```python
import torch

def _softmax(x, axis=-1):
    return torch.softmax(x, dim=axis)
def _gelu(x):
    return torch.nn.functional.gelu(x, approximate="tanh")
def _silu(x):
    return torch.nn.functional.silu(x)
def _sigmoid(x):
    return torch.sigmoid(x)
def _layernorm(x, axis=-1, eps=1e-5):
    m = x.mean(dim=axis, keepdim=True)
    v = x.var(dim=axis, keepdim=True, unbiased=False)
    return (x - m) / torch.sqrt(v + eps)
def _scatter_add(tgt, idx, src, axis=0):
    return tgt.clone().index_add_(axis, idx, src)
def _segment_sum(src, seg, n):
    out = torch.zeros((n,) + src.shape[1:], dtype=src.dtype)
    return out.index_add_(0, seg, src)
def _pad(x, pw):
    flat = tuple(v for pair in reversed(pw) for v in pair)
    return torch.nn.functional.pad(x, flat)

kernel_name = 'interleave_layers__spectral_gfnet__grouped_kv_mqa__prompt'

def setup_inputs():

    _w = (lambda a: (lambda t: t.to(torch.bfloat16) if t.is_floating_point() else t)(torch.as_tensor(a)))
    def _A_setup():
        B, S, D = 1, 2048, 512
        Df = D // 2 + 1
        return {
            "x": _w((lambda *s: torch.randn(*s, dtype=torch.bfloat16) * 0.05)(B, S, D)),
            "w_r": _w((lambda *s: torch.randn(*s, dtype=torch.bfloat16) * 0.8)(S, Df) * 0.02),
            "w_i": _w((lambda *s: torch.randn(*s, dtype=torch.bfloat16) * 0.8)(S, Df) * 0.02),
        }
    _A_in = _A_setup()

    def _B_setup():
        B, S, D, H = 1, 2048, 512, 32
        Dh = D // H
        return {
            "x": _w((lambda *s: torch.randn(*s, dtype=torch.bfloat16) * 0.05)(B, S, D)),
            "wq": _w((lambda *s: torch.randn(*s, dtype=torch.bfloat16) * 0.05)(D, H * Dh)),
            "wk": _w((lambda *s: torch.randn(*s, dtype=torch.bfloat16) * 0.05)(D, Dh)),
            "wv": _w((lambda *s: torch.randn(*s, dtype=torch.bfloat16) * 0.05)(D, Dh)),
            "wo": _w((lambda *s: torch.randn(*s, dtype=torch.bfloat16) * 0.05)(H * Dh, D)),
        }
    _B_in = _B_setup()

    return {
        "A_x": _A_in["x"],
        "A_w_r": _A_in["w_r"],
        "A_w_i": _A_in["w_i"],
        "B_x": _B_in["x"],
        "B_wq": _B_in["wq"],
        "B_wk": _B_in["wk"],
        "B_wv": _B_in["wv"],
        "B_wo": _B_in["wo"],
    }

def reference(A_x, A_w_r, A_w_i, B_x, B_wq, B_wk, B_wv, B_wo):
    def _A_ref(x, w_r, w_i):
        B, S, D = x.shape
        X = (lambda x, axes=(-2,-1): torch.fft.rfft2(x.float(), dim=axes))(x, axes=(-2, -1))
        W = w_r + 1j * w_i
        X = X * W
        y = (lambda x, s, axes=(-2,-1): torch.fft.irfft2(x, s=s, dim=axes).to(torch.bfloat16))(X, (S, D), axes=(-2, -1))
        return y

    def _B_ref(x, wq, wk, wv, wo):
        B, S, D = x.shape
        H = 32
        Dh = D // H
        q = (lambda x, *d: x.permute(*d))((x @ wq).reshape(B, S, H, Dh), 0, 2, 1, 3)
        k = x @ wk
        v = x @ wv
        scores = torch.einsum("bhqd,bkd->bhqk", q, k) * (Dh ** -0.5)
        p = _softmax(scores, axis=-1)
        o = torch.einsum("bhqk,bkd->bhqd", p, v)
        return (lambda x, *d: x.permute(*d))(o, 0, 2, 1, 3).reshape(B, S, H * Dh) @ wo

    x = A_x
    x = x + _A_ref(_layernorm(x, axis=-1), A_w_r, A_w_i)
    x = x + _B_ref(_layernorm(x, axis=-1), B_wq, B_wk, B_wv, B_wo)
    x = x + _A_ref(_layernorm(x, axis=-1), A_w_r, A_w_i)
    x = x + _B_ref(_layernorm(x, axis=-1), B_wq, B_wk, B_wv, B_wo)
    return x
```

```cpp
#include <hip/hip_runtime.h>
#include <hip/hip_bf16.h>
#include <stdint.h>

typedef unsigned u32;
typedef u32 v2u __attribute__((ext_vector_type(2)));
typedef u32 v4u __attribute__((ext_vector_type(4)));
typedef u32 v8u __attribute__((ext_vector_type(8)));
typedef float f2 __attribute__((ext_vector_type(2)));
typedef float f4 __attribute__((ext_vector_type(4)));
typedef float f8 __attribute__((ext_vector_type(8)));
typedef float f16v __attribute__((ext_vector_type(16)));
typedef _Float16 h2 __attribute__((ext_vector_type(2)));
typedef _Float16 h8 __attribute__((ext_vector_type(8)));
typedef __bf16 b2 __attribute__((ext_vector_type(2)));
typedef __bf16 b8 __attribute__((ext_vector_type(8)));
typedef __bf16 b16v __attribute__((ext_vector_type(16)));
typedef short s4 __attribute__((ext_vector_type(4)));
#define LAS __attribute__((address_space(3)))
#if defined(__HIP_DEVICE_COMPILE__)
#define NOPK __attribute__((target("no-packed-fp32-ops")))
#else
#define NOPK
#endif
#define DEV NOPK __device__ __forceinline__

constexpr int NWG = 256, NT = 512;
constexpr u32 SENT = 0xFFFFFFFFu;
constexpr u32 SPIN_CAP = 1u << 16;
constexpr u32 PSTR = 8192 + 128;
constexpr u32 ZB_SZ = 256 * PSTR, YB_SZ = 256 * PSTR, KV_SZ = 256 * 512, PO_SZ = 256 * 3 * 8192, XH_SZ = 256 * 8192;
constexpr u32 LAYER_SZ = ZB_SZ + YB_SZ + KV_SZ + PO_SZ;
constexpr u32 OFF_ZB = 0, OFF_YB = ZB_SZ, OFF_KV = OFF_YB + YB_SZ, OFF_PO = OFF_KV + KV_SZ;
constexpr u32 OFF_XH = 2 * LAYER_SZ;
constexpr u32 OFF_WQ = OFF_XH + 2 * XH_SZ;
constexpr u32 OFF_WO = OFF_WQ + 34 * 16384;
constexpr u32 OFF_X0 = OFF_WO + 524288;
constexpr u32 WS_USED = OFF_X0 + 256 * 16384;
constexpr int WQ_LDS_KS = 14;
constexpr u32 L_WQ = 0;
constexpr u32 L_R = 8 * WQ_LDS_KS * 1024;
constexpr u32 L_XN = L_R, L_OP = L_R, L_ZI = L_R, L_YO = L_R + 16384;
constexpr u32 L_KP = L_R + 32768, L_Q = L_R + 32768;
constexpr u32 L_SS = L_R + 40960;
constexpr u32 L_LP = L_SS + 1024;
constexpr u32 L_OS = L_LP + 2304;
constexpr u32 L_MISC = L_OS + 2176;
constexpr u32 LDS_BYTES = 163840;
static_assert(L_MISC + 256 <= LDS_BYTES, "lds");

extern __shared__ __attribute__((aligned(16))) char smem_raw[];
template <class T> DEV LAS T* lds(u32 off) { return (LAS T*)((LAS char*)smem_raw + off); }

DEV f4 mfma16h(v4u a, v4u b, f4 c) { return __builtin_amdgcn_mfma_f32_16x16x32_f16(__builtin_bit_cast(h8, a), __builtin_bit_cast(h8, b), c, 0, 0, 0); }
DEV f4 mfma16b(v4u a, v4u b, f4 c) { return __builtin_amdgcn_mfma_f32_16x16x32_bf16(__builtin_bit_cast(b8, a), __builtin_bit_cast(b8, b), c, 0, 0, 0); }
DEV f16v mfma32h(v4u a, v4u b, f16v c) { return __builtin_amdgcn_mfma_f32_32x32x16_f16(__builtin_bit_cast(h8, a), __builtin_bit_cast(h8, b), c, 0, 0, 0); }
DEV f16v mfma32b(v4u a, v4u b, f16v c) { return __builtin_amdgcn_mfma_f32_32x32x16_bf16(__builtin_bit_cast(b8, a), __builtin_bit_cast(b8, b), c, 0, 0, 0); }
DEV f16v smf32(v4u a, f8 b, f16v c, u32 idx) { return __builtin_amdgcn_smfmac_f32_32x32x32_bf16(__builtin_bit_cast(b8, a), __builtin_bit_cast(b16v, b), c, idx, 0, 0); }
DEV f4 smf16(v4u a, v8u b, f4 c, u32 idx) { return __builtin_amdgcn_smfmac_f32_16x16x64_bf16(__builtin_bit_cast(b8, a), __builtin_bit_cast(b16v, b), c, idx, 0, 0); }
DEV u32 pkh(float a, float b) { f2 v = {a, b}; return __builtin_bit_cast(u32, __builtin_convertvector(v, h2)); }
DEV u32 pkb(float a, float b) { f2 v = {a, b}; return __builtin_bit_cast(u32, __builtin_convertvector(v, b2)); }
DEV float bflo(u32 x) { return __builtin_bit_cast(float, x << 16); }
DEV float bfhi(u32 x) { return __builtin_bit_cast(float, x & 0xffff0000u); }
DEV float cosr(u32 num, u32 den) { return __builtin_amdgcn_cosf((float)(num & (den - 1)) * (1.0f / (float)den)); }
DEV float sinr(u32 num, u32 den) { return __builtin_amdgcn_sinf((float)(num & (den - 1)) * (1.0f / (float)den)); }
template <int CTRL> DEV float dppf(float x) { return __builtin_bit_cast(float, __builtin_amdgcn_update_dpp(0, __builtin_bit_cast(int, x), CTRL, 0xf, 0xf, true)); }
DEV float row_sum(float x) { x += dppf<0x128>(x); x += dppf<0x124>(x); x += dppf<0x122>(x); x += dppf<0x121>(x); return x; }
DEV float row_max(float x) { x = __builtin_fmaxf(x, dppf<0x128>(x)); x = __builtin_fmaxf(x, dppf<0x124>(x)); x = __builtin_fmaxf(x, dppf<0x122>(x)); x = __builtin_fmaxf(x, dppf<0x121>(x)); return x; }
DEV f2 swap16(float x) { u32 u = __builtin_bit_cast(u32, x); auto r = __builtin_amdgcn_permlane16_swap(u, u, false, false); return f2{__builtin_bit_cast(float, (u32)r[0]), __builtin_bit_cast(float, (u32)r[1])}; }
DEV f2 swap32(float x) { u32 u = __builtin_bit_cast(u32, x); auto r = __builtin_amdgcn_permlane32_swap(u, u, false, false); return f2{__builtin_bit_cast(float, (u32)r[0]), __builtin_bit_cast(float, (u32)r[1])}; }
DEV float wave_sum(float x) { x = row_sum(x); f2 a = swap16(x); x = a[0] + a[1]; a = swap32(x); return a[0] + a[1]; }
DEV float wave_max(float x) { x = row_max(x); f2 a = swap16(x); x = __builtin_fmaxf(a[0], a[1]); a = swap32(x); return __builtin_fmaxf(a[0], a[1]); }
DEV u32 first_lane(u32 x) { u32 r; asm volatile("s_nop 1\n\tv_readfirstlane_b32 %0, %1\n\ts_nop 4" : "=s"(r) : "v"(x)); return r; }
DEV void bar() { asm volatile("s_waitcnt lgkmcnt(0)\n\ts_barrier" ::: "memory"); }
DEV void lds_fence() { asm volatile("s_waitcnt lgkmcnt(0)" ::: "memory"); }
DEV bool any_lane(bool c) { return __builtin_amdgcn_ballot_w64(c) != 0; }
DEV u32 umin(u32 a, u32 b) { return a < b ? a : b; }
DEV u32 umax(u32 a, u32 b) { return a > b ? a : b; }
DEV v4u unsent(v4u v) { return v4u{umin(v[0], SENT - 1), umin(v[1], SENT - 1), umin(v[2], SENT - 1), umin(v[3], SENT - 1)}; }
DEV u32 umax4(v4u v) { return umax(umax(v[0], v[1]), umax(v[2], v[3])); }

typedef __amdgpu_buffer_rsrc_t rsrc_t;
constexpr int SC1 = 16;
DEV v4u ld16(rsrc_t rs, u32 voff, u32 soff) { return __builtin_amdgcn_raw_buffer_load_b128(rs, voff, soff, SC1); }
DEV void st16(rsrc_t rs, u32 voff, u32 soff, v4u v) { __builtin_amdgcn_raw_buffer_store_b128(v, rs, voff, soff, SC1); asm volatile("s_nop 2" :: "v"(v) : "memory"); }
DEV void st8(rsrc_t rs, u32 voff, u32 soff, v2u v) { __builtin_amdgcn_raw_buffer_store_b64(v, rs, voff, soff, SC1); }
DEV void sleepn(int n) { for (int i = 0; i < n; ++i) __builtin_amdgcn_s_sleep(1); }

DEV int fresh_lane() { int l; asm volatile("v_mbcnt_lo_u32_b32 %0, -1, 0\n\tv_mbcnt_hi_u32_b32 %0, -1, %0" : "=v"(l)); return l; }
DEV int fresh_s(int x) { asm volatile("" : "+s"(x)); return x; }
#define PHASE_IDS \
  const int lane = fresh_lane(), w = fresh_s(w_), b = fresh_s(b_), tid = w * 64 + lane, i16 = lane & 15, g = lane >> 4; \
  const int gam = (b & 7) + 8 * (b >> 5), j = (b >> 3) & 3, n2 = 4 * gam + j, k1 = b & 7, cg = b >> 3; \
  (void)tid; (void)i16; (void)g; (void)gam; (void)j; (void)n2; (void)k1; (void)cg;

struct Params {
  const unsigned short* x; const unsigned short* wr; const unsigned short* wi;
  const unsigned short* wq; const unsigned short* wk; const unsigned short* wv; const unsigned short* wo;
  unsigned short* out; char* ws;
};

DEV void row_fft2(const float (&v)[2][8], float eps, u32 stage, int lane, float (&z)[2][8], float (&sig)[2], float (&mu)[2]) {
  const int cp = lane & 31, h = lane >> 5;
  v4u fc0, fs0, fc1, fs1;
  {
    float c[8], s[8];
#pragma unroll
    for (int j = 0; j < 8; ++j) { c[j] = cosr((8 * h + j) * cp, 32); s[j] = -sinr((8 * h + j) * cp, 32); }
    const u32 sg = (cp & 1) ? 0x80008000u : 0u;
#pragma unroll
    for (int j = 0; j < 4; ++j) { fc0[j] = pkh(c[2 * j], c[2 * j + 1]); fs0[j] = pkh(s[2 * j], s[2 * j + 1]); fc1[j] = fc0[j] ^ sg; fs1[j] = fs0[j] ^ sg; }
  }
  float tc[8], ts[8];
  v4u a2r, a2i;
  {
    float c[8], s[8];
    const int d = cp >> 1, ri = cp & 1;
#pragma unroll
    for (int j = 0; j < 8; ++j) {
      const int b = 8 * (j >> 2) + 4 * h + (j & 3);
      tc[j] = cosr(b * cp, 512) * 0.0625f; ts[j] = sinr(b * cp, 512) * 0.0625f;
      const float cc = cosr(b * d, 16), ss = sinr(b * d, 16);
      c[j] = ri ? -ss : cc; s[j] = ri ? cc : ss;
    }
#pragma unroll
    for (int j = 0; j < 4; ++j) { a2r[j] = pkh(c[2 * j], c[2 * j + 1]); a2i[j] = pkh(s[2 * j], s[2 * j + 1]); }
  }
#pragma unroll
  for (int p = 0; p < 2; ++p) {
    const float piv = __builtin_bit_cast(float, first_lane(__builtin_bit_cast(u32, v[p][0])));
    float u[8], s1 = 0.f, s2 = 0.f;
#pragma unroll
    for (int i = 0; i < 8; ++i) { u[i] = v[p][i] - piv; s1 += u[i]; s2 += u[i] * u[i]; }
    s1 = wave_sum(s1) * (1.0f / 512.0f); s2 = wave_sum(s2) * (1.0f / 512.0f);
    const float var = __builtin_fmaxf(s2 - s1 * s1, 0.f) + eps;
    const float rs = __builtin_amdgcn_rsqf(var);
    sig[p] = var * rs; mu[p] = s1 + piv;
    v4u o;
#pragma unroll
    for (int i = 0; i < 4; ++i) o[i] = pkh((u[2 * i] - s1) * rs, (u[2 * i + 1] - s1) * rs);
    *lds<v4u>(stage + p * 1152 + lane * 16) = o;
  }
  lds_fence();
  v4u a0, a1;
  {
    const u32 base = stage + ((lane >> 4) & 1) * 1152 + h * 256 + (lane & 15) * 8;
    v2u t0 = __builtin_bit_cast(v2u, __builtin_amdgcn_ds_read_tr16_b64_v4i16(lds<s4>(base)));
    v2u t1 = __builtin_bit_cast(v2u, __builtin_amdgcn_ds_read_tr16_b64_v4i16(lds<s4>(base + 128)));
    v2u t2 = __builtin_bit_cast(v2u, __builtin_amdgcn_ds_read_tr16_b64_v4i16(lds<s4>(base + 512)));
    v2u t3 = __builtin_bit_cast(v2u, __builtin_amdgcn_ds_read_tr16_b64_v4i16(lds<s4>(base + 640)));
    a0 = v4u{t0[0], t0[1], t1[0], t1[1]}; a1 = v4u{t2[0], t2[1], t3[0], t3[1]};
  }
  const f16v zero = {0};
  f16v dr = mfma32h(a0, fc0, zero), di = mfma32h(a0, fs0, zero);
  dr = mfma32h(a1, fc1, dr); di = mfma32h(a1, fs1, di);
#pragma unroll
  for (int p = 0; p < 2; ++p) {
    float tr[8], ti[8];
#pragma unroll
    for (int j = 0; j < 8; ++j) { tr[j] = dr[8 * p + j] * tc[j] + di[8 * p + j] * ts[j]; ti[j] = di[8 * p + j] * tc[j] - dr[8 * p + j] * ts[j]; }
    v4u br, bi;
#pragma unroll
    for (int j = 0; j < 4; ++j) { br[j] = pkh(tr[2 * j], tr[2 * j + 1]); bi[j] = pkh(ti[2 * j], ti[2 * j + 1]); }
    f16v o = mfma32h(a2r, br, zero); o = mfma32h(a2i, bi, o);
#pragma unroll
    for (int r = 0; r < 8; ++r) z[p][r] = o[r];
    if (lane == 0) { z[p][0] = o[8] * 0.70710678f; z[p][1] = 0.f; }
  }
}

DEV void z_publish(rsrc_t rs, u32 zoff, v4u a, int n2, int w, int lane) {
  const int g = lane >> 4, k1 = lane & 7, tp = (lane >> 3) & 1;
  const float c0 = cosr(2 * g * k1, 8) * 0.35355339f, s0 = sinr(2 * g * k1, 8) * 0.35355339f;
  const float c1 = cosr((2 * g + 1) * k1, 8) * 0.35355339f, s1 = sinr((2 * g + 1) * k1, 8) * 0.35355339f;
  const u32 r0 = pkh(c0, s0), r1 = pkh(c1, s1), i0 = pkh(-s0, c0), i1 = pkh(-s1, c1);
  const v4u bre = tp ? v4u{0, 0, r0, r1} : v4u{r0, r1, 0, 0};
  const v4u bim = tp ? v4u{0, 0, i0, i1} : v4u{i0, i1, 0, 0};
  const f4 zero = {0};
  const f4 dr = mfma16h(a, bre, zero), di = mfma16h(a, bim, zero);
  v4u o = {pkh(dr[0], di[0]), pkh(dr[1], di[1]), pkh(dr[2], di[2]), pkh(dr[3], di[3])};
  st16(rs, k1 * 1024 + w * 128 + tp * 64 + g * 16, zoff + n2 * PSTR, unsent(o));
}

DEV void prologue(const Params& p, rsrc_t rs, int b, int n2, int w, int lane, f4 (&X)[2]) {
  const int tid = w * 64 + lane;
  constexpr u32 L_ST = 0, L_XT = 20480, L_WT = 40960, L_SG = 51200;
  constexpr u32 XT_STR = 2048 + 64, WT_STR = 1024 + 16;
  float v[2][8];
  bool active = true;
  if (w < 4) {
#pragma unroll
    for (int q = 0; q < 2; ++q) {
      const v4u u = *(const v4u*)(p.x + (size_t)(n2 + 256 * (2 * w + q)) * 512 + lane * 8);
#pragma unroll
      for (int i = 0; i < 4; ++i) { v[q][2 * i] = bflo(u[i]); v[q][2 * i + 1] = bfhi(u[i]); }
    }
  } else if (b < 64) {
#pragma unroll
    for (int q = 0; q < 2; ++q) {
      const v4u u = *(const v4u*)(p.wo + (size_t)(8 * b + 2 * (w - 4) + q) * 512 + lane * 8);
#pragma unroll
      for (int i = 0; i < 4; ++i) { v[q][2 * i] = bflo(u[i]); v[q][2 * i + 1] = bfhi(u[i]); }
    }
  } else if (b < 132) {
    const int cb = b < 128 ? ((b & 7) * 8 + ((b - 64) >> 3)) : (b & 1);
    const unsigned short* src = b < 128 ? p.wq : (b < 130 ? p.wk : p.wv);
    const int str = b < 128 ? 512 : 16;
#pragma unroll
    for (int i = 0; i < 8; ++i) {
      const u32 u = *(const u32*)(src + (size_t)(8 * lane + i) * str + 8 * cb + 2 * (w - 4));
      v[0][i] = bflo(u); v[1][i] = bfhi(u);
    }
  } else active = false;
  if (active) {
    float z[2][8], sig[2], mu[2];
    row_fft2(v, w < 4 ? 1e-5f : 1e-30f, L_ST + w * 2304, lane, z, sig, mu);
    const int cp = lane & 31, h = lane >> 5;
#pragma unroll
    for (int q = 0; q < 2; ++q) {
      const int row = 2 * (w & 3) + q;
      if (w < 4) {
#pragma unroll
        for (int e = 0; e < 4; ++e) *lds<f2>(L_XT + row * XT_STR + (cp + 32 * (4 * (e >> 1) + 2 * h + (e & 1))) * 8) = f2{z[q][2 * e], z[q][2 * e + 1]};
        if (lane == 0) *lds<float>(L_SG + row * 4) = sig[q];
      } else {
#pragma unroll
        for (int e = 0; e < 4; ++e) {
          float re = z[q][2 * e] * sig[q], im = z[q][2 * e + 1] * sig[q];
          if (e == 0 && lane == 0 && b < 64) im = mu[q] * 22.627417f;
          *lds<u32>(L_WT + row * WT_STR + (cp + 32 * (4 * (e >> 1) + 2 * h + (e & 1))) * 4) = pkb(re, im);
        }
      }
    }
  }
  bar();
  {
    const int i = lane & 15, g = lane >> 4;
    const f2 sg = *lds<f2>(L_SG + g * 8);
    v4u a;
#pragma unroll
    for (int t = 0; t < 2; ++t) {
      const f2 z0 = *lds<f2>(L_XT + (2 * g) * XT_STR + (32 * w + 16 * t + i) * 8);
      const f2 z1 = *lds<f2>(L_XT + (2 * g + 1) * XT_STR + (32 * w + 16 * t + i) * 8);
      a[2 * t] = pkh(z0[0], z0[1]); a[2 * t + 1] = pkh(z1[0], z1[1]);
      X[t] = f4{z0[0] * sg[0], z0[1] * sg[0], z1[0] * sg[1], z1[1] * sg[1]};
    }
    z_publish(rs, OFF_ZB, a, n2, w, lane);
  }
  if (b < 64) {
    const int gs = tid >> 8, col = tid & 255, k = 8 * b + 4 * gs;
    v4u o;
#pragma unroll
    for (int e = 0; e < 4; ++e) o[e] = *lds<u32>(L_WT + (4 * gs + e) * WT_STR + col * 4);
    const u32 off = ((((((k >> 7) * 8 + (col >> 5)) * 2 + ((col >> 4) & 1)) * 4 + ((k >> 5) & 3)) * 2 + ((k >> 4) & 1)) * 64 + ((k >> 2) & 3) * 16 + (col & 15)) * 16;
    st16(rs, off, OFF_WO, o);
  } else if (b < 132) {
    const int cb = b < 128 ? ((b & 7) * 8 + ((b - 64) >> 3)) : (64 + (b - 128));
    const int r8 = tid & 7, pc = tid >> 3, col = 8 * cb + r8;
    const v4u o = *lds<v4u>(L_WT + r8 * WT_STR + pc * 16);
    st16(rs, (((col >> 4) * 16 + (pc >> 2)) * 64 + (pc & 3) * 16 + (col & 15)) * 16, OFF_WQ, o);
  }
}

NOPK __global__ void __launch_bounds__(NT, 2) k_pro(Params p) {
  const int b = blockIdx.x, lane = threadIdx.x & 63, w = __builtin_amdgcn_readfirstlane(threadIdx.x >> 6);
  const int n2 = 4 * ((b & 7) + 8 * (b >> 5)) + ((b >> 3) & 3);
  const rsrc_t rs = __builtin_amdgcn_make_buffer_rsrc(p.ws, 0, WS_USED, 0x00020000);
  f4 X[2];
  prologue(p, rs, b, n2, w, lane, X);
  const int tid = threadIdx.x;
  st16(rs, tid * 16, OFF_X0 + b * 16384, __builtin_bit_cast(v4u, X[0]));
  st16(rs, tid * 16 + 8192, OFF_X0 + b * 16384, __builtin_bit_cast(v4u, X[1]));
  st16(rs, tid * 16, OFF_XH + n2 * 8192, v4u{pkb(X[0][0], X[0][1]), pkb(X[0][2], X[0][3]), pkb(X[1][0], X[1][1]), pkb(X[1][2], X[1][3])});
}

DEV f16v attn_tile(const v4u (&K)[8], const v4u (&V)[16], v4u q) {
  const f16v zero = {0};
  const u32 idx = 0xDDDDDDDDu;
  f16v acc = zero;
  f16v cur = mfma32b(K[0], q, zero), nxt = zero;
  f8 hi = {0};
#define EX(i) cur[i] = __builtin_amdgcn_exp2f(cur[i])
#define SB __builtin_amdgcn_sched_barrier(0)
#pragma unroll
  for (int kb = 0; kb < 8; ++kb) {
    EX(0); EX(1); SB;
    if (kb > 0) acc = smf32(V[2 * kb - 1], hi, acc, idx);
    SB; EX(2); EX(3); EX(4); EX(5); EX(6); SB;
    if (kb < 7) nxt = mfma32b(K[kb + 1], q, zero);
    SB; EX(7); EX(8); EX(9); EX(10); EX(11); SB;
    acc = smf32(V[2 * kb], __builtin_shufflevector(cur, cur, 0, 1, 2, 3, 4, 5, 6, 7), acc, idx);
    SB; EX(12); EX(13); EX(14); EX(15); SB;
    hi = __builtin_shufflevector(cur, cur, 8, 9, 10, 11, 12, 13, 14, 15);
    cur = nxt;
  }
  acc = smf32(V[15], hi, acc, idx);
#undef EX
  return acc;
}
DEV f16v attn_tile_shifted(const v4u (&K)[8], const v4u (&V)[16], v4u q, float m) {
  const f16v zero = {0};
  f16v acc = zero;
#pragma unroll
  for (int kb = 0; kb < 8; ++kb) {
    f16v s = mfma32b(K[kb], q, zero);
#pragma unroll
    for (int r = 0; r < 16; ++r) s[r] = __builtin_amdgcn_exp2f(s[r] - m);
    acc = smf32(V[2 * kb], __builtin_shufflevector(s, s, 0, 1, 2, 3, 4, 5, 6, 7), acc, 0xDDDDDDDDu);
    acc = smf32(V[2 * kb + 1], __builtin_shufflevector(s, s, 8, 9, 10, 11, 12, 13, 14, 15), acc, 0xDDDDDDDDu);
    __builtin_amdgcn_sched_barrier(0);
  }
  return acc;
}
DEV void put_partial(const f16v& acc, int tau, int w, int lane) {
  *lds<f4>(L_OP + ((tau * 8 + w) * 2 + 0) * 1024 + lane * 16) = f4{acc[0], acc[1], acc[2], acc[3]};
  *lds<f4>(L_OP + ((tau * 8 + w) * 2 + 1) * 1024 + lane * 16) = f4{acc[4], acc[5], acc[6], acc[7]};
  if (lane < 32) *lds<float>(L_LP + (tau * 32 + lane) * 36 + w * 4) = acc[8];
}
template <bool CHECK>
DEV void reduce_pass(int w, int lane) {
  const int tau = w >> 1, quad = w & 1, col = lane & 31, h = lane >> 5;
  f4 s = {0}; float l = 0.f;
#pragma unroll
  for (int u = 0; u < 8; ++u) { s += *lds<f4>(L_OP + ((tau * 8 + u) * 2 + quad) * 1024 + lane * 16); l += *lds<float>(L_LP + (tau * 32 + col) * 36 + u * 4); }
  const float r = __builtin_amdgcn_rcpf(l);
  s *= r;
  if (CHECK) {
    const float mx = __builtin_fmaxf(__builtin_fmaxf(__builtin_fabsf(s[0]), __builtin_fabsf(s[1])), __builtin_fmaxf(__builtin_fabsf(s[2]), __builtin_fabsf(s[3])));
    const bool ok = (l > 9.094947e-13f) && (l < 1.0995116e12f) && (mx < 3.0e38f);
    if (!ok) *lds<u32>(L_MISC) = 1u;
  }
  *lds<v2u>(L_OS + (col & 7) * 272 + (4 * tau + (col >> 3)) * 32 + quad * 16 + h * 8) = v2u{pkb(s[0], s[1]), pkb(s[2], s[3])};
}

NOPK __global__ void __launch_bounds__(NT, 2) k_main(Params p) {
  const int b_ = blockIdx.x, w_ = __builtin_amdgcn_readfirstlane(threadIdx.x >> 6);
  const rsrc_t rs = __builtin_amdgcn_make_buffer_rsrc(p.ws, 0, WS_USED, 0x00020000);
  const f4 zero4 = {0};

  f4 X[2]; v8u WO[2][4]; v4u WQR[16 - WQ_LDS_KS]; v4u WF; float wscale;
  {
  PHASE_IDS
  X[0] = __builtin_bit_cast(f4, ld16(rs, tid * 16, OFF_X0 + b * 16384));
  X[1] = __builtin_bit_cast(f4, ld16(rs, tid * 16 + 8192, OFF_X0 + b * 16384));
#pragma unroll
  for (int t = 0; t < 2; ++t)
#pragma unroll
    for (int ks = 0; ks < 4; ++ks) {
      const u32 so = OFF_WO + (((j * 8 + w) * 2 + t) * 4 + ks) * 2048;
      const v4u a = ld16(rs, lane * 16, so), c = ld16(rs, lane * 16 + 1024, so);
      WO[t][ks] = __builtin_shufflevector(a, c, 0, 1, 2, 3, 4, 5, 6, 7);
    }
#pragma unroll
  for (int ks = 0; ks < 16; ++ks) {
    const v4u a = ld16(rs, lane * 16, OFF_WQ + ((8 * j + w) * 16 + ks) * 1024);
    if (ks < WQ_LDS_KS) *lds<v4u>(L_WQ + (w * WQ_LDS_KS + ks) * 1024 + lane * 16) = a; else WQR[ks - WQ_LDS_KS] = a;
  }
  {
    const int col = 8 * cg + w, wc = col == 0 ? 256 : col;
    float mx = 0.f; float wr[4], wi[4];
#pragma unroll
    for (int r = 0; r < 4; ++r) {
      const size_t o = (size_t)(k1 + 8 * (i16 + 16 * (4 * g + r))) * 257 + wc;
      const u32 a = p.wr[o], c = p.wi[o];
      WF[r] = a | (c << 16);
      wr[r] = bflo(WF[r]); wi[r] = bfhi(WF[r]);
      mx = __builtin_fmaxf(mx, __builtin_fmaxf(__builtin_fabsf(wr[r]), __builtin_fabsf(wi[r])));
    }
    mx = wave_max(mx);
    u32 e = __builtin_bit_cast(u32, mx) & 0x7f800000u;
    e = umin(umax(e, 0x10000000u), 0x6f000000u);
    wscale = __builtin_bit_cast(float, first_lane(e));
  }
  if (tid == 0) *lds<u32>(L_MISC) = 0u;
  }

#pragma unroll 1
  for (int L = 0; L < 2; ++L) {
    const u32 lo = L * LAYER_SZ;
    {
      PHASE_IDS
      v4u f1r, f1i, f2r, f2i, g2r, g2i, g1r, g1i; float tfc[4], tfs[4], tic[4], tis[4], wr[4], wi[4];
      asm volatile("" : "+v"(WF), "+s"(wscale));
      const float winv = __builtin_amdgcn_rcpf(wscale) * 0.5f;
#pragma unroll
      for (int e = 0; e < 4; ++e) {
        const int y = 4 * g + e;
        float c = cosr(y * (k1 + 8 * i16), 128) * 0.25f, s = sinr(y * (k1 + 8 * i16), 128) * 0.25f;
        f1r[e] = pkh(c, s); f1i[e] = pkh(-s, c);
        c = cosr(y * i16, 16) * 0.25f; s = sinr(y * i16, 16) * 0.25f;
        f2r[e] = pkh(c, s); f2i[e] = pkh(-s, c); g2r[e] = pkh(c, -s); g2i[e] = pkh(s, c);
        c = cosr(i16 * (k1 + 8 * y), 128) * 0.25f; s = sinr(i16 * (k1 + 8 * y), 128) * 0.25f;
        g1r[e] = pkh(c, -s); g1i[e] = pkh(s, c);
        tfc[e] = cosr(y * (k1 + 8 * i16), 2048); tfs[e] = sinr(y * (k1 + 8 * i16), 2048);
        tic[e] = cosr(i16 * (k1 + 8 * y), 2048); tis[e] = sinr(i16 * (k1 + 8 * y), 2048);
        wr[e] = bflo(WF[e]) * winv; wi[e] = bfhi(WF[e]) * winv;
      }
      const int pp = tid >> 1, lam = pp >> 2;
      const int src = 16 * (4 * (lam >> 4) + (pp & 3)) + (lam & 15);
      const u32 zo = src * PSTR + (tid & 1) * 16, zs = lo + OFF_ZB + k1 * 1024 + cg * 32;
      v4u zin;
      for (u32 sp = 0; sp < SPIN_CAP; ++sp) {
        asm volatile("" ::: "memory");
        zin = ld16(rs, zo, zs);
        if (!any_lane(umax4(zin) == SENT)) break;
      }
#pragma unroll
      for (int e = 0; e < 4; ++e) *lds<u32>(L_ZI + (4 * (tid & 1) + e) * 1040 + pp * 4) = zin[e];
      bar();
      v4u a = *lds<v4u>(L_ZI + w * 1040 + lane * 16);
      f4 dr = mfma16h(a, f1r, zero4), di = mfma16h(a, f1i, zero4);
#pragma unroll
      for (int e = 0; e < 4; ++e) a[e] = pkh(dr[e] * tfc[e] + di[e] * tfs[e], di[e] * tfc[e] - dr[e] * tfs[e]);
      dr = mfma16h(f2r, a, zero4); di = mfma16h(f2i, a, zero4);
#pragma unroll
      for (int e = 0; e < 4; ++e) a[e] = pkh(dr[e] * wr[e] - di[e] * wi[e], dr[e] * wi[e] + di[e] * wr[e]);
      dr = mfma16h(a, g2r, zero4); di = mfma16h(a, g2i, zero4);
#pragma unroll
      for (int e = 0; e < 4; ++e) a[e] = pkh(dr[e] * tic[e] - di[e] * tis[e], dr[e] * tis[e] + di[e] * tic[e]);
      dr = mfma16h(a, g1r, zero4); di = mfma16h(a, g1i, zero4);
      const float os = wscale * (2.0f * 0.35355339f);
#pragma unroll
      for (int e = 0; e < 4; ++e) a[e] = pkb(dr[e] * os, di[e] * os);
      const int gp = 4 * i16 + g;
      *lds<v4u>(L_YO + gp * 128 + ((w ^ (i16 & 7)) * 16)) = unsent(a);
      bar();
      const v4u o = *lds<v4u>(L_YO + (tid >> 3) * 128 + (((tid & 7) ^ ((tid >> 5) & 7)) * 16));
      st16(rs, tid * 16, lo + OFF_YB + b * PSTR, o);
    }
    {
      PHASE_IDS
      v4u wkv[2][2];
#pragma unroll
      for (int m = 0; m < 2; ++m)
#pragma unroll
        for (int s = 0; s < 2; ++s) wkv[m][s] = ld16(rs, lane * 16, OFF_WQ + ((32 + m) * 16 + 2 * w + s) * 1024);
      v4u ainv;
      {
        const int n1 = i16 >> 1, ri = i16 & 1;
        const float c0 = cosr(n1 * 2 * g, 8), s0 = sinr(n1 * 2 * g, 8), c1 = cosr(n1 * (2 * g + 1), 8), s1 = sinr(n1 * (2 * g + 1), 8);
        ainv[0] = ri ? pkb(s0, c0) : pkb(c0, -s0); ainv[1] = ri ? pkb(s1, c1) : pkb(c1, -s1);
        const u32 one = ri ? 0x3f800000u : 0x00003f80u;
        ainv[2] = n1 == 2 * g ? one : 0u; ainv[3] = n1 == 2 * g + 1 ? one : 0u;
      }
      sleepn(8);
      v4u y[2][2];
      for (u32 sp = 0; sp < SPIN_CAP; ++sp) {
        asm volatile("" ::: "memory");
        u32 m = 0;
#pragma unroll
        for (int t = 0; t < 2; ++t)
#pragma unroll
          for (int e = 0; e < 2; ++e) {
            const int col = 32 * w + 16 * t + i16;
            y[t][e] = ld16(rs, ((col >> 3) * 8 + 2 * g + e) * PSTR + (col & 7) * 16, lo + OFF_YB + gam * 128);
            m = umax(m, umax4(y[t][e]));
          }
        if (!any_lane(m == SENT)) break;
      }
      const bool dcl = (w == 0) && (i16 == 0);
#pragma unroll
      for (int t = 0; t < 2; ++t) {
        const u32 y0 = j == 0 ? y[t][0][0] : j == 1 ? y[t][0][1] : j == 2 ? y[t][0][2] : y[t][0][3];
        const u32 y1 = j == 0 ? y[t][1][0] : j == 1 ? y[t][1][1] : j == 2 ? y[t][1][2] : y[t][1][3];
        f4 d = mfma16b(ainv, v4u{y0, y1, 0, 0}, X[t]);
        u32 u0 = pkb(d[0], d[1]), u1 = pkb(d[2], d[3]);
        if (t == 0 && dcl) { d[1] = X[0][1]; d[3] = X[0][3]; u0 &= 0xffffu; u1 &= 0xffffu; }
        X[t] = d;
        const int col = 32 * w + 16 * t + i16, r0 = 8 * j + 2 * g;
        *lds<u32>(L_XN + r0 * 1024 + (((col >> 2) ^ (r0 & 15)) * 16) + (col & 3) * 4) = u0;
        *lds<u32>(L_XN + (r0 + 1) * 1024 + (((col >> 2) ^ ((r0 + 1) & 15)) * 16) + (col & 3) * 4) = u1;
      }
      lds_fence();
      {
        const int row = 8 * j + (lane & 7);
        const v4u x0 = *lds<v4u>(L_XN + row * 1024 + (((8 * w + g) ^ (row & 15)) * 16));
        const v4u x1 = *lds<v4u>(L_XN + row * 1024 + (((8 * w + 4 + g) ^ (row & 15)) * 16));
        f4 dk = mfma16b(wkv[0][0], x0, zero4), dv = mfma16b(x0, wkv[1][0], zero4), dg = mfma16b(x0, x0, zero4);
        dk = mfma16b(wkv[0][1], x1, dk); dv = mfma16b(x1, wkv[1][1], dv); dg = mfma16b(x1, x1, dg);
        const bool klane = ((lane >> 3) & 1) == (g >> 1);
        *lds<f4>(L_KP + w * 1024 + lane * 16) = klane ? dk : dv;
        const int r = lane & 3;
        const float ds = r == 0 ? dg[0] : r == 1 ? dg[1] : r == 2 ? dg[2] : dg[3];
        if ((i16 >> 2) == g && g < 2) *lds<float>(L_SS + (8 * j + i16) * 32 + w * 4) = ds;
      }
      bar();
      v4u xh[4];
#pragma unroll
      for (int be = 0; be < 4; ++be) xh[be] = ld16(rs, tid * 16, OFF_XH + L * XH_SZ + (4 * gam + be) * 8192);
      if (w == 0) {
        const bool klane = ((lane >> 3) & 1) == (g >> 1);
        f4 s = zero4;
#pragma unroll
        for (int u = 0; u < 8; ++u) s += *lds<f4>(L_KP + u * 1024 + lane * 16);
        const int row = 8 * j + (klane ? (lane & 7) : 4 * (g & 1) + (lane & 3));
        const f4 q0 = *lds<f4>(L_SS + row * 32), q1 = *lds<f4>(L_SS + row * 32 + 16);
        const float ss = (q0[0] + q0[1]) + (q0[2] + q0[3]) + (q1[0] + q1[1]) + (q1[2] + q1[3]);
        const float rstd = __builtin_amdgcn_rsqf(ss * (1.0f / 512.0f) + 1e-5f);
        const float r0 = dppf<0x00>(rstd), r1 = dppf<0x55>(rstd), r2 = dppf<0xaa>(rstd), r3 = dppf<0xff>(rstd);
        s[0] *= klane ? rstd : r0; s[1] *= klane ? rstd : r1; s[2] *= klane ? rstd : r2; s[3] *= klane ? rstd : r3;
        const u32 off = klane ? (lane & 7) * 32 + g * 8 : 256 + i16 * 16 + (g & 1) * 8;
        st8(rs, off, lo + OFF_KV + b * 512, v2u{umin(pkb(s[0], s[1]), SENT - 1), umin(pkb(s[2], s[3]), SENT - 1)});
      }
#pragma unroll
      for (int be = 0; be < 4; ++be) {
        if (be != j) {
#pragma unroll
          for (int t = 0; t < 2; ++t) {
            const f4 d = mfma16b(ainv, v4u{y[t][0][be], y[t][1][be], xh[be][2 * t], xh[be][2 * t + 1]}, zero4);
            u32 u0 = pkb(d[0], d[1]), u1 = pkb(d[2], d[3]);
            if (t == 0 && dcl) { u0 &= 0xffffu; u1 &= 0xffffu; }
            const int col = 32 * w + 16 * t + i16, r0 = 8 * be + 2 * g;
            *lds<u32>(L_XN + r0 * 1024 + (((col >> 2) ^ (r0 & 15)) * 16) + (col & 3) * 4) = u0;
            *lds<u32>(L_XN + (r0 + 1) * 1024 + (((col >> 2) ^ ((r0 + 1) & 15)) * 16) + (col & 3) * 4) = u1;
          }
        }
      }
      lds_fence();
#pragma unroll
      for (int nu = 0; nu < 2; ++nu) {
        const int row = 16 * nu + i16;
        const v4u x0 = *lds<v4u>(L_XN + row * 1024 + (((8 * w + g) ^ i16) * 16));
        const v4u x1 = *lds<v4u>(L_XN + row * 1024 + (((8 * w + 4 + g) ^ i16) * 16));
        f4 dg = mfma16b(x0, x0, zero4); dg = mfma16b(x1, x1, dg);
        const int r = lane & 3;
        const float ds = r == 0 ? dg[0] : r == 1 ? dg[1] : r == 2 ? dg[2] : dg[3];
        if ((i16 >> 2) == g && (row >> 3) != j) *lds<float>(L_SS + row * 32 + w * 4) = ds;
      }
      bar();
      f4 q[2] = {zero4, zero4};
#pragma unroll
      for (int ks = 0; ks < 16; ++ks) {
        const v4u a = ks < WQ_LDS_KS ? *lds<v4u>(L_WQ + (w * WQ_LDS_KS + ks) * 1024 + lane * 16) : WQR[ks < WQ_LDS_KS ? 0 : ks - WQ_LDS_KS];
#pragma unroll
        for (int nu = 0; nu < 2; ++nu) q[nu] = mfma16b(a, *lds<v4u>(L_XN + (16 * nu + i16) * 1024 + (((4 * ks + g) ^ i16) * 16)), q[nu]);
      }
#pragma unroll
      for (int nu = 0; nu < 2; ++nu) {
        const int row = 16 * nu + i16;
        const f4 q0 = *lds<f4>(L_SS + row * 32), q1 = *lds<f4>(L_SS + row * 32 + 16);
        const float ss = (q0[0] + q0[1]) + (q0[2] + q0[3]) + (q1[0] + q1[1]) + (q1[2] + q1[3]);
        const float sc = __builtin_amdgcn_rsqf(ss * (1.0f / 512.0f) + 1e-5f) * (0.25f * 1.44269504f);
        *lds<v2u>(L_Q + (((row >> 3) * 2 + (w >> 2)) * 32 + (w & 3) * 8 + (row & 7)) * 32 + g * 8) = v2u{pkb(q[nu][0] * sc, q[nu][1] * sc), pkb(q[nu][2] * sc, q[nu][3] * sc)};
      }
    }
    {
      v4u K[8], V[16];
      {
      PHASE_IDS
      const int rho = lane & 31, h = lane >> 5;
      const u32 cst = rho == 16 ? 0x3f803f80u : 0u;
      for (u32 sp = 0; sp < SPIN_CAP; ++sp) {
        asm volatile("" ::: "memory");
        u32 m = 0;
#pragma unroll
        for (int kb = 0; kb < 8; ++kb) {
          const u32 so = lo + OFF_KV + (w * 8 + kb) * 2048;
          K[kb] = ld16(rs, (rho >> 3) * 512 + (rho & 7) * 32 + h * 16, so);
          V[2 * kb] = v4u{cst, cst, cst, cst}; V[2 * kb + 1] = v4u{cst, cst, cst, cst};
          if (rho < 16) { V[2 * kb] = ld16(rs, h * 512 + 256 + rho * 16, so); V[2 * kb + 1] = ld16(rs, 1024 + h * 512 + 256 + rho * 16, so); }
        }
#pragma unroll
        for (int kb = 0; kb < 8; ++kb) m = umax(m, umax(umax4(K[kb]), umax(umax4(V[2 * kb]), umax4(V[2 * kb + 1]))));
        if (!any_lane(m == SENT)) break;
      }
      bar();
      }
#pragma unroll 1
      for (int ps = 0; ps < 5; ++ps) {
        PHASE_IDS
        const int rho = lane & 31, h = lane >> 5;
        const u32 oidx = (lane & 1) ? 0xDDDDu : 0x8888u;
        const int be = (j + 1 + ps) & 3;
        f16v acc;
        if (ps < 4) acc = attn_tile(K, V, *lds<v4u>(L_Q + (be * 2) * 1024 + rho * 32 + h * 16));
        if (ps > 0) {
          bar();
          if (__builtin_amdgcn_readfirstlane(*lds<u32>(L_MISC))) {
            const int bp = (j + ps) & 3;
            bar();
            if (tid == 0) *lds<u32>(L_MISC) = 0u;
#pragma unroll 1
            for (int tau = 0; tau < 2; ++tau) {
              const v4u q = *lds<v4u>(L_Q + (bp * 2 + tau) * 1024 + rho * 32 + h * 16);
              const f16v zero = {0};
              float m = -3.0e38f;
#pragma unroll
              for (int kb = 0; kb < 8; ++kb) {
                const f16v s = mfma32b(K[kb], q, zero);
#pragma unroll
                for (int r = 0; r < 16; ++r) m = __builtin_fmaxf(m, s[r]);
                __builtin_amdgcn_sched_barrier(0);
              }
              const f2 mm = swap32(m); m = __builtin_fmaxf(mm[0], mm[1]);
              if (lane < 32) *lds<float>(L_SS + lane * 32 + w * 4) = m;
              bar();
              const f4 m0 = *lds<f4>(L_SS + rho * 32), m1 = *lds<f4>(L_SS + rho * 32 + 16);
              m = __builtin_fmaxf(__builtin_fmaxf(__builtin_fmaxf(m0[0], m0[1]), __builtin_fmaxf(m0[2], m0[3])), __builtin_fmaxf(__builtin_fmaxf(m1[0], m1[1]), __builtin_fmaxf(m1[2], m1[3])));
              const f16v a2 = attn_tile_shifted(K, V, q, m);
              put_partial(a2, tau, w, lane);
              bar();
            }
            if (w < 4) reduce_pass<false>(w, lane);
            bar();
          }
        }
        if (ps < 4) put_partial(acc, 0, w, lane);
        if (ps > 0) {
          f4 d[2];
          d[0] = ps == 4 ? X[0] : zero4; d[1] = ps == 4 ? X[1] : zero4;
#pragma unroll
          for (int ks = 0; ks < 4; ++ks) {
            const v4u a = *lds<v4u>(L_OS + (i16 >> 1) * 272 + ks * 64 + g * 16);
            d[0] = smf16(a, WO[0][ks], d[0], oidx); d[1] = smf16(a, WO[1][ks], d[1], oidx);
          }
          if (ps == 4) { X[0] = d[0]; X[1] = d[1]; break; }
          const int dst = 4 * gam + ((j + ps) & 3);
          st16(rs, tid * 16, lo + OFF_PO + (dst * 3 + ps - 1) * 8192, unsent(v4u{pkb(d[0][0], d[0][1]), pkb(d[0][2], d[0][3]), pkb(d[1][0], d[1][1]), pkb(d[1][2], d[1][3])}));
        }
        acc = attn_tile(K, V, *lds<v4u>(L_Q + (be * 2 + 1) * 1024 + rho * 32 + h * 16));
        put_partial(acc, 1, w, lane);
        bar();
        if (w < 4) reduce_pass<true>(w, lane);
      }
      PHASE_IDS
      v4u po[3];
      for (u32 sp = 0; sp < SPIN_CAP; ++sp) {
        asm volatile("" ::: "memory");
#pragma unroll
        for (int s = 0; s < 3; ++s) po[s] = ld16(rs, tid * 16, lo + OFF_PO + (n2 * 3 + s) * 8192);
        if (!any_lane(umax(umax4(po[0]), umax(umax4(po[1]), umax4(po[2]))) == SENT)) break;
      }
#pragma unroll
      for (int s = 0; s < 3; ++s) {
        X[0] += f4{bflo(po[s][0]), bfhi(po[s][0]), bflo(po[s][1]), bfhi(po[s][1])};
        X[1] += f4{bflo(po[s][2]), bfhi(po[s][2]), bflo(po[s][3]), bfhi(po[s][3])};
      }
    }
    if (L == 0) {
      PHASE_IDS
      const bool dcl = (w == 0) && (i16 == 0);
      const float a1 = dcl ? 0.f : X[0][1], a3 = dcl ? 0.f : X[0][3];
      float s0 = X[0][0] * X[0][0] + a1 * a1 + X[1][0] * X[1][0] + X[1][1] * X[1][1];
      float s1 = X[0][2] * X[0][2] + a3 * a3 + X[1][2] * X[1][2] + X[1][3] * X[1][3];
      s0 = row_sum(s0); s1 = row_sum(s1);
      if (i16 < 2) *lds<float>(L_SS + (2 * g + i16) * 32 + w * 4) = i16 ? s1 : s0;
      bar();
      const f4 q0 = *lds<f4>(L_SS + (2 * g) * 32), q1 = *lds<f4>(L_SS + (2 * g) * 32 + 16), q2 = *lds<f4>(L_SS + (2 * g + 1) * 32), q3 = *lds<f4>(L_SS + (2 * g + 1) * 32 + 16);
      const float r0 = __builtin_amdgcn_rsqf(((q0[0] + q0[1]) + (q0[2] + q0[3]) + (q1[0] + q1[1]) + (q1[2] + q1[3])) * (1.0f / 512.0f) + 1e-5f);
      const float r1 = __builtin_amdgcn_rsqf(((q2[0] + q2[1]) + (q2[2] + q2[3]) + (q3[0] + q3[1]) + (q3[2] + q3[3])) * (1.0f / 512.0f) + 1e-5f);
      const v4u a = {pkh(X[0][0] * r0, a1 * r0), pkh(X[0][2] * r1, a3 * r1), pkh(X[1][0] * r0, X[1][1] * r0), pkh(X[1][2] * r1, X[1][3] * r1)};
      z_publish(rs, LAYER_SZ + OFF_ZB, a, n2, w, lane);
      st16(rs, tid * 16, OFF_XH + XH_SZ + n2 * 8192, v4u{pkb(X[0][0], X[0][1]), pkb(X[0][2], X[0][3]), pkb(X[1][0], X[1][1]), pkb(X[1][2], X[1][3])});
    }
  }
  {
    PHASE_IDS
    constexpr u32 L_DT = L_R, DT_STR = 1024 + 16;
    const f4 x00 = __builtin_bit_cast(f4, ld16(rs, tid * 16, OFF_X0 + b * 16384));
    const f4 x01 = __builtin_bit_cast(f4, ld16(rs, tid * 16 + 8192, OFF_X0 + b * 16384));
    f4 d0 = X[0] - x00; const f4 d1 = X[1] - x01;
    if (w == 0 && i16 == 0) {
      *lds<f2>(L_SS + g * 8) = f2{d0[0] * 0.044194174f, d0[2] * 0.044194174f};
      d0[0] = d0[1] * 0.70710678f; d0[1] = 0.f; d0[2] = d0[3] * 0.70710678f; d0[3] = 0.f;
    }
    *lds<u32>(L_DT + (2 * g) * DT_STR + i16 * 32 + w * 4) = pkb(d0[0], d0[1]);
    *lds<u32>(L_DT + (2 * g + 1) * DT_STR + i16 * 32 + w * 4) = pkb(d0[2], d0[3]);
    *lds<u32>(L_DT + (2 * g) * DT_STR + (16 + i16) * 32 + w * 4) = pkb(d1[0], d1[1]);
    *lds<u32>(L_DT + (2 * g + 1) * DT_STR + (16 + i16) * 32 + w * 4) = pkb(d1[2], d1[3]);
    bar();
    if (w < 4) {
      const int cp = lane & 31, h = lane >> 5, bb = lane & 15, rho = (lane >> 4) & 1;
      v4u fr, fi;
#pragma unroll
      for (int e = 0; e < 4; ++e) { const float c = cosr(bb * (4 * h + e), 16), s = sinr(bb * (4 * h + e), 16); fr[e] = pkb(c, -s); fi[e] = pkb(s, c); }
      const v4u z4 = {0, 0, 0, 0};
      const v4u a0 = *lds<v4u>(L_DT + (2 * w) * DT_STR + cp * 32 + h * 16), a1 = *lds<v4u>(L_DT + (2 * w + 1) * DT_STR + cp * 32 + h * 16);
      const f16v zero = {0};
      f16v dr = mfma32b(a0, rho ? z4 : fr, zero), di = mfma32b(a0, rho ? z4 : fi, zero);
      dr = mfma32b(a1, rho ? fr : z4, dr); di = mfma32b(a1, rho ? fi : z4, di);
      f16v o = zero;
#pragma unroll
      for (int s = 0; s < 2; ++s) {
        float tr[8], ti[8], bc[8], bs[8];
#pragma unroll
        for (int e = 0; e < 8; ++e) {
          const int c = 16 * s + 8 * (e >> 2) + 4 * h + (e & 3);
          const float tc = cosr(bb * c, 512) * 0.0625f, ts = sinr(bb * c, 512) * 0.0625f;
          tr[e] = dr[8 * s + e] * tc - di[8 * s + e] * ts; ti[e] = dr[8 * s + e] * ts + di[8 * s + e] * tc;
          bc[e] = cosr(cp * c, 32); bs[e] = -sinr(cp * c, 32);
        }
        v4u ar, ai, mc, ms;
#pragma unroll
        for (int e = 0; e < 4; ++e) { ar[e] = pkb(tr[2 * e], tr[2 * e + 1]); ai[e] = pkb(ti[2 * e], ti[2 * e + 1]); mc[e] = pkb(bc[2 * e], bc[2 * e + 1]); ms[e] = pkb(bs[2 * e], bs[2 * e + 1]); }
        o = mfma32b(ar, mc, o); o = mfma32b(ai, ms, o);
      }
      const f2 ny = *lds<f2>(L_SS + w * 8);
#pragma unroll
      for (int q = 0; q < 2; ++q)
#pragma unroll
        for (int u = 0; u < 2; ++u) {
          const size_t go = (size_t)(n2 + 256 * (2 * w + q)) * 512 + 16 * cp + 8 * u + 4 * h;
          const v2u xv = *(const v2u*)(p.x + go);
          const int r = 8 * q + 4 * u;
          *(v2u*)(p.out + go) = v2u{pkb(bflo(xv[0]) + o[r] + ny[q], bfhi(xv[0]) + o[r + 1] - ny[q]), pkb(bflo(xv[1]) + o[r + 2] + ny[q], bfhi(xv[1]) + o[r + 3] - ny[q])};
        }
    }
  }
}

extern "C" void kernel_launch(void* const* d_in, const int* in_sizes, int n_in, void* d_out, int out_size, void* d_ws, size_t ws_size, hipStream_t stream) {
  Params p;
  p.x = (const unsigned short*)d_in[0]; p.wr = (const unsigned short*)d_in[1]; p.wi = (const unsigned short*)d_in[2];
  p.wq = (const unsigned short*)d_in[4]; p.wk = (const unsigned short*)d_in[5]; p.wv = (const unsigned short*)d_in[6]; p.wo = (const unsigned short*)d_in[7];
  p.out = (unsigned short*)d_out; p.ws = (char*)d_ws;
  (void)hipFuncSetAttribute((const void*)k_main, hipFuncAttributeMaxDynamicSharedMemorySize, LDS_BYTES);
  (void)hipMemsetAsync((char*)d_ws + OFF_YB, 0xFF, 2 * LAYER_SZ - OFF_YB, stream);
  hipLaunchKernelGGL(k_pro, dim3(NWG), dim3(NT), 53248, stream, p);
  hipLaunchKernelGGL(k_main, dim3(NWG), dim3(NT), LDS_BYTES, stream, p);
}
```

```cpp
#include <hip/hip_runtime.h>
#include <hip/hip_bf16.h>
#include <stdint.h>

#define ADUMP ""
#define ACOUNT(n) ""
#define ASTAMP(n, l) ""

#define KERNEL_BODY \
  "s_load_dwordx16 s[4:19], %[ka], 0x0\n" \
  "s_load_dwordx2 s[20:21], %[ka], 0x40\n" \
  "v_readfirstlane_b32 s22, v0\n" \
  "s_mov_b32 s33, %[b]\n" \
  "v_and_b32 v12, 63, v0\n" \
  "s_lshr_b32 s22, s22, 6\n" \
  "v_lshlrev_b32 v21, 4, v12\n" \
  "s_and_b32 s88, s33, 7\n" \
  "s_cmp_lg_u32 s22, 1\n" \
  "s_cbranch_scc1 1f\n" \
  "s_getpc_b64 s[94:95]\n" \
  ".Lk_pf0:\n" \
  "v_and_b32 v22, 0xf0, v21\n" \
  "s_bfe_u32 s87, s33, 0x40003\n" \
  "v_add_lshl_u32 v22, v22, s87, 7\n" \
  "v_min_u32 v22, .Lk_pf1-.Lk_pf0-4, v22\n" \
  "global_load_dword v22, v22, s[94:95]\n" \
  "1:\n" \
  "s_mov_b64 s[90:91], %[tab]\n" \
  "s_add_u32 s92, s90, 86016\n" \
  "s_addc_u32 s93, s91, 0\n" \
  "s_mul_i32 s81, s88, 12288\n" \
  "s_add_u32 s90, s90, s81\n" \
  "s_addc_u32 s91, s91, 0\n" \
  "s_sub_u32 s85, 32, s22\n" \
  "s_cmp_lt_u32 s85, 12\n" \
  "s_cselect_b64 s[94:95], s[90:91], s[92:93]\n" \
  "s_lshl_b32 s87, s85, 10\n" \
  "s_add_u32 s94, s94, s87\n" \
  "s_addc_u32 s95, s95, 0\n" \
  "s_add_u32 m0, s87, 57216\n" \
  "s_nop 0\n" \
  "global_load_lds_dwordx4 v21, s[94:95]\n" \
  "s_sub_u32 s85, 24, s22\n" \
  "s_cmp_lt_u32 s85, 12\n" \
  "s_cselect_b64 s[94:95], s[90:91], s[92:93]\n" \
  "s_lshl_b32 s87, s85, 10\n" \
  "s_add_u32 s94, s94, s87\n" \
  "s_addc_u32 s95, s95, 0\n" \
  "s_add_u32 m0, s87, 57216\n" \
  "s_nop 0\n" \
  "global_load_lds_dwordx4 v21, s[94:95]\n" \
  "s_bfe_u32 s34, s33, 0x20003\n" \
  "s_lshl_b32 s89, s88, 2\n" \
  "s_and_b32 s81, s33, 0xe0\n" \
  "s_or_b32 s89, s89, s81\n" \
  "s_or_b32 s84, s89, s34\n" \
  "s_lshl_b32 s81, s22, 8\n" \
  "s_add_u32 s23, s84, s81\n" \
  "s_lshl_b32 s23, s23, 10\n" \
  "v_add_u32 v13, s23, v21\n" \
  "s_mov_b32 s26, 22489344\n" \
  "s_mov_b32 s27, 0x20000\n" \
  "s_mov_b32 s81, 22489088\n" \
  "s_waitcnt lgkmcnt(0)\n" \
  ASTAMP(48648, 0) \
  "global_load_dwordx4 v[24:27], v13, s[4:5]\n" \
  "s_mov_b32 s24, s20\n" \
  "s_and_b32 s25, s21, 0xffff\n" \
  "buffer_load_dwordx2 v[102:103], off, s[24:27], s81 sc1\n" \
  "s_mul_i32 s45, s84, 8320\n" \
  "s_mul_i32 s41, s33, 8320\n" \
  "s_add_u32 s41, s41, 2129920\n" \
  "v_lshlrev_b32 v14, 4, v0\n" \
  "v_mov_b64 v[16:17], -1\n" \
  "v_mov_b64 v[18:19], -1\n" \
  "buffer_store_dwordx4 v[16:19], v14, s[24:27], s45 offen sc1\n" \
  "buffer_store_dwordx4 v[16:19], v14, s[24:27], s41 offen sc1\n" \
  "s_lshr_b32 s65, s33, 3\n" \
  "s_lshl_b32 s85, s88, 4\n" \
  "s_add_u32 s85, s85, s65\n" \
  "s_add_u32 s85, s85, 112\n" \
  "s_add_u32 s86, s33, 248\n" \
  "s_cmp_eq_u32 s65, 1\n" \
  "s_cselect_b32 s86, s86, 999\n" \
  "s_cmp_ge_u32 s33, 128\n" \
  "s_cselect_b32 s64, s85, s33\n" \
  "s_cselect_b32 s85, s85, s86\n" \
  "s_cmp_lt_u32 s22, 4\n" \
  "s_cselect_b32 s64, s64, s86\n" \
  "s_lshr_b32 s86, s33, 2\n" \
  "s_lshl_b32 s86, s86, 10\n" \
  "s_and_b32 s81, s33, 1\n" \
  "s_lshl_b32 s81, s81, 9\n" \
  "s_add_u32 s86, s86, s81\n" \
  "s_and_b32 s81, s33, 2\n" \
  "s_lshl_b32 s81, s81, 7\n" \
  "s_add_u32 s86, s86, s81\n" \
  "s_add_u32 s86, s86, 4325376\n" \
  "s_lshl_b32 s50, s33, 8\n" \
  "s_add_u32 s50, s50, 4259840\n" \
  "s_cmp_ge_u32 s22, 4\n" \
  "s_cbranch_scc1 2f\n" \
  "s_cmp_eq_u32 s22, 1\n" \
  "s_cbranch_scc1 5f\n" \
  "s_cmp_eq_u32 s22, 0\n" \
  "s_cbranch_scc1 4f\n" \
  "v_lshrrev_b32 v13, 5, v12\n" \
  "s_mov_b32 s81, 10687488\n" \
  "v_mul_lo_u32 v13, v13, s81\n" \
  "v_and_b32 v15, 0xf0, v21\n" \
  "v_add_u32 v13, v13, v15\n" \
  "s_cmp_eq_u32 s22, 3\n" \
  "s_cbranch_scc1 3f\n" \
  "s_mov_b32 exec_lo, 0xffff\n" \
  "s_mov_b32 exec_hi, 0xffff\n" \
  "v_add_u32 v13, s50, v13\n" \
  "s_not_b64 exec, exec\n" \
  "v_add_u32 v13, s86, v13\n" \
  "s_mov_b64 exec, -1\n" \
  "buffer_store_dwordx4 v[16:19], v13, s[24:27], 0 offen sc1\n" \
  "s_branch 5f\n" \
  "3:\n" \
  "s_mov_b32 exec_lo, 3\n" \
  "s_mov_b32 exec_hi, 3\n" \
  "s_add_u32 s81, s45, 8192\n" \
  "buffer_store_dwordx4 v[16:19], v13, s[24:27], s81 offen sc1\n" \
  "s_mov_b64 exec, -1\n" \
  "s_branch 5f\n" \
  "4:\n" \
  "s_cmp_lt_u32 s22, 12\n" \
  "s_cselect_b64 s[94:95], s[90:91], s[92:93]\n" \
  "s_lshl_b32 s87, s22, 10\n" \
  "s_add_u32 s94, s94, s87\n" \
  "s_addc_u32 s95, s95, 0\n" \
  "s_add_u32 m0, s87, 57216\n" \
  "s_nop 0\n" \
  "global_load_lds_dwordx4 v21, s[94:95]\n" \
  "s_branch 5f\n" \
  "2:\n" \
  "s_cmp_eq_u32 s85, 999\n" \
  "s_cbranch_scc1 5f\n" \
  "s_lshl_b32 s81, s85, 12\n" \
  "s_add_u32 s81, s81, 21370880\n" \
  "buffer_store_dwordx4 v[16:19], v14, s[24:27], s81 offen sc1\n" \
  "5:\n" \
  "s_lshl_b32 s81, s65, 5\n" \
  "v_lshrrev_b32 v15, 1, v0\n" \
  "v_add_u32 v15, s81, v15\n" \
  "v_and_b32 v15, 255, v15\n" \
  "v_add_u32 v23, s81, v0\n" \
  "v_and_b32 v23, 511, v23\n" \
  "v_mul_u32_u24 v13, 4112, v15\n" \
  "s_mul_i32 s81, s88, 514\n" \
  "s_lshl_b32 s87, s65, 4\n" \
  "s_add_u32 s81, s81, s87\n" \
  "s_add_u32 s81, s81, 2\n" \
  "v_add_u32 v13, s81, v13\n" \
  "s_cmp_ge_u32 s33, 128\n" \
  "s_cbranch_scc1 6f\n" \
  "s_and_b32 s81, s22, 3\n" \
  "s_lshl_b32 s87, s33, 2\n" \
  "s_add_u32 s81, s81, s87\n" \
  "s_lshl_b32 s81, s81, 10\n" \
  "v_add_u32 v14, s81, v21\n" \
  "s_mov_b64 s[94:95], s[16:17]\n" \
  "s_cmp_lt_u32 s33, 12\n" \
  "s_cselect_b64 s[96:97], s[12:13], s[14:15]\n" \
  "s_and_b32 s81, s33, 3\n" \
  "s_lshl_b32 s81, s81, 3\n" \
  "v_lshl_add_u32 v157, v23, 5, s81\n" \
  "s_branch 7f\n" \
  "6:\n" \
  "v_lshrrev_b32 v14, 4, v0\n" \
  "s_and_b32 s81, s65, 15\n" \
  "s_lshl_b32 s81, s81, 15\n" \
  "s_lshl_b32 s87, s88, 7\n" \
  "s_add_u32 s81, s81, s87\n" \
  "v_lshl_add_u32 v14, v14, 10, s81\n" \
  "v_and_b32 v18, 0x70, v21\n" \
  "v_add_u32 v14, v14, v18\n" \
  "s_mov_b64 s[94:95], s[10:11]\n" \
  "s_mov_b64 s[96:97], s[10:11]\n" \
  "s_lshl_b32 s81, s85, 3\n" \
  "s_sub_u32 s81, s81, 1024\n" \
  "v_lshl_add_u32 v157, v23, 10, s81\n" \
  "7:\n" \
  "s_barrier\n" \
  "global_load_dwordx4 v[28:31], v14, s[94:95]\n" \
  "s_mov_b32 s68, 0xaaaaaaaa\n" \
  "s_mov_b32 s69, s68\n" \
  "s_mov_b64 exec, s[68:69]\n" \
  "global_load_dwordx4 v[96:99], v13, s[8:9]\n" \
  "s_not_b64 exec, exec\n" \
  "global_load_dwordx4 v[96:99], v13, s[6:7]\n" \
  "s_mov_b64 exec, -1\n" \
  "s_sub_u32 s80, 16, s22\n" \
  "s_cmp_lt_u32 s80, 12\n" \
  "s_cselect_b64 s[94:95], s[90:91], s[92:93]\n" \
  "s_lshl_b32 s87, s80, 10\n" \
  "s_add_u32 s94, s94, s87\n" \
  "s_addc_u32 s95, s95, 0\n" \
  "s_add_u32 m0, s87, 57216\n" \
  "s_nop 0\n" \
  "global_load_lds_dwordx4 v21, s[94:95]\n" \
  "s_sub_u32 s80, 8, s22\n" \
  "s_cmp_lt_u32 s80, 12\n" \
  "s_cselect_b64 s[94:95], s[90:91], s[92:93]\n" \
  "s_lshl_b32 s87, s80, 10\n" \
  "s_add_u32 s94, s94, s87\n" \
  "s_addc_u32 s95, s95, 0\n" \
  "s_add_u32 m0, s87, 57216\n" \
  "s_nop 0\n" \
  "global_load_lds_dwordx4 v21, s[94:95]\n" \
  "s_mov_b64 s[28:29], s[24:25]\n" \
  "s_mov_b64 s[30:31], s[26:27]\n" \
  "s_mov_b32 s35, 0\n" \
  "s_lshl_b32 s40, s88, 10\n" \
  "s_lshl_b32 s81, s65, 5\n" \
  "s_add_u32 s40, s40, s81\n" \
  "s_add_u32 s42, s34, 1\n" \
  "s_and_b32 s42, s42, 3\n" \
  "s_or_b32 s42, s42, s89\n" \
  "s_mul_i32 s42, s42, 8320\n" \
  "s_add_u32 s43, s34, 2\n" \
  "s_and_b32 s43, s43, 3\n" \
  "s_or_b32 s43, s43, s89\n" \
  "s_mul_i32 s43, s43, 8320\n" \
  "s_add_u32 s44, s34, 3\n" \
  "s_and_b32 s44, s44, 3\n" \
  "s_or_b32 s44, s44, s89\n" \
  "s_mul_i32 s44, s44, 8320\n" \
  "s_lshl_b32 s46, s89, 5\n" \
  "s_add_u32 s46, s46, 2129920\n" \
  "s_add_u32 s47, s46, 8320\n" \
  "s_add_u32 s48, s46, 133120\n" \
  "s_add_u32 s49, s46, 141440\n" \
  "s_cmp_lt_u32 s22, 2\n" \
  "s_cselect_b32 s50, s50, s86\n" \
  "s_and_b32 s81, s22, 1\n" \
  "s_lshl_b32 s81, s81, 7\n" \
  "s_add_u32 s50, s50, s81\n" \
  "s_lshl_b32 s81, s22, 13\n" \
  "s_add_u32 s51, s81, 4227072\n" \
  "s_add_u32 s52, s81, 4231168\n" \
  "s_add_u32 s53, s81, 4283392\n" \
  "s_add_u32 s54, s81, 4287488\n" \
  "s_lshl_b32 s81, s22, 10\n" \
  "s_add_u32 s81, s81, 4348928\n" \
  "s_add_u32 s55, s34, 1\n" \
  "s_and_b32 s55, s55, 3\n" \
  "s_or_b32 s55, s55, s89\n" \
  "s_mul_i32 s55, s55, 24576\n" \
  "s_add_u32 s55, s55, s81\n" \
  "s_add_u32 s58, s34, 0\n" \
  "s_and_b32 s58, s58, 3\n" \
  "s_or_b32 s58, s58, s89\n" \
  "s_mul_i32 s58, s58, 24576\n" \
  "s_add_u32 s58, s58, s81\n" \
  "s_add_u32 s81, s81, 8192\n" \
  "s_add_u32 s56, s34, 2\n" \
  "s_and_b32 s56, s56, 3\n" \
  "s_or_b32 s56, s56, s89\n" \
  "s_mul_i32 s56, s56, 24576\n" \
  "s_add_u32 s56, s56, s81\n" \
  "s_add_u32 s81, s81, 8192\n" \
  "s_add_u32 s57, s34, 3\n" \
  "s_and_b32 s57, s57, 3\n" \
  "s_or_b32 s57, s57, s89\n" \
  "s_mul_i32 s57, s57, 24576\n" \
  "s_add_u32 s57, s57, s81\n" \
  "s_lshl_b32 s59, s89, 4\n" \
  "s_add_u32 s59, s59, 10640384\n" \
  "s_and_b32 s60, s22, 3\n" \
  "s_lshl_b32 s60, s60, 10\n" \
  "s_add_u32 s60, s60, 10640384\n" \
  "s_lshl_b32 s61, s34, 3\n" \
  "s_add_u32 s61, s61, s22\n" \
  "s_lshl_b32 s61, s61, 14\n" \
  "s_add_u32 s61, s61, 21899264\n" \
  "s_lshl_b32 s81, s22, 9\n" \
  "s_add_u32 s62, s81, 22423552\n" \
  "s_lshl_b32 s63, s34, 17\n" \
  "s_add_u32 s63, s63, s81\n" \
  "s_add_u32 s63, s63, 21374976\n" \
  "s_mov_b32 s66, 0xffff\n" \
  "s_mov_b32 s67, s66\n" \
  "s_mov_b32 s70, 0xcccccccc\n" \
  "s_mov_b32 s71, s70\n" \
  "s_cmp_eq_u32 s22, 7\n" \
  "s_cselect_b32 s72, 0x80008000, 0\n" \
  "s_mov_b32 s73, s72\n" \
  "s_lshl_b64 s[74:75], 1, s34\n" \
  "v_and_b32 v13, 15, v12\n" \
  "v_lshrrev_b32 v14, 4, v12\n" \
  "s_lshl_b32 s81, s22, 10\n" \
  "s_add_u32 s81, s81, 8704\n" \
  "v_lshl_add_u32 v148, v14, 8, s81\n" \
  "v_lshl_add_u32 v148, v13, 3, v148\n" \
  "v_lshl_add_u32 v16, v14, 6, v13\n" \
  "s_mul_i32 s81, s22, 1056\n" \
  "v_lshl_add_u32 v149, v16, 2, s81\n" \
  "v_add_u32 v16, -1, v16\n" \
  "v_and_b32 v16, 255, v16\n" \
  "v_lshl_add_u32 v150, v16, 2, s81\n" \
  "v_mul_u32_u24 v16, 2112, v14\n" \
  "s_lshl_b32 s81, s22, 7\n" \
  "v_lshl_add_u32 v16, v13, 2, v16\n" \
  "v_add_u32 v151, s81, v16\n" \
  "v_and_b32 v16, 7, v12\n" \
  "v_lshl_add_u32 v16, v16, 10, s81\n" \
  "v_lshl_add_u32 v16, v14, 4, v16\n" \
  "v_and_b32 v17, 8, v12\n" \
  "v_lshl_add_u32 v152, v17, 3, v16\n" \
  "v_and_b32 v16, 15, v15\n" \
  "v_lshlrev_b32 v16, 4, v16\n" \
  "v_lshrrev_b32 v17, 6, v15\n" \
  "v_lshl_add_u32 v16, v17, 8, v16\n" \
  "v_bfe_u32 v17, v15, 4, 2\n" \
  "v_lshl_add_u32 v16, v17, 2, v16\n" \
  "v_and_b32 v17, 1, v12\n" \
  "v_lshl_add_u32 v153, v17, 1, v16\n" \
  "v_lshlrev_b32 v154, 1, v23\n" \
  "v_and_b32 v16, 255, v0\n" \
  "v_lshlrev_b32 v156, 7, v16\n" \
  "v_lshlrev_b32 v155, 2, v16\n" \
  "s_cmp_lt_u32 s64, 128\n" \
  "s_cbranch_scc1 8f\n" \
  "v_and_b32 v17, 3, v12\n" \
  "s_and_b32 s81, s22, 4\n" \
  "v_add_u32 v17, s81, v17\n" \
  "v_mul_u32_u24 v17, 1056, v17\n" \
  "v_and_b32 v16, 0xfc, v16\n" \
  "v_lshl_add_u32 v155, v16, 2, v17\n" \
  "8:\n" \
  "v_mbcnt_lo_u32_b32 v12, -1, 0\n" \
  "v_mbcnt_hi_u32_b32 v12, -1, v12\n" \
  "s_lshl_b32 s81, s22, 6\n" \
  "v_or_b32 v17, s81, v12\n" \
  "v_and_b32 v13, 15, v12\n" \
  "v_lshrrev_b32 v14, 4, v12\n" \
  "v_bfe_u32 v15, v17, 3, 4\n" \
  "v_and_b32 v16, 6, v12\n" \
  "v_lshl_add_u32 v15, v16, 3, v15\n" \
  "s_lshr_b32 s81, s22, 1\n" \
  "s_lshl_b32 s81, s81, 6\n" \
  "v_add_u32 v15, s81, v15\n" \
  "v_mul_u32_u24 v15, 8320, v15\n" \
  "v_and_b32 v16, 1, v12\n" \
  "v_lshl_add_u32 v168, v16, 4, v15\n" \
  "v_mul_u32_u24 v15, 4158, v16\n" \
  "v_lshl_add_u32 v169, v17, 1, v15\n" \
  "v_mul_u32_u24 v15, 1405584, v16\n" \
  "v_lshlrev_b32 v15, 4, v15\n" \
  "v_lshl_add_u32 v190, v12, 1, v15\n" \
  "v_lshlrev_b32 v186, 4, v17\n" \
  "s_lshl_b32 s81, s22, 4\n" \
  "v_add_u32 v170, s81, v186\n" \
  "v_lshlrev_b32 v15, 4, v12\n" \
  "v_add_u32 v4, 41984, v15\n" \
  "s_mul_i32 s81, s22, 10240\n" \
  "s_add_u32 s81, s81, 81792\n" \
  "v_add_u32 v178, s81, v15\n" \
  "v_and_b32 v15, 7, v12\n" \
  "v_xor_b32 v16, s22, v15\n" \
  "v_lshlrev_b32 v16, 4, v16\n" \
  "v_lshl_add_u32 v16, v13, 9, v16\n" \
  "v_lshl_add_u32 v171, v14, 7, v16\n" \
  "v_bfe_u32 v16, v17, 5, 3\n" \
  "v_lshlrev_b32 v16, 4, v16\n" \
  "v_xor_b32 v172, v16, v186\n" \
  "v_lshrrev_b32 v16, 1, v12\n" \
  "v_and_b32 v16, 4, v16\n" \
  "v_xor_b32 v16, v16, v15\n" \
  "s_lshl_b32 s81, s22, 7\n" \
  "v_lshl_add_u32 v173, v16, 4, s81\n" \
  "v_and_b32 v16, 24, v12\n" \
  "v_lshl_add_u32 v173, v16, 8, v173\n" \
  "v_and_b32 v16, 32, v12\n" \
  "v_lshl_add_u32 v173, v16, 5, v173\n" \
  "v_lshrrev_b32 v16, 1, v16\n" \
  "v_and_b32 v8, 31, v12\n" \
  "v_lshl_add_u32 v7, v8, 5, v16\n" \
  "v_add_u32 v7, 32768, v7\n" \
  "v_cmp_eq_u32 vcc, 16, v8\n" \
  "v_mov_b32 v9, 0x3f803f80\n" \
  "v_xor_b32 v16, v14, v13\n" \
  "v_cndmask_b32 v8, 0, v9, vcc\n" \
  "v_lshlrev_b32 v16, 4, v16\n" \
  "v_lshl_add_u32 v20, v13, 10, v16\n" \
  "v_xor_b32 v185, s81, v20\n" \
  "v_mov_b32 v9, v8\n" \
  "v_bfe_u32 v16, v12, 3, 1\n" \
  "v_mul_u32_u24 v184, 8320, v16\n" \
  "v_xor_b32 v184, 8320, v184\n" \
  "v_xor_b32 v184, v184, v185\n" \
  "v_lshl_add_u32 v16, v16, 2, v14\n" \
  "v_mul_u32_u24 v16, 16640, v16\n" \
  "s_mul_i32 s87, s22, 266240\n" \
  "v_lshl_add_u32 v16, v15, 4, v16\n" \
  "v_add_u32 v181, s87, v16\n" \
  "v_mul_u32_u24 v16, 2080, v14\n" \
  "v_lshlrev_b32 v15, 2, v13\n" \
  "s_xor_b32 s81, s81, 128\n" \
  "v_xor_b32 v16, v16, v15\n" \
  "v_xor_b32 v182, s81, v16\n" \
  "v_xor_b32 v183, 1040, v182\n" \
  "s_lshl_b32 s81, s22, 10\n" \
  "v_lshl_add_u32 v15, v14, 7, v15\n" \
  "v_and_b32 v16, 16, v12\n" \
  "v_lshl_add_u32 v15, v16, 2, v15\n" \
  "v_add_u32 v176, s81, v15\n" \
  "v_xor_b32 v177, 64, v176\n" \
  "v_and_b32 v15, 3, v12\n" \
  "v_and_b32 v16, 12, v12\n" \
  "v_lshlrev_b32 v16, 10, v16\n" \
  "v_lshl_add_u32 v16, v14, 6, v16\n" \
  "v_lshl_add_u32 v180, v15, 4, v16\n" \
  "s_lshl_b32 s81, s22, 2\n" \
  "v_lshl_add_u32 v187, v13, 5, s81\n" \
  "s_add_u32 s87, s34, 1\n" \
  "v_add_u32 v16, s87, v15\n" \
  "v_and_b32 v16, 3, v16\n" \
  "v_mul_u32_u24 v16, 8320, v16\n" \
  "v_lshl_add_u32 v16, v14, 3, v16\n" \
  "s_mul_i32 s87, s34, 8320\n" \
  "s_sub_u32 s87, s45, s87\n" \
  "s_add_u32 s87, s87, 8192\n" \
  "v_add_u32 v174, s87, v16\n" \
  "s_cmp_lt_u32 s22, 2\n" \
  "s_cbranch_scc0 31f\n" \
  "v_add_u32 v14, s81, v14\n" \
  "v_lshlrev_b32 v189, 5, v14\n" \
  "v_and_b32 v16, 8, v12\n" \
  "v_add_u32 v14, v16, v14\n" \
  "v_and_b32 v16, 12, v12\n" \
  "v_lshl_add_u32 v14, v16, 2, v14\n" \
  "s_branch 32f\n" \
  "31:\n" \
  "v_and_b32 v16, 7, v12\n" \
  "v_lshlrev_b32 v189, 5, v16\n" \
  "v_lshrrev_b32 v14, 3, v12\n" \
  "v_and_b32 v16, 4, v12\n" \
  "v_lshl_add_u32 v14, v16, 2, v14\n" \
  "s_mul_i32 s87, s22, -24\n" \
  "s_add_u32 s87, s87, 80\n" \
  "v_add_u32 v14, s87, v14\n" \
  "32:\n" \
  "v_lshlrev_b32 v15, 2, v15\n" \
  "v_lshl_add_u32 v188, v14, 4, v15\n" \
  ASTAMP(48652, 0) \
  "s_waitcnt vmcnt(5)\n" \
  "s_barrier\n" \
  ASTAMP(48656, 0) \
  "v_readfirstlane_b32 s94, v102\n" \
  "v_readfirstlane_b32 s95, v103\n" \
  "ds_read_b128 v[104:107], v4 offset:39808\n" \
  "ds_read_b128 v[108:111], v4 offset:40832\n" \
  "ds_read_b128 v[112:115], v4 offset:41856\n" \
  "ds_read_b128 v[116:119], v4 offset:42880\n" \
  "ds_read_b128 v[120:123], v4 offset:43904\n" \
  "ds_read_b128 v[124:127], v4 offset:44928\n" \
  "ds_read_b128 v[128:131], v4 offset:45952\n" \
  "ds_read_b128 v[132:135], v4 offset:46976\n" \
  "ds_read_b128 v[136:139], v4 offset:48000\n" \
  "s_mul_i32 s95, s95, 0x7f4a7c15\n" \
  "s_mul_i32 s87, s94, 0x9e3779b9\n" \
  "s_mul_hi_u32 s88, s94, 0x7f4a7c15\n" \
  "s_mul_i32 s94, s94, 0x7f4a7c15\n" \
  "s_add_u32 s95, s95, s87\n" \
  "s_add_u32 s95, s95, s88\n" \
  "s_add_u32 s36, s94, 0xd192ed03\n" \
  "s_addc_u32 s37, s95, 0xd1b54a32\n" \
  "s_cmp_lg_u32 s22, 0\n" \
  "s_cbranch_scc1 1f\n" \
  "v_mov_b64 v[102:103], s[36:37]\n" \
  "s_lshl_b32 s81, s33, 7\n" \
  "s_add_u32 s81, s81, 22456320\n" \
  "s_mov_b64 exec, 1\n" \
  "buffer_store_dwordx2 v[102:103], off, s[24:27], s81 sc1\n" \
  "s_mov_b64 exec, -1\n" \
  "1:\n" \
  "ds_read_b128 v[140:143], v4 offset:37760\n" \
  "ds_read_b128 v[144:147], v4 offset:38784\n" \
  "v_readfirstlane_b32 s94, v24\n" \
  "v_lshlrev_b32 v192, 16, v24\n" \
  "v_lshlrev_b32 v194, 16, v25\n" \
  "v_lshlrev_b32 v196, 16, v26\n" \
  "v_lshlrev_b32 v198, 16, v27\n" \
  "s_lshl_b32 s94, s94, 16\n" \
  "v_and_b32 v193, 0xffff0000, v24\n" \
  "v_and_b32 v195, 0xffff0000, v25\n" \
  "v_and_b32 v197, 0xffff0000, v26\n" \
  "v_and_b32 v199, 0xffff0000, v27\n" \
  "v_pk_add_f32 v[192:193], v[192:193], s[94:95] op_sel_hi:[1,0] neg_lo:[0,1] neg_hi:[0,1]\n" \
  "v_pk_add_f32 v[194:195], v[194:195], s[94:95] op_sel_hi:[1,0] neg_lo:[0,1] neg_hi:[0,1]\n" \
  "v_pk_add_f32 v[196:197], v[196:197], s[94:95] op_sel_hi:[1,0] neg_lo:[0,1] neg_hi:[0,1]\n" \
  "v_pk_add_f32 v[198:199], v[198:199], s[94:95] op_sel_hi:[1,0] neg_lo:[0,1] neg_hi:[0,1]\n" \
  "s_bitcmp0_b32 0xffff0002, s65\n" \
  "s_cbranch_scc1 2f\n" \
  "global_load_dwordx2 v[100:101], v157, s[96:97]\n" \
  "2:\n" \
  "v_pk_mul_f32 v[204:205], v[192:193], v[192:193]\n" \
  "v_pk_add_f32 v[200:201], v[192:193], v[194:195]\n" \
  "v_pk_fma_f32 v[204:205], v[194:195], v[194:195], v[204:205]\n" \
  "v_pk_add_f32 v[202:203], v[196:197], v[198:199]\n" \
  "v_pk_fma_f32 v[204:205], v[196:197], v[196:197], v[204:205]\n" \
  "v_pk_add_f32 v[200:201], v[200:201], v[202:203]\n" \
  "v_pk_fma_f32 v[204:205], v[198:199], v[198:199], v[204:205]\n" \
  "v_add_f32 v202, v200, v201\n" \
  "v_add_f32 v204, v204, v205\n" \
  "v_sub_f32 v203, v200, v201\n" \
  "v_add_f32_dpp v202, v202, v202 row_ror:8 row_mask:0xf bank_mask:0xf\n" \
  "v_add_f32_dpp v204, v204, v204 row_ror:8 row_mask:0xf bank_mask:0xf\n" \
  "v_add_f32_dpp v203, v203, v203 row_ror:8 row_mask:0xf bank_mask:0xf\n" \
  "v_add_f32_dpp v202, v202, v202 row_ror:4 row_mask:0xf bank_mask:0xf\n" \
  "v_add_f32_dpp v204, v204, v204 row_ror:4 row_mask:0xf bank_mask:0xf\n" \
  "v_add_f32_dpp v203, v203, v203 row_ror:4 row_mask:0xf bank_mask:0xf\n" \
  "v_add_f32_dpp v202, v202, v202 row_ror:2 row_mask:0xf bank_mask:0xf\n" \
  "v_add_f32_dpp v204, v204, v204 row_ror:2 row_mask:0xf bank_mask:0xf\n" \
  "v_add_f32_dpp v203, v203, v203 row_ror:2 row_mask:0xf bank_mask:0xf\n" \
  "v_add_f32_dpp v202, v202, v202 row_ror:1 row_mask:0xf bank_mask:0xf\n" \
  "v_add_f32_dpp v204, v204, v204 row_ror:1 row_mask:0xf bank_mask:0xf\n" \
  "v_add_f32_dpp v203, v203, v203 row_ror:1 row_mask:0xf bank_mask:0xf\n" \
  "v_add_f32_dpp v202, v202, v202 row_bcast:15 row_mask:0xa bank_mask:0xf\n" \
  "v_add_f32_dpp v204, v204, v204 row_bcast:15 row_mask:0xa bank_mask:0xf\n" \
  "v_add_f32_dpp v203, v203, v203 row_bcast:15 row_mask:0xa bank_mask:0xf\n" \
  "v_add_f32_dpp v202, v202, v202 row_bcast:31 row_mask:0xc bank_mask:0xf\n" \
  "v_add_f32_dpp v204, v204, v204 row_bcast:31 row_mask:0xc bank_mask:0xf\n" \
  "v_add_f32_dpp v203, v203, v203 row_bcast:31 row_mask:0xc bank_mask:0xf\n" \
  "v_readlane_b32 s87, v202, 63\n" \
  "v_readlane_b32 s88, v204, 63\n" \
  "v_readlane_b32 s89, v203, 63\n" \
  "v_mov_b32 v200, 0x3b000000\n" \
  "v_mul_f32 v202, s87, v200\n" \
  "v_mul_f32 v204, s88, v200\n" \
  "v_fma_f32 v204, -v202, v202, v204\n" \
  "v_max_f32 v204, 0, v204\n" \
  "v_add_f32 v204, 0x3727c5ac, v204\n" \
  "v_rsq_f32 v200, v204\n" \
  "v_add_f32 v233, s94, v202\n" \
  "v_mul_f32 v202, v202, v200\n" \
  "v_pk_fma_f32 v[192:193], v[192:193], v[200:201], v[202:203] op_sel_hi:[1,0,0] neg_lo:[0,0,1] neg_hi:[0,0,1]\n" \
  "v_pk_fma_f32 v[194:195], v[194:195], v[200:201], v[202:203] op_sel_hi:[1,0,0] neg_lo:[0,0,1] neg_hi:[0,0,1]\n" \
  "v_pk_fma_f32 v[196:197], v[196:197], v[200:201], v[202:203] op_sel_hi:[1,0,0] neg_lo:[0,0,1] neg_hi:[0,0,1]\n" \
  "v_pk_fma_f32 v[198:199], v[198:199], v[200:201], v[202:203] op_sel_hi:[1,0,0] neg_lo:[0,0,1] neg_hi:[0,0,1]\n" \
  "v_cvt_pk_f16_f32 v192, v192, v193\n" \
  "v_cvt_pk_f16_f32 v193, v194, v195\n" \
  "v_cvt_pk_f16_f32 v194, v196, v197\n" \
  "v_cvt_pk_f16_f32 v195, v198, v199\n" \
  "ds_write_b128 v186, v[192:195] offset:8704\n" \
  "ds_read_b64_tr_b16 v[196:197], v148\n" \
  "ds_read_b64_tr_b16 v[198:199], v148 offset:128\n" \
  "v_mul_f32 v232, v204, v200\n" \
  "v_mul_f32 v200, 0x3d3504f3, v200\n" \
  "v_mul_f32 v234, s89, v200\n" \
  "s_waitcnt lgkmcnt(0)\n" \
  "v_mfma_f32_16x16x32_f16 v[208:211], v[196:199], v[104:107], 0\n" \
  "v_mfma_f32_16x16x32_f16 v[212:215], v[196:199], v[112:115], 0\n" \
  "v_mfma_f32_16x16x32_f16 v[216:219], v[196:199], v[108:111], 0\n" \
  "v_mfma_f32_16x16x32_f16 v[220:223], v[196:199], v[116:119], 0\n" \
  "s_nop 5\n" \
  "v_pk_mul_f32 v[204:205], v[212:213], v[128:129]\n" \
  "v_pk_mul_f32 v[206:207], v[208:209], v[128:129]\n" \
  "v_pk_mul_f32 v[192:193], v[214:215], v[130:131]\n" \
  "v_pk_mul_f32 v[194:195], v[210:211], v[130:131]\n" \
  "v_pk_fma_f32 v[208:209], v[208:209], v[124:125], v[204:205]\n" \
  "v_pk_fma_f32 v[212:213], v[212:213], v[124:125], v[206:207] neg_lo:[0,0,1] neg_hi:[0,0,1]\n" \
  "v_pk_fma_f32 v[210:211], v[210:211], v[126:127], v[192:193]\n" \
  "v_pk_fma_f32 v[214:215], v[214:215], v[126:127], v[194:195] neg_lo:[0,0,1] neg_hi:[0,0,1]\n" \
  "v_cvt_pk_f16_f32 v192, v208, v212\n" \
  "v_cvt_pk_f16_f32 v193, v209, v213\n" \
  "v_cvt_pk_f16_f32 v194, v210, v214\n" \
  "v_cvt_pk_f16_f32 v195, v211, v215\n" \
  "v_pk_mul_f32 v[204:205], v[220:221], v[136:137]\n" \
  "v_pk_mul_f32 v[206:207], v[216:217], v[136:137]\n" \
  "v_mfma_f32_16x16x32_f16 v[224:227], v[120:123], v[192:195], 0\n" \
  "v_pk_mul_f32 v[200:201], v[222:223], v[138:139]\n" \
  "v_pk_mul_f32 v[202:203], v[218:219], v[138:139]\n" \
  "v_pk_fma_f32 v[216:217], v[216:217], v[132:133], v[204:205]\n" \
  "v_pk_fma_f32 v[220:221], v[220:221], v[132:133], v[206:207] neg_lo:[0,0,1] neg_hi:[0,0,1]\n" \
  "v_pk_fma_f32 v[218:219], v[218:219], v[134:135], v[200:201]\n" \
  "v_pk_fma_f32 v[222:223], v[222:223], v[134:135], v[202:203] neg_lo:[0,0,1] neg_hi:[0,0,1]\n" \
  "v_cvt_pk_f16_f32 v200, v216, v220\n" \
  "v_cvt_pk_f16_f32 v201, v217, v221\n" \
  "v_cvt_pk_f16_f32 v202, v218, v222\n" \
  "v_cvt_pk_f16_f32 v203, v219, v223\n" \
  "s_mov_b64 exec, 1\n" \
  "v_mov_b32 v224, v234\n" \
  "v_mov_b32 v225, 0\n" \
  "s_mov_b64 exec, -1\n" \
  "v_mfma_f32_16x16x32_f16 v[228:231], v[120:123], v[200:203], 0\n" \
  "v_cvt_pk_f16_f32 v192, v224, v225\n" \
  "v_cvt_pk_f16_f32 v193, v226, v227\n" \
  "v_min_u32 v232, -2, v232\n" \
  "ds_write_b32 v150, v192 offset:0\n" \
  "ds_write_b32 v149, v193 offset:124\n" \
  "s_lshl_b32 s81, s22, 2\n" \
  "s_add_u32 s81, s81, s45\n" \
  "s_addk_i32 s81, 0x2000\n" \
  "v_cvt_pk_f16_f32 v194, v228, v229\n" \
  "v_cvt_pk_f16_f32 v195, v230, v231\n" \
  "ds_write_b32 v149, v194 offset:60\n" \
  "ds_write_b32 v149, v195 offset:188\n" \
  "s_waitcnt lgkmcnt(0)\n" \
  "s_barrier\n" \
  "ds_read_b32 v192, v151\n" \
  "ds_read_b32 v193, v151 offset:1056\n" \
  "ds_read_b32 v194, v151 offset:64\n" \
  "ds_read_b32 v195, v151 offset:1120\n" \
  "s_mov_b64 exec, 1\n" \
  "buffer_store_dword v232, off, s[24:27], s81 sc1\n" \
  "s_mov_b64 exec, -1\n" \
  "s_waitcnt lgkmcnt(0)\n" \
  "v_mfma_f32_16x16x32_f16 v[208:211], v[192:195], v[140:143], 0\n" \
  "v_mfma_f32_16x16x32_f16 v[212:215], v[192:195], v[144:147], 0\n" \
  "s_nop 7\n" \
  "v_cvt_pk_f16_f32 v236, v208, v212\n" \
  "v_cvt_pk_f16_f32 v237, v209, v213\n" \
  "v_cvt_pk_f16_f32 v238, v210, v214\n" \
  "v_cvt_pk_f16_f32 v239, v211, v215\n" \
  "buffer_store_dwordx4 v[236:239], v152, s[24:27], s45 offen sc1\n" \
  ASTAMP(48660, 0) \
  "s_waitcnt vmcnt(2)\n" \
  "s_bitcmp0_b32 0xffff0002, s65\n" \
  "s_cbranch_scc1 3f\n" \
  "ds_write_b16 v154, v100 offset:25344\n" \
  "ds_write_b16_d16_hi v154, v100 offset:26384\n" \
  "ds_write_b16 v154, v101 offset:27424\n" \
  "ds_write_b16_d16_hi v154, v101 offset:28464\n" \
  "s_waitcnt lgkmcnt(0)\n" \
  "s_barrier\n" \
  "3:\n" \
  "ds_write_b16 v153, v96 offset:48896\n" \
  "ds_write_b16_d16_hi v153, v96 offset:49936\n" \
  "ds_write_b16 v153, v97 offset:50976\n" \
  "ds_write_b16_d16_hi v153, v97 offset:52016\n" \
  "ds_write_b16 v153, v98 offset:53056\n" \
  "ds_write_b16_d16_hi v153, v98 offset:54096\n" \
  "ds_write_b16 v153, v99 offset:55136\n" \
  "ds_write_b16_d16_hi v153, v99 offset:56176\n" \
  "s_mov_b32 s80, 22456320\n" \
  "s_cmp_lt_u32 s22, 4\n" \
  "s_cbranch_scc1 4f\n" \
  "s_sleep 2\n" \
  "buffer_load_dwordx2 v[240:241], v156, s[24:27], s80 offen sc1\n" \
  "s_cmp_lg_u32 s65, 1\n" \
  "s_cbranch_scc1 6f\n" \
  "4:\n" \
  "s_cmp_lt_u32 s64, 128\n" \
  "s_cbranch_scc1 5f\n" \
  "s_and_b32 s81, s22, 3\n" \
  "s_mul_i32 s81, s81, 1040\n" \
  "v_add_u32 v192, s81, v21\n" \
  "ds_read_b128 v[28:31], v192 offset:25344\n" \
  "s_waitcnt lgkmcnt(0)\n" \
  "5:\n" \
  "v_readfirstlane_b32 s94, v28\n" \
  "v_lshlrev_b32 v192, 16, v28\n" \
  "v_lshlrev_b32 v194, 16, v29\n" \
  "v_lshlrev_b32 v196, 16, v30\n" \
  "v_lshlrev_b32 v198, 16, v31\n" \
  "s_lshl_b32 s94, s94, 16\n" \
  "v_and_b32 v193, 0xffff0000, v28\n" \
  "v_and_b32 v195, 0xffff0000, v29\n" \
  "v_and_b32 v197, 0xffff0000, v30\n" \
  "v_and_b32 v199, 0xffff0000, v31\n" \
  "v_pk_add_f32 v[192:193], v[192:193], s[94:95] op_sel_hi:[1,0] neg_lo:[0,1] neg_hi:[0,1]\n" \
  "v_pk_add_f32 v[194:195], v[194:195], s[94:95] op_sel_hi:[1,0] neg_lo:[0,1] neg_hi:[0,1]\n" \
  "v_pk_add_f32 v[196:197], v[196:197], s[94:95] op_sel_hi:[1,0] neg_lo:[0,1] neg_hi:[0,1]\n" \
  "v_pk_add_f32 v[198:199], v[198:199], s[94:95] op_sel_hi:[1,0] neg_lo:[0,1] neg_hi:[0,1]\n" \
  "v_pk_mul_f32 v[204:205], v[192:193], v[192:193]\n" \
  "v_pk_add_f32 v[200:201], v[192:193], v[194:195]\n" \
  "v_pk_fma_f32 v[204:205], v[194:195], v[194:195], v[204:205]\n" \
  "v_pk_add_f32 v[202:203], v[196:197], v[198:199]\n" \
  "v_pk_fma_f32 v[204:205], v[196:197], v[196:197], v[204:205]\n" \
  "v_pk_add_f32 v[200:201], v[200:201], v[202:203]\n" \
  "v_pk_fma_f32 v[204:205], v[198:199], v[198:199], v[204:205]\n" \
  "v_add_f32 v202, v200, v201\n" \
  "v_add_f32 v204, v204, v205\n" \
  "v_sub_f32 v203, v200, v201\n" \
  "v_add_f32_dpp v202, v202, v202 row_ror:8 row_mask:0xf bank_mask:0xf\n" \
  "v_add_f32_dpp v204, v204, v204 row_ror:8 row_mask:0xf bank_mask:0xf\n" \
  "v_add_f32_dpp v203, v203, v203 row_ror:8 row_mask:0xf bank_mask:0xf\n" \
  "v_add_f32_dpp v202, v202, v202 row_ror:4 row_mask:0xf bank_mask:0xf\n" \
  "v_add_f32_dpp v204, v204, v204 row_ror:4 row_mask:0xf bank_mask:0xf\n" \
  "v_add_f32_dpp v203, v203, v203 row_ror:4 row_mask:0xf bank_mask:0xf\n" \
  "v_add_f32_dpp v202, v202, v202 row_ror:2 row_mask:0xf bank_mask:0xf\n" \
  "v_add_f32_dpp v204, v204, v204 row_ror:2 row_mask:0xf bank_mask:0xf\n" \
  "v_add_f32_dpp v203, v203, v203 row_ror:2 row_mask:0xf bank_mask:0xf\n" \
  "v_add_f32_dpp v202, v202, v202 row_ror:1 row_mask:0xf bank_mask:0xf\n" \
  "v_add_f32_dpp v204, v204, v204 row_ror:1 row_mask:0xf bank_mask:0xf\n" \
  "v_add_f32_dpp v203, v203, v203 row_ror:1 row_mask:0xf bank_mask:0xf\n" \
  "v_add_f32_dpp v202, v202, v202 row_bcast:15 row_mask:0xa bank_mask:0xf\n" \
  "v_add_f32_dpp v204, v204, v204 row_bcast:15 row_mask:0xa bank_mask:0xf\n" \
  "v_add_f32_dpp v203, v203, v203 row_bcast:15 row_mask:0xa bank_mask:0xf\n" \
  "v_add_f32_dpp v202, v202, v202 row_bcast:31 row_mask:0xc bank_mask:0xf\n" \
  "v_add_f32_dpp v204, v204, v204 row_bcast:31 row_mask:0xc bank_mask:0xf\n" \
  "v_add_f32_dpp v203, v203, v203 row_bcast:31 row_mask:0xc bank_mask:0xf\n" \
  "v_readlane_b32 s87, v202, 63\n" \
  "v_readlane_b32 s88, v204, 63\n" \
  "v_readlane_b32 s89, v203, 63\n" \
  "v_mov_b32 v200, 0x3b000000\n" \
  "v_mul_f32 v202, s87, v200\n" \
  "v_mul_f32 v204, s88, v200\n" \
  "v_fma_f32 v204, -v202, v202, v204\n" \
  "v_max_f32 v204, 0, v204\n" \
  "v_add_f32 v204, 0x0da24260, v204\n" \
  "v_rsq_f32 v200, v204\n" \
  "v_add_f32 v233, s94, v202\n" \
  "v_mul_f32 v202, v202, v200\n" \
  "v_pk_fma_f32 v[192:193], v[192:193], v[200:201], v[202:203] op_sel_hi:[1,0,0] neg_lo:[0,0,1] neg_hi:[0,0,1]\n" \
  "v_pk_fma_f32 v[194:195], v[194:195], v[200:201], v[202:203] op_sel_hi:[1,0,0] neg_lo:[0,0,1] neg_hi:[0,0,1]\n" \
  "v_pk_fma_f32 v[196:197], v[196:197], v[200:201], v[202:203] op_sel_hi:[1,0,0] neg_lo:[0,0,1] neg_hi:[0,0,1]\n" \
  "v_pk_fma_f32 v[198:199], v[198:199], v[200:201], v[202:203] op_sel_hi:[1,0,0] neg_lo:[0,0,1] neg_hi:[0,0,1]\n" \
  "v_cvt_pk_f16_f32 v192, v192, v193\n" \
  "v_cvt_pk_f16_f32 v193, v194, v195\n" \
  "v_cvt_pk_f16_f32 v194, v196, v197\n" \
  "v_cvt_pk_f16_f32 v195, v198, v199\n" \
  "ds_write_b128 v186, v[192:195] offset:8704\n" \
  "ds_read_b64_tr_b16 v[196:197], v148\n" \
  "ds_read_b64_tr_b16 v[198:199], v148 offset:128\n" \
  "v_mul_f32 v232, v204, v200\n" \
  "v_mul_f32 v200, 0x3d3504f3, v200\n" \
  "v_mul_f32 v234, s89, v200\n" \
  "s_waitcnt lgkmcnt(0)\n" \
  "v_mfma_f32_16x16x32_f16 v[208:211], v[196:199], v[104:107], 0\n" \
  "v_mfma_f32_16x16x32_f16 v[212:215], v[196:199], v[112:115], 0\n" \
  "v_mfma_f32_16x16x32_f16 v[216:219], v[196:199], v[108:111], 0\n" \
  "v_mfma_f32_16x16x32_f16 v[220:223], v[196:199], v[116:119], 0\n" \
  "s_nop 5\n" \
  "v_pk_mul_f32 v[204:205], v[212:213], v[128:129]\n" \
  "v_pk_mul_f32 v[206:207], v[208:209], v[128:129]\n" \
  "v_pk_mul_f32 v[192:193], v[214:215], v[130:131]\n" \
  "v_pk_mul_f32 v[194:195], v[210:211], v[130:131]\n" \
  "v_pk_fma_f32 v[208:209], v[208:209], v[124:125], v[204:205]\n" \
  "v_pk_fma_f32 v[212:213], v[212:213], v[124:125], v[206:207] neg_lo:[0,0,1] neg_hi:[0,0,1]\n" \
  "v_pk_fma_f32 v[210:211], v[210:211], v[126:127], v[192:193]\n" \
  "v_pk_fma_f32 v[214:215], v[214:215], v[126:127], v[194:195] neg_lo:[0,0,1] neg_hi:[0,0,1]\n" \
  "v_cvt_pk_f16_f32 v192, v208, v212\n" \
  "v_cvt_pk_f16_f32 v193, v209, v213\n" \
  "v_cvt_pk_f16_f32 v194, v210, v214\n" \
  "v_cvt_pk_f16_f32 v195, v211, v215\n" \
  "v_pk_mul_f32 v[204:205], v[220:221], v[136:137]\n" \
  "v_pk_mul_f32 v[206:207], v[216:217], v[136:137]\n" \
  "v_mfma_f32_16x16x32_f16 v[224:227], v[120:123], v[192:195], 0\n" \
  "v_pk_mul_f32 v[200:201], v[222:223], v[138:139]\n" \
  "v_pk_mul_f32 v[202:203], v[218:219], v[138:139]\n" \
  "v_pk_fma_f32 v[216:217], v[216:217], v[132:133], v[204:205]\n" \
  "v_pk_fma_f32 v[220:221], v[220:221], v[132:133], v[206:207] neg_lo:[0,0,1] neg_hi:[0,0,1]\n" \
  "v_pk_fma_f32 v[218:219], v[218:219], v[134:135], v[200:201]\n" \
  "v_pk_fma_f32 v[222:223], v[222:223], v[134:135], v[202:203] neg_lo:[0,0,1] neg_hi:[0,0,1]\n" \
  "v_cvt_pk_f16_f32 v200, v216, v220\n" \
  "v_cvt_pk_f16_f32 v201, v217, v221\n" \
  "v_cvt_pk_f16_f32 v202, v218, v222\n" \
  "v_cvt_pk_f16_f32 v203, v219, v223\n" \
  "s_mov_b64 exec, 1\n" \
  "v_mov_b32 v224, v234\n" \
  "v_mov_b32 v225, 0\n" \
  "s_mov_b64 exec, -1\n" \
  "v_mfma_f32_16x16x32_f16 v[228:231], v[120:123], v[200:203], 0\n" \
  "v_pk_mul_f32 v[224:225], v[224:225], v[232:233] op_sel_hi:[1,0]\n" \
  "v_pk_mul_f32 v[226:227], v[226:227], v[232:233] op_sel_hi:[1,0]\n" \
  "s_cmp_lt_u32 s64, 128\n" \
  "s_cselect_b64 exec, 1, 0\n" \
  "v_mul_f32 v225, 0x41b504f3, v233\n" \
  "s_mov_b64 exec, -1\n" \
  "v_cvt_pk_bf16_f32 v192, v224, v225\n" \
  "v_cvt_pk_bf16_f32 v193, v226, v227\n" \
  "v_pk_mul_f32 v[228:229], v[228:229], v[232:233] op_sel_hi:[1,0]\n" \
  "v_pk_mul_f32 v[230:231], v[230:231], v[232:233] op_sel_hi:[1,0]\n" \
  "v_cvt_pk_bf16_f32 v194, v228, v229\n" \
  "v_cvt_pk_bf16_f32 v195, v230, v231\n" \
  "v_min_u32 v192, -2, v192\n" \
  "v_min_u32 v193, -2, v193\n" \
  "v_min_u32 v194, -2, v194\n" \
  "v_min_u32 v195, -2, v195\n" \
  "ds_write_b32 v150, v192 offset:16896\n" \
  "ds_write_b32 v149, v193 offset:17020\n" \
  "ds_write_b32 v149, v194 offset:16956\n" \
  "ds_write_b32 v149, v195 offset:17084\n" \
  "s_cmp_lt_u32 s22, 4\n" \
  "s_cbranch_scc1 7f\n" \
  "6:\n" \
  "s_mov_b32 s78, 0x10000\n" \
  "8:\n" \
  "s_waitcnt vmcnt(0)\n" \
  "v_cmp_ne_u64 vcc, s[36:37], v[240:241]\n" \
  "s_cbranch_vccz 7f\n" \
  "s_sleep 2\n" \
  "buffer_load_dwordx2 v[240:241], v156, s[24:27], s80 offen sc1\n" \
  "s_sub_u32 s78, s78, 1\n" \
  "s_cbranch_scc0 8b\n" \
  "7:\n" \
  ASTAMP(48664, 0) \
  "s_waitcnt lgkmcnt(0)\n" \
  "s_barrier\n" \
  "s_mov_b32 s78, 0x10000\n" \
  ASTAMP(48668, 0) \
  "buffer_load_dwordx4 v[0:3], v168, s[28:31], s40 offen sc1\n" \
  "s_cmp_ge_u32 s64, 264\n" \
  "s_cbranch_scc1 2f\n" \
  "s_cmp_lt_u32 s64, 128\n" \
  "s_cbranch_scc1 1f\n" \
  "ds_read_b128 v[116:119], v155 offset:16896\n" \
  "s_branch 3f\n" \
  "1:\n" \
  "ds_read_b32 v116, v155 offset:16896\n" \
  "ds_read_b32 v117, v155 offset:17952\n" \
  "ds_read_b32 v118, v155 offset:19008\n" \
  "ds_read_b32 v119, v155 offset:20064\n" \
  "3:\n" \
  "s_lshl_b32 s81, s64, 12\n" \
  "s_add_u32 s81, s81, 21374976\n" \
  "v_lshrrev_b32 v156, 3, v156\n" \
  "s_waitcnt lgkmcnt(0)\n" \
  "buffer_store_dwordx4 v[116:119], v156, s[24:27], s81 offen sc1\n" \
  "2:\n" \
  "ds_read_b128 v[12:15], v170 offset:48896\n" \
  "ds_read_b128 v[32:35], v4 offset:15232\n" \
  "ds_read_b128 v[36:39], v4 offset:16256\n" \
  "ds_read_b128 v[40:43], v4 offset:17280\n" \
  "ds_read_b128 v[44:47], v4 offset:18304\n" \
  "ds_read_b128 v[48:51], v4 offset:19328\n" \
  "ds_read_b128 v[52:55], v4 offset:20352\n" \
  "ds_read_b128 v[56:59], v4 offset:21376\n" \
  "ds_read_b128 v[60:63], v4 offset:22400\n" \
  "ds_read_b128 v[64:67], v4 offset:23424\n" \
  "ds_read_b128 v[68:71], v4 offset:24448\n" \
  "ds_read_b128 v[72:75], v4 offset:25472\n" \
  "ds_read_b128 v[76:79], v4 offset:26496\n" \
  "s_waitcnt lgkmcnt(12)\n" \
  "v_lshlrev_b32 v80, 16, v12\n" \
  "v_lshlrev_b32 v81, 16, v13\n" \
  "v_lshlrev_b32 v82, 16, v14\n" \
  "v_lshlrev_b32 v83, 16, v15\n" \
  "v_and_b32 v84, 0xffff0000, v12\n" \
  "v_and_b32 v85, 0xffff0000, v13\n" \
  "v_and_b32 v86, 0xffff0000, v14\n" \
  "buffer_load_dwordx4 v[96:99], v173, s[28:31], s42 offen sc1\n" \
  "v_and_b32 v87, 0xffff0000, v15\n" \
  "v_max3_f32 v10, |v80|, |v81|, |v82|\n" \
  "v_max3_f32 v10, v10, |v83|, |v84|\n" \
  "v_max3_f32 v10, v10, |v85|, |v86|\n" \
  "v_max_f32 v10, v10, |v87|\n" \
  "s_nop 1\n" \
  "buffer_load_dwordx4 v[100:103], v173, s[28:31], s43 offen sc1\n" \
  "v_max_f32_dpp v10, v10, v10 row_ror:8 row_mask:0xf bank_mask:0xf\n" \
  "s_nop 1\n" \
  "v_max_f32_dpp v10, v10, v10 row_ror:4 row_mask:0xf bank_mask:0xf\n" \
  "s_nop 1\n" \
  "v_max_f32_dpp v10, v10, v10 row_ror:2 row_mask:0xf bank_mask:0xf\n" \
  "s_nop 1\n" \
  "v_max_f32_dpp v10, v10, v10 row_ror:1 row_mask:0xf bank_mask:0xf\n" \
  "buffer_load_dwordx4 v[104:107], v173, s[28:31], s44 offen sc1\n" \
  "s_nop 1\n" \
  "v_max_f32_dpp v10, v10, v10 row_bcast:15 row_mask:0xa bank_mask:0xf\n" \
  "s_nop 1\n" \
  "v_max_f32_dpp v10, v10, v10 row_bcast:31 row_mask:0xc bank_mask:0xf\n" \
  "s_nop 0\n" \
  "v_readlane_b32 s39, v10, 63\n" \
  "buffer_load_dwordx4 v[108:111], v173, s[24:27], s45 offen sc1\n" \
  "s_and_b32 s39, s39, 0x7f800000\n" \
  "s_max_u32 s39, s39, 0x10000000\n" \
  "s_min_u32 s39, s39, 0x6f000000\n" \
  "s_sub_u32 s38, 0x7e800000, s39\n" \
  "s_sub_u32 s39, s39, 0x4afb0d\n" \
  "v_mul_f32 v80, s38, v80\n" \
  "v_mul_f32 v81, s38, v81\n" \
  "buffer_load_dwordx2 v[112:113], v174, s[24:27], 0 offen sc1\n" \
  "v_mul_f32 v82, s38, v82\n" \
  "v_mul_f32 v83, s38, v83\n" \
  "v_mul_f32 v84, s38, v84\n" \
  "v_mul_f32 v85, s38, v85\n" \
  "v_mul_f32 v86, s38, v86\n" \
  "v_mul_f32 v87, s38, v87\n" \
  "s_waitcnt vmcnt(5)\n" \
  "s_branch 87f\n" \
  "86:\n" \
  "buffer_load_dwordx4 v[0:3], v168, s[28:31], s40 offen sc1\n" \
  "s_waitcnt vmcnt(0)\n" \
  "87:\n" \
  "ds_write_b32 v169, v0 offset:0\n" \
  "ds_write_b32 v169, v1 offset:1040\n" \
  "ds_write_b32 v169, v2 offset:2080\n" \
  "ds_write_b32 v169, v3 offset:3120\n" \
  "v_max3_u32 v10, v0, v1, v2\n" \
  "v_max3_u32 v10, v10, v3, v3\n" \
  "v_cmp_eq_u32 vcc, -1, v10\n" \
  "s_cbranch_vccz 88f\n" \
  "s_sub_u32 s78, s78, 1\n" \
  "s_cbranch_scc0 86b\n" \
  "88:\n" \
  ASTAMP(48672, 0) \
  "s_waitcnt lgkmcnt(0)\n" \
  "s_barrier\n" \
  "ds_read_b128 v[0:3], v170\n" \
  "s_waitcnt lgkmcnt(0)\n" \
  "v_mfma_f32_16x16x32_f16 v[12:15], v[0:3], v[32:35], 0\n" \
  "v_mfma_f32_16x16x32_f16 v[16:19], v[0:3], v[36:39], 0\n" \
  "buffer_load_dwordx4 v[152:155], v180, s[24:27], s62 offen offset:0 sc1\n" \
  "s_nop 6\n" \
  "v_pk_mul_f32 v[10:11], v[16:17], v[68:69]\n" \
  "v_pk_mul_f32 v[22:23], v[12:13], v[68:69]\n" \
  "v_pk_mul_f32 v[0:1], v[18:19], v[70:71]\n" \
  "v_pk_mul_f32 v[2:3], v[14:15], v[70:71]\n" \
  "v_pk_fma_f32 v[12:13], v[12:13], v[64:65], v[10:11]\n" \
  "v_pk_fma_f32 v[16:17], v[16:17], v[64:65], v[22:23] neg_lo:[0,0,1] neg_hi:[0,0,1]\n" \
  "v_pk_fma_f32 v[14:15], v[14:15], v[66:67], v[0:1]\n" \
  "v_pk_fma_f32 v[18:19], v[18:19], v[66:67], v[2:3] neg_lo:[0,0,1] neg_hi:[0,0,1]\n" \
  "v_cvt_pk_f16_f32 v0, v12, v16\n" \
  "v_cvt_pk_f16_f32 v1, v13, v17\n" \
  "v_cvt_pk_f16_f32 v2, v14, v18\n" \
  "v_cvt_pk_f16_f32 v3, v15, v19\n" \
  "s_nop 1\n" \
  "v_mfma_f32_16x16x32_f16 v[12:15], v[40:43], v[0:3], 0\n" \
  "v_mfma_f32_16x16x32_f16 v[16:19], v[44:47], v[0:3], 0\n" \
  "buffer_load_dwordx4 v[156:159], v180, s[24:27], s62 offen offset:256 sc1\n" \
  "s_nop 6\n" \
  "v_pk_mul_f32 v[10:11], v[16:17], v[84:85]\n" \
  "v_pk_mul_f32 v[22:23], v[12:13], v[84:85]\n" \
  "v_pk_mul_f32 v[0:1], v[18:19], v[86:87]\n" \
  "v_pk_mul_f32 v[2:3], v[14:15], v[86:87]\n" \
  "v_pk_fma_f32 v[12:13], v[12:13], v[80:81], v[10:11] neg_lo:[0,0,1] neg_hi:[0,0,1]\n" \
  "v_pk_fma_f32 v[16:17], v[16:17], v[80:81], v[22:23]\n" \
  "v_pk_fma_f32 v[14:15], v[14:15], v[82:83], v[0:1] neg_lo:[0,0,1] neg_hi:[0,0,1]\n" \
  "v_pk_fma_f32 v[18:19], v[18:19], v[82:83], v[2:3]\n" \
  "v_cvt_pk_f16_f32 v0, v12, v16\n" \
  "v_cvt_pk_f16_f32 v1, v13, v17\n" \
  "v_cvt_pk_f16_f32 v2, v14, v18\n" \
  "v_cvt_pk_f16_f32 v3, v15, v19\n" \
  "s_nop 1\n" \
  "v_mfma_f32_16x16x32_f16 v[12:15], v[0:3], v[48:51], 0\n" \
  "v_mfma_f32_16x16x32_f16 v[16:19], v[0:3], v[52:55], 0\n" \
  "s_add_u32 s87, s62, 16384\n" \
  "buffer_load_dwordx4 v[160:163], v180, s[24:27], s87 offen offset:0 sc1\n" \
  "s_nop 6\n" \
  "v_pk_mul_f32 v[10:11], v[16:17], v[76:77]\n" \
  "v_pk_mul_f32 v[22:23], v[12:13], v[76:77]\n" \
  "v_pk_mul_f32 v[0:1], v[18:19], v[78:79]\n" \
  "v_pk_mul_f32 v[2:3], v[14:15], v[78:79]\n" \
  "v_pk_fma_f32 v[12:13], v[12:13], v[72:73], v[10:11] neg_lo:[0,0,1] neg_hi:[0,0,1]\n" \
  "v_pk_fma_f32 v[16:17], v[16:17], v[72:73], v[22:23]\n" \
  "v_pk_fma_f32 v[14:15], v[14:15], v[74:75], v[0:1] neg_lo:[0,0,1] neg_hi:[0,0,1]\n" \
  "v_pk_fma_f32 v[18:19], v[18:19], v[74:75], v[2:3]\n" \
  "v_cvt_pk_f16_f32 v0, v12, v16\n" \
  "v_cvt_pk_f16_f32 v1, v13, v17\n" \
  "v_cvt_pk_f16_f32 v2, v14, v18\n" \
  "v_cvt_pk_f16_f32 v3, v15, v19\n" \
  "s_nop 1\n" \
  "v_mfma_f32_16x16x32_f16 v[12:15], v[0:3], v[56:59], 0\n" \
  "v_mfma_f32_16x16x32_f16 v[16:19], v[0:3], v[60:63], 0\n" \
  "v_mov_b32 v10, s39\n" \
  "buffer_load_dwordx4 v[164:167], v180, s[24:27], s87 offen offset:256 sc1\n" \
  "s_nop 5\n" \
  "v_pk_mul_f32 v[12:13], v[12:13], v[10:11] op_sel_hi:[1,0]\n" \
  "v_pk_mul_f32 v[16:17], v[16:17], v[10:11] op_sel_hi:[1,0]\n" \
  "v_pk_mul_f32 v[14:15], v[14:15], v[10:11] op_sel_hi:[1,0]\n" \
  "v_pk_mul_f32 v[18:19], v[18:19], v[10:11] op_sel_hi:[1,0]\n" \
  "v_cvt_pk_bf16_f32 v0, v12, v16\n" \
  "v_cvt_pk_bf16_f32 v1, v13, v17\n" \
  "v_cvt_pk_bf16_f32 v2, v14, v18\n" \
  "v_cvt_pk_bf16_f32 v3, v15, v19\n" \
  "ds_write_b128 v171, v[0:3] offset:16384\n" \
  "s_waitcnt lgkmcnt(0)\n" \
  "s_barrier\n" \
  "ds_read_b128 v[0:3], v172 offset:16384\n" \
  "s_waitcnt lgkmcnt(0)\n" \
  "buffer_store_dwordx4 v[0:3], v186, s[28:31], s41 offen sc1\n" \
  ASTAMP(48676, 0) \
  "ds_read_b128 v[136:139], v4 offset:27520\n" \
  "s_waitcnt vmcnt(5)\n" \
  "s_mov_b32 s78, 0x10000\n" \
  "89:\n" \
  "v_max3_u32 v10, v96, v97, v98\n" \
  "v_max3_u32 v10, v10, v99, v100\n" \
  "v_max3_u32 v10, v10, v101, v102\n" \
  "v_max3_u32 v10, v10, v103, v104\n" \
  "v_max3_u32 v10, v10, v105, v106\n" \
  "v_max3_u32 v10, v10, v107, v108\n" \
  "v_max3_u32 v10, v10, v109, v110\n" \
  "v_max3_u32 v10, v10, v111, v112\n" \
  "v_max3_u32 v10, v10, v113, v113\n" \
  "v_cmp_eq_u32 vcc, -1, v10\n" \
  "s_cbranch_vccz 90f\n" \
  "buffer_load_dwordx4 v[96:99], v173, s[28:31], s42 offen sc1\n" \
  "buffer_load_dwordx4 v[100:103], v173, s[28:31], s43 offen sc1\n" \
  "buffer_load_dwordx4 v[104:107], v173, s[28:31], s44 offen sc1\n" \
  "buffer_load_dwordx4 v[108:111], v173, s[24:27], s45 offen sc1\n" \
  "buffer_load_dwordx2 v[112:113], v174, s[24:27], 0 offen sc1\n" \
  "s_sub_u32 s78, s78, 1\n" \
  "s_waitcnt vmcnt(0)\n" \
  "s_cbranch_scc0 89b\n" \
  "90:\n" \
  "ds_write_b128 v186, v[96:99] offset:0\n" \
  "ds_write_b128 v186, v[100:103] offset:8192\n" \
  "ds_write_b128 v186, v[104:107] offset:32768\n" \
  "ds_read2st64_b32 v[96:97], v176 offset0:0 offset1:2\n" \
  "ds_read2st64_b32 v[98:99], v177 offset0:0 offset1:2\n" \
  "ds_write_b128 v186, v[108:111] offset:0\n" \
  "ds_read2st64_b32 v[100:101], v176 offset0:32 offset1:34\n" \
  "ds_read2st64_b32 v[102:103], v177 offset0:32 offset1:34\n" \
  "ds_read2st64_b32 v[104:105], v176 offset0:128 offset1:130\n" \
  "buffer_load_dwordx4 v[192:195], v180, s[24:27], s61 offen offset:0 sc1\n" \
  "ds_read2st64_b32 v[106:107], v177 offset0:128 offset1:130\n" \
  "ds_read2st64_b32 v[108:109], v176 offset0:0 offset1:2\n" \
  "ds_read2st64_b32 v[110:111], v177 offset0:0 offset1:2\n" \
  "v_mul_f32 v22, 0x3eb504f3, v112\n" \
  "v_mul_f32 v23, 0x3eb504f3, v113\n" \
  "v_xor_b32 v5, 64, v182\n" \
  "v_xor_b32 v6, 64, v183\n" \
  "v_xor_b32 v179, 0x80, v182\n" \
  "buffer_load_dwordx4 v[196:199], v180, s[24:27], s61 offen offset:256 sc1\n" \
  "v_xor_b32 v191, 0x80, v183\n" \
  "v_xor_b32 v140, 0xc0, v182\n" \
  "v_xor_b32 v141, 0xc0, v183\n" \
  "v_xor_b32 v142, 64, v184\n" \
  "v_xor_b32 v143, 64, v185\n" \
  "v_mov_b32_dpp v12, v22 quad_perm:[0,0,0,0] row_mask:0xf bank_mask:0xf\n" \
  "v_mov_b32_dpp v14, v23 quad_perm:[0,0,0,0] row_mask:0xf bank_mask:0xf\n" \
  "v_mov_b32_dpp v16, v22 quad_perm:[1,1,1,1] row_mask:0xf bank_mask:0xf\n" \
  "buffer_load_dwordx4 v[200:203], v180, s[24:27], s61 offen offset:512 sc1\n" \
  "v_mov_b32_dpp v18, v23 quad_perm:[1,1,1,1] row_mask:0xf bank_mask:0xf\n" \
  "v_mov_b32_dpp v0, v22 quad_perm:[2,2,2,2] row_mask:0xf bank_mask:0xf\n" \
  "v_mov_b32_dpp v2, v23 quad_perm:[2,2,2,2] row_mask:0xf bank_mask:0xf\n" \
  "v_mov_b32_dpp v168, v22 quad_perm:[3,3,3,3] row_mask:0xf bank_mask:0xf\n" \
  "v_mov_b32_dpp v170, v23 quad_perm:[3,3,3,3] row_mask:0xf bank_mask:0xf\n" \
  "s_waitcnt lgkmcnt(7)\n" \
  "v_mfma_f32_16x16x16_f16 v[112:115], v[138:139], v[96:97], 0\n" \
  "v_mfma_f32_16x16x16_f16 v[116:119], v[138:139], v[98:99], 0\n" \
  "s_waitcnt lgkmcnt(4)\n" \
  "buffer_load_dwordx4 v[204:207], v180, s[24:27], s61 offen offset:768 sc1\n" \
  "v_mfma_f32_16x16x16_f16 v[120:123], v[138:139], v[100:101], 0\n" \
  "v_mfma_f32_16x16x16_f16 v[124:127], v[138:139], v[102:103], 0\n" \
  "s_waitcnt lgkmcnt(2)\n" \
  "v_mfma_f32_16x16x16_f16 v[128:131], v[138:139], v[104:105], 0\n" \
  "v_mfma_f32_16x16x16_f16 v[132:135], v[138:139], v[106:107], 0\n" \
  "s_waitcnt lgkmcnt(0)\n" \
  "v_mfma_f32_16x16x16_f16 v[24:27], v[138:139], v[108:109], 0\n" \
  "v_mfma_f32_16x16x16_f16 v[28:31], v[138:139], v[110:111], 0\n" \
  "buffer_load_dwordx4 v[208:211], v180, s[24:27], s61 offen offset:1024 sc1\n" \
  "v_pk_mul_f32 v[112:113], v[112:113], v[12:13] op_sel_hi:[1,0]\n" \
  "v_pk_mul_f32 v[114:115], v[114:115], v[14:15] op_sel_hi:[1,0]\n" \
  "v_pk_mul_f32 v[116:117], v[116:117], v[12:13] op_sel_hi:[1,0]\n" \
  "v_pk_mul_f32 v[118:119], v[118:119], v[14:15] op_sel_hi:[1,0]\n" \
  "v_pk_mul_f32 v[120:121], v[120:121], v[16:17] op_sel_hi:[1,0]\n" \
  "v_pk_mul_f32 v[122:123], v[122:123], v[18:19] op_sel_hi:[1,0]\n" \
  "v_pk_mul_f32 v[124:125], v[124:125], v[16:17] op_sel_hi:[1,0]\n" \
  "v_pk_mul_f32 v[126:127], v[126:127], v[18:19] op_sel_hi:[1,0]\n" \
  "buffer_load_dwordx4 v[212:215], v180, s[24:27], s61 offen offset:1280 sc1\n" \
  "v_pk_mul_f32 v[128:129], v[128:129], v[0:1] op_sel_hi:[1,0]\n" \
  "v_pk_mul_f32 v[130:131], v[130:131], v[2:3] op_sel_hi:[1,0]\n" \
  "v_pk_mul_f32 v[132:133], v[132:133], v[0:1] op_sel_hi:[1,0]\n" \
  "v_pk_mul_f32 v[134:135], v[134:135], v[2:3] op_sel_hi:[1,0]\n" \
  "v_pk_mul_f32 v[24:25], v[24:25], v[168:169] op_sel_hi:[1,0]\n" \
  "v_pk_mul_f32 v[26:27], v[26:27], v[170:171] op_sel_hi:[1,0]\n" \
  "v_pk_mul_f32 v[28:29], v[28:29], v[168:169] op_sel_hi:[1,0]\n" \
  "v_pk_mul_f32 v[30:31], v[30:31], v[170:171] op_sel_hi:[1,0]\n" \
  "s_waitcnt vmcnt(7)\n" \
  "s_mov_b32 s78, 0x10000\n" \
  "91:\n" \
  "v_max3_u32 v10, v152, v153, v154\n" \
  "v_max3_u32 v10, v10, v155, v156\n" \
  "v_max3_u32 v10, v10, v157, v158\n" \
  "v_max3_u32 v10, v10, v159, v160\n" \
  "v_max3_u32 v10, v10, v161, v162\n" \
  "v_max3_u32 v10, v10, v163, v164\n" \
  "v_max3_u32 v10, v10, v165, v166\n" \
  "v_max3_u32 v10, v10, v167, v167\n" \
  "v_cmp_eq_u32 vcc, -1, v10\n" \
  "s_cbranch_vccz 92f\n" \
  "buffer_load_dwordx4 v[152:155], v180, s[24:27], s62 offen offset:0 sc1\n" \
  "buffer_load_dwordx4 v[156:159], v180, s[24:27], s62 offen offset:256 sc1\n" \
  "s_add_u32 s87, s62, 16384\n" \
  "buffer_load_dwordx4 v[160:163], v180, s[24:27], s87 offen offset:0 sc1\n" \
  "buffer_load_dwordx4 v[164:167], v180, s[24:27], s87 offen offset:256 sc1\n" \
  "s_sub_u32 s78, s78, 1\n" \
  "s_waitcnt vmcnt(0)\n" \
  "s_cbranch_scc0 91b\n" \
  "92:\n" \
  ASTAMP(48680, 0) \
  "s_mov_b32 s78, 0x10000\n" \
  "buffer_load_dwordx4 v[96:99], v181, s[28:31], s46 offen sc1\n" \
  "buffer_load_dwordx4 v[100:103], v181, s[28:31], s47 offen sc1\n" \
  "buffer_load_dwordx4 v[104:107], v181, s[28:31], s48 offen sc1\n" \
  "buffer_load_dwordx4 v[108:111], v181, s[28:31], s49 offen sc1\n" \
  "s_sleep 6\n" \
  "buffer_load_dwordx4 v[216:219], v180, s[24:27], s61 offen offset:1536 sc1\n" \
  "buffer_load_dwordx4 v[220:223], v180, s[24:27], s61 offen offset:1792 sc1\n" \
  "buffer_load_dwordx4 v[224:227], v180, s[24:27], s61 offen offset:2048 sc1\n" \
  "s_branch 94f\n" \
  "93:\n" \
  "buffer_load_dwordx4 v[96:99], v181, s[28:31], s46 offen sc1\n" \
  "buffer_load_dwordx4 v[100:103], v181, s[28:31], s47 offen sc1\n" \
  "buffer_load_dwordx4 v[104:107], v181, s[28:31], s48 offen sc1\n" \
  "buffer_load_dwordx4 v[108:111], v181, s[28:31], s49 offen sc1\n" \
  "s_waitcnt vmcnt(0)\n" \
  "94:\n" \
  "s_cmp_eq_u32 s34, 1\n" \
  "s_cbranch_scc1 96f\n" \
  "s_cmp_eq_u32 s34, 2\n" \
  "s_cbranch_scc1 97f\n" \
  "s_cmp_eq_u32 s34, 3\n" \
  "s_cbranch_scc1 98f\n" \
  "95:\n" \
  "s_waitcnt vmcnt(3)\n" \
  "v_mov_b32 v0, v96\n" \
  "v_mov_b32 v1, v100\n" \
  "v_mov_b32 v2, v104\n" \
  "v_mov_b32 v3, v108\n" \
  "v_mfma_f32_16x16x16_bf16 v[12:15], v[136:137], v[0:1], v[24:27]\n" \
  "v_mov_b32 v168, v97\n" \
  "v_mfma_f32_16x16x16_bf16 v[16:19], v[136:137], v[2:3], v[28:31]\n" \
  "v_mov_b32 v169, v101\n" \
  "v_mov_b32 v170, v105\n" \
  "v_mov_b32 v171, v109\n" \
  "v_mov_b32 v172, v98\n" \
  "v_mov_b32 v173, v102\n" \
  "v_mov_b32 v174, v106\n" \
  "v_mov_b32 v175, v110\n" \
  "v_mov_b32 v10, v99\n" \
  "v_mov_b32 v11, v103\n" \
  "v_mov_b32 v22, v107\n" \
  "v_mov_b32 v23, v111\n" \
  "s_branch 99f\n" \
  "96:\n" \
  "s_waitcnt vmcnt(3)\n" \
  "v_mov_b32 v0, v97\n" \
  "v_mov_b32 v1, v101\n" \
  "v_mov_b32 v2, v105\n" \
  "v_mov_b32 v3, v109\n" \
  "v_mfma_f32_16x16x16_bf16 v[12:15], v[136:137], v[0:1], v[24:27]\n" \
  "v_mov_b32 v168, v98\n" \
  "v_mfma_f32_16x16x16_bf16 v[16:19], v[136:137], v[2:3], v[28:31]\n" \
  "v_mov_b32 v169, v102\n" \
  "v_mov_b32 v170, v106\n" \
  "v_mov_b32 v171, v110\n" \
  "v_mov_b32 v172, v99\n" \
  "v_mov_b32 v173, v103\n" \
  "v_mov_b32 v174, v107\n" \
  "v_mov_b32 v175, v111\n" \
  "v_mov_b32 v10, v96\n" \
  "v_mov_b32 v11, v100\n" \
  "v_mov_b32 v22, v104\n" \
  "v_mov_b32 v23, v108\n" \
  "s_branch 99f\n" \
  "97:\n" \
  "s_waitcnt vmcnt(3)\n" \
  "v_mov_b32 v0, v98\n" \
  "v_mov_b32 v1, v102\n" \
  "v_mov_b32 v2, v106\n" \
  "v_mov_b32 v3, v110\n" \
  "v_mfma_f32_16x16x16_bf16 v[12:15], v[136:137], v[0:1], v[24:27]\n" \
  "v_mov_b32 v168, v99\n" \
  "v_mfma_f32_16x16x16_bf16 v[16:19], v[136:137], v[2:3], v[28:31]\n" \
  "v_mov_b32 v169, v103\n" \
  "v_mov_b32 v170, v107\n" \
  "v_mov_b32 v171, v111\n" \
  "v_mov_b32 v172, v96\n" \
  "v_mov_b32 v173, v100\n" \
  "v_mov_b32 v174, v104\n" \
  "v_mov_b32 v175, v108\n" \
  "v_mov_b32 v10, v97\n" \
  "v_mov_b32 v11, v101\n" \
  "v_mov_b32 v22, v105\n" \
  "v_mov_b32 v23, v109\n" \
  "s_branch 99f\n" \
  "98:\n" \
  "s_waitcnt vmcnt(3)\n" \
  "v_mov_b32 v0, v99\n" \
  "v_mov_b32 v1, v103\n" \
  "v_mov_b32 v2, v107\n" \
  "v_mov_b32 v3, v111\n" \
  "v_mfma_f32_16x16x16_bf16 v[12:15], v[136:137], v[0:1], v[24:27]\n" \
  "v_mov_b32 v168, v96\n" \
  "v_mfma_f32_16x16x16_bf16 v[16:19], v[136:137], v[2:3], v[28:31]\n" \
  "v_mov_b32 v169, v100\n" \
  "v_mov_b32 v170, v104\n" \
  "v_mov_b32 v171, v108\n" \
  "v_mov_b32 v172, v97\n" \
  "v_mov_b32 v173, v101\n" \
  "v_mov_b32 v174, v105\n" \
  "v_mov_b32 v175, v109\n" \
  "v_mov_b32 v10, v98\n" \
  "v_mov_b32 v11, v102\n" \
  "v_mov_b32 v22, v106\n" \
  "v_mov_b32 v23, v110\n" \
  "99:\n" \
  "s_mov_b64 exec, s[72:73]\n" \
  "v_mov_b32 v17, 0\n" \
  "v_mov_b32 v19, 0\n" \
  "s_mov_b64 exec, -1\n" \
  "v_cvt_pk_bf16_f32 v0, v12, v13\n" \
  "v_cvt_pk_bf16_f32 v1, v14, v15\n" \
  "v_cvt_pk_bf16_f32 v2, v16, v17\n" \
  "v_cvt_pk_bf16_f32 v3, v18, v19\n" \
  "ds_write_b32 v182, v0 offset:24576\n" \
  "ds_write_b32 v183, v1 offset:24576\n" \
  "ds_write_b32 v5, v2 offset:24576\n" \
  "ds_write_b32 v6, v3 offset:24576\n" \
  "ds_read_b128 v[0:3], v184 offset:16384\n" \
  "v_max3_u32 v21, v96, v97, v98\n" \
  "v_max3_u32 v21, v21, v99, v100\n" \
  "v_max3_u32 v21, v21, v101, v102\n" \
  "v_max3_u32 v21, v21, v103, v104\n" \
  "v_max3_u32 v21, v21, v105, v106\n" \
  "v_max3_u32 v21, v21, v107, v108\n" \
  "v_max3_u32 v21, v21, v109, v110\n" \
  "v_max3_u32 v21, v21, v111, v111\n" \
  "v_cmp_eq_u32 vcc, -1, v21\n" \
  "s_cbranch_vccz 100f\n" \
  "s_sub_u32 s78, s78, 1\n" \
  "s_cbranch_scc0 93b\n" \
  "100:\n" \
  ASTAMP(48684, 0) \
  "ds_read_b128 v[108:111], v142 offset:16384\n" \
  "v_mfma_f32_16x16x16_bf16 v[112:115], v[136:137], v[168:169], v[112:115]\n" \
  "v_mfma_f32_16x16x16_bf16 v[116:119], v[136:137], v[170:171], v[116:119]\n" \
  "v_mfma_f32_16x16x16_bf16 v[120:123], v[136:137], v[172:173], v[120:123]\n" \
  "v_mfma_f32_16x16x16_bf16 v[124:127], v[136:137], v[174:175], v[124:127]\n" \
  "v_mfma_f32_16x16x16_bf16 v[128:131], v[136:137], v[10:11], v[128:131]\n" \
  "v_mfma_f32_16x16x16_bf16 v[132:135], v[136:137], v[22:23], v[132:135]\n" \
  "buffer_load_dwordx4 v[228:231], v180, s[24:27], s61 offen offset:2304 sc1\n" \
  "s_waitcnt lgkmcnt(1)\n" \
  "v_mfma_f32_16x16x32_bf16 v[104:107], v[0:3], v[0:3], 0\n" \
  "v_mfma_f32_16x16x32_bf16 v[96:99], v[152:155], v[0:3], 0\n" \
  "v_mfma_f32_16x16x32_bf16 v[100:103], v[0:3], v[160:163], 0\n" \
  "s_waitcnt lgkmcnt(0)\n" \
  "v_mfma_f32_16x16x32_bf16 v[104:107], v[108:111], v[108:111], v[104:107]\n" \
  "v_mfma_f32_16x16x32_bf16 v[96:99], v[156:159], v[108:111], v[96:99]\n" \
  "v_mfma_f32_16x16x32_bf16 v[100:103], v[108:111], v[164:167], v[100:103]\n" \
  "v_cvt_pk_bf16_f32 v112, v112, v113\n" \
  "v_cvt_pk_bf16_f32 v113, v114, v115\n" \
  "v_cvt_pk_bf16_f32 v116, v116, v117\n" \
  "v_cvt_pk_bf16_f32 v117, v118, v119\n" \
  "v_cvt_pk_bf16_f32 v120, v120, v121\n" \
  "v_cvt_pk_bf16_f32 v121, v122, v123\n" \
  "v_cndmask_b32 v104, v104, v105, s[68:69]\n" \
  "v_cndmask_b32 v106, v106, v107, s[68:69]\n" \
  "v_cndmask_b32 v104, v104, v106, s[70:71]\n" \
  "s_mov_b32 exec_lo, 0x00ff00ff\n" \
  "s_mov_b32 exec_hi, 0xff00ff00\n" \
  "ds_write_b128 v186, v[96:99] offset:32768\n" \
  "s_not_b64 exec, exec\n" \
  "ds_write_b128 v186, v[100:103] offset:32768\n" \
  "s_mov_b32 exec_lo, 0\n" \
  "s_mov_b32 exec_hi, 0xf0000f00\n" \
  "ds_write_b32 v187, v104 offset:41472\n" \
  "s_mov_b64 exec, -1\n" \
  "v_cvt_pk_bf16_f32 v124, v124, v125\n" \
  "v_cvt_pk_bf16_f32 v125, v126, v127\n" \
  "v_cvt_pk_bf16_f32 v128, v128, v129\n" \
  "v_cvt_pk_bf16_f32 v129, v130, v131\n" \
  "v_cvt_pk_bf16_f32 v132, v132, v133\n" \
  "v_cvt_pk_bf16_f32 v133, v134, v135\n" \
  "buffer_load_dwordx4 v[232:235], v180, s[24:27], s61 offen offset:2560 sc1\n" \
  "s_waitcnt lgkmcnt(0)\n" \
  "s_barrier\n" \
  ASTAMP(48688, 0) \
  "s_cmp_lt_u32 s22, 4\n" \
  "s_cbranch_scc1 101f\n" \
  "s_mov_b64 exec, s[72:73]\n" \
  "v_and_b32 v116, 0xffff, v116\n" \
  "v_and_b32 v117, 0xffff, v117\n" \
  "v_and_b32 v124, 0xffff, v124\n" \
  "v_and_b32 v125, 0xffff, v125\n" \
  "v_and_b32 v132, 0xffff, v132\n" \
  "v_and_b32 v133, 0xffff, v133\n" \
  "s_mov_b64 exec, -1\n" \
  "ds_write_b32 v179, v112 offset:0\n" \
  "ds_write_b32 v191, v113 offset:0\n" \
  "ds_write_b32 v140, v116 offset:0\n" \
  "ds_write_b32 v141, v117 offset:0\n" \
  "ds_write_b32 v182, v120 offset:8192\n" \
  "ds_write_b32 v183, v121 offset:8192\n" \
  "ds_write_b32 v5, v124 offset:8192\n" \
  "ds_write_b32 v6, v125 offset:8192\n" \
  "s_branch 102f\n" \
  "101:\n" \
  "ds_read_b128 v[104:107], v189 offset:41728\n" \
  "ds_read_b128 v[108:111], v189 offset:41744\n" \
  "ds_read2st64_b32 v[96:97], v188 offset0:128 offset1:132\n" \
  "ds_read2st64_b32 v[98:99], v188 offset0:136 offset1:140\n" \
  "ds_read2st64_b32 v[100:101], v188 offset0:144 offset1:148\n" \
  "ds_read2st64_b32 v[102:103], v188 offset0:152 offset1:156\n" \
  "ds_write_b32 v179, v112 offset:0\n" \
  "ds_write_b32 v191, v113 offset:0\n" \
  "ds_write_b32 v140, v116 offset:0\n" \
  "ds_write_b32 v141, v117 offset:0\n" \
  "ds_write_b32 v182, v120 offset:8192\n" \
  "ds_write_b32 v183, v121 offset:8192\n" \
  "ds_write_b32 v5, v124 offset:8192\n" \
  "ds_write_b32 v6, v125 offset:8192\n" \
  "s_waitcnt lgkmcnt(12)\n" \
  "v_pk_add_f32 v[104:105], v[104:105], v[108:109]\n" \
  "v_pk_add_f32 v[106:107], v[106:107], v[110:111]\n" \
  "v_mov_b32 v21, 0x3727c5ac\n" \
  "v_pk_add_f32 v[104:105], v[104:105], v[106:107]\n" \
  "s_waitcnt lgkmcnt(8)\n" \
  "v_pk_add_f32 v[96:97], v[96:97], v[98:99]\n" \
  "v_add_f32 v104, v104, v105\n" \
  "v_pk_add_f32 v[100:101], v[100:101], v[102:103]\n" \
  "v_fmamk_f32 v104, v104, 0x3b000000, v21\n" \
  "v_pk_add_f32 v[96:97], v[96:97], v[100:101]\n" \
  "v_rsq_f32 v104, v104\n" \
  "v_add_f32 v96, v96, v97\n" \
  "s_nop 0\n" \
  "v_mul_f32 v96, v96, v104\n" \
  "s_nop 1\n" \
  "v_mov_b32_dpp v97, v96 quad_perm:[1,0,3,2] row_mask:0xf bank_mask:0xf\n" \
  "v_cvt_pk_bf16_f32 v96, v96, v97\n" \
  "buffer_store_dword v96, v190, s[28:31], s50 offen sc1\n" \
  "102:\n" \
  ASTAMP(48692, 0) \
  "ds_write_b32 v179, v128 offset:16384\n" \
  "ds_write_b32 v191, v129 offset:16384\n" \
  "buffer_load_dwordx4 v[236:239], v180, s[24:27], s61 offen offset:2816 sc1\n" \
  "ds_write_b32 v140, v132 offset:16384\n" \
  "ds_write_b32 v141, v133 offset:16384\n" \
  "buffer_load_dwordx4 v[240:243], v180, s[24:27], s61 offen offset:3072 sc1\n" \
  "ds_read_b128 v[112:115], v185\n" \
  "ds_read_b128 v[116:119], v143\n" \
  "buffer_load_dwordx4 v[244:247], v180, s[24:27], s61 offen offset:3328 sc1\n" \
  "ds_read_b128 v[120:123], v185 offset:16384\n" \
  "ds_read_b128 v[124:127], v143 offset:16384\n" \
  "buffer_load_dwordx4 v[248:251], v180, s[24:27], s61 offen offset:3584 sc1\n" \
  "v_mov_b64 v[24:25], v[12:13]\n" \
  "v_mov_b64 v[26:27], v[14:15]\n" \
  "buffer_load_dwordx4 v[252:255], v180, s[24:27], s61 offen offset:3840 sc1\n" \
  "v_mov_b64 v[28:29], v[16:17]\n" \
  "v_mov_b64 v[30:31], v[18:19]\n" \
  "s_waitcnt lgkmcnt(2)\n" \
  "v_mfma_f32_16x16x32_bf16 v[96:99], v[112:115], v[112:115], 0\n" \
  "v_mfma_f32_16x16x32_bf16 v[96:99], v[116:119], v[116:119], v[96:99]\n" \
  "s_waitcnt lgkmcnt(0)\n" \
  "v_mfma_f32_16x16x32_bf16 v[100:103], v[120:123], v[120:123], 0\n" \
  "v_mfma_f32_16x16x32_bf16 v[100:103], v[124:127], v[124:127], v[100:103]\n" \
  "v_mov_b32 v21, 0\n" \
  "s_mov_b64 exec, 0xffffff\n" \
  "ds_write_b32 v4, v21 offset:4096\n" \
  "s_mov_b64 exec, -1\n" \
  "s_nop 1\n" \
  "v_cndmask_b32 v96, v96, v97, s[68:69]\n" \
  "v_cndmask_b32 v98, v98, v99, s[68:69]\n" \
  "v_cndmask_b32 v96, v96, v98, s[70:71]\n" \
  "v_cndmask_b32 v100, v100, v101, s[68:69]\n" \
  "v_cndmask_b32 v102, v102, v103, s[68:69]\n" \
  "v_cndmask_b32 v100, v100, v102, s[70:71]\n" \
  "s_mov_b32 exec_lo, 0x00f0000f\n" \
  "s_mov_b32 exec_hi, 0xf0000f00\n" \
  "ds_write_b32 v187, v96 offset:40960\n" \
  "s_mov_b32 exec_hi, 0\n" \
  "ds_write_b32 v187, v100 offset:41472\n" \
  "s_mov_b64 exec, -1\n" \
  "s_waitcnt vmcnt(5)\n" \
  "v_max3_u32 v21, v192, v193, v194\n" \
  "v_max3_u32 v21, v21, v195, v196\n" \
  "v_max3_u32 v21, v21, v197, v198\n" \
  "ds_write_b128 v178, v[192:195] offset:0\n" \
  "v_max3_u32 v21, v21, v199, v200\n" \
  "v_max3_u32 v21, v21, v201, v202\n" \
  "ds_write_b128 v178, v[196:199] offset:1024\n" \
  "v_max3_u32 v21, v21, v203, v204\n" \
  "v_max3_u32 v21, v21, v205, v206\n" \
  "ds_write_b128 v178, v[200:203] offset:2048\n" \
  "v_max3_u32 v21, v21, v207, v208\n" \
  "v_max3_u32 v21, v21, v209, v210\n" \
  "ds_write_b128 v178, v[204:207] offset:3072\n" \
  "v_max3_u32 v21, v21, v211, v212\n" \
  "v_max3_u32 v21, v21, v213, v214\n" \
  "ds_write_b128 v178, v[208:211] offset:4096\n" \
  "v_max3_u32 v21, v21, v215, v216\n" \
  "v_max3_u32 v21, v21, v217, v218\n" \
  "ds_write_b128 v178, v[212:215] offset:5120\n" \
  "v_max3_u32 v21, v21, v219, v220\n" \
  "v_max3_u32 v21, v21, v221, v222\n" \
  "ds_write_b128 v178, v[216:219] offset:6144\n" \
  "v_max3_u32 v21, v21, v223, v224\n" \
  "v_max3_u32 v21, v21, v225, v226\n" \
  "ds_write_b128 v178, v[220:223] offset:7168\n" \
  "v_max3_u32 v21, v21, v227, v228\n" \
  "v_max3_u32 v21, v21, v229, v230\n" \
  "ds_write_b128 v178, v[224:227] offset:8192\n" \
  "v_max3_u32 v21, v21, v231, v232\n" \
  "v_max3_u32 v21, v21, v233, v234\n" \
  "ds_write_b128 v178, v[228:231] offset:9216\n" \
  "v_max3_u32 v21, v21, v235, v235\n" \
  "v_cmp_eq_u32 vcc, -1, v21\n" \
  "s_cbranch_vccz 103f\n" \
  "buffer_load_dwordx4 v[192:195], v180, s[24:27], s61 offen offset:0 sc1\n" \
  "buffer_load_dwordx4 v[196:199], v180, s[24:27], s61 offen offset:256 sc1\n" \
  "buffer_load_dwordx4 v[200:203], v180, s[24:27], s61 offen offset:512 sc1\n" \
  "buffer_load_dwordx4 v[204:207], v180, s[24:27], s61 offen offset:768 sc1\n" \
  "buffer_load_dwordx4 v[208:211], v180, s[24:27], s61 offen offset:1024 sc1\n" \
  "buffer_load_dwordx4 v[212:215], v180, s[24:27], s61 offen offset:1280 sc1\n" \
  "buffer_load_dwordx4 v[216:219], v180, s[24:27], s61 offen offset:1536 sc1\n" \
  "buffer_load_dwordx4 v[220:223], v180, s[24:27], s61 offen offset:1792 sc1\n" \
  "buffer_load_dwordx4 v[224:227], v180, s[24:27], s61 offen offset:2048 sc1\n" \
  "buffer_load_dwordx4 v[228:231], v180, s[24:27], s61 offen offset:2304 sc1\n" \
  "buffer_load_dwordx4 v[232:235], v180, s[24:27], s61 offen offset:2560 sc1\n" \
  "s_waitcnt vmcnt(0)\n" \
  "ds_write_b128 v178, v[192:195] offset:0\n" \
  "ds_write_b128 v178, v[196:199] offset:1024\n" \
  "ds_write_b128 v178, v[200:203] offset:2048\n" \
  "ds_write_b128 v178, v[204:207] offset:3072\n" \
  "ds_write_b128 v178, v[208:211] offset:4096\n" \
  "ds_write_b128 v178, v[212:215] offset:5120\n" \
  "ds_write_b128 v178, v[216:219] offset:6144\n" \
  "ds_write_b128 v178, v[220:223] offset:7168\n" \
  "ds_write_b128 v178, v[224:227] offset:8192\n" \
  "ds_write_b128 v178, v[228:231] offset:9216\n" \
  "103:\n" \
  "s_waitcnt vmcnt(0)\n" \
  "29:\n" \
  "v_xor_b32 v21, 64, v20\n" \
  "v_xor_b32 v22, 0x80, v20\n" \
  "v_xor_b32 v23, 0xc0, v20\n" \
  "s_mov_b64 s[76:77], -1\n" \
  "s_mov_b32 s78, 0x10000\n" \
  ASTAMP(48696, s35) \
  "s_waitcnt lgkmcnt(0)\n" \
  "s_barrier\n" \
  ASTAMP(48712, s35) \
  "ds_read_b128 v[160:163], v20 offset:0\n" \
  "ds_read_b128 v[164:167], v20 offset:16384\n" \
  "ds_read_b128 v[168:171], v21 offset:0\n" \
  "ds_read_b128 v[172:175], v21 offset:16384\n" \
  "ds_read_b128 v[176:179], v22 offset:0\n" \
  "ds_read_b128 v[180:183], v22 offset:16384\n" \
  "s_waitcnt lgkmcnt(5)\n" \
  "v_mfma_f32_16x16x32_bf16 v[12:15], v[192:195], v[160:163], 0\n" \
  "v_mov_b64 v[128:129], v[8:9]\n" \
  "v_mov_b64 v[130:131], v[8:9]\n" \
  "v_mov_b64 v[132:133], v[8:9]\n" \
  "v_mov_b64 v[134:135], v[8:9]\n" \
  "s_waitcnt lgkmcnt(4)\n" \
  "v_mfma_f32_16x16x32_bf16 v[16:19], v[192:195], v[164:167], 0\n" \
  "ds_read_b128 v[184:187], v23 offset:0\n" \
  "ds_read_b128 v[188:191], v23 offset:16384\n" \
  "s_waitcnt lgkmcnt(5)\n" \
  "v_mfma_f32_16x16x32_bf16 v[12:15], v[196:199], v[168:171], v[12:15]\n" \
  "v_mov_b64 v[136:137], v[8:9]\n" \
  "v_mov_b64 v[138:139], v[8:9]\n" \
  "v_mov_b64 v[140:141], v[8:9]\n" \
  "v_mov_b64 v[142:143], v[8:9]\n" \
  "s_waitcnt lgkmcnt(4)\n" \
  "v_mfma_f32_16x16x32_bf16 v[16:19], v[196:199], v[172:175], v[16:19]\n" \
  "ds_read_b128 v[160:163], v20 offset:256\n" \
  "ds_read_b128 v[164:167], v20 offset:16640\n" \
  "s_waitcnt lgkmcnt(5)\n" \
  "v_mfma_f32_16x16x32_bf16 v[12:15], v[200:203], v[176:179], v[12:15]\n" \
  "v_mov_b64 v[144:145], v[8:9]\n" \
  "v_mov_b64 v[146:147], v[8:9]\n" \
  "v_mov_b64 v[148:149], v[8:9]\n" \
  "v_mov_b64 v[150:151], v[8:9]\n" \
  "s_waitcnt lgkmcnt(4)\n" \
  "v_mfma_f32_16x16x32_bf16 v[16:19], v[200:203], v[180:183], v[16:19]\n" \
  "ds_read_b128 v[168:171], v21 offset:256\n" \
  "ds_read_b128 v[172:175], v21 offset:16640\n" \
  "s_waitcnt lgkmcnt(5)\n" \
  "v_mfma_f32_16x16x32_bf16 v[12:15], v[204:207], v[184:187], v[12:15]\n" \
  "v_mov_b64 v[152:153], v[8:9]\n" \
  "v_mov_b64 v[154:155], v[8:9]\n" \
  "v_mov_b64 v[156:157], v[8:9]\n" \
  "v_mov_b64 v[158:159], v[8:9]\n" \
  "s_waitcnt lgkmcnt(4)\n" \
  "v_mfma_f32_16x16x32_bf16 v[16:19], v[204:207], v[188:191], v[16:19]\n" \
  "ds_read_b128 v[176:179], v22 offset:256\n" \
  "ds_read_b128 v[180:183], v22 offset:16640\n" \
  "s_waitcnt lgkmcnt(5)\n" \
  "v_mfma_f32_16x16x32_bf16 v[12:15], v[208:211], v[160:163], v[12:15]\n" \
  "v_mbcnt_lo_u32_b32 v124, -1, 0\n" \
  "v_mbcnt_hi_u32_b32 v124, -1, v124\n" \
  "v_and_b32 v125, 15, v124\n" \
  "v_lshrrev_b32 v126, 4, v124\n" \
  "v_lshlrev_b32 v10, 5, v125\n" \
  "s_waitcnt lgkmcnt(4)\n" \
  "v_mfma_f32_16x16x32_bf16 v[16:19], v[208:211], v[164:167], v[16:19]\n" \
  "ds_read_b128 v[184:187], v23 offset:256\n" \
  "ds_read_b128 v[188:191], v23 offset:16640\n" \
  "s_waitcnt lgkmcnt(5)\n" \
  "v_mfma_f32_16x16x32_bf16 v[12:15], v[212:215], v[168:171], v[12:15]\n" \
  "s_lshl_b32 s81, s22, 8\n" \
  "v_lshl_add_u32 v11, v126, 3, s81\n" \
  "v_and_b32 v127, 7, v124\n" \
  "v_lshl_add_u32 v11, v127, 5, v11\n" \
  "v_and_b32 v127, 8, v124\n" \
  "v_lshl_add_u32 v11, v127, 8, v11\n" \
  "s_waitcnt lgkmcnt(4)\n" \
  "v_mfma_f32_16x16x32_bf16 v[16:19], v[212:215], v[172:175], v[16:19]\n" \
  "ds_read_b128 v[192:195], v20 offset:512\n" \
  "ds_read_b128 v[196:199], v20 offset:16896\n" \
  "s_waitcnt lgkmcnt(5)\n" \
  "v_mfma_f32_16x16x32_bf16 v[12:15], v[216:219], v[176:179], v[12:15]\n" \
  "v_and_b32 v127, 1, v124\n" \
  "v_mul_u32_u24 v0, 0x5555, v127\n" \
  "v_add_u32 v0, 0xdddd8888, v0\n" \
  "s_waitcnt lgkmcnt(4)\n" \
  "v_mfma_f32_16x16x32_bf16 v[16:19], v[216:219], v[180:183], v[16:19]\n" \
  "ds_read_b128 v[200:203], v21 offset:512\n" \
  "ds_read_b128 v[204:207], v21 offset:16896\n" \
  "s_waitcnt lgkmcnt(5)\n" \
  "v_mfma_f32_16x16x32_bf16 v[12:15], v[220:223], v[184:187], v[12:15]\n" \
  "s_lshl_b32 s81, s22, 3\n" \
  "s_add_u32 s81, s81, 46464\n" \
  "v_lshl_add_u32 v5, v127, 2, s81\n" \
  "v_lshl_add_u32 v5, v126, 5, v5\n" \
  "s_waitcnt lgkmcnt(4)\n" \
  "v_mfma_f32_16x16x32_bf16 v[16:19], v[220:223], v[188:191], v[16:19]\n" \
  "ds_read_b128 v[208:211], v22 offset:512\n" \
  "ds_read_b128 v[212:215], v22 offset:16896\n" \
  "s_waitcnt lgkmcnt(5)\n" \
  "v_mfma_f32_16x16x32_bf16 v[12:15], v[224:227], v[192:195], v[12:15]\n" \
  "v_mov_b64 v[160:161], v[8:9]\n" \
  "v_mov_b64 v[162:163], v[8:9]\n" \
  "v_mov_b64 v[164:165], v[8:9]\n" \
  "v_mov_b64 v[166:167], v[8:9]\n" \
  "s_waitcnt lgkmcnt(4)\n" \
  "v_mfma_f32_16x16x32_bf16 v[16:19], v[224:227], v[196:199], v[16:19]\n" \
  "ds_read_b128 v[216:219], v23 offset:512\n" \
  "ds_read_b128 v[220:223], v23 offset:16896\n" \
  "s_waitcnt lgkmcnt(5)\n" \
  "v_mfma_f32_16x16x32_bf16 v[12:15], v[228:231], v[200:203], v[12:15]\n" \
  "v_mov_b64 v[168:169], v[8:9]\n" \
  "v_mov_b64 v[170:171], v[8:9]\n" \
  "v_mov_b64 v[172:173], v[8:9]\n" \
  "v_mov_b64 v[174:175], v[8:9]\n" \
  "s_waitcnt lgkmcnt(4)\n" \
  "v_mfma_f32_16x16x32_bf16 v[16:19], v[228:231], v[204:207], v[16:19]\n" \
  "ds_read_b128 v[192:195], v20 offset:768\n" \
  "ds_read_b128 v[196:199], v20 offset:17152\n" \
  "s_waitcnt lgkmcnt(5)\n" \
  "v_mfma_f32_16x16x32_bf16 v[12:15], v[232:235], v[208:211], v[12:15]\n" \
  "v_mov_b64 v[176:177], v[8:9]\n" \
  "v_mov_b64 v[178:179], v[8:9]\n" \
  "v_mov_b64 v[180:181], v[8:9]\n" \
  "v_mov_b64 v[182:183], v[8:9]\n" \
  "buffer_load_dwordx4 v[96:99], v7, s[28:31], s51 offen offset:0 sc1\n" \
  "s_waitcnt lgkmcnt(4)\n" \
  "v_mfma_f32_16x16x32_bf16 v[16:19], v[232:235], v[212:215], v[16:19]\n" \
  "ds_read_b128 v[200:203], v21 offset:768\n" \
  "ds_read_b128 v[204:207], v21 offset:17152\n" \
  "s_waitcnt lgkmcnt(5)\n" \
  "v_mfma_f32_16x16x32_bf16 v[12:15], v[236:239], v[216:219], v[12:15]\n" \
  "v_mov_b64 v[184:185], v[8:9]\n" \
  "v_mov_b64 v[186:187], v[8:9]\n" \
  "v_mov_b64 v[188:189], v[8:9]\n" \
  "v_mov_b64 v[190:191], v[8:9]\n" \
  "buffer_load_dwordx4 v[100:103], v7, s[28:31], s51 offen offset:1024 sc1\n" \
  "s_waitcnt lgkmcnt(4)\n" \
  "v_mfma_f32_16x16x32_bf16 v[16:19], v[236:239], v[220:223], v[16:19]\n" \
  "ds_read_b128 v[208:211], v22 offset:768\n" \
  "ds_read_b128 v[212:215], v22 offset:17152\n" \
  "s_waitcnt lgkmcnt(5)\n" \
  "v_mfma_f32_16x16x32_bf16 v[12:15], v[240:243], v[192:195], v[12:15]\n" \
  "v_lshrrev_b32 v127, 1, v125\n" \
  "v_mul_u32_u24 v127, 0x110, v127\n" \
  "v_add_u32 v5, v127, v5\n" \
  "v_lshl_add_u32 v6, v126, 4, v127\n" \
  "v_add_u32 v6, 46464, v6\n" \
  "s_mov_b64 exec, s[66:67]\n" \
  "buffer_load_dwordx4 v[128:131], v4, s[28:31], s53 offen offset:0 sc1\n" \
  "s_mov_b64 exec, -1\n" \
  "s_waitcnt lgkmcnt(4)\n" \
  "v_mfma_f32_16x16x32_bf16 v[16:19], v[240:243], v[196:199], v[16:19]\n" \
  "ds_read_b128 v[216:219], v23 offset:768\n" \
  "ds_read_b128 v[220:223], v23 offset:17152\n" \
  "ds_read_b128 v[224:227], v10 offset:40960\n" \
  "ds_read_b128 v[228:231], v10 offset:40976\n" \
  "ds_read_b128 v[232:235], v10 offset:41472\n" \
  "ds_read_b128 v[236:239], v10 offset:41488\n" \
  "s_waitcnt lgkmcnt(9)\n" \
  "v_mfma_f32_16x16x32_bf16 v[12:15], v[244:247], v[200:203], v[12:15]\n" \
  "s_lshl_b32 s81, s22, 11\n" \
  "v_lshl_add_u32 v1, v124, 4, s81\n" \
  "s_lshl_b32 s81, s22, 9\n" \
  "v_lshl_add_u32 v3, v124, 3, s81\n" \
  "s_mov_b64 exec, s[66:67]\n" \
  "buffer_load_dwordx4 v[132:135], v4, s[28:31], s53 offen offset:256 sc1\n" \
  "s_mov_b64 exec, -1\n" \
  "s_waitcnt lgkmcnt(8)\n" \
  "v_mfma_f32_16x16x32_bf16 v[16:19], v[244:247], v[204:207], v[16:19]\n" \
  "s_waitcnt lgkmcnt(7)\n" \
  "v_mfma_f32_16x16x32_bf16 v[12:15], v[248:251], v[208:211], v[12:15]\n" \
  "s_lshl_b32 s81, s22, 2\n" \
  "v_and_b32 v127, 28, v124\n" \
  "v_xor_b32 v127, s81, v127\n" \
  "v_lshl_add_u32 v2, v124, 5, v127\n" \
  "v_add_u32 v2, 41984, v2\n" \
  "buffer_load_dwordx4 v[104:107], v7, s[28:31], s51 offen offset:2048 sc1\n" \
  "s_waitcnt lgkmcnt(6)\n" \
  "v_mfma_f32_16x16x32_bf16 v[16:19], v[248:251], v[212:215], v[16:19]\n" \
  "s_waitcnt lgkmcnt(5)\n" \
  "v_mfma_f32_16x16x32_bf16 v[12:15], v[252:255], v[216:219], v[12:15]\n" \
  "s_mov_b64 exec, s[66:67]\n" \
  "buffer_load_dwordx4 v[136:139], v4, s[28:31], s53 offen offset:1024 sc1\n" \
  "s_mov_b64 exec, -1\n" \
  "s_waitcnt lgkmcnt(4)\n" \
  "v_mfma_f32_16x16x32_bf16 v[16:19], v[252:255], v[220:223], v[16:19]\n" \
  "s_waitcnt lgkmcnt(0)\n" \
  "v_pk_add_f32 v[224:225], v[224:225], v[228:229]\n" \
  "v_pk_add_f32 v[232:233], v[232:233], v[236:237]\n" \
  "v_pk_add_f32 v[226:227], v[226:227], v[230:231]\n" \
  "v_pk_add_f32 v[234:235], v[234:235], v[238:239]\n" \
  "v_pk_add_f32 v[224:225], v[224:225], v[226:227]\n" \
  "v_pk_add_f32 v[232:233], v[232:233], v[234:235]\n" \
  "v_add_f32 v224, v224, v225\n" \
  "v_add_f32 v232, v232, v233\n" \
  "v_mov_b32 v225, 0x38a1369a\n" \
  "v_fmamk_f32 v224, v224, 0x3c75fdf0, v225\n" \
  "v_fmamk_f32 v232, v232, 0x3c75fdf0, v225\n" \
  "v_rsq_f32 v224, v224\n" \
  "v_rsq_f32 v232, v232\n" \
  "v_pk_mul_f32 v[12:13], v[12:13], v[224:225] op_sel_hi:[1,0]\n" \
  "v_pk_mul_f32 v[14:15], v[14:15], v[224:225] op_sel_hi:[1,0]\n" \
  "v_pk_mul_f32 v[16:17], v[16:17], v[232:233] op_sel_hi:[1,0]\n" \
  "v_pk_mul_f32 v[18:19], v[18:19], v[232:233] op_sel_hi:[1,0]\n" \
  "v_cvt_pk_bf16_f32 v12, v12, v13\n" \
  "v_cvt_pk_bf16_f32 v13, v14, v15\n" \
  "v_cvt_pk_bf16_f32 v14, v16, v17\n" \
  "v_cvt_pk_bf16_f32 v15, v18, v19\n" \
  "ds_write_b64 v11, v[12:13] offset:32768\n" \
  "ds_write_b64 v11, v[14:15] offset:36864\n" \
  ASTAMP(48720, s35) \
  "s_waitcnt lgkmcnt(0)\n" \
  "s_barrier\n" \
  "11:\n" \
  ASTAMP(48728, s35) \
  "ds_read_b128 v[12:15], v7\n" \
  "ds_read_b128 v[16:19], v7 offset:1024\n" \
  "v_mov_b64 v[10:11], 0\n" \
  "s_mov_b32 s80, 1\n" \
  "s_mov_b32 s79, 0\n" \
  "s_mov_b32 s84, s55\n" \
  "s_mov_b32 s85, s56\n" \
  "s_setprio 3\n" \
  "s_waitcnt vmcnt(5)\n" \
  "s_waitcnt lgkmcnt(0)\n" \
  "v_mfma_f32_32x32x16_bf16 v[192:207], v[96:99], v[12:15], 0\n" \
  "v_mfma_f32_32x32x8_f16 v[224:239], v[10:11], v[10:11], 0\n" \
  "s_nop 10\n" \
  "s_mov_b64 exec, s[66:67]\n" \
  "buffer_load_dwordx4 v[140:143], v4, s[28:31], s53 offen offset:1280 sc1\n" \
  "s_mov_b64 exec, -1\n" \
  "buffer_load_dwordx4 v[108:111], v7, s[28:31], s51 offen offset:3072 sc1\n" \
  "s_mov_b64 exec, s[66:67]\n" \
  "buffer_load_dwordx4 v[144:147], v4, s[28:31], s53 offen offset:2048 sc1\n" \
  "buffer_load_dwordx4 v[148:151], v4, s[28:31], s53 offen offset:2304 sc1\n" \
  "s_mov_b64 exec, -1\n" \
  "v_exp_f32 v192, v192\n" \
  "v_exp_f32 v193, v193\n" \
  "v_exp_f32 v194, v194\n" \
  "v_exp_f32 v195, v195\n" \
  "v_exp_f32 v196, v196\n" \
  "s_waitcnt vmcnt(8)\n" \
  "v_mfma_f32_32x32x16_bf16 v[208:223], v[100:103], v[12:15], 0\n" \
  "v_exp_f32 v197, v197\n" \
  "v_exp_f32 v198, v198\n" \
  "v_exp_f32 v199, v199\n" \
  "v_exp_f32 v200, v200\n" \
  "v_exp_f32 v201, v201\n" \
  "v_exp_f32 v202, v202\n" \
  "s_waitcnt vmcnt(7)\n" \
  "v_smfmac_f32_32x32x32_bf16 v[224:239], v[128:131], v[192:199], v0 abid:1\n" \
  "v_exp_f32 v203, v203\n" \
  "v_exp_f32 v204, v204\n" \
  "v_exp_f32 v205, v205\n" \
  "v_exp_f32 v206, v206\n" \
  "v_exp_f32 v207, v207\n" \
  "buffer_load_dwordx4 v[112:115], v7, s[28:31], s52 offen offset:0 sc1\n" \
  "s_mov_b64 exec, s[66:67]\n" \
  "buffer_load_dwordx4 v[152:155], v4, s[28:31], s53 offen offset:3072 sc1\n" \
  "buffer_load_dwordx4 v[156:159], v4, s[28:31], s53 offen offset:3328 sc1\n" \
  "s_mov_b64 exec, -1\n" \
  "buffer_load_dwordx4 v[116:119], v7, s[28:31], s52 offen offset:1024 sc1\n" \
  "v_exp_f32 v208, v208\n" \
  "v_exp_f32 v209, v209\n" \
  "s_waitcnt vmcnt(10)\n" \
  "v_smfmac_f32_32x32x32_bf16 v[224:239], v[132:135], v[200:207], v0 abid:1\n" \
  "v_exp_f32 v210, v210\n" \
  "v_exp_f32 v211, v211\n" \
  "v_exp_f32 v212, v212\n" \
  "s_waitcnt vmcnt(9)\n" \
  "v_mfma_f32_32x32x16_bf16 v[192:207], v[104:107], v[12:15], 0\n" \
  "v_exp_f32 v213, v213\n" \
  "v_exp_f32 v214, v214\n" \
  "v_exp_f32 v215, v215\n" \
  "v_exp_f32 v216, v216\n" \
  "v_exp_f32 v217, v217\n" \
  "v_exp_f32 v218, v218\n" \
  "s_waitcnt vmcnt(8)\n" \
  "v_smfmac_f32_32x32x32_bf16 v[224:239], v[136:139], v[208:215], v0 abid:1\n" \
  "v_exp_f32 v219, v219\n" \
  "v_exp_f32 v220, v220\n" \
  "v_exp_f32 v221, v221\n" \
  "v_exp_f32 v222, v222\n" \
  "v_exp_f32 v223, v223\n" \
  "s_mov_b64 exec, s[66:67]\n" \
  "buffer_load_dwordx4 v[160:163], v4, s[28:31], s54 offen offset:0 sc1\n" \
  "buffer_load_dwordx4 v[164:167], v4, s[28:31], s54 offen offset:256 sc1\n" \
  "s_mov_b64 exec, -1\n" \
  "buffer_load_dwordx4 v[120:123], v7, s[28:31], s52 offen offset:2048 sc1\n" \
  "s_mov_b64 exec, s[66:67]\n" \
  "buffer_load_dwordx4 v[168:171], v4, s[28:31], s54 offen offset:1024 sc1\n" \
  "s_mov_b64 exec, -1\n" \
  "v_exp_f32 v192, v192\n" \
  "v_exp_f32 v193, v193\n" \
  "s_waitcnt vmcnt(11)\n" \
  "v_smfmac_f32_32x32x32_bf16 v[224:239], v[140:143], v[216:223], v0 abid:1\n" \
  "v_exp_f32 v194, v194\n" \
  "v_exp_f32 v195, v195\n" \
  "v_exp_f32 v196, v196\n" \
  "s_waitcnt vmcnt(10)\n" \
  "v_mfma_f32_32x32x16_bf16 v[208:223], v[108:111], v[12:15], 0\n" \
  "v_exp_f32 v197, v197\n" \
  "v_exp_f32 v198, v198\n" \
  "v_exp_f32 v199, v199\n" \
  "v_exp_f32 v200, v200\n" \
  "v_exp_f32 v201, v201\n" \
  "v_exp_f32 v202, v202\n" \
  "s_waitcnt vmcnt(9)\n" \
  "v_smfmac_f32_32x32x32_bf16 v[224:239], v[144:147], v[192:199], v0 abid:1\n" \
  "v_exp_f32 v203, v203\n" \
  "v_exp_f32 v204, v204\n" \
  "v_exp_f32 v205, v205\n" \
  "v_exp_f32 v206, v206\n" \
  "v_exp_f32 v207, v207\n" \
  "s_mov_b64 exec, s[66:67]\n" \
  "buffer_load_dwordx4 v[172:175], v4, s[28:31], s54 offen offset:1280 sc1\n" \
  "s_mov_b64 exec, -1\n" \
  "buffer_load_dwordx4 v[124:127], v7, s[28:31], s52 offen offset:3072 sc1\n" \
  "s_mov_b64 exec, s[66:67]\n" \
  "buffer_load_dwordx4 v[176:179], v4, s[28:31], s54 offen offset:2048 sc1\n" \
  "buffer_load_dwordx4 v[180:183], v4, s[28:31], s54 offen offset:2304 sc1\n" \
  "s_mov_b64 exec, -1\n" \
  "s_setprio 2\n" \
  "v_exp_f32 v208, v208\n" \
  "v_exp_f32 v209, v209\n" \
  "s_waitcnt vmcnt(12)\n" \
  "v_smfmac_f32_32x32x32_bf16 v[224:239], v[148:151], v[200:207], v0 abid:1\n" \
  "v_exp_f32 v210, v210\n" \
  "v_exp_f32 v211, v211\n" \
  "v_exp_f32 v212, v212\n" \
  "s_waitcnt vmcnt(11)\n" \
  "v_mfma_f32_32x32x16_bf16 v[192:207], v[112:115], v[12:15], 0\n" \
  "v_exp_f32 v213, v213\n" \
  "v_exp_f32 v214, v214\n" \
  "v_exp_f32 v215, v215\n" \
  "v_exp_f32 v216, v216\n" \
  "v_exp_f32 v217, v217\n" \
  "v_exp_f32 v218, v218\n" \
  "s_waitcnt vmcnt(10)\n" \
  "v_smfmac_f32_32x32x32_bf16 v[224:239], v[152:155], v[208:215], v0 abid:1\n" \
  "v_exp_f32 v219, v219\n" \
  "v_exp_f32 v220, v220\n" \
  "v_exp_f32 v221, v221\n" \
  "v_exp_f32 v222, v222\n" \
  "v_exp_f32 v223, v223\n" \
  "s_mov_b64 exec, s[66:67]\n" \
  "buffer_load_dwordx4 v[184:187], v4, s[28:31], s54 offen offset:3072 sc1\n" \
  "buffer_load_dwordx4 v[188:191], v4, s[28:31], s54 offen offset:3328 sc1\n" \
  "s_mov_b64 exec, -1\n" \
  "v_exp_f32 v192, v192\n" \
  "v_exp_f32 v193, v193\n" \
  "s_waitcnt vmcnt(11)\n" \
  "v_smfmac_f32_32x32x32_bf16 v[224:239], v[156:159], v[216:223], v0 abid:1\n" \
  "v_exp_f32 v194, v194\n" \
  "v_exp_f32 v195, v195\n" \
  "v_exp_f32 v196, v196\n" \
  "s_waitcnt vmcnt(10)\n" \
  "v_mfma_f32_32x32x16_bf16 v[208:223], v[116:119], v[12:15], 0\n" \
  "v_exp_f32 v197, v197\n" \
  "v_exp_f32 v198, v198\n" \
  "v_exp_f32 v199, v199\n" \
  "v_exp_f32 v200, v200\n" \
  "v_exp_f32 v201, v201\n" \
  "v_exp_f32 v202, v202\n" \
  "s_waitcnt vmcnt(9)\n" \
  "v_smfmac_f32_32x32x32_bf16 v[224:239], v[160:163], v[192:199], v0 abid:1\n" \
  "v_exp_f32 v203, v203\n" \
  "v_exp_f32 v204, v204\n" \
  "v_exp_f32 v205, v205\n" \
  "v_exp_f32 v206, v206\n" \
  "v_mfma_f32_32x32x8_f16 v[240:255], v[10:11], v[10:11], 0\n" \
  "v_exp_f32 v207, v207\n" \
  "v_exp_f32 v208, v208\n" \
  "v_exp_f32 v209, v209\n" \
  "s_waitcnt vmcnt(8)\n" \
  "v_smfmac_f32_32x32x32_bf16 v[224:239], v[164:167], v[200:207], v0 abid:1\n" \
  "v_exp_f32 v210, v210\n" \
  "v_exp_f32 v211, v211\n" \
  "v_exp_f32 v212, v212\n" \
  "s_waitcnt vmcnt(7)\n" \
  "v_mfma_f32_32x32x16_bf16 v[192:207], v[120:123], v[12:15], 0\n" \
  "v_exp_f32 v213, v213\n" \
  "v_exp_f32 v214, v214\n" \
  "v_exp_f32 v215, v215\n" \
  "v_exp_f32 v216, v216\n" \
  "v_exp_f32 v217, v217\n" \
  "v_exp_f32 v218, v218\n" \
  "s_waitcnt vmcnt(6)\n" \
  "v_smfmac_f32_32x32x32_bf16 v[224:239], v[168:171], v[208:215], v0 abid:1\n" \
  "v_exp_f32 v219, v219\n" \
  "v_exp_f32 v220, v220\n" \
  "v_exp_f32 v221, v221\n" \
  "v_exp_f32 v222, v222\n" \
  "v_exp_f32 v223, v223\n" \
  "s_waitcnt vmcnt(0)\n" \
  "s_setprio 1\n" \
  "v_exp_f32 v192, v192\n" \
  "s_cmp_eq_u32 s35, 0\n" \
  "s_cbranch_scc0 35f\n" \
  "v_mbcnt_lo_u32_b32 v8, -1, 0\n" \
  "v_mbcnt_hi_u32_b32 v8, -1, v8\n" \
  "v_lshrrev_b32 v9, 4, v8\n" \
  "v_and_b32 v8, 15, v8\n" \
  "v_lshlrev_b32 v8, 4, v8\n" \
  "v_lshl_or_b32 v8, v9, 12, v8\n" \
  "s_mov_b32 s86, s63\n" \
  "buffer_load_dwordx4 v[32:35], v8, s[24:27], s86 offen offset:0 sc1\n" \
  "35:\n" \
  "v_exp_f32 v193, v193\n" \
  "v_smfmac_f32_32x32x32_bf16 v[224:239], v[172:175], v[216:223], v0 abid:1\n" \
  "v_exp_f32 v194, v194\n" \
  "v_exp_f32 v195, v195\n" \
  "v_exp_f32 v196, v196\n" \
  "v_mfma_f32_32x32x16_bf16 v[208:223], v[124:127], v[12:15], 0\n" \
  "v_exp_f32 v197, v197\n" \
  "v_exp_f32 v198, v198\n" \
  "v_exp_f32 v199, v199\n" \
  "v_exp_f32 v200, v200\n" \
  "s_cmp_eq_u32 s35, 0\n" \
  "s_cbranch_scc0 36f\n" \
  "buffer_load_dwordx4 v[64:67], v8, s[24:27], s86 offen offset:256 sc1\n" \
  "36:\n" \
  "v_exp_f32 v201, v201\n" \
  "v_exp_f32 v202, v202\n" \
  "v_smfmac_f32_32x32x32_bf16 v[224:239], v[176:179], v[192:199], v0 abid:1\n" \
  "v_exp_f32 v203, v203\n" \
  "v_exp_f32 v204, v204\n" \
  "v_exp_f32 v205, v205\n" \
  "v_exp_f32 v206, v206\n" \
  "v_exp_f32 v207, v207\n" \
  "v_exp_f32 v208, v208\n" \
  "s_cmp_eq_u32 s35, 0\n" \
  "s_cbranch_scc0 37f\n" \
  "s_add_u32 s86, s86, 16384\n" \
  "buffer_load_dwordx4 v[36:39], v8, s[24:27], s86 offen offset:0 sc1\n" \
  "37:\n" \
  "v_exp_f32 v209, v209\n" \
  "v_smfmac_f32_32x32x32_bf16 v[224:239], v[180:183], v[200:207], v0 abid:1\n" \
  "v_exp_f32 v210, v210\n" \
  "v_exp_f32 v211, v211\n" \
  "v_exp_f32 v212, v212\n" \
  "v_mfma_f32_32x32x16_bf16 v[192:207], v[96:99], v[16:19], 0\n" \
  "v_exp_f32 v213, v213\n" \
  "v_exp_f32 v214, v214\n" \
  "v_exp_f32 v215, v215\n" \
  "v_exp_f32 v216, v216\n" \
  "s_cmp_eq_u32 s35, 0\n" \
  "s_cbranch_scc0 38f\n" \
  "buffer_load_dwordx4 v[68:71], v8, s[24:27], s86 offen offset:256 sc1\n" \
  "38:\n" \
  "v_exp_f32 v217, v217\n" \
  "v_add_u32 v7, 2048, v7\n" \
  "v_exp_f32 v218, v218\n" \
  "v_smfmac_f32_32x32x32_bf16 v[224:239], v[184:187], v[208:215], v0 abid:1\n" \
  "v_exp_f32 v219, v219\n" \
  "v_exp_f32 v220, v220\n" \
  "v_exp_f32 v221, v221\n" \
  "v_exp_f32 v222, v222\n" \
  "v_exp_f32 v223, v223\n" \
  "v_exp_f32 v192, v192\n" \
  "ds_read_b128 v[12:15], v7 offset:0\n" \
  "v_exp_f32 v193, v193\n" \
  "v_smfmac_f32_32x32x32_bf16 v[224:239], v[188:191], v[216:223], v0 abid:1\n" \
  "v_exp_f32 v194, v194\n" \
  "v_exp_f32 v195, v195\n" \
  "v_exp_f32 v196, v196\n" \
  "v_mfma_f32_32x32x16_bf16 v[208:223], v[100:103], v[16:19], 0\n" \
  "v_exp_f32 v197, v197\n" \
  "v_exp_f32 v198, v198\n" \
  "v_exp_f32 v199, v199\n" \
  "v_exp_f32 v200, v200\n" \
  "v_exp_f32 v201, v201\n" \
  "v_exp_f32 v202, v202\n" \
  "v_smfmac_f32_32x32x32_bf16 v[240:255], v[128:131], v[192:199], v0 abid:1\n" \
  "v_exp_f32 v203, v203\n" \
  "v_exp_f32 v204, v204\n" \
  "v_cmp_u_f32 vcc, v224, v225\n" \
  "v_cmp_u_f32 s[82:83], v226, v227\n" \
  "v_exp_f32 v205, v205\n" \
  "s_or_b64 s[82:83], s[82:83], vcc\n" \
  "v_cmp_u_f32 vcc, v228, v229\n" \
  "v_exp_f32 v206, v206\n" \
  "s_or_b64 s[82:83], s[82:83], vcc\n" \
  "v_cmp_u_f32 vcc, v230, v231\n" \
  "v_exp_f32 v207, v207\n" \
  "s_or_b64 s[82:83], s[82:83], vcc\n" \
  "s_and_b64 s[82:83], s[82:83], s[76:77]\n" \
  "s_cbranch_scc1 8f\n" \
  "ds_write_b128 v1, v[224:227] offset:0\n" \
  "ds_write_b128 v1, v[228:231] offset:1024\n" \
  "ds_write_b32 v2, v232 offset:0\n" \
  ASTAMP(48736, s35) \
  ACOUNT(48848) \
  "s_setprio 0\n" \
  "v_exp_f32 v208, v208\n" \
  "v_mov_b64 v[20:21], -1\n" \
  "v_mov_b64 v[22:23], -1\n" \
  "v_exp_f32 v209, v209\n" \
  "buffer_store_dwordx4 v[20:23], v4, s[28:31], s55 offen sc1\n" \
  "v_smfmac_f32_32x32x32_bf16 v[240:255], v[132:135], v[200:207], v0 abid:1\n" \
  "v_exp_f32 v210, v210\n" \
  "v_exp_f32 v211, v211\n" \
  "v_exp_f32 v212, v212\n" \
  "v_mfma_f32_32x32x16_bf16 v[192:207], v[104:107], v[16:19], 0\n" \
  "v_exp_f32 v213, v213\n" \
  "v_exp_f32 v214, v214\n" \
  "v_exp_f32 v215, v215\n" \
  "v_exp_f32 v216, v216\n" \
  "v_exp_f32 v217, v217\n" \
  "v_exp_f32 v218, v218\n" \
  "v_smfmac_f32_32x32x32_bf16 v[240:255], v[136:139], v[208:215], v0 abid:1\n" \
  "v_exp_f32 v219, v219\n" \
  "v_exp_f32 v220, v220\n" \
  "v_exp_f32 v221, v221\n" \
  "v_exp_f32 v222, v222\n" \
  "v_exp_f32 v223, v223\n" \
  "s_lshl_b64 exec, 1, s22\n" \
  "ds_write_b32 v4, v7 offset:4096\n" \
  "s_mov_b64 exec, -1\n" \
  "s_setprio 3\n" \
  "s_cmp_lt_u32 s22, 4\n" \
  "s_cbranch_scc1 1f\n" \
  "v_exp_f32 v192, v192\n" \
  "v_exp_f32 v193, v193\n" \
  "v_smfmac_f32_32x32x32_bf16 v[240:255], v[140:143], v[216:223], v0 abid:1\n" \
  "v_exp_f32 v194, v194\n" \
  "s_cmp_eq_u32 s35, 0\n" \
  "s_cbranch_scc0 39f\n" \
  "s_add_u32 s86, s86, 16384\n" \
  "buffer_load_dwordx4 v[40:43], v8, s[24:27], s86 offen offset:0 sc1\n" \
  "39:\n" \
  "v_exp_f32 v195, v195\n" \
  "v_exp_f32 v196, v196\n" \
  "v_mfma_f32_32x32x16_bf16 v[208:223], v[108:111], v[16:19], 0\n" \
  "v_exp_f32 v197, v197\n" \
  "v_exp_f32 v198, v198\n" \
  "v_exp_f32 v199, v199\n" \
  "v_exp_f32 v200, v200\n" \
  "v_exp_f32 v201, v201\n" \
  "v_exp_f32 v202, v202\n" \
  "v_smfmac_f32_32x32x32_bf16 v[240:255], v[144:147], v[192:199], v0 abid:1\n" \
  "v_exp_f32 v203, v203\n" \
  "v_exp_f32 v204, v204\n" \
  "s_cmp_eq_u32 s35, 0\n" \
  "s_cbranch_scc0 40f\n" \
  "buffer_load_dwordx4 v[72:75], v8, s[24:27], s86 offen offset:256 sc1\n" \
  "40:\n" \
  "v_exp_f32 v205, v205\n" \
  "v_exp_f32 v206, v206\n" \
  "v_exp_f32 v207, v207\n" \
  "v_exp_f32 v208, v208\n" \
  "v_exp_f32 v209, v209\n" \
  "v_smfmac_f32_32x32x32_bf16 v[240:255], v[148:151], v[200:207], v0 abid:1\n" \
  "v_exp_f32 v210, v210\n" \
  "s_cmp_eq_u32 s35, 0\n" \
  "s_cbranch_scc0 41f\n" \
  "s_add_u32 s86, s86, 16384\n" \
  "buffer_load_dwordx4 v[44:47], v8, s[24:27], s86 offen offset:0 sc1\n" \
  "41:\n" \
  "v_exp_f32 v211, v211\n" \
  "v_exp_f32 v212, v212\n" \
  "v_mfma_f32_32x32x16_bf16 v[192:207], v[112:115], v[16:19], 0\n" \
  "v_exp_f32 v213, v213\n" \
  "v_exp_f32 v214, v214\n" \
  "v_exp_f32 v215, v215\n" \
  "v_exp_f32 v216, v216\n" \
  "v_exp_f32 v217, v217\n" \
  "v_exp_f32 v218, v218\n" \
  "v_smfmac_f32_32x32x32_bf16 v[240:255], v[152:155], v[208:215], v0 abid:1\n" \
  "v_exp_f32 v219, v219\n" \
  "v_exp_f32 v220, v220\n" \
  "s_cmp_eq_u32 s35, 0\n" \
  "s_cbranch_scc0 42f\n" \
  "buffer_load_dwordx4 v[76:79], v8, s[24:27], s86 offen offset:256 sc1\n" \
  "42:\n" \
  "v_exp_f32 v221, v221\n" \
  "v_exp_f32 v222, v222\n" \
  "v_exp_f32 v223, v223\n" \
  "s_branch 2f\n" \
  "1:\n" \
  "51:\n" \
  "ds_read_b32 v20, v4 offset:4096\n" \
  "s_waitcnt lgkmcnt(0)\n" \
  "v_cmp_gt_u32 vcc, v7, v20\n" \
  "s_and_b32 vcc_lo, vcc_lo, 0xff\n" \
  "s_cbranch_scc1 51b\n" \
  "v_exp_f32 v192, v192\n" \
  "ds_read_b128 v[20:23], v4 offset:0\n" \
  "v_exp_f32 v193, v193\n" \
  "ds_read_b64 v[224:225], v3 offset:0\n" \
  "v_smfmac_f32_32x32x32_bf16 v[240:255], v[140:143], v[216:223], v0 abid:1\n" \
  "v_exp_f32 v194, v194\n" \
  "s_cmp_eq_u32 s35, 0\n" \
  "s_cbranch_scc0 39f\n" \
  "s_add_u32 s86, s86, 16384\n" \
  "buffer_load_dwordx4 v[40:43], v8, s[24:27], s86 offen offset:0 sc1\n" \
  "39:\n" \
  "v_exp_f32 v195, v195\n" \
  "ds_read_b64 v[226:227], v3 offset:2048\n" \
  "v_exp_f32 v196, v196\n" \
  "ds_read_b64 v[228:229], v3 offset:4096\n" \
  "v_mfma_f32_32x32x16_bf16 v[208:223], v[108:111], v[16:19], 0\n" \
  "v_exp_f32 v197, v197\n" \
  "v_exp_f32 v198, v198\n" \
  "ds_read_b64 v[230:231], v3 offset:6144\n" \
  "v_exp_f32 v199, v199\n" \
  "ds_read_b64 v[232:233], v3 offset:8192\n" \
  "v_exp_f32 v200, v200\n" \
  "ds_read_b64 v[234:235], v3 offset:10240\n" \
  "v_exp_f32 v201, v201\n" \
  "ds_read_b64 v[236:237], v3 offset:12288\n" \
  "v_exp_f32 v202, v202\n" \
  "ds_read_b64 v[238:239], v3 offset:14336\n" \
  "v_smfmac_f32_32x32x32_bf16 v[240:255], v[144:147], v[192:199], v0 abid:1\n" \
  "v_exp_f32 v203, v203\n" \
  "v_exp_f32 v204, v204\n" \
  "s_cmp_eq_u32 s35, 0\n" \
  "s_cbranch_scc0 40f\n" \
  "buffer_load_dwordx4 v[72:75], v8, s[24:27], s86 offen offset:256 sc1\n" \
  "40:\n" \
  "v_exp_f32 v205, v205\n" \
  "v_exp_f32 v206, v206\n" \
  "v_exp_f32 v207, v207\n" \
  "v_exp_f32 v208, v208\n" \
  "s_waitcnt lgkmcnt(0)\n" \
  "v_pk_add_f32 v[20:21], v[20:21], v[22:23]\n" \
  "v_exp_f32 v209, v209\n" \
  "v_pk_add_f32 v[224:225], v[224:225], v[232:233]\n" \
  "v_smfmac_f32_32x32x32_bf16 v[240:255], v[148:151], v[200:207], v0 abid:1\n" \
  "v_exp_f32 v210, v210\n" \
  "s_cmp_eq_u32 s35, 0\n" \
  "s_cbranch_scc0 41f\n" \
  "s_add_u32 s86, s86, 16384\n" \
  "buffer_load_dwordx4 v[44:47], v8, s[24:27], s86 offen offset:0 sc1\n" \
  "41:\n" \
  "v_exp_f32 v211, v211\n" \
  "v_pk_add_f32 v[226:227], v[226:227], v[234:235]\n" \
  "v_add_f32 v20, v20, v21\n" \
  "v_exp_f32 v212, v212\n" \
  "v_pk_add_f32 v[228:229], v[228:229], v[236:237]\n" \
  "v_mfma_f32_32x32x16_bf16 v[192:207], v[112:115], v[16:19], 0\n" \
  "v_exp_f32 v213, v213\n" \
  "v_exp_f32 v214, v214\n" \
  "v_pk_add_f32 v[230:231], v[230:231], v[238:239]\n" \
  "v_add_f32_dpp v20, v20, v20 quad_perm:[1,0,3,2] row_mask:0xf bank_mask:0xf\n" \
  "v_exp_f32 v215, v215\n" \
  "v_pk_add_f32 v[224:225], v[224:225], v[228:229]\n" \
  "v_exp_f32 v216, v216\n" \
  "v_pk_add_f32 v[226:227], v[226:227], v[230:231]\n" \
  "v_rcp_f32 v21, v20\n" \
  "v_exp_f32 v217, v217\n" \
  "v_pk_add_f32 v[224:225], v[224:225], v[226:227]\n" \
  "v_exp_f32 v218, v218\n" \
  "v_max_f32 v20, v20, v21\n" \
  "v_mul_f32 v20, 0x6b800000, v20\n" \
  "v_smfmac_f32_32x32x32_bf16 v[240:255], v[152:155], v[208:215], v0 abid:1\n" \
  "v_exp_f32 v219, v219\n" \
  "v_exp_f32 v220, v220\n" \
  "v_fma_f32 v21, v20, 0, v21\n" \
  "s_cmp_eq_u32 s35, 0\n" \
  "s_cbranch_scc0 42f\n" \
  "buffer_load_dwordx4 v[76:79], v8, s[24:27], s86 offen offset:256 sc1\n" \
  "42:\n" \
  "v_exp_f32 v221, v221\n" \
  "v_mul_f32 v224, v224, v21\n" \
  "v_mul_f32 v225, v225, v21\n" \
  "v_exp_f32 v222, v222\n" \
  "v_cvt_pk_bf16_f32 v224, v224, v225\n" \
  "v_exp_f32 v223, v223\n" \
  "ds_write_b32 v5, v224 offset:0\n" \
  "2:\n" \
  "s_setprio 2\n" \
  "v_exp_f32 v192, v192\n" \
  "v_exp_f32 v193, v193\n" \
  "v_smfmac_f32_32x32x32_bf16 v[240:255], v[156:159], v[216:223], v0 abid:1\n" \
  "v_exp_f32 v194, v194\n" \
  "s_cmp_eq_u32 s35, 0\n" \
  "s_cbranch_scc0 43f\n" \
  "s_add_u32 s86, s86, 16384\n" \
  "buffer_load_dwordx4 v[48:51], v8, s[24:27], s86 offen offset:0 sc1\n" \
  "43:\n" \
  "v_exp_f32 v195, v195\n" \
  "v_exp_f32 v196, v196\n" \
  "v_mfma_f32_32x32x16_bf16 v[208:223], v[116:119], v[16:19], 0\n" \
  "v_exp_f32 v197, v197\n" \
  "v_exp_f32 v198, v198\n" \
  "v_exp_f32 v199, v199\n" \
  "v_exp_f32 v200, v200\n" \
  "v_exp_f32 v201, v201\n" \
  "v_exp_f32 v202, v202\n" \
  "v_smfmac_f32_32x32x32_bf16 v[240:255], v[160:163], v[192:199], v0 abid:1\n" \
  "v_exp_f32 v203, v203\n" \
  "v_exp_f32 v204, v204\n" \
  "s_cmp_eq_u32 s35, 0\n" \
  "s_cbranch_scc0 44f\n" \
  "buffer_load_dwordx4 v[80:83], v8, s[24:27], s86 offen offset:256 sc1\n" \
  "44:\n" \
  "v_exp_f32 v205, v205\n" \
  "v_exp_f32 v206, v206\n" \
  "v_mfma_f32_32x32x8_f16 v[224:239], v[10:11], v[10:11], 0\n" \
  "v_exp_f32 v207, v207\n" \
  "v_exp_f32 v208, v208\n" \
  "v_exp_f32 v209, v209\n" \
  "v_smfmac_f32_32x32x32_bf16 v[240:255], v[164:167], v[200:207], v0 abid:1\n" \
  "v_exp_f32 v210, v210\n" \
  "s_cmp_eq_u32 s35, 0\n" \
  "s_cbranch_scc0 45f\n" \
  "s_add_u32 s86, s86, 16384\n" \
  "buffer_load_dwordx4 v[52:55], v8, s[24:27], s86 offen offset:0 sc1\n" \
  "45:\n" \
  "v_exp_f32 v211, v211\n" \
  "v_exp_f32 v212, v212\n" \
  "v_mfma_f32_32x32x16_bf16 v[192:207], v[120:123], v[16:19], 0\n" \
  "v_exp_f32 v213, v213\n" \
  "v_exp_f32 v214, v214\n" \
  "v_exp_f32 v215, v215\n" \
  "v_exp_f32 v216, v216\n" \
  "v_exp_f32 v217, v217\n" \
  "v_exp_f32 v218, v218\n" \
  "v_smfmac_f32_32x32x32_bf16 v[240:255], v[168:171], v[208:215], v0 abid:1\n" \
  "v_exp_f32 v219, v219\n" \
  "v_exp_f32 v220, v220\n" \
  "s_cmp_eq_u32 s35, 0\n" \
  "s_cbranch_scc0 46f\n" \
  "buffer_load_dwordx4 v[84:87], v8, s[24:27], s86 offen offset:256 sc1\n" \
  "46:\n" \
  "v_exp_f32 v221, v221\n" \
  "v_exp_f32 v222, v222\n" \
  "v_exp_f32 v223, v223\n" \
  "s_setprio 1\n" \
  "v_exp_f32 v192, v192\n" \
  "v_exp_f32 v193, v193\n" \
  "v_smfmac_f32_32x32x32_bf16 v[240:255], v[172:175], v[216:223], v0 abid:1\n" \
  "v_exp_f32 v194, v194\n" \
  "s_cmp_eq_u32 s35, 0\n" \
  "s_cbranch_scc0 47f\n" \
  "s_add_u32 s86, s86, 16384\n" \
  "buffer_load_dwordx4 v[56:59], v8, s[24:27], s86 offen offset:0 sc1\n" \
  "47:\n" \
  "v_exp_f32 v195, v195\n" \
  "v_exp_f32 v196, v196\n" \
  "v_mfma_f32_32x32x16_bf16 v[208:223], v[124:127], v[16:19], 0\n" \
  "v_exp_f32 v197, v197\n" \
  "v_exp_f32 v198, v198\n" \
  "v_exp_f32 v199, v199\n" \
  "v_exp_f32 v200, v200\n" \
  "v_exp_f32 v201, v201\n" \
  "v_exp_f32 v202, v202\n" \
  "v_smfmac_f32_32x32x32_bf16 v[240:255], v[176:179], v[192:199], v0 abid:1\n" \
  "v_exp_f32 v203, v203\n" \
  "v_exp_f32 v204, v204\n" \
  "s_cmp_eq_u32 s35, 0\n" \
  "s_cbranch_scc0 48f\n" \
  "buffer_load_dwordx4 v[88:91], v8, s[24:27], s86 offen offset:256 sc1\n" \
  "48:\n" \
  "v_exp_f32 v205, v205\n" \
  "v_exp_f32 v206, v206\n" \
  "v_exp_f32 v207, v207\n" \
  "v_exp_f32 v208, v208\n" \
  "s_waitcnt lgkmcnt(0)\n" \
  "v_exp_f32 v209, v209\n" \
  "v_smfmac_f32_32x32x32_bf16 v[240:255], v[180:183], v[200:207], v0 abid:1\n" \
  "v_exp_f32 v210, v210\n" \
  "s_cmp_eq_u32 s35, 0\n" \
  "s_cbranch_scc0 49f\n" \
  "s_add_u32 s86, s86, 16384\n" \
  "buffer_load_dwordx4 v[60:63], v8, s[24:27], s86 offen offset:0 sc1\n" \
  "49:\n" \
  "v_exp_f32 v211, v211\n" \
  "v_exp_f32 v212, v212\n" \
  "v_mfma_f32_32x32x16_bf16 v[192:207], v[96:99], v[12:15], 0\n" \
  "v_exp_f32 v213, v213\n" \
  "v_exp_f32 v214, v214\n" \
  "v_exp_f32 v215, v215\n" \
  "v_exp_f32 v216, v216\n" \
  "v_exp_f32 v217, v217\n" \
  "v_exp_f32 v218, v218\n" \
  "v_smfmac_f32_32x32x32_bf16 v[240:255], v[184:187], v[208:215], v0 abid:1\n" \
  "v_exp_f32 v219, v219\n" \
  "v_exp_f32 v220, v220\n" \
  "s_cmp_eq_u32 s35, 0\n" \
  "s_cbranch_scc0 50f\n" \
  "buffer_load_dwordx4 v[92:95], v8, s[24:27], s86 offen offset:256 sc1\n" \
  "50:\n" \
  "v_exp_f32 v221, v221\n" \
  "v_exp_f32 v222, v222\n" \
  "v_exp_f32 v223, v223\n" \
  ASTAMP(48744, s35) \
  "s_branch 7f\n" \
  "0:\n" \
  "s_cmp_eq_u32 s80, 2\n" \
  "s_cbranch_scc0 67f\n" \
  ASTAMP(48752, s35) \
  "67:\n" \
  "v_exp_f32 v192, v192\n" \
  "ds_read_b128 v[12:15], v7 offset:0\n" \
  "v_exp_f32 v193, v193\n" \
  "v_smfmac_f32_32x32x32_bf16 v[224:239], v[188:191], v[216:223], v0 abid:1\n" \
  "v_exp_f32 v194, v194\n" \
  "v_exp_f32 v195, v195\n" \
  "v_exp_f32 v196, v196\n" \
  "v_mfma_f32_32x32x16_bf16 v[208:223], v[100:103], v[16:19], 0\n" \
  "v_exp_f32 v197, v197\n" \
  "v_exp_f32 v198, v198\n" \
  "v_exp_f32 v199, v199\n" \
  "v_exp_f32 v200, v200\n" \
  "v_exp_f32 v201, v201\n" \
  "v_exp_f32 v202, v202\n" \
  "v_smfmac_f32_32x32x32_bf16 v[240:255], v[128:131], v[192:199], v0 abid:1\n" \
  "v_exp_f32 v203, v203\n" \
  "v_exp_f32 v204, v204\n" \
  "v_exp_f32 v205, v205\n" \
  "ds_write_b128 v1, v[224:227] offset:0\n" \
  "v_exp_f32 v206, v206\n" \
  "ds_write_b128 v1, v[228:231] offset:1024\n" \
  "v_exp_f32 v207, v207\n" \
  "ds_write_b32 v2, v232 offset:0\n" \
  "s_setprio 0\n" \
  "v_exp_f32 v208, v208\n" \
  "s_cmp_eq_u32 s80, 2\n" \
  "s_cbranch_scc0 54f\n" \
  "v_mov_b64 v[20:21], -1\n" \
  "v_mov_b64 v[22:23], -1\n" \
  "buffer_store_dwordx4 v[20:23], v4, s[28:31], s56 offen sc1\n" \
  "54:\n" \
  "v_exp_f32 v209, v209\n" \
  "v_smfmac_f32_32x32x32_bf16 v[240:255], v[132:135], v[200:207], v0 abid:1\n" \
  "v_exp_f32 v210, v210\n" \
  "v_exp_f32 v211, v211\n" \
  "v_exp_f32 v212, v212\n" \
  "v_mfma_f32_32x32x16_bf16 v[192:207], v[104:107], v[16:19], 0\n" \
  "v_exp_f32 v213, v213\n" \
  "v_exp_f32 v214, v214\n" \
  "v_exp_f32 v215, v215\n" \
  "v_exp_f32 v216, v216\n" \
  "v_exp_f32 v217, v217\n" \
  "v_exp_f32 v218, v218\n" \
  "v_smfmac_f32_32x32x32_bf16 v[240:255], v[136:139], v[208:215], v0 abid:1\n" \
  "v_exp_f32 v219, v219\n" \
  "v_exp_f32 v220, v220\n" \
  "v_exp_f32 v221, v221\n" \
  "v_exp_f32 v222, v222\n" \
  "v_exp_f32 v223, v223\n" \
  "s_lshl_b64 exec, 1, s22\n" \
  "ds_write_b32 v4, v7 offset:4096\n" \
  "s_mov_b64 exec, -1\n" \
  "s_setprio 3\n" \
  "s_cmp_lt_u32 s22, 4\n" \
  "s_cbranch_scc1 1f\n" \
  "v_exp_f32 v192, v192\n" \
  "v_exp_f32 v193, v193\n" \
  "v_smfmac_f32_32x32x32_bf16 v[240:255], v[140:143], v[216:223], v0 abid:1\n" \
  "v_exp_f32 v194, v194\n" \
  "v_exp_f32 v195, v195\n" \
  "v_exp_f32 v196, v196\n" \
  "v_mfma_f32_32x32x16_bf16 v[208:223], v[108:111], v[16:19], 0\n" \
  "v_exp_f32 v197, v197\n" \
  "v_exp_f32 v198, v198\n" \
  "v_exp_f32 v199, v199\n" \
  "v_exp_f32 v200, v200\n" \
  "v_exp_f32 v201, v201\n" \
  "v_exp_f32 v202, v202\n" \
  "v_smfmac_f32_32x32x32_bf16 v[240:255], v[144:147], v[192:199], v0 abid:1\n" \
  "v_exp_f32 v203, v203\n" \
  "v_exp_f32 v204, v204\n" \
  "v_exp_f32 v205, v205\n" \
  "v_exp_f32 v206, v206\n" \
  "v_exp_f32 v207, v207\n" \
  "v_exp_f32 v208, v208\n" \
  "v_exp_f32 v209, v209\n" \
  "v_smfmac_f32_32x32x32_bf16 v[240:255], v[148:151], v[200:207], v0 abid:1\n" \
  "v_exp_f32 v210, v210\n" \
  "v_exp_f32 v211, v211\n" \
  "v_exp_f32 v212, v212\n" \
  "v_mfma_f32_32x32x16_bf16 v[192:207], v[112:115], v[16:19], 0\n" \
  "v_exp_f32 v213, v213\n" \
  "v_exp_f32 v214, v214\n" \
  "v_exp_f32 v215, v215\n" \
  "v_exp_f32 v216, v216\n" \
  "v_exp_f32 v217, v217\n" \
  "v_exp_f32 v218, v218\n" \
  "v_smfmac_f32_32x32x32_bf16 v[240:255], v[152:155], v[208:215], v0 abid:1\n" \
  "v_exp_f32 v219, v219\n" \
  "v_exp_f32 v220, v220\n" \
  "v_exp_f32 v221, v221\n" \
  "v_exp_f32 v222, v222\n" \
  "v_exp_f32 v223, v223\n" \
  "s_branch 2f\n" \
  "1:\n" \
  "68:\n" \
  "ds_read_b32 v20, v4 offset:4096\n" \
  "s_waitcnt lgkmcnt(0)\n" \
  "v_cmp_gt_u32 vcc, v7, v20\n" \
  "s_and_b32 vcc_lo, vcc_lo, 0xff\n" \
  "s_cbranch_scc1 68b\n" \
  "s_lshl_b32 s81, s22, 4\n" \
  "s_or_b32 s81, s81, s80\n" \
  "s_cmp_eq_u32 s81, 4\n" \
  "s_cbranch_scc0 70f\n" \
  "s_mov_b64 exec, s[74:75]\n" \
  "v_mov_b64 v[8:9], s[36:37]\n" \
  "buffer_store_dwordx2 v[8:9], v4, s[28:31], s59 offen sc1\n" \
  "s_mov_b64 exec, -1\n" \
  "70:\n" \
  "v_exp_f32 v192, v192\n" \
  "ds_read_b128 v[20:23], v4 offset:0\n" \
  "v_exp_f32 v193, v193\n" \
  "ds_read_b64 v[224:225], v3 offset:0\n" \
  "v_smfmac_f32_32x32x32_bf16 v[240:255], v[140:143], v[216:223], v0 abid:1\n" \
  "v_exp_f32 v194, v194\n" \
  "v_exp_f32 v195, v195\n" \
  "ds_read_b64 v[226:227], v3 offset:2048\n" \
  "v_exp_f32 v196, v196\n" \
  "ds_read_b64 v[228:229], v3 offset:4096\n" \
  "v_mfma_f32_32x32x16_bf16 v[208:223], v[108:111], v[16:19], 0\n" \
  "v_exp_f32 v197, v197\n" \
  "v_exp_f32 v198, v198\n" \
  "ds_read_b64 v[230:231], v3 offset:6144\n" \
  "v_exp_f32 v199, v199\n" \
  "ds_read_b64 v[232:233], v3 offset:8192\n" \
  "v_exp_f32 v200, v200\n" \
  "ds_read_b64 v[234:235], v3 offset:10240\n" \
  "v_exp_f32 v201, v201\n" \
  "ds_read_b64 v[236:237], v3 offset:12288\n" \
  "v_exp_f32 v202, v202\n" \
  "ds_read_b64 v[238:239], v3 offset:14336\n" \
  "v_smfmac_f32_32x32x32_bf16 v[240:255], v[144:147], v[192:199], v0 abid:1\n" \
  "v_exp_f32 v203, v203\n" \
  "v_exp_f32 v204, v204\n" \
  "v_exp_f32 v205, v205\n" \
  "v_exp_f32 v206, v206\n" \
  "v_exp_f32 v207, v207\n" \
  "v_exp_f32 v208, v208\n" \
  "s_waitcnt lgkmcnt(0)\n" \
  "v_pk_add_f32 v[20:21], v[20:21], v[22:23]\n" \
  "v_exp_f32 v209, v209\n" \
  "v_pk_add_f32 v[224:225], v[224:225], v[232:233]\n" \
  "v_smfmac_f32_32x32x32_bf16 v[240:255], v[148:151], v[200:207], v0 abid:1\n" \
  "v_exp_f32 v210, v210\n" \
  "v_exp_f32 v211, v211\n" \
  "v_pk_add_f32 v[226:227], v[226:227], v[234:235]\n" \
  "v_add_f32 v20, v20, v21\n" \
  "v_exp_f32 v212, v212\n" \
  "v_pk_add_f32 v[228:229], v[228:229], v[236:237]\n" \
  "v_mfma_f32_32x32x16_bf16 v[192:207], v[112:115], v[16:19], 0\n" \
  "v_exp_f32 v213, v213\n" \
  "v_exp_f32 v214, v214\n" \
  "v_pk_add_f32 v[230:231], v[230:231], v[238:239]\n" \
  "v_add_f32_dpp v20, v20, v20 quad_perm:[1,0,3,2] row_mask:0xf bank_mask:0xf\n" \
  "v_exp_f32 v215, v215\n" \
  "v_pk_add_f32 v[224:225], v[224:225], v[228:229]\n" \
  "v_exp_f32 v216, v216\n" \
  "v_pk_add_f32 v[226:227], v[226:227], v[230:231]\n" \
  "v_rcp_f32 v21, v20\n" \
  "v_exp_f32 v217, v217\n" \
  "v_pk_add_f32 v[224:225], v[224:225], v[226:227]\n" \
  "v_exp_f32 v218, v218\n" \
  "v_max_f32 v20, v20, v21\n" \
  "v_mul_f32 v20, 0x6b800000, v20\n" \
  "v_smfmac_f32_32x32x32_bf16 v[240:255], v[152:155], v[208:215], v0 abid:1\n" \
  "v_exp_f32 v219, v219\n" \
  "v_exp_f32 v220, v220\n" \
  "v_fma_f32 v21, v20, 0, v21\n" \
  "v_exp_f32 v221, v221\n" \
  "v_mul_f32 v224, v224, v21\n" \
  "v_mul_f32 v225, v225, v21\n" \
  "v_exp_f32 v222, v222\n" \
  "v_cvt_pk_bf16_f32 v224, v224, v225\n" \
  "v_exp_f32 v223, v223\n" \
  "ds_write_b32 v5, v224 offset:0\n" \
  "2:\n" \
  "s_setprio 2\n" \
  "v_exp_f32 v192, v192\n" \
  "v_exp_f32 v193, v193\n" \
  "v_smfmac_f32_32x32x32_bf16 v[240:255], v[156:159], v[216:223], v0 abid:1\n" \
  "v_exp_f32 v194, v194\n" \
  "v_exp_f32 v195, v195\n" \
  "v_exp_f32 v196, v196\n" \
  "v_mfma_f32_32x32x16_bf16 v[208:223], v[116:119], v[16:19], 0\n" \
  "v_exp_f32 v197, v197\n" \
  "v_exp_f32 v198, v198\n" \
  "v_exp_f32 v199, v199\n" \
  "v_exp_f32 v200, v200\n" \
  "v_exp_f32 v201, v201\n" \
  "v_exp_f32 v202, v202\n" \
  "v_smfmac_f32_32x32x32_bf16 v[240:255], v[160:163], v[192:199], v0 abid:1\n" \
  "v_exp_f32 v203, v203\n" \
  "v_exp_f32 v204, v204\n" \
  "v_exp_f32 v205, v205\n" \
  "v_exp_f32 v206, v206\n" \
  "v_mfma_f32_32x32x8_f16 v[224:239], v[10:11], v[10:11], 0\n" \
  "v_exp_f32 v207, v207\n" \
  "v_exp_f32 v208, v208\n" \
  "s_cmp_eq_u32 s80, 2\n" \
  "s_cbranch_scc0 55f\n" \
  "v_mov_b64 v[20:21], -1\n" \
  "v_mov_b64 v[22:23], -1\n" \
  "buffer_store_dwordx4 v[20:23], v4, s[28:31], s57 offen sc1\n" \
  "55:\n" \
  "v_exp_f32 v209, v209\n" \
  "v_smfmac_f32_32x32x32_bf16 v[240:255], v[164:167], v[200:207], v0 abid:1\n" \
  "v_exp_f32 v210, v210\n" \
  "v_exp_f32 v211, v211\n" \
  "v_exp_f32 v212, v212\n" \
  "v_mfma_f32_32x32x16_bf16 v[192:207], v[120:123], v[16:19], 0\n" \
  "v_exp_f32 v213, v213\n" \
  "v_exp_f32 v214, v214\n" \
  "v_exp_f32 v215, v215\n" \
  "v_exp_f32 v216, v216\n" \
  "v_exp_f32 v217, v217\n" \
  "v_exp_f32 v218, v218\n" \
  "v_smfmac_f32_32x32x32_bf16 v[240:255], v[168:171], v[208:215], v0 abid:1\n" \
  "v_exp_f32 v219, v219\n" \
  "v_exp_f32 v220, v220\n" \
  "v_exp_f32 v221, v221\n" \
  "v_exp_f32 v222, v222\n" \
  "v_exp_f32 v223, v223\n" \
  "s_setprio 1\n" \
  "v_exp_f32 v192, v192\n" \
  "v_exp_f32 v193, v193\n" \
  "v_smfmac_f32_32x32x32_bf16 v[240:255], v[172:175], v[216:223], v0 abid:1\n" \
  "v_exp_f32 v194, v194\n" \
  "v_exp_f32 v195, v195\n" \
  "v_exp_f32 v196, v196\n" \
  "v_mfma_f32_32x32x16_bf16 v[208:223], v[124:127], v[16:19], 0\n" \
  "v_exp_f32 v197, v197\n" \
  "v_exp_f32 v198, v198\n" \
  "v_exp_f32 v199, v199\n" \
  "v_exp_f32 v200, v200\n" \
  "v_exp_f32 v201, v201\n" \
  "v_exp_f32 v202, v202\n" \
  "v_smfmac_f32_32x32x32_bf16 v[240:255], v[176:179], v[192:199], v0 abid:1\n" \
  "v_exp_f32 v203, v203\n" \
  "v_exp_f32 v204, v204\n" \
  "v_exp_f32 v205, v205\n" \
  "v_exp_f32 v206, v206\n" \
  "v_exp_f32 v207, v207\n" \
  "v_exp_f32 v208, v208\n" \
  "s_waitcnt lgkmcnt(0)\n" \
  "v_exp_f32 v209, v209\n" \
  "v_smfmac_f32_32x32x32_bf16 v[240:255], v[180:183], v[200:207], v0 abid:1\n" \
  "v_exp_f32 v210, v210\n" \
  "v_exp_f32 v211, v211\n" \
  "v_exp_f32 v212, v212\n" \
  "v_mfma_f32_32x32x16_bf16 v[192:207], v[96:99], v[12:15], 0\n" \
  "v_exp_f32 v213, v213\n" \
  "v_exp_f32 v214, v214\n" \
  "v_exp_f32 v215, v215\n" \
  "v_exp_f32 v216, v216\n" \
  "v_exp_f32 v217, v217\n" \
  "v_exp_f32 v218, v218\n" \
  "v_smfmac_f32_32x32x32_bf16 v[240:255], v[184:187], v[208:215], v0 abid:1\n" \
  "v_exp_f32 v219, v219\n" \
  "v_exp_f32 v220, v220\n" \
  "v_exp_f32 v221, v221\n" \
  "v_exp_f32 v222, v222\n" \
  "v_exp_f32 v223, v223\n" \
  "7:\n" \
  "v_exp_f32 v192, v192\n" \
  "ds_read_b128 v[16:19], v7 offset:1024\n" \
  "v_exp_f32 v193, v193\n" \
  "v_smfmac_f32_32x32x32_bf16 v[240:255], v[188:191], v[216:223], v0 abid:1\n" \
  "v_exp_f32 v194, v194\n" \
  "v_exp_f32 v195, v195\n" \
  "v_exp_f32 v196, v196\n" \
  "v_mfma_f32_32x32x16_bf16 v[208:223], v[100:103], v[12:15], 0\n" \
  "v_exp_f32 v197, v197\n" \
  "v_exp_f32 v198, v198\n" \
  "v_exp_f32 v199, v199\n" \
  "v_exp_f32 v200, v200\n" \
  "v_exp_f32 v201, v201\n" \
  "v_exp_f32 v202, v202\n" \
  "v_smfmac_f32_32x32x32_bf16 v[224:239], v[128:131], v[192:199], v0 abid:1\n" \
  "v_exp_f32 v203, v203\n" \
  "v_exp_f32 v204, v204\n" \
  "v_exp_f32 v205, v205\n" \
  "ds_write_b128 v1, v[240:243] offset:16384\n" \
  "v_exp_f32 v206, v206\n" \
  "ds_write_b128 v1, v[244:247] offset:17408\n" \
  "v_exp_f32 v207, v207\n" \
  "ds_write_b32 v2, v248 offset:2048\n" \
  "s_setprio 0\n" \
  "v_exp_f32 v208, v208\n" \
  "s_lshl_b32 s81, s35, 2\n" \
  "s_or_b32 s81, s81, s80\n" \
  "s_cmp_gt_u32 s81, 2\n" \
  "s_cbranch_scc1 53f\n" \
  "s_cmp_eq_u32 s80, 1\n" \
  "s_cselect_b32 s87, s45, s41\n" \
  "s_lshl_b32 s81, s22, 10\n" \
  "s_add_u32 s87, s87, s81\n" \
  "s_add_u32 s87, s87, 10645504\n" \
  "v_mov_b64 v[20:21], -1\n" \
  "v_mov_b64 v[22:23], -1\n" \
  "buffer_store_dwordx4 v[20:23], v4, s[28:31], s87 offen sc1\n" \
  "53:\n" \
  "v_exp_f32 v209, v209\n" \
  "v_smfmac_f32_32x32x32_bf16 v[224:239], v[132:135], v[200:207], v0 abid:1\n" \
  "v_exp_f32 v210, v210\n" \
  "v_exp_f32 v211, v211\n" \
  "v_exp_f32 v212, v212\n" \
  "v_mfma_f32_32x32x16_bf16 v[192:207], v[104:107], v[12:15], 0\n" \
  "v_exp_f32 v213, v213\n" \
  "v_exp_f32 v214, v214\n" \
  "v_exp_f32 v215, v215\n" \
  "v_exp_f32 v216, v216\n" \
  "v_exp_f32 v217, v217\n" \
  "v_exp_f32 v218, v218\n" \
  "v_smfmac_f32_32x32x32_bf16 v[224:239], v[136:139], v[208:215], v0 abid:1\n" \
  "v_exp_f32 v219, v219\n" \
  "v_exp_f32 v220, v220\n" \
  "v_exp_f32 v221, v221\n" \
  "v_exp_f32 v222, v222\n" \
  "v_exp_f32 v223, v223\n" \
  "s_lshl_b64 exec, 1, s22\n" \
  "ds_write_b32 v4, v7 offset:4224\n" \
  "s_mov_b64 exec, -1\n" \
  "s_setprio 3\n" \
  "s_cmp_lt_u32 s22, 4\n" \
  "s_cbranch_scc1 1f\n" \
  "v_exp_f32 v192, v192\n" \
  "v_exp_f32 v193, v193\n" \
  "v_smfmac_f32_32x32x32_bf16 v[224:239], v[140:143], v[216:223], v0 abid:1\n" \
  "v_exp_f32 v194, v194\n" \
  "v_exp_f32 v195, v195\n" \
  "v_exp_f32 v196, v196\n" \
  "v_mfma_f32_32x32x16_bf16 v[208:223], v[108:111], v[12:15], 0\n" \
  "v_exp_f32 v197, v197\n" \
  "v_exp_f32 v198, v198\n" \
  "v_exp_f32 v199, v199\n" \
  "v_exp_f32 v200, v200\n" \
  "v_exp_f32 v201, v201\n" \
  "v_exp_f32 v202, v202\n" \
  "v_smfmac_f32_32x32x32_bf16 v[224:239], v[144:147], v[192:199], v0 abid:1\n" \
  "v_exp_f32 v203, v203\n" \
  "v_exp_f32 v204, v204\n" \
  "v_exp_f32 v205, v205\n" \
  "v_exp_f32 v206, v206\n" \
  "v_exp_f32 v207, v207\n" \
  "v_exp_f32 v208, v208\n" \
  "v_exp_f32 v209, v209\n" \
  "v_smfmac_f32_32x32x32_bf16 v[224:239], v[148:151], v[200:207], v0 abid:1\n" \
  "v_exp_f32 v210, v210\n" \
  "v_exp_f32 v211, v211\n" \
  "v_exp_f32 v212, v212\n" \
  "v_mfma_f32_32x32x16_bf16 v[192:207], v[112:115], v[12:15], 0\n" \
  "v_exp_f32 v213, v213\n" \
  "v_exp_f32 v214, v214\n" \
  "v_exp_f32 v215, v215\n" \
  "v_exp_f32 v216, v216\n" \
  "v_exp_f32 v217, v217\n" \
  "v_exp_f32 v218, v218\n" \
  "v_smfmac_f32_32x32x32_bf16 v[224:239], v[152:155], v[208:215], v0 abid:1\n" \
  "v_exp_f32 v219, v219\n" \
  "v_exp_f32 v220, v220\n" \
  "v_exp_f32 v221, v221\n" \
  "v_exp_f32 v222, v222\n" \
  "v_exp_f32 v223, v223\n" \
  "s_branch 2f\n" \
  "1:\n" \
  "71:\n" \
  "ds_read_b32 v20, v4 offset:4224\n" \
  "s_waitcnt lgkmcnt(0)\n" \
  "v_cmp_gt_u32 vcc, v7, v20\n" \
  "s_and_b32 vcc_lo, vcc_lo, 0xff\n" \
  "s_cbranch_scc1 71b\n" \
  "v_exp_f32 v192, v192\n" \
  "ds_read_b128 v[20:23], v4 offset:2048\n" \
  "v_exp_f32 v193, v193\n" \
  "ds_read_b64 v[240:241], v3 offset:16384\n" \
  "v_smfmac_f32_32x32x32_bf16 v[224:239], v[140:143], v[216:223], v0 abid:1\n" \
  "v_exp_f32 v194, v194\n" \
  "v_exp_f32 v195, v195\n" \
  "ds_read_b64 v[242:243], v3 offset:18432\n" \
  "v_exp_f32 v196, v196\n" \
  "ds_read_b64 v[244:245], v3 offset:20480\n" \
  "v_mfma_f32_32x32x16_bf16 v[208:223], v[108:111], v[12:15], 0\n" \
  "v_exp_f32 v197, v197\n" \
  "v_exp_f32 v198, v198\n" \
  "ds_read_b64 v[246:247], v3 offset:22528\n" \
  "v_exp_f32 v199, v199\n" \
  "ds_read_b64 v[248:249], v3 offset:24576\n" \
  "v_exp_f32 v200, v200\n" \
  "ds_read_b64 v[250:251], v3 offset:26624\n" \
  "v_exp_f32 v201, v201\n" \
  "ds_read_b64 v[252:253], v3 offset:28672\n" \
  "v_exp_f32 v202, v202\n" \
  "ds_read_b64 v[254:255], v3 offset:30720\n" \
  "v_smfmac_f32_32x32x32_bf16 v[224:239], v[144:147], v[192:199], v0 abid:1\n" \
  "v_exp_f32 v203, v203\n" \
  "v_exp_f32 v204, v204\n" \
  "v_exp_f32 v205, v205\n" \
  "v_exp_f32 v206, v206\n" \
  "v_exp_f32 v207, v207\n" \
  "v_exp_f32 v208, v208\n" \
  "s_waitcnt lgkmcnt(0)\n" \
  "v_pk_add_f32 v[20:21], v[20:21], v[22:23]\n" \
  "v_exp_f32 v209, v209\n" \
  "v_pk_add_f32 v[240:241], v[240:241], v[248:249]\n" \
  "v_smfmac_f32_32x32x32_bf16 v[224:239], v[148:151], v[200:207], v0 abid:1\n" \
  "v_exp_f32 v210, v210\n" \
  "v_exp_f32 v211, v211\n" \
  "v_pk_add_f32 v[242:243], v[242:243], v[250:251]\n" \
  "v_add_f32 v20, v20, v21\n" \
  "v_exp_f32 v212, v212\n" \
  "v_pk_add_f32 v[244:245], v[244:245], v[252:253]\n" \
  "v_mfma_f32_32x32x16_bf16 v[192:207], v[112:115], v[12:15], 0\n" \
  "v_exp_f32 v213, v213\n" \
  "v_exp_f32 v214, v214\n" \
  "v_pk_add_f32 v[246:247], v[246:247], v[254:255]\n" \
  "v_add_f32_dpp v20, v20, v20 quad_perm:[1,0,3,2] row_mask:0xf bank_mask:0xf\n" \
  "v_exp_f32 v215, v215\n" \
  "v_pk_add_f32 v[240:241], v[240:241], v[244:245]\n" \
  "v_exp_f32 v216, v216\n" \
  "v_pk_add_f32 v[242:243], v[242:243], v[246:247]\n" \
  "v_rcp_f32 v21, v20\n" \
  "v_exp_f32 v217, v217\n" \
  "v_pk_add_f32 v[240:241], v[240:241], v[242:243]\n" \
  "v_exp_f32 v218, v218\n" \
  "v_max_f32 v20, v20, v21\n" \
  "v_mul_f32 v20, 0x6b800000, v20\n" \
  "v_smfmac_f32_32x32x32_bf16 v[224:239], v[152:155], v[208:215], v0 abid:1\n" \
  "v_exp_f32 v219, v219\n" \
  "v_exp_f32 v220, v220\n" \
  "v_fma_f32 v21, v20, 0, v21\n" \
  "v_exp_f32 v221, v221\n" \
  "v_mul_f32 v240, v240, v21\n" \
  "v_mul_f32 v241, v241, v21\n" \
  "v_exp_f32 v222, v222\n" \
  "v_cvt_pk_bf16_f32 v240, v240, v241\n" \
  "v_exp_f32 v223, v223\n" \
  "ds_write_b32 v5, v240 offset:128\n" \
  "s_lshl_b64 exec, 1, s22\n" \
  "ds_write_b32 v4, v7 offset:4352\n" \
  "s_mov_b64 exec, -1\n" \
  "2:\n" \
  "s_setprio 2\n" \
  "v_exp_f32 v192, v192\n" \
  "ds_read_b32 v20, v4 offset:4352\n" \
  "v_exp_f32 v193, v193\n" \
  "v_smfmac_f32_32x32x32_bf16 v[224:239], v[156:159], v[216:223], v0 abid:1\n" \
  "v_exp_f32 v194, v194\n" \
  "v_exp_f32 v195, v195\n" \
  "v_exp_f32 v196, v196\n" \
  "v_mfma_f32_32x32x16_bf16 v[208:223], v[116:119], v[12:15], 0\n" \
  "v_exp_f32 v197, v197\n" \
  "v_exp_f32 v198, v198\n" \
  "v_exp_f32 v199, v199\n" \
  "v_mfma_f32_16x16x16_f16 v[248:251], v[10:11], v[10:11], 0\n" \
  "v_exp_f32 v200, v200\n" \
  "v_exp_f32 v201, v201\n" \
  "v_mfma_f32_16x16x16_f16 v[252:255], v[10:11], v[10:11], 0\n" \
  "v_exp_f32 v202, v202\n" \
  "v_smfmac_f32_32x32x32_bf16 v[224:239], v[160:163], v[192:199], v0 abid:1\n" \
  "v_exp_f32 v203, v203\n" \
  "v_exp_f32 v204, v204\n" \
  "s_waitcnt lgkmcnt(0)\n" \
  "v_cmp_gt_u32 vcc, v7, v20\n" \
  "s_and_b32 vcc_lo, vcc_lo, 0xf\n" \
  "s_cbranch_scc0 74f\n" \
  "73:\n" \
  "s_sleep 1\n" \
  "ds_read_b32 v20, v4 offset:4352\n" \
  "s_waitcnt lgkmcnt(0)\n" \
  "v_cmp_gt_u32 vcc, v7, v20\n" \
  "s_and_b32 vcc_lo, vcc_lo, 0xf\n" \
  "s_cbranch_scc1 73b\n" \
  "74:\n" \
  "ds_read_b128 v[240:243], v6\n" \
  "v_exp_f32 v205, v205\n" \
  "ds_read_b128 v[244:247], v6 offset:64\n" \
  "v_exp_f32 v206, v206\n" \
  "v_exp_f32 v207, v207\n" \
  "v_exp_f32 v208, v208\n" \
  "v_exp_f32 v209, v209\n" \
  "v_smfmac_f32_32x32x32_bf16 v[224:239], v[164:167], v[200:207], v0 abid:1\n" \
  "v_exp_f32 v210, v210\n" \
  "v_exp_f32 v211, v211\n" \
  "v_exp_f32 v212, v212\n" \
  "v_mfma_f32_32x32x16_bf16 v[192:207], v[120:123], v[12:15], 0\n" \
  "v_exp_f32 v213, v213\n" \
  "v_exp_f32 v214, v214\n" \
  "v_exp_f32 v215, v215\n" \
  "s_waitcnt vmcnt(0) lgkmcnt(0)\n" \
  "v_smfmac_f32_16x16x64_bf16 v[248:251], v[240:243], v[32:39], v0\n" \
  "v_exp_f32 v216, v216\n" \
  "v_exp_f32 v217, v217\n" \
  "v_smfmac_f32_16x16x64_bf16 v[252:255], v[240:243], v[64:71], v0\n" \
  "v_exp_f32 v218, v218\n" \
  "ds_read_b128 v[240:243], v6 offset:128\n" \
  "v_smfmac_f32_32x32x32_bf16 v[224:239], v[168:171], v[208:215], v0 abid:1\n" \
  "v_exp_f32 v219, v219\n" \
  "v_exp_f32 v220, v220\n" \
  "v_exp_f32 v221, v221\n" \
  "v_smfmac_f32_16x16x64_bf16 v[248:251], v[244:247], v[40:47], v0\n" \
  "v_exp_f32 v222, v222\n" \
  "v_exp_f32 v223, v223\n" \
  "v_smfmac_f32_16x16x64_bf16 v[252:255], v[244:247], v[72:79], v0\n" \
  "s_setprio 1\n" \
  "v_exp_f32 v192, v192\n" \
  "ds_read_b128 v[244:247], v6 offset:192\n" \
  "v_exp_f32 v193, v193\n" \
  "v_smfmac_f32_32x32x32_bf16 v[224:239], v[172:175], v[216:223], v0 abid:1\n" \
  "v_exp_f32 v194, v194\n" \
  "v_exp_f32 v195, v195\n" \
  "s_cmp_eq_u32 s80, 4\n" \
  "s_cbranch_scc0 75f\n" \
  "buffer_load_dwordx2 v[8:9], v4, s[28:31], s59 offen sc1\n" \
  "75:\n" \
  "v_exp_f32 v196, v196\n" \
  "v_mfma_f32_32x32x16_bf16 v[208:223], v[124:127], v[12:15], 0\n" \
  "v_exp_f32 v197, v197\n" \
  "v_exp_f32 v198, v198\n" \
  "v_exp_f32 v199, v199\n" \
  "s_waitcnt lgkmcnt(1)\n" \
  "v_smfmac_f32_16x16x64_bf16 v[248:251], v[240:243], v[48:55], v0\n" \
  "v_exp_f32 v200, v200\n" \
  "v_exp_f32 v201, v201\n" \
  "v_smfmac_f32_16x16x64_bf16 v[252:255], v[240:243], v[80:87], v0\n" \
  "v_exp_f32 v202, v202\n" \
  "v_smfmac_f32_32x32x32_bf16 v[224:239], v[176:179], v[192:199], v0 abid:1\n" \
  "v_exp_f32 v203, v203\n" \
  "v_exp_f32 v204, v204\n" \
  "v_exp_f32 v205, v205\n" \
  "s_waitcnt lgkmcnt(0)\n" \
  "v_smfmac_f32_16x16x64_bf16 v[248:251], v[244:247], v[56:63], v0\n" \
  "v_exp_f32 v206, v206\n" \
  "v_exp_f32 v207, v207\n" \
  "v_smfmac_f32_16x16x64_bf16 v[252:255], v[244:247], v[88:95], v0\n" \
  "v_exp_f32 v208, v208\n" \
  "v_exp_f32 v209, v209\n" \
  "v_smfmac_f32_32x32x32_bf16 v[224:239], v[180:183], v[200:207], v0 abid:1\n" \
  "v_exp_f32 v210, v210\n" \
  "v_exp_f32 v211, v211\n" \
  "v_exp_f32 v212, v212\n" \
  "v_mfma_f32_32x32x16_bf16 v[192:207], v[96:99], v[16:19], 0\n" \
  "v_exp_f32 v213, v213\n" \
  "v_exp_f32 v214, v214\n" \
  "v_add_f32 v240, |v248|, |v250|\n" \
  "v_cvt_pk_bf16_f32 v20, v248, v249\n" \
  "v_exp_f32 v215, v215\n" \
  "v_cmp_ngt_f32 vcc, 0x7f000000, v240\n" \
  "v_cvt_pk_bf16_f32 v21, v250, v251\n" \
  "v_exp_f32 v216, v216\n" \
  "v_cvt_pk_bf16_f32 v22, v252, v253\n" \
  "v_exp_f32 v217, v217\n" \
  "v_cvt_pk_bf16_f32 v23, v254, v255\n" \
  "v_exp_f32 v218, v218\n" \
  "v_smfmac_f32_32x32x32_bf16 v[224:239], v[184:187], v[208:215], v0 abid:1\n" \
  "v_exp_f32 v219, v219\n" \
  "v_exp_f32 v220, v220\n" \
  "s_cbranch_vccnz 5f\n" \
  "buffer_store_dwordx4 v[20:23], v4, s[28:31], s84 offen sc1\n" \
  "s_branch 6f\n" \
  "5:\n" \
  "s_or_b32 s79, s79, s80\n" \
  "6:\n" \
  "v_exp_f32 v221, v221\n" \
  "v_exp_f32 v222, v222\n" \
  "v_mfma_f32_32x32x8_f16 v[240:255], v[10:11], v[10:11], 0\n" \
  "v_exp_f32 v223, v223\n" \
  "v_add_u32 v7, 2048, v7\n" \
  "s_mov_b32 s84, s85\n" \
  "s_mov_b32 s85, s57\n" \
  "s_lshl_b32 s80, s80, 1\n" \
  "s_cmp_eq_u32 s80, 8\n" \
  "s_cbranch_scc0 0b\n" \
  ASTAMP(48760, s35) \
  "v_exp_f32 v192, v192\n" \
  "s_cmp_eq_u32 s35, 0\n" \
  "s_cbranch_scc0 56f\n" \
  "v_mbcnt_lo_u32_b32 v13, -1, 0\n" \
  "v_mbcnt_hi_u32_b32 v13, -1, v13\n" \
  "v_lshrrev_b32 v14, 4, v13\n" \
  "v_and_b32 v12, 12, v13\n" \
  "v_lshlrev_b32 v12, 10, v12\n" \
  "v_lshl_add_u32 v12, v14, 6, v12\n" \
  "v_and_b32 v14, 3, v13\n" \
  "v_lshl_add_u32 v12, v14, 4, v12\n" \
  "56:\n" \
  "v_exp_f32 v193, v193\n" \
  "v_smfmac_f32_32x32x32_bf16 v[224:239], v[188:191], v[216:223], v0 abid:1\n" \
  "v_exp_f32 v194, v194\n" \
  "v_exp_f32 v195, v195\n" \
  "v_exp_f32 v196, v196\n" \
  "v_mfma_f32_32x32x16_bf16 v[208:223], v[100:103], v[16:19], 0\n" \
  "v_exp_f32 v197, v197\n" \
  "v_exp_f32 v198, v198\n" \
  "v_exp_f32 v199, v199\n" \
  "v_exp_f32 v200, v200\n" \
  "v_exp_f32 v201, v201\n" \
  "v_exp_f32 v202, v202\n" \
  "v_smfmac_f32_32x32x32_bf16 v[240:255], v[128:131], v[192:199], v0 abid:1\n" \
  "v_exp_f32 v203, v203\n" \
  "v_exp_f32 v204, v204\n" \
  "v_exp_f32 v205, v205\n" \
  "ds_write_b128 v1, v[224:227] offset:0\n" \
  "v_exp_f32 v206, v206\n" \
  "ds_write_b128 v1, v[228:231] offset:1024\n" \
  "v_exp_f32 v207, v207\n" \
  "ds_write_b32 v2, v232 offset:0\n" \
  "s_setprio 0\n" \
  "v_exp_f32 v208, v208\n" \
  "v_exp_f32 v209, v209\n" \
  "v_smfmac_f32_32x32x32_bf16 v[240:255], v[132:135], v[200:207], v0 abid:1\n" \
  "v_exp_f32 v210, v210\n" \
  "v_exp_f32 v211, v211\n" \
  "s_cmp_eq_u32 s35, 0\n" \
  "s_cbranch_scc0 61f\n" \
  "buffer_load_dwordx4 v[128:131], v12, s[24:27], s61 offen offset:2560 sc1\n" \
  "61:\n" \
  "v_exp_f32 v212, v212\n" \
  "v_mfma_f32_32x32x16_bf16 v[192:207], v[104:107], v[16:19], 0\n" \
  "v_exp_f32 v213, v213\n" \
  "v_exp_f32 v214, v214\n" \
  "v_exp_f32 v215, v215\n" \
  "v_exp_f32 v216, v216\n" \
  "v_exp_f32 v217, v217\n" \
  "v_exp_f32 v218, v218\n" \
  "v_smfmac_f32_32x32x32_bf16 v[240:255], v[136:139], v[208:215], v0 abid:1\n" \
  "v_exp_f32 v219, v219\n" \
  "v_exp_f32 v220, v220\n" \
  "s_cmp_eq_u32 s35, 0\n" \
  "s_cbranch_scc0 62f\n" \
  "buffer_load_dwordx4 v[132:135], v12, s[24:27], s61 offen offset:2816 sc1\n" \
  "62:\n" \
  "v_exp_f32 v221, v221\n" \
  "v_exp_f32 v222, v222\n" \
  "v_exp_f32 v223, v223\n" \
  "s_lshl_b64 exec, 1, s22\n" \
  "ds_write_b32 v4, v7 offset:4096\n" \
  "s_mov_b64 exec, -1\n" \
  "s_setprio 3\n" \
  "s_cmp_lt_u32 s22, 4\n" \
  "s_cbranch_scc1 1f\n" \
  "v_exp_f32 v192, v192\n" \
  "v_exp_f32 v193, v193\n" \
  "v_smfmac_f32_32x32x32_bf16 v[240:255], v[140:143], v[216:223], v0 abid:1\n" \
  "v_exp_f32 v194, v194\n" \
  "v_exp_f32 v195, v195\n" \
  "s_cmp_eq_u32 s35, 0\n" \
  "s_cbranch_scc0 63f\n" \
  "buffer_load_dwordx4 v[136:139], v12, s[24:27], s61 offen offset:3072 sc1\n" \
  "63:\n" \
  "v_exp_f32 v196, v196\n" \
  "v_mfma_f32_32x32x16_bf16 v[208:223], v[108:111], v[16:19], 0\n" \
  "v_exp_f32 v197, v197\n" \
  "v_exp_f32 v198, v198\n" \
  "v_exp_f32 v199, v199\n" \
  "v_exp_f32 v200, v200\n" \
  "v_exp_f32 v201, v201\n" \
  "v_exp_f32 v202, v202\n" \
  "v_smfmac_f32_32x32x32_bf16 v[240:255], v[144:147], v[192:199], v0 abid:1\n" \
  "v_exp_f32 v203, v203\n" \
  "v_exp_f32 v204, v204\n" \
  "s_cmp_eq_u32 s35, 0\n" \
  "s_cbranch_scc0 64f\n" \
  "buffer_load_dwordx4 v[140:143], v12, s[24:27], s61 offen offset:3328 sc1\n" \
  "64:\n" \
  "v_exp_f32 v205, v205\n" \
  "v_exp_f32 v206, v206\n" \
  "v_exp_f32 v207, v207\n" \
  "v_exp_f32 v208, v208\n" \
  "v_exp_f32 v209, v209\n" \
  "v_smfmac_f32_32x32x32_bf16 v[240:255], v[148:151], v[200:207], v0 abid:1\n" \
  "v_exp_f32 v210, v210\n" \
  "v_exp_f32 v211, v211\n" \
  "s_cmp_eq_u32 s35, 0\n" \
  "s_cbranch_scc0 65f\n" \
  "buffer_load_dwordx4 v[144:147], v12, s[24:27], s61 offen offset:3584 sc1\n" \
  "65:\n" \
  "v_exp_f32 v212, v212\n" \
  "v_mfma_f32_32x32x16_bf16 v[192:207], v[112:115], v[16:19], 0\n" \
  "v_exp_f32 v213, v213\n" \
  "v_exp_f32 v214, v214\n" \
  "v_exp_f32 v215, v215\n" \
  "v_exp_f32 v216, v216\n" \
  "v_exp_f32 v217, v217\n" \
  "v_exp_f32 v218, v218\n" \
  "v_smfmac_f32_32x32x32_bf16 v[240:255], v[152:155], v[208:215], v0 abid:1\n" \
  "v_exp_f32 v219, v219\n" \
  "v_exp_f32 v220, v220\n" \
  "s_cmp_eq_u32 s35, 0\n" \
  "s_cbranch_scc0 66f\n" \
  "buffer_load_dwordx4 v[148:151], v12, s[24:27], s61 offen offset:3840 sc1\n" \
  "66:\n" \
  "v_exp_f32 v221, v221\n" \
  "v_exp_f32 v222, v222\n" \
  "v_exp_f32 v223, v223\n" \
  "s_branch 2f\n" \
  "1:\n" \
  "76:\n" \
  "ds_read_b32 v20, v4 offset:4096\n" \
  "s_waitcnt lgkmcnt(0)\n" \
  "v_cmp_gt_u32 vcc, v7, v20\n" \
  "s_and_b32 vcc_lo, vcc_lo, 0xff\n" \
  "s_cbranch_scc1 76b\n" \
  "v_exp_f32 v192, v192\n" \
  "ds_read_b128 v[20:23], v4 offset:0\n" \
  "v_exp_f32 v193, v193\n" \
  "ds_read_b64 v[224:225], v3 offset:0\n" \
  "v_smfmac_f32_32x32x32_bf16 v[240:255], v[140:143], v[216:223], v0 abid:1\n" \
  "v_exp_f32 v194, v194\n" \
  "v_exp_f32 v195, v195\n" \
  "ds_read_b64 v[226:227], v3 offset:2048\n" \
  "s_cmp_eq_u32 s35, 0\n" \
  "s_cbranch_scc0 63f\n" \
  "buffer_load_dwordx4 v[136:139], v12, s[24:27], s61 offen offset:3072 sc1\n" \
  "63:\n" \
  "v_exp_f32 v196, v196\n" \
  "ds_read_b64 v[228:229], v3 offset:4096\n" \
  "v_mfma_f32_32x32x16_bf16 v[208:223], v[108:111], v[16:19], 0\n" \
  "v_exp_f32 v197, v197\n" \
  "v_exp_f32 v198, v198\n" \
  "ds_read_b64 v[230:231], v3 offset:6144\n" \
  "v_exp_f32 v199, v199\n" \
  "ds_read_b64 v[232:233], v3 offset:8192\n" \
  "v_exp_f32 v200, v200\n" \
  "ds_read_b64 v[234:235], v3 offset:10240\n" \
  "v_exp_f32 v201, v201\n" \
  "ds_read_b64 v[236:237], v3 offset:12288\n" \
  "v_exp_f32 v202, v202\n" \
  "ds_read_b64 v[238:239], v3 offset:14336\n" \
  "v_smfmac_f32_32x32x32_bf16 v[240:255], v[144:147], v[192:199], v0 abid:1\n" \
  "v_exp_f32 v203, v203\n" \
  "v_exp_f32 v204, v204\n" \
  "s_cmp_eq_u32 s35, 0\n" \
  "s_cbranch_scc0 64f\n" \
  "buffer_load_dwordx4 v[140:143], v12, s[24:27], s61 offen offset:3328 sc1\n" \
  "64:\n" \
  "v_exp_f32 v205, v205\n" \
  "v_exp_f32 v206, v206\n" \
  "v_exp_f32 v207, v207\n" \
  "v_exp_f32 v208, v208\n" \
  "s_waitcnt lgkmcnt(0)\n" \
  "v_pk_add_f32 v[20:21], v[20:21], v[22:23]\n" \
  "v_exp_f32 v209, v209\n" \
  "v_pk_add_f32 v[224:225], v[224:225], v[232:233]\n" \
  "v_smfmac_f32_32x32x32_bf16 v[240:255], v[148:151], v[200:207], v0 abid:1\n" \
  "v_exp_f32 v210, v210\n" \
  "v_exp_f32 v211, v211\n" \
  "v_pk_add_f32 v[226:227], v[226:227], v[234:235]\n" \
  "v_add_f32 v20, v20, v21\n" \
  "s_cmp_eq_u32 s35, 0\n" \
  "s_cbranch_scc0 65f\n" \
  "buffer_load_dwordx4 v[144:147], v12, s[24:27], s61 offen offset:3584 sc1\n" \
  "65:\n" \
  "v_exp_f32 v212, v212\n" \
  "v_pk_add_f32 v[228:229], v[228:229], v[236:237]\n" \
  "v_mfma_f32_32x32x16_bf16 v[192:207], v[112:115], v[16:19], 0\n" \
  "v_exp_f32 v213, v213\n" \
  "v_exp_f32 v214, v214\n" \
  "v_pk_add_f32 v[230:231], v[230:231], v[238:239]\n" \
  "v_add_f32_dpp v20, v20, v20 quad_perm:[1,0,3,2] row_mask:0xf bank_mask:0xf\n" \
  "v_exp_f32 v215, v215\n" \
  "v_pk_add_f32 v[224:225], v[224:225], v[228:229]\n" \
  "v_exp_f32 v216, v216\n" \
  "v_pk_add_f32 v[226:227], v[226:227], v[230:231]\n" \
  "v_rcp_f32 v21, v20\n" \
  "v_exp_f32 v217, v217\n" \
  "v_pk_add_f32 v[224:225], v[224:225], v[226:227]\n" \
  "v_exp_f32 v218, v218\n" \
  "v_max_f32 v20, v20, v21\n" \
  "v_mul_f32 v20, 0x6b800000, v20\n" \
  "v_smfmac_f32_32x32x32_bf16 v[240:255], v[152:155], v[208:215], v0 abid:1\n" \
  "v_exp_f32 v219, v219\n" \
  "v_exp_f32 v220, v220\n" \
  "v_fma_f32 v21, v20, 0, v21\n" \
  "s_cmp_eq_u32 s35, 0\n" \
  "s_cbranch_scc0 66f\n" \
  "buffer_load_dwordx4 v[148:151], v12, s[24:27], s61 offen offset:3840 sc1\n" \
  "66:\n" \
  "v_exp_f32 v221, v221\n" \
  "v_mul_f32 v224, v224, v21\n" \
  "v_mul_f32 v225, v225, v21\n" \
  "v_exp_f32 v222, v222\n" \
  "v_cvt_pk_bf16_f32 v224, v224, v225\n" \
  "v_exp_f32 v223, v223\n" \
  "ds_write_b32 v5, v224 offset:0\n" \
  "2:\n" \
  "s_setprio 2\n" \
  "v_exp_f32 v192, v192\n" \
  "v_mbcnt_lo_u32_b32 v20, -1, 0\n" \
  "v_mbcnt_hi_u32_b32 v20, -1, v20\n" \
  "v_exp_f32 v193, v193\n" \
  "v_lshrrev_b32 v21, 4, v20\n" \
  "v_and_b32 v22, 15, v20\n" \
  "s_cmp_eq_u32 s35, 0\n" \
  "s_cbranch_scc0 33f\n" \
  "s_waitcnt vmcnt(6)\n" \
  "s_branch 34f\n" \
  "33:\n" \
  "s_waitcnt vmcnt(0)\n" \
  "34:\n" \
  "buffer_load_dwordx4 v[224:227], v4, s[28:31], s58 offen sc1\n" \
  "v_smfmac_f32_32x32x32_bf16 v[240:255], v[156:159], v[216:223], v0 abid:1\n" \
  "v_exp_f32 v194, v194\n" \
  "v_exp_f32 v195, v195\n" \
  "v_lshlrev_b32 v23, 2, v21\n" \
  "v_sub_u32 v22, v22, v23\n" \
  "v_exp_f32 v196, v196\n" \
  "v_cmp_gt_u32 vcc, 4, v22\n" \
  "v_lshlrev_b32 v23, 4, v22\n" \
  "v_mfma_f32_32x32x16_bf16 v[208:223], v[116:119], v[16:19], 0\n" \
  "v_exp_f32 v197, v197\n" \
  "v_exp_f32 v198, v198\n" \
  "v_mov_b32 v236, 0x3f80\n" \
  "v_mov_b32 v237, 0\n" \
  "s_add_u32 s85, s58, 8192\n" \
  "buffer_load_dwordx4 v[228:231], v4, s[28:31], s85 offen sc1\n" \
  "v_exp_f32 v199, v199\n" \
  "v_cndmask_b32 v236, 0, v236, vcc\n" \
  "v_lshlrev_b64 v[236:237], v23, v[236:237]\n" \
  "v_exp_f32 v200, v200\n" \
  "v_exp_f32 v201, v201\n" \
  "v_exp_f32 v202, v202\n" \
  "s_add_u32 s86, s58, 16384\n" \
  "buffer_load_dwordx4 v[232:235], v4, s[28:31], s86 offen sc1\n" \
  "v_smfmac_f32_32x32x32_bf16 v[240:255], v[160:163], v[192:199], v0 abid:1\n" \
  "v_exp_f32 v203, v203\n" \
  "v_exp_f32 v204, v204\n" \
  "v_exp_f32 v205, v205\n" \
  "v_exp_f32 v206, v206\n" \
  "buffer_load_dwordx2 v[238:239], v4, s[24:27], s60 offen sc1\n" \
  "v_exp_f32 v207, v207\n" \
  "v_exp_f32 v208, v208\n" \
  "v_exp_f32 v209, v209\n" \
  "v_smfmac_f32_32x32x32_bf16 v[240:255], v[164:167], v[200:207], v0 abid:1\n" \
  "v_exp_f32 v210, v210\n" \
  "v_exp_f32 v211, v211\n" \
  "v_exp_f32 v212, v212\n" \
  "v_mfma_f32_32x32x16_bf16 v[192:207], v[120:123], v[16:19], 0\n" \
  "v_exp_f32 v213, v213\n" \
  "v_exp_f32 v214, v214\n" \
  "v_exp_f32 v215, v215\n" \
  "v_exp_f32 v216, v216\n" \
  "v_exp_f32 v217, v217\n" \
  "s_cmp_eq_u32 s35, 0\n" \
  "s_cbranch_scc0 57f\n" \
  "buffer_load_dwordx4 v[152:155], v12, s[24:27], s62 offen offset:0 sc1\n" \
  "57:\n" \
  "v_exp_f32 v218, v218\n" \
  "v_smfmac_f32_32x32x32_bf16 v[240:255], v[168:171], v[208:215], v0 abid:1\n" \
  "v_exp_f32 v219, v219\n" \
  "v_exp_f32 v220, v220\n" \
  "v_exp_f32 v221, v221\n" \
  "v_exp_f32 v222, v222\n" \
  "v_exp_f32 v223, v223\n" \
  "s_setprio 1\n" \
  "v_exp_f32 v192, v192\n" \
  "s_cmp_eq_u32 s35, 0\n" \
  "s_cbranch_scc0 58f\n" \
  "buffer_load_dwordx4 v[156:159], v12, s[24:27], s62 offen offset:256 sc1\n" \
  "58:\n" \
  "v_exp_f32 v193, v193\n" \
  "v_smfmac_f32_32x32x32_bf16 v[240:255], v[172:175], v[216:223], v0 abid:1\n" \
  "v_exp_f32 v194, v194\n" \
  "v_exp_f32 v195, v195\n" \
  "v_exp_f32 v196, v196\n" \
  "v_mfma_f32_32x32x16_bf16 v[208:223], v[124:127], v[16:19], 0\n" \
  "v_exp_f32 v197, v197\n" \
  "v_exp_f32 v198, v198\n" \
  "v_exp_f32 v199, v199\n" \
  "v_exp_f32 v200, v200\n" \
  "v_exp_f32 v201, v201\n" \
  "s_cmp_eq_u32 s35, 0\n" \
  "s_cbranch_scc0 59f\n" \
  "s_add_u32 s87, s62, 16384\n" \
  "buffer_load_dwordx4 v[160:163], v12, s[24:27], s87 offen offset:0 sc1\n" \
  "59:\n" \
  "v_exp_f32 v202, v202\n" \
  "v_smfmac_f32_32x32x32_bf16 v[240:255], v[176:179], v[192:199], v0 abid:1\n" \
  "v_exp_f32 v203, v203\n" \
  "v_exp_f32 v204, v204\n" \
  "v_exp_f32 v205, v205\n" \
  "v_exp_f32 v206, v206\n" \
  "v_exp_f32 v207, v207\n" \
  "v_exp_f32 v208, v208\n" \
  "s_cmp_eq_u32 s35, 0\n" \
  "s_cbranch_scc0 60f\n" \
  "buffer_load_dwordx4 v[164:167], v12, s[24:27], s87 offen offset:256 sc1\n" \
  "60:\n" \
  "v_exp_f32 v209, v209\n" \
  "v_smfmac_f32_32x32x32_bf16 v[240:255], v[180:183], v[200:207], v0 abid:1\n" \
  "v_exp_f32 v210, v210\n" \
  "v_exp_f32 v211, v211\n" \
  "v_exp_f32 v212, v212\n" \
  "v_exp_f32 v213, v213\n" \
  "v_exp_f32 v214, v214\n" \
  "v_exp_f32 v215, v215\n" \
  "v_exp_f32 v216, v216\n" \
  "v_exp_f32 v217, v217\n" \
  "v_exp_f32 v218, v218\n" \
  "v_smfmac_f32_32x32x32_bf16 v[240:255], v[184:187], v[208:215], v0 abid:1\n" \
  "v_exp_f32 v219, v219\n" \
  "v_exp_f32 v220, v220\n" \
  "v_exp_f32 v221, v221\n" \
  "v_exp_f32 v222, v222\n" \
  "v_exp_f32 v223, v223\n" \
  "s_waitcnt vmcnt(4)\n" \
  "s_cmp_eq_u32 s35, 0\n" \
  "v_smfmac_f32_32x32x32_bf16 v[240:255], v[188:191], v[216:223], v0 abid:1\n" \
  "s_cbranch_scc1 3f\n" \
  "s_waitcnt vmcnt(0)\n" \
  "3:\n" \
  "v_mfma_f32_16x16x16_f16 v[12:15], v[10:11], v[10:11], 0\n" \
  "v_mfma_f32_16x16x16_f16 v[16:19], v[10:11], v[10:11], 0\n" \
  "v_mfma_f32_16x16x16_bf16 v[208:211], v[236:237], v[224:225], v[24:27]\n" \
  "v_mfma_f32_16x16x16_bf16 v[212:215], v[236:237], v[226:227], v[28:31]\n" \
  "v_mfma_f32_16x16x16_bf16 v[208:211], v[236:237], v[228:229], v[208:211]\n" \
  "v_mfma_f32_16x16x16_bf16 v[212:215], v[236:237], v[230:231], v[212:215]\n" \
  "v_mfma_f32_16x16x16_bf16 v[208:211], v[236:237], v[232:233], v[208:211]\n" \
  "v_mfma_f32_16x16x16_bf16 v[212:215], v[236:237], v[234:235], v[212:215]\n" \
  "v_cmp_ne_u64 vcc, s[36:37], v[8:9]\n" \
  "v_cmp_ne_u64 s[88:89], s[36:37], v[238:239]\n" \
  "s_and_b64 vcc, vcc, 15\n" \
  "s_or_b64 s[88:89], s[88:89], vcc\n" \
  "ds_write_b128 v1, v[240:243] offset:16384\n" \
  "ds_write_b128 v1, v[244:247] offset:17408\n" \
  "ds_write_b32 v2, v248 offset:2048\n" \
  "s_cmp_eq_u32 s35, 0\n" \
  "s_cbranch_scc0 78f\n" \
  "v_mbcnt_lo_u32_b32 v8, -1, 0\n" \
  "v_mbcnt_hi_u32_b32 v8, -1, v8\n" \
  "v_lshrrev_b32 v9, 4, v8\n" \
  "v_lshlrev_b32 v2, 6, v9\n" \
  "s_lshl_b32 s81, s22, 3\n" \
  "v_add_u32 v1, s81, v2\n" \
  "v_and_b32 v7, 7, v8\n" \
  "v_lshlrev_b32 v7, 10, v7\n" \
  "v_lshl_add_u32 v7, v9, 4, v7\n" \
  "v_and_b32 v8, 8, v8\n" \
  "v_lshl_add_u32 v7, v8, 3, v7\n" \
  "s_lshl_b32 s84, s22, 7\n" \
  "s_add_u32 s84, s84, s45\n" \
  "s_add_u32 s84, s84, 10687488\n" \
  "s_branch 79f\n" \
  "78:\n" \
  "v_mbcnt_lo_u32_b32 v8, -1, 0\n" \
  "v_mbcnt_hi_u32_b32 v8, -1, v8\n" \
  "v_lshrrev_b32 v9, 4, v8\n" \
  "v_and_b32 v8, 15, v8\n" \
  "v_lshl_add_u32 v7, v8, 5, s23\n" \
  "v_lshl_add_u32 v7, v9, 3, v7\n" \
  "global_load_dwordx2 v[10:11], v7, s[4:5]\n" \
  "global_load_dwordx2 v[238:239], v7, s[4:5] offset:512\n" \
  "v_lshlrev_b32 v8, 2, v8\n" \
  "v_mul_u32_u24 v1, 0x840, v9\n" \
  "s_lshl_b32 s81, s22, 7\n" \
  "v_add3_u32 v1, v1, v8, s81\n" \
  "s_mul_i32 s81, s22, 0x420\n" \
  "v_lshl_add_u32 v2, v9, 8, v8\n" \
  "v_add_u32 v2, s81, v2\n" \
  "79:\n" \
  ASTAMP(48776, s35) \
  "s_waitcnt lgkmcnt(0)\n" \
  "s_barrier\n" \
  "s_setprio 0\n" \
  ASTAMP(48784, s35) \
  "s_cmp_lt_u32 s22, 4\n" \
  "s_cbranch_scc0 3f\n" \
  "ds_read_b128 v[20:23], v4 offset:2048\n" \
  "ds_read_b64 v[192:193], v3 offset:16384\n" \
  "ds_read_b64 v[194:195], v3 offset:18432\n" \
  "ds_read_b64 v[196:197], v3 offset:20480\n" \
  "ds_read_b64 v[198:199], v3 offset:22528\n" \
  "ds_read_b64 v[200:201], v3 offset:24576\n" \
  "ds_read_b64 v[202:203], v3 offset:26624\n" \
  "ds_read_b64 v[204:205], v3 offset:28672\n" \
  "ds_read_b64 v[206:207], v3 offset:30720\n" \
  "ds_read_b128 v[216:219], v6\n" \
  "ds_read_b128 v[220:223], v6 offset:64\n" \
  "s_waitcnt lgkmcnt(0)\n" \
  "v_pk_add_f32 v[20:21], v[20:21], v[22:23]\n" \
  "v_pk_add_f32 v[192:193], v[192:193], v[200:201]\n" \
  "v_pk_add_f32 v[194:195], v[194:195], v[202:203]\n" \
  "v_add_f32 v20, v20, v21\n" \
  "s_nop 0\n" \
  "v_pk_add_f32 v[196:197], v[196:197], v[204:205]\n" \
  "v_pk_add_f32 v[198:199], v[198:199], v[206:207]\n" \
  "v_add_f32_dpp v20, v20, v20 quad_perm:[1,0,3,2] row_mask:0xf bank_mask:0xf\n" \
  "v_pk_add_f32 v[192:193], v[192:193], v[196:197]\n" \
  "v_pk_add_f32 v[194:195], v[194:195], v[198:199]\n" \
  "v_rcp_f32 v21, v20\n" \
  "v_pk_add_f32 v[192:193], v[192:193], v[194:195]\n" \
  "s_nop 0\n" \
  "v_max_f32 v20, v20, v21\n" \
  "v_mul_f32 v20, 0x6b800000, v20\n" \
  "v_fma_f32 v21, v20, 0, v21\n" \
  "v_mul_f32 v192, v192, v21\n" \
  "v_mul_f32 v193, v193, v21\n" \
  "v_cvt_pk_bf16_f32 v192, v192, v193\n" \
  "ds_write_b32 v5, v192 offset:128\n" \
  "s_branch 4f\n" \
  "3:\n" \
  "ds_read_b128 v[216:219], v6\n" \
  "ds_read_b128 v[220:223], v6 offset:64\n" \
  "s_waitcnt lgkmcnt(0)\n" \
  "4:\n" \
  "v_smfmac_f32_16x16x64_bf16 v[12:15], v[216:219], v[32:39], v0\n" \
  "s_cmp_eq_u32 s35, 0\n" \
  "s_cbranch_scc0 80f\n" \
  "ds_read_b128 v[240:243], v4 offset:37760\n" \
  "ds_read_b128 v[244:247], v4 offset:38784\n" \
  "s_waitcnt lgkmcnt(2)\n" \
  "s_branch 81f\n" \
  "80:\n" \
  "ds_read_b128 v[224:227], v4 offset:28544\n" \
  "ds_read_b128 v[228:231], v4 offset:29568\n" \
  "ds_read_b128 v[232:235], v4 offset:30592\n" \
  "ds_read_b128 v[240:243], v4 offset:31616\n" \
  "ds_read_b128 v[244:247], v4 offset:32640\n" \
  "ds_read_b128 v[248:251], v4 offset:33664\n" \
  "ds_read_b128 v[252:255], v4 offset:34688\n" \
  "s_waitcnt lgkmcnt(7)\n" \
  "81:\n" \
  "s_barrier\n" \
  "ds_read_b128 v[200:203], v6 offset:128\n" \
  "ds_read_b128 v[204:207], v6 offset:192\n" \
  "ds_read_b128 v[192:195], v4 offset:35712\n" \
  "ds_read_b128 v[196:199], v4 offset:36736\n" \
  "v_smfmac_f32_16x16x64_bf16 v[16:19], v[216:219], v[64:71], v0\n" \
  "v_smfmac_f32_16x16x64_bf16 v[12:15], v[220:223], v[40:47], v0\n" \
  "v_smfmac_f32_16x16x64_bf16 v[16:19], v[220:223], v[72:79], v0\n" \
  "v_cmp_u_f32 vcc, v208, v212\n" \
  "s_or_b64 s[88:89], s[88:89], vcc\n" \
  "s_cselect_b32 s81, 16, 0\n" \
  "s_or_b32 s79, s79, s81\n" \
  "s_waitcnt lgkmcnt(3)\n" \
  "v_smfmac_f32_16x16x64_bf16 v[12:15], v[200:203], v[48:55], v0\n" \
  "v_smfmac_f32_16x16x64_bf16 v[16:19], v[200:203], v[80:87], v0\n" \
  "s_waitcnt lgkmcnt(2)\n" \
  "v_smfmac_f32_16x16x64_bf16 v[12:15], v[204:207], v[56:63], v0\n" \
  "v_smfmac_f32_16x16x64_bf16 v[16:19], v[204:207], v[88:95], v0\n" \
  "s_nop 6\n" \
  "v_add_f32 v8, |v12|, |v14|\n" \
  "v_cmp_ngt_f32 vcc, 0x7f000000, v8\n" \
  "s_cbranch_vccnz 12f\n" \
  "s_cmp_lg_u32 s79, 0\n" \
  "s_cbranch_scc1 13f\n" \
  "14:\n" \
  ASTAMP(48792, s35) \
  "v_pk_add_f32 v[24:25], v[12:13], v[208:209]\n" \
  "v_pk_add_f32 v[26:27], v[14:15], v[210:211]\n" \
  "v_pk_add_f32 v[28:29], v[16:17], v[212:213]\n" \
  "v_pk_add_f32 v[30:31], v[18:19], v[214:215]\n" \
  "s_cmp_eq_u32 s35, 0\n" \
  "s_cbranch_scc0 21f\n" \
  "s_mov_b64 exec, s[72:73]\n" \
  "ds_write_b32 v2, v29 offset:48640\n" \
  "ds_write_b32 v2, v31 offset:48644\n" \
  "v_mov_b32 v29, 0\n" \
  "v_mov_b32 v31, 0\n" \
  "s_mov_b64 exec, -1\n" \
  "v_pk_mul_f32 v[12:13], v[24:25], v[24:25]\n" \
  "v_pk_mul_f32 v[14:15], v[26:27], v[26:27]\n" \
  "v_pk_fma_f32 v[12:13], v[28:29], v[28:29], v[12:13]\n" \
  "v_pk_fma_f32 v[14:15], v[30:31], v[30:31], v[14:15]\n" \
  "v_add_f32 v12, v12, v13\n" \
  "v_add_f32 v13, v14, v15\n" \
  "s_nop 0\n" \
  "v_add_f32_dpp v12, v12, v12 row_ror:8 row_mask:0xf bank_mask:0xf\n" \
  "v_add_f32_dpp v13, v13, v13 row_ror:8 row_mask:0xf bank_mask:0xf\n" \
  "s_nop 0\n" \
  "v_add_f32_dpp v12, v12, v12 row_ror:4 row_mask:0xf bank_mask:0xf\n" \
  "v_add_f32_dpp v13, v13, v13 row_ror:4 row_mask:0xf bank_mask:0xf\n" \
  "s_nop 0\n" \
  "v_add_f32_dpp v12, v12, v12 row_ror:2 row_mask:0xf bank_mask:0xf\n" \
  "v_add_f32_dpp v13, v13, v13 row_ror:2 row_mask:0xf bank_mask:0xf\n" \
  "s_nop 0\n" \
  "v_add_f32_dpp v12, v12, v12 row_ror:1 row_mask:0xf bank_mask:0xf\n" \
  "v_add_f32_dpp v13, v13, v13 row_ror:1 row_mask:0xf bank_mask:0xf\n" \
  "s_mov_b32 exec_lo, 0x10001\n" \
  "s_mov_b32 exec_hi, 0x10001\n" \
  "ds_write_b64 v1, v[12:13] offset:40960\n" \
  "s_mov_b64 exec, -1\n" \
  "v_mov_b32 v8, 0x3727c5ac\n" \
  "s_waitcnt lgkmcnt(0)\n" \
  "s_barrier\n" \
  "ds_read_b128 v[192:195], v2 offset:40960\n" \
  "ds_read_b128 v[196:199], v2 offset:40976\n" \
  "ds_read_b128 v[200:203], v2 offset:40992\n" \
  "ds_read_b128 v[204:207], v2 offset:41008\n" \
  "s_waitcnt lgkmcnt(2)\n" \
  "v_pk_add_f32 v[192:193], v[192:193], v[194:195]\n" \
  "v_pk_add_f32 v[196:197], v[196:197], v[198:199]\n" \
  "s_waitcnt lgkmcnt(0)\n" \
  "v_pk_add_f32 v[200:201], v[200:201], v[202:203]\n" \
  "v_pk_add_f32 v[204:205], v[204:205], v[206:207]\n" \
  "v_pk_add_f32 v[192:193], v[192:193], v[196:197]\n" \
  "v_pk_add_f32 v[200:201], v[200:201], v[204:205]\n" \
  "v_pk_add_f32 v[192:193], v[192:193], v[200:201]\n" \
  "v_fmamk_f32 v192, v192, 0x3b000000, v8\n" \
  "v_fmamk_f32 v193, v193, 0x3b000000, v8\n" \
  "v_rsq_f32 v194, v192\n" \
  "v_rsq_f32 v196, v193\n" \
  "v_pk_mul_f32 v[12:13], v[24:25], v[194:195] op_sel_hi:[1,0]\n" \
  "v_pk_mul_f32 v[16:17], v[28:29], v[194:195] op_sel_hi:[1,0]\n" \
  "v_pk_mul_f32 v[14:15], v[26:27], v[196:197] op_sel_hi:[1,0]\n" \
  "v_pk_mul_f32 v[18:19], v[30:31], v[196:197] op_sel_hi:[1,0]\n" \
  "v_cvt_pk_f16_f32 v200, v12, v13\n" \
  "v_cvt_pk_f16_f32 v201, v14, v15\n" \
  "v_cvt_pk_f16_f32 v202, v16, v17\n" \
  "v_cvt_pk_f16_f32 v203, v18, v19\n" \
  "s_nop 1\n" \
  "v_mfma_f32_16x16x32_f16 v[12:15], v[200:203], v[240:243], 0\n" \
  "v_mfma_f32_16x16x32_f16 v[16:19], v[200:203], v[244:247], 0\n" \
  "v_mul_f32 v248, v192, v194\n" \
  "v_mul_f32 v249, v193, v196\n" \
  "v_lshrrev_b32 v8, 3, v2\n" \
  "s_nop 4\n" \
  "v_cvt_pk_f16_f32 v252, v12, v16\n" \
  "v_cvt_pk_f16_f32 v253, v13, v17\n" \
  "v_cvt_pk_f16_f32 v254, v14, v18\n" \
  "v_cvt_pk_f16_f32 v255, v15, v19\n" \
  "buffer_store_dwordx4 v[252:255], v7, s[24:27], s84 offen sc1\n" \
  "s_cmp_lg_u32 s22, 0\n" \
  "s_cbranch_scc1 5f\n" \
  "s_add_u32 s84, s84, 8192\n" \
  "s_mov_b32 exec_lo, 0x10001\n" \
  "s_mov_b32 exec_hi, 0x10001\n" \
  "buffer_store_dwordx2 v[248:249], v8, s[24:27], s84 offen sc1\n" \
  "s_mov_b64 exec, -1\n" \
  "5:\n" \
  ASTAMP(48800, 0) \
  "s_add_u32 s28, s28, 10687488\n" \
  "s_addc_u32 s29, s29, 0\n" \
  "s_sub_u32 s30, s30, 10687488\n" \
  "s_mov_b32 s35, 1\n" \
  "s_mov_b32 s78, 0x10000\n" \
  "v_mbcnt_lo_u32_b32 v12, -1, 0\n" \
  "v_mbcnt_hi_u32_b32 v12, -1, v12\n" \
  "s_lshl_b32 s81, s22, 6\n" \
  "v_or_b32 v17, s81, v12\n" \
  "v_and_b32 v13, 15, v12\n" \
  "v_lshrrev_b32 v14, 4, v12\n" \
  "v_bfe_u32 v15, v17, 3, 4\n" \
  "v_and_b32 v16, 6, v12\n" \
  "v_lshl_add_u32 v15, v16, 3, v15\n" \
  "s_lshr_b32 s81, s22, 1\n" \
  "s_lshl_b32 s81, s81, 6\n" \
  "v_add_u32 v15, s81, v15\n" \
  "v_mul_u32_u24 v15, 8320, v15\n" \
  "v_and_b32 v16, 1, v12\n" \
  "v_lshl_add_u32 v168, v16, 4, v15\n" \
  "v_mul_u32_u24 v15, 4158, v16\n" \
  "v_lshl_add_u32 v169, v17, 1, v15\n" \
  "v_mul_u32_u24 v15, 1405584, v16\n" \
  "v_lshlrev_b32 v15, 4, v15\n" \
  "v_lshl_add_u32 v190, v12, 1, v15\n" \
  "v_lshlrev_b32 v186, 4, v17\n" \
  "s_lshl_b32 s81, s22, 4\n" \
  "v_add_u32 v170, s81, v186\n" \
  "v_lshlrev_b32 v15, 4, v12\n" \
  "v_add_u32 v4, 41984, v15\n" \
  "s_mul_i32 s81, s22, 10240\n" \
  "s_add_u32 s81, s81, 81792\n" \
  "v_add_u32 v178, s81, v15\n" \
  "v_and_b32 v15, 7, v12\n" \
  "v_xor_b32 v16, s22, v15\n" \
  "v_lshlrev_b32 v16, 4, v16\n" \
  "v_lshl_add_u32 v16, v13, 9, v16\n" \
  "v_lshl_add_u32 v171, v14, 7, v16\n" \
  "v_bfe_u32 v16, v17, 5, 3\n" \
  "v_lshlrev_b32 v16, 4, v16\n" \
  "v_xor_b32 v172, v16, v186\n" \
  "v_lshrrev_b32 v16, 1, v12\n" \
  "v_and_b32 v16, 4, v16\n" \
  "v_xor_b32 v16, v16, v15\n" \
  "s_lshl_b32 s81, s22, 7\n" \
  "v_lshl_add_u32 v173, v16, 4, s81\n" \
  "v_and_b32 v16, 24, v12\n" \
  "v_lshl_add_u32 v173, v16, 8, v173\n" \
  "v_and_b32 v16, 32, v12\n" \
  "v_lshl_add_u32 v173, v16, 5, v173\n" \
  "v_lshrrev_b32 v16, 1, v16\n" \
  "v_and_b32 v8, 31, v12\n" \
  "v_lshl_add_u32 v7, v8, 5, v16\n" \
  "v_add_u32 v7, 32768, v7\n" \
  "v_cmp_eq_u32 vcc, 16, v8\n" \
  "v_mov_b32 v9, 0x3f803f80\n" \
  "v_xor_b32 v16, v14, v13\n" \
  "v_cndmask_b32 v8, 0, v9, vcc\n" \
  "v_lshlrev_b32 v16, 4, v16\n" \
  "v_lshl_add_u32 v20, v13, 10, v16\n" \
  "v_xor_b32 v185, s81, v20\n" \
  "v_mov_b32 v9, v8\n" \
  "v_bfe_u32 v16, v12, 3, 1\n" \
  "v_mul_u32_u24 v184, 8320, v16\n" \
  "v_xor_b32 v184, 8320, v184\n" \
  "v_xor_b32 v184, v184, v185\n" \
  "v_lshl_add_u32 v16, v16, 2, v14\n" \
  "v_mul_u32_u24 v16, 16640, v16\n" \
  "s_mul_i32 s87, s22, 266240\n" \
  "v_lshl_add_u32 v16, v15, 4, v16\n" \
  "v_add_u32 v181, s87, v16\n" \
  "v_mul_u32_u24 v16, 2080, v14\n" \
  "v_lshlrev_b32 v15, 2, v13\n" \
  "s_xor_b32 s81, s81, 128\n" \
  "v_xor_b32 v16, v16, v15\n" \
  "v_xor_b32 v182, s81, v16\n" \
  "v_xor_b32 v183, 1040, v182\n" \
  "s_lshl_b32 s81, s22, 10\n" \
  "v_lshl_add_u32 v15, v14, 7, v15\n" \
  "v_and_b32 v16, 16, v12\n" \
  "v_lshl_add_u32 v15, v16, 2, v15\n" \
  "v_add_u32 v176, s81, v15\n" \
  "v_xor_b32 v177, 64, v176\n" \
  "v_and_b32 v15, 3, v12\n" \
  "v_and_b32 v16, 12, v12\n" \
  "v_lshlrev_b32 v16, 10, v16\n" \
  "v_lshl_add_u32 v16, v14, 6, v16\n" \
  "v_lshl_add_u32 v180, v15, 4, v16\n" \
  "s_lshl_b32 s81, s22, 2\n" \
  "v_lshl_add_u32 v187, v13, 5, s81\n" \
  "s_add_u32 s87, s34, 1\n" \
  "v_add_u32 v16, s87, v15\n" \
  "v_and_b32 v16, 3, v16\n" \
  "v_mul_u32_u24 v16, 8320, v16\n" \
  "v_lshl_add_u32 v16, v14, 3, v16\n" \
  "s_mul_i32 s87, s34, 8320\n" \
  "s_sub_u32 s87, s45, s87\n" \
  "s_add_u32 s87, s87, 8192\n" \
  "v_add_u32 v174, s87, v16\n" \
  "v_cmp_ne_u32 vcc, 3, v15\n" \
  "v_add_u32 v16, 10687488, v174\n" \
  "s_nop 0\n" \
  "v_cndmask_b32 v174, v174, v16, vcc\n" \
  "s_cmp_lt_u32 s22, 2\n" \
  "s_cbranch_scc0 106f\n" \
  "v_add_u32 v14, s81, v14\n" \
  "v_lshlrev_b32 v189, 5, v14\n" \
  "v_and_b32 v16, 8, v12\n" \
  "v_add_u32 v14, v16, v14\n" \
  "v_and_b32 v16, 12, v12\n" \
  "v_lshl_add_u32 v14, v16, 2, v14\n" \
  "s_branch 107f\n" \
  "106:\n" \
  "v_and_b32 v16, 7, v12\n" \
  "v_lshlrev_b32 v189, 5, v16\n" \
  "v_lshrrev_b32 v14, 3, v12\n" \
  "v_and_b32 v16, 4, v12\n" \
  "v_lshl_add_u32 v14, v16, 2, v14\n" \
  "s_mul_i32 s87, s22, -24\n" \
  "s_add_u32 s87, s87, 80\n" \
  "v_add_u32 v14, s87, v14\n" \
  "107:\n" \
  "v_lshlrev_b32 v15, 2, v15\n" \
  "v_lshl_add_u32 v188, v14, 4, v15\n" \
  "ds_read_b128 v[12:15], v170 offset:48896\n" \
  "ds_read_b128 v[192:195], v4 offset:15232\n" \
  "ds_read_b128 v[196:199], v4 offset:16256\n" \
  "ds_read_b128 v[200:203], v4 offset:17280\n" \
  "ds_read_b128 v[204:207], v4 offset:18304\n" \
  "ds_read_b128 v[208:211], v4 offset:19328\n" \
  "ds_read_b128 v[212:215], v4 offset:20352\n" \
  "ds_read_b128 v[216:219], v4 offset:21376\n" \
  "ds_read_b128 v[220:223], v4 offset:22400\n" \
  "ds_read_b128 v[224:227], v4 offset:23424\n" \
  "ds_read_b128 v[228:231], v4 offset:24448\n" \
  "ds_read_b128 v[232:235], v4 offset:25472\n" \
  "ds_read_b128 v[236:239], v4 offset:26496\n" \
  "s_waitcnt lgkmcnt(12)\n" \
  "v_lshlrev_b32 v240, 16, v12\n" \
  "v_lshlrev_b32 v241, 16, v13\n" \
  "v_lshlrev_b32 v242, 16, v14\n" \
  "v_lshlrev_b32 v243, 16, v15\n" \
  "v_and_b32 v244, 0xffff0000, v12\n" \
  "v_and_b32 v245, 0xffff0000, v13\n" \
  "v_and_b32 v246, 0xffff0000, v14\n" \
  "v_and_b32 v247, 0xffff0000, v15\n" \
  "v_mul_f32 v240, s38, v240\n" \
  "v_mul_f32 v241, s38, v241\n" \
  "v_mul_f32 v242, s38, v242\n" \
  "v_mul_f32 v243, s38, v243\n" \
  "v_mul_f32 v244, s38, v244\n" \
  "v_mul_f32 v245, s38, v245\n" \
  "v_mul_f32 v246, s38, v246\n" \
  "v_mul_f32 v247, s38, v247\n" \
  "s_sleep 12\n" \
  ASTAMP(48804, 0) \
  "buffer_load_dwordx4 v[0:3], v168, s[28:31], s40 offen sc1\n" \
  "s_sleep 2\n" \
  "buffer_load_dwordx4 v[96:99], v173, s[28:31], s42 offen sc1\n" \
  "buffer_load_dwordx4 v[100:103], v173, s[28:31], s43 offen sc1\n" \
  "buffer_load_dwordx4 v[104:107], v173, s[28:31], s44 offen sc1\n" \
  "buffer_load_dwordx4 v[108:111], v173, s[24:27], s45 offen sc1\n" \
  "buffer_load_dwordx2 v[112:113], v174, s[24:27], 0 offen sc1\n" \
  "s_waitcnt vmcnt(5)\n" \
  "s_branch 105f\n" \
  "104:\n" \
  "buffer_load_dwordx4 v[0:3], v168, s[28:31], s40 offen sc1\n" \
  "s_waitcnt vmcnt(0)\n" \
  "105:\n" \
  "ds_write_b32 v169, v0 offset:0\n" \
  "ds_write_b32 v169, v1 offset:1040\n" \
  "ds_write_b32 v169, v2 offset:2080\n" \
  "ds_write_b32 v169, v3 offset:3120\n" \
  "v_max3_u32 v10, v0, v1, v2\n" \
  "v_max3_u32 v10, v10, v3, v3\n" \
  "v_cmp_eq_u32 vcc, -1, v10\n" \
  "s_cbranch_vccz 108f\n" \
  "s_sub_u32 s78, s78, 1\n" \
  "s_cbranch_scc0 104b\n" \
  "108:\n" \
  ASTAMP(48808, 0) \
  "s_waitcnt lgkmcnt(0)\n" \
  "s_barrier\n" \
  "ds_read_b128 v[0:3], v170\n" \
  "s_waitcnt lgkmcnt(0)\n" \
  "v_mfma_f32_16x16x32_f16 v[12:15], v[0:3], v[192:195], 0\n" \
  "v_mfma_f32_16x16x32_f16 v[16:19], v[0:3], v[196:199], 0\n" \
  "s_nop 7\n" \
  "v_pk_mul_f32 v[10:11], v[16:17], v[228:229]\n" \
  "v_pk_mul_f32 v[22:23], v[12:13], v[228:229]\n" \
  "v_pk_mul_f32 v[0:1], v[18:19], v[230:231]\n" \
  "v_pk_mul_f32 v[2:3], v[14:15], v[230:231]\n" \
  "v_pk_fma_f32 v[12:13], v[12:13], v[224:225], v[10:11]\n" \
  "v_pk_fma_f32 v[16:17], v[16:17], v[224:225], v[22:23] neg_lo:[0,0,1] neg_hi:[0,0,1]\n" \
  "v_pk_fma_f32 v[14:15], v[14:15], v[226:227], v[0:1]\n" \
  "v_pk_fma_f32 v[18:19], v[18:19], v[226:227], v[2:3] neg_lo:[0,0,1] neg_hi:[0,0,1]\n" \
  "v_cvt_pk_f16_f32 v0, v12, v16\n" \
  "v_cvt_pk_f16_f32 v1, v13, v17\n" \
  "v_cvt_pk_f16_f32 v2, v14, v18\n" \
  "v_cvt_pk_f16_f32 v3, v15, v19\n" \
  "s_nop 1\n" \
  "v_mfma_f32_16x16x32_f16 v[12:15], v[200:203], v[0:3], 0\n" \
  "v_mfma_f32_16x16x32_f16 v[16:19], v[204:207], v[0:3], 0\n" \
  "s_nop 7\n" \
  "v_pk_mul_f32 v[10:11], v[16:17], v[244:245]\n" \
  "v_pk_mul_f32 v[22:23], v[12:13], v[244:245]\n" \
  "v_pk_mul_f32 v[0:1], v[18:19], v[246:247]\n" \
  "v_pk_mul_f32 v[2:3], v[14:15], v[246:247]\n" \
  "v_pk_fma_f32 v[12:13], v[12:13], v[240:241], v[10:11] neg_lo:[0,0,1] neg_hi:[0,0,1]\n" \
  "v_pk_fma_f32 v[16:17], v[16:17], v[240:241], v[22:23]\n" \
  "v_pk_fma_f32 v[14:15], v[14:15], v[242:243], v[0:1] neg_lo:[0,0,1] neg_hi:[0,0,1]\n" \
  "v_pk_fma_f32 v[18:19], v[18:19], v[242:243], v[2:3]\n" \
  "v_cvt_pk_f16_f32 v0, v12, v16\n" \
  "v_cvt_pk_f16_f32 v1, v13, v17\n" \
  "v_cvt_pk_f16_f32 v2, v14, v18\n" \
  "v_cvt_pk_f16_f32 v3, v15, v19\n" \
  "s_nop 1\n" \
  "v_mfma_f32_16x16x32_f16 v[12:15], v[0:3], v[208:211], 0\n" \
  "v_mfma_f32_16x16x32_f16 v[16:19], v[0:3], v[212:215], 0\n" \
  "s_nop 7\n" \
  "v_pk_mul_f32 v[10:11], v[16:17], v[236:237]\n" \
  "v_pk_mul_f32 v[22:23], v[12:13], v[236:237]\n" \
  "v_pk_mul_f32 v[0:1], v[18:19], v[238:239]\n" \
  "v_pk_mul_f32 v[2:3], v[14:15], v[238:239]\n" \
  "v_pk_fma_f32 v[12:13], v[12:13], v[232:233], v[10:11] neg_lo:[0,0,1] neg_hi:[0,0,1]\n" \
  "v_pk_fma_f32 v[16:17], v[16:17], v[232:233], v[22:23]\n" \
  "v_pk_fma_f32 v[14:15], v[14:15], v[234:235], v[0:1] neg_lo:[0,0,1] neg_hi:[0,0,1]\n" \
  "v_pk_fma_f32 v[18:19], v[18:19], v[234:235], v[2:3]\n" \
  "v_cvt_pk_f16_f32 v0, v12, v16\n" \
  "v_cvt_pk_f16_f32 v1, v13, v17\n" \
  "v_cvt_pk_f16_f32 v2, v14, v18\n" \
  "v_cvt_pk_f16_f32 v3, v15, v19\n" \
  "s_nop 1\n" \
  "v_mfma_f32_16x16x32_f16 v[12:15], v[0:3], v[216:219], 0\n" \
  "v_mfma_f32_16x16x32_f16 v[16:19], v[0:3], v[220:223], 0\n" \
  "v_mov_b32 v10, s39\n" \
  "s_nop 6\n" \
  "v_pk_mul_f32 v[12:13], v[12:13], v[10:11] op_sel_hi:[1,0]\n" \
  "v_pk_mul_f32 v[16:17], v[16:17], v[10:11] op_sel_hi:[1,0]\n" \
  "v_pk_mul_f32 v[14:15], v[14:15], v[10:11] op_sel_hi:[1,0]\n" \
  "v_pk_mul_f32 v[18:19], v[18:19], v[10:11] op_sel_hi:[1,0]\n" \
  "v_cvt_pk_bf16_f32 v0, v12, v16\n" \
  "v_cvt_pk_bf16_f32 v1, v13, v17\n" \
  "v_cvt_pk_bf16_f32 v2, v14, v18\n" \
  "v_cvt_pk_bf16_f32 v3, v15, v19\n" \
  "ds_write_b128 v171, v[0:3] offset:16384\n" \
  "s_waitcnt lgkmcnt(0)\n" \
  "s_barrier\n" \
  "ds_read_b128 v[0:3], v172 offset:16384\n" \
  "v_mov_b64 v[232:233], v[128:129]\n" \
  "v_mov_b64 v[234:235], v[130:131]\n" \
  "v_mov_b64 v[236:237], v[132:133]\n" \
  "v_mov_b64 v[238:239], v[134:135]\n" \
  "v_mov_b64 v[240:241], v[136:137]\n" \
  "v_mov_b64 v[242:243], v[138:139]\n" \
  "v_mov_b64 v[244:245], v[140:141]\n" \
  "v_mov_b64 v[246:247], v[142:143]\n" \
  "v_mov_b64 v[248:249], v[144:145]\n" \
  "v_mov_b64 v[250:251], v[146:147]\n" \
  "v_mov_b64 v[252:253], v[148:149]\n" \
  "v_mov_b64 v[254:255], v[150:151]\n" \
  "s_waitcnt lgkmcnt(0)\n" \
  "buffer_store_dwordx4 v[0:3], v186, s[28:31], s41 offen sc1\n" \
  ASTAMP(48812, 0) \
  "ds_read_b128 v[136:139], v4 offset:27520\n" \
  "s_waitcnt vmcnt(1)\n" \
  "s_mov_b32 s78, 0x10000\n" \
  "109:\n" \
  "v_max3_u32 v10, v96, v97, v98\n" \
  "v_max3_u32 v10, v10, v99, v100\n" \
  "v_max3_u32 v10, v10, v101, v102\n" \
  "v_max3_u32 v10, v10, v103, v104\n" \
  "v_max3_u32 v10, v10, v105, v106\n" \
  "v_max3_u32 v10, v10, v107, v108\n" \
  "v_max3_u32 v10, v10, v109, v110\n" \
  "v_max3_u32 v10, v10, v111, v112\n" \
  "v_max3_u32 v10, v10, v113, v113\n" \
  "v_cmp_eq_u32 vcc, -1, v10\n" \
  "s_cbranch_vccz 110f\n" \
  "buffer_load_dwordx4 v[96:99], v173, s[28:31], s42 offen sc1\n" \
  "buffer_load_dwordx4 v[100:103], v173, s[28:31], s43 offen sc1\n" \
  "buffer_load_dwordx4 v[104:107], v173, s[28:31], s44 offen sc1\n" \
  "buffer_load_dwordx4 v[108:111], v173, s[24:27], s45 offen sc1\n" \
  "buffer_load_dwordx2 v[112:113], v174, s[24:27], 0 offen sc1\n" \
  "s_sub_u32 s78, s78, 1\n" \
  "s_waitcnt vmcnt(0)\n" \
  "s_cbranch_scc0 109b\n" \
  "110:\n" \
  "ds_write_b128 v186, v[96:99] offset:0\n" \
  "ds_write_b128 v186, v[100:103] offset:8192\n" \
  "ds_write_b128 v186, v[104:107] offset:32768\n" \
  "ds_read2st64_b32 v[96:97], v176 offset0:0 offset1:2\n" \
  "ds_read2st64_b32 v[98:99], v177 offset0:0 offset1:2\n" \
  "ds_write_b128 v186, v[108:111] offset:0\n" \
  "ds_read2st64_b32 v[100:101], v176 offset0:32 offset1:34\n" \
  "ds_read2st64_b32 v[102:103], v177 offset0:32 offset1:34\n" \
  "ds_read2st64_b32 v[104:105], v176 offset0:128 offset1:130\n" \
  "ds_read2st64_b32 v[106:107], v177 offset0:128 offset1:130\n" \
  "ds_read2st64_b32 v[108:109], v176 offset0:0 offset1:2\n" \
  "ds_read2st64_b32 v[110:111], v177 offset0:0 offset1:2\n" \
  "v_mul_f32 v22, 0x3eb504f3, v112\n" \
  "v_mul_f32 v23, 0x3eb504f3, v113\n" \
  "v_xor_b32 v5, 64, v182\n" \
  "v_xor_b32 v6, 64, v183\n" \
  "v_xor_b32 v179, 0x80, v182\n" \
  "v_xor_b32 v191, 0x80, v183\n" \
  "v_xor_b32 v140, 0xc0, v182\n" \
  "v_xor_b32 v141, 0xc0, v183\n" \
  "v_xor_b32 v142, 64, v184\n" \
  "v_xor_b32 v143, 64, v185\n" \
  "v_mov_b32_dpp v12, v22 quad_perm:[0,0,0,0] row_mask:0xf bank_mask:0xf\n" \
  "v_mov_b32_dpp v14, v23 quad_perm:[0,0,0,0] row_mask:0xf bank_mask:0xf\n" \
  "v_mov_b32_dpp v16, v22 quad_perm:[1,1,1,1] row_mask:0xf bank_mask:0xf\n" \
  "v_mov_b32_dpp v18, v23 quad_perm:[1,1,1,1] row_mask:0xf bank_mask:0xf\n" \
  "v_mov_b32_dpp v0, v22 quad_perm:[2,2,2,2] row_mask:0xf bank_mask:0xf\n" \
  "v_mov_b32_dpp v2, v23 quad_perm:[2,2,2,2] row_mask:0xf bank_mask:0xf\n" \
  "v_mov_b32_dpp v168, v22 quad_perm:[3,3,3,3] row_mask:0xf bank_mask:0xf\n" \
  "v_mov_b32_dpp v170, v23 quad_perm:[3,3,3,3] row_mask:0xf bank_mask:0xf\n" \
  "s_waitcnt lgkmcnt(7)\n" \
  "v_mfma_f32_16x16x16_f16 v[112:115], v[138:139], v[96:97], 0\n" \
  "v_mfma_f32_16x16x16_f16 v[116:119], v[138:139], v[98:99], 0\n" \
  "s_waitcnt lgkmcnt(4)\n" \
  "v_mfma_f32_16x16x16_f16 v[120:123], v[138:139], v[100:101], 0\n" \
  "v_mfma_f32_16x16x16_f16 v[124:127], v[138:139], v[102:103], 0\n" \
  "s_waitcnt lgkmcnt(2)\n" \
  "v_mfma_f32_16x16x16_f16 v[128:131], v[138:139], v[104:105], 0\n" \
  "v_mfma_f32_16x16x16_f16 v[132:135], v[138:139], v[106:107], 0\n" \
  "s_waitcnt lgkmcnt(0)\n" \
  "v_mfma_f32_16x16x16_f16 v[144:147], v[138:139], v[108:109], 0\n" \
  "v_mfma_f32_16x16x16_f16 v[148:151], v[138:139], v[110:111], 0\n" \
  "ds_read_b128 v[192:195], v178 offset:0\n" \
  "ds_read_b128 v[196:199], v178 offset:1024\n" \
  "ds_read_b128 v[200:203], v178 offset:2048\n" \
  "ds_read_b128 v[204:207], v178 offset:3072\n" \
  "ds_read_b128 v[208:211], v178 offset:4096\n" \
  "ds_read_b128 v[212:215], v178 offset:5120\n" \
  "ds_read_b128 v[216:219], v178 offset:6144\n" \
  "ds_read_b128 v[220:223], v178 offset:7168\n" \
  "ds_read_b128 v[224:227], v178 offset:8192\n" \
  "ds_read_b128 v[228:231], v178 offset:9216\n" \
  "v_pk_mul_f32 v[112:113], v[112:113], v[12:13] op_sel_hi:[1,0]\n" \
  "v_pk_mul_f32 v[114:115], v[114:115], v[14:15] op_sel_hi:[1,0]\n" \
  "v_pk_mul_f32 v[116:117], v[116:117], v[12:13] op_sel_hi:[1,0]\n" \
  "v_pk_mul_f32 v[118:119], v[118:119], v[14:15] op_sel_hi:[1,0]\n" \
  "v_pk_mul_f32 v[120:121], v[120:121], v[16:17] op_sel_hi:[1,0]\n" \
  "v_pk_mul_f32 v[122:123], v[122:123], v[18:19] op_sel_hi:[1,0]\n" \
  "v_pk_mul_f32 v[124:125], v[124:125], v[16:17] op_sel_hi:[1,0]\n" \
  "v_pk_mul_f32 v[126:127], v[126:127], v[18:19] op_sel_hi:[1,0]\n" \
  "v_pk_mul_f32 v[128:129], v[128:129], v[0:1] op_sel_hi:[1,0]\n" \
  "v_pk_mul_f32 v[130:131], v[130:131], v[2:3] op_sel_hi:[1,0]\n" \
  "v_pk_mul_f32 v[132:133], v[132:133], v[0:1] op_sel_hi:[1,0]\n" \
  "v_pk_mul_f32 v[134:135], v[134:135], v[2:3] op_sel_hi:[1,0]\n" \
  "v_pk_mul_f32 v[144:145], v[144:145], v[168:169] op_sel_hi:[1,0]\n" \
  "v_pk_mul_f32 v[146:147], v[146:147], v[170:171] op_sel_hi:[1,0]\n" \
  "v_pk_mul_f32 v[148:149], v[148:149], v[168:169] op_sel_hi:[1,0]\n" \
  "v_pk_mul_f32 v[150:151], v[150:151], v[170:171] op_sel_hi:[1,0]\n" \
  ASTAMP(48816, 0) \
  "s_mov_b32 s78, 0x10000\n" \
  "111:\n" \
  "buffer_load_dwordx4 v[96:99], v181, s[28:31], s46 offen sc1\n" \
  "buffer_load_dwordx4 v[100:103], v181, s[28:31], s47 offen sc1\n" \
  "buffer_load_dwordx4 v[104:107], v181, s[28:31], s48 offen sc1\n" \
  "buffer_load_dwordx4 v[108:111], v181, s[28:31], s49 offen sc1\n" \
  "s_cmp_eq_u32 s34, 1\n" \
  "s_cbranch_scc1 114f\n" \
  "s_cmp_eq_u32 s34, 2\n" \
  "s_cbranch_scc1 115f\n" \
  "s_cmp_eq_u32 s34, 3\n" \
  "s_cbranch_scc1 116f\n" \
  "113:\n" \
  "s_waitcnt vmcnt(0)\n" \
  "v_mov_b32 v0, v96\n" \
  "v_mov_b32 v1, v100\n" \
  "v_mov_b32 v2, v104\n" \
  "v_mov_b32 v3, v108\n" \
  "v_mfma_f32_16x16x16_bf16 v[12:15], v[136:137], v[0:1], v[24:27]\n" \
  "v_mov_b32 v168, v97\n" \
  "v_mfma_f32_16x16x16_bf16 v[16:19], v[136:137], v[2:3], v[28:31]\n" \
  "v_mov_b32 v169, v101\n" \
  "v_mov_b32 v170, v105\n" \
  "v_mov_b32 v171, v109\n" \
  "v_mov_b32 v172, v98\n" \
  "v_mov_b32 v173, v102\n" \
  "v_mov_b32 v174, v106\n" \
  "v_mov_b32 v175, v110\n" \
  "v_mov_b32 v10, v99\n" \
  "v_mov_b32 v11, v103\n" \
  "v_mov_b32 v22, v107\n" \
  "v_mov_b32 v23, v111\n" \
  "s_branch 117f\n" \
  "114:\n" \
  "s_waitcnt vmcnt(0)\n" \
  "v_mov_b32 v0, v97\n" \
  "v_mov_b32 v1, v101\n" \
  "v_mov_b32 v2, v105\n" \
  "v_mov_b32 v3, v109\n" \
  "v_mfma_f32_16x16x16_bf16 v[12:15], v[136:137], v[0:1], v[24:27]\n" \
  "v_mov_b32 v168, v98\n" \
  "v_mfma_f32_16x16x16_bf16 v[16:19], v[136:137], v[2:3], v[28:31]\n" \
  "v_mov_b32 v169, v102\n" \
  "v_mov_b32 v170, v106\n" \
  "v_mov_b32 v171, v110\n" \
  "v_mov_b32 v172, v99\n" \
  "v_mov_b32 v173, v103\n" \
  "v_mov_b32 v174, v107\n" \
  "v_mov_b32 v175, v111\n" \
  "v_mov_b32 v10, v96\n" \
  "v_mov_b32 v11, v100\n" \
  "v_mov_b32 v22, v104\n" \
  "v_mov_b32 v23, v108\n" \
  "s_branch 117f\n" \
  "115:\n" \
  "s_waitcnt vmcnt(0)\n" \
  "v_mov_b32 v0, v98\n" \
  "v_mov_b32 v1, v102\n" \
  "v_mov_b32 v2, v106\n" \
  "v_mov_b32 v3, v110\n" \
  "v_mfma_f32_16x16x16_bf16 v[12:15], v[136:137], v[0:1], v[24:27]\n" \
  "v_mov_b32 v168, v99\n" \
  "v_mfma_f32_16x16x16_bf16 v[16:19], v[136:137], v[2:3], v[28:31]\n" \
  "v_mov_b32 v169, v103\n" \
  "v_mov_b32 v170, v107\n" \
  "v_mov_b32 v171, v111\n" \
  "v_mov_b32 v172, v96\n" \
  "v_mov_b32 v173, v100\n" \
  "v_mov_b32 v174, v104\n" \
  "v_mov_b32 v175, v108\n" \
  "v_mov_b32 v10, v97\n" \
  "v_mov_b32 v11, v101\n" \
  "v_mov_b32 v22, v105\n" \
  "v_mov_b32 v23, v109\n" \
  "s_branch 117f\n" \
  "116:\n" \
  "s_waitcnt vmcnt(0)\n" \
  "v_mov_b32 v0, v99\n" \
  "v_mov_b32 v1, v103\n" \
  "v_mov_b32 v2, v107\n" \
  "v_mov_b32 v3, v111\n" \
  "v_mfma_f32_16x16x16_bf16 v[12:15], v[136:137], v[0:1], v[24:27]\n" \
  "v_mov_b32 v168, v96\n" \
  "v_mfma_f32_16x16x16_bf16 v[16:19], v[136:137], v[2:3], v[28:31]\n" \
  "v_mov_b32 v169, v100\n" \
  "v_mov_b32 v170, v104\n" \
  "v_mov_b32 v171, v108\n" \
  "v_mov_b32 v172, v97\n" \
  "v_mov_b32 v173, v101\n" \
  "v_mov_b32 v174, v105\n" \
  "v_mov_b32 v175, v109\n" \
  "v_mov_b32 v10, v98\n" \
  "v_mov_b32 v11, v102\n" \
  "v_mov_b32 v22, v106\n" \
  "v_mov_b32 v23, v110\n" \
  "117:\n" \
  "s_mov_b64 exec, s[72:73]\n" \
  "v_mov_b32 v17, 0\n" \
  "v_mov_b32 v19, 0\n" \
  "s_mov_b64 exec, -1\n" \
  "v_cvt_pk_bf16_f32 v0, v12, v13\n" \
  "v_cvt_pk_bf16_f32 v1, v14, v15\n" \
  "v_cvt_pk_bf16_f32 v2, v16, v17\n" \
  "v_cvt_pk_bf16_f32 v3, v18, v19\n" \
  "ds_write_b32 v182, v0 offset:24576\n" \
  "ds_write_b32 v183, v1 offset:24576\n" \
  "ds_write_b32 v5, v2 offset:24576\n" \
  "ds_write_b32 v6, v3 offset:24576\n" \
  "ds_read_b128 v[0:3], v184 offset:16384\n" \
  "v_max3_u32 v21, v96, v97, v98\n" \
  "v_max3_u32 v21, v21, v99, v100\n" \
  "v_max3_u32 v21, v21, v101, v102\n" \
  "v_max3_u32 v21, v21, v103, v104\n" \
  "v_max3_u32 v21, v21, v105, v106\n" \
  "v_max3_u32 v21, v21, v107, v108\n" \
  "v_max3_u32 v21, v21, v109, v110\n" \
  "v_max3_u32 v21, v21, v111, v111\n" \
  "v_cmp_eq_u32 vcc, -1, v21\n" \
  "s_cbranch_vccz 118f\n" \
  "s_sub_u32 s78, s78, 1\n" \
  "s_cbranch_scc0 111b\n" \
  "118:\n" \
  ASTAMP(48820, 0) \
  "ds_read_b128 v[108:111], v142 offset:16384\n" \
  "v_mfma_f32_16x16x16_bf16 v[112:115], v[136:137], v[168:169], v[112:115]\n" \
  "v_mfma_f32_16x16x16_bf16 v[116:119], v[136:137], v[170:171], v[116:119]\n" \
  "v_mfma_f32_16x16x16_bf16 v[120:123], v[136:137], v[172:173], v[120:123]\n" \
  "v_mfma_f32_16x16x16_bf16 v[124:127], v[136:137], v[174:175], v[124:127]\n" \
  "v_mfma_f32_16x16x16_bf16 v[128:131], v[136:137], v[10:11], v[128:131]\n" \
  "v_mfma_f32_16x16x16_bf16 v[132:135], v[136:137], v[22:23], v[132:135]\n" \
  "s_waitcnt lgkmcnt(1)\n" \
  "v_mfma_f32_16x16x32_bf16 v[104:107], v[0:3], v[0:3], 0\n" \
  "v_mfma_f32_16x16x32_bf16 v[96:99], v[152:155], v[0:3], 0\n" \
  "v_mfma_f32_16x16x32_bf16 v[100:103], v[0:3], v[160:163], 0\n" \
  "s_waitcnt lgkmcnt(0)\n" \
  "v_mfma_f32_16x16x32_bf16 v[104:107], v[108:111], v[108:111], v[104:107]\n" \
  "v_mfma_f32_16x16x32_bf16 v[96:99], v[156:159], v[108:111], v[96:99]\n" \
  "v_mfma_f32_16x16x32_bf16 v[100:103], v[108:111], v[164:167], v[100:103]\n" \
  "v_cvt_pk_bf16_f32 v112, v112, v113\n" \
  "v_cvt_pk_bf16_f32 v113, v114, v115\n" \
  "v_cvt_pk_bf16_f32 v116, v116, v117\n" \
  "v_cvt_pk_bf16_f32 v117, v118, v119\n" \
  "v_cvt_pk_bf16_f32 v120, v120, v121\n" \
  "v_cvt_pk_bf16_f32 v121, v122, v123\n" \
  "v_cndmask_b32 v104, v104, v105, s[68:69]\n" \
  "v_cndmask_b32 v106, v106, v107, s[68:69]\n" \
  "v_cndmask_b32 v104, v104, v106, s[70:71]\n" \
  "s_mov_b32 exec_lo, 0x00ff00ff\n" \
  "s_mov_b32 exec_hi, 0xff00ff00\n" \
  "ds_write_b128 v186, v[96:99] offset:32768\n" \
  "s_not_b64 exec, exec\n" \
  "ds_write_b128 v186, v[100:103] offset:32768\n" \
  "s_mov_b32 exec_lo, 0\n" \
  "s_mov_b32 exec_hi, 0xf0000f00\n" \
  "ds_write_b32 v187, v104 offset:41472\n" \
  "s_mov_b64 exec, -1\n" \
  "v_cvt_pk_bf16_f32 v124, v124, v125\n" \
  "v_cvt_pk_bf16_f32 v125, v126, v127\n" \
  "v_cvt_pk_bf16_f32 v128, v128, v129\n" \
  "v_cvt_pk_bf16_f32 v129, v130, v131\n" \
  "v_cvt_pk_bf16_f32 v132, v132, v133\n" \
  "v_cvt_pk_bf16_f32 v133, v134, v135\n" \
  "s_waitcnt lgkmcnt(0)\n" \
  "s_barrier\n" \
  ASTAMP(48824, 0) \
  "s_cmp_lt_u32 s22, 4\n" \
  "s_cbranch_scc1 119f\n" \
  "s_mov_b64 exec, s[72:73]\n" \
  "v_and_b32 v116, 0xffff, v116\n" \
  "v_and_b32 v117, 0xffff, v117\n" \
  "v_and_b32 v124, 0xffff, v124\n" \
  "v_and_b32 v125, 0xffff, v125\n" \
  "v_and_b32 v132, 0xffff, v132\n" \
  "v_and_b32 v133, 0xffff, v133\n" \
  "s_mov_b64 exec, -1\n" \
  "ds_write_b32 v179, v112 offset:0\n" \
  "ds_write_b32 v191, v113 offset:0\n" \
  "ds_write_b32 v140, v116 offset:0\n" \
  "ds_write_b32 v141, v117 offset:0\n" \
  "ds_write_b32 v182, v120 offset:8192\n" \
  "ds_write_b32 v183, v121 offset:8192\n" \
  "ds_write_b32 v5, v124 offset:8192\n" \
  "ds_write_b32 v6, v125 offset:8192\n" \
  "s_branch 120f\n" \
  "119:\n" \
  "ds_read_b128 v[104:107], v189 offset:41728\n" \
  "ds_read_b128 v[108:111], v189 offset:41744\n" \
  "ds_read2st64_b32 v[96:97], v188 offset0:128 offset1:132\n" \
  "ds_read2st64_b32 v[98:99], v188 offset0:136 offset1:140\n" \
  "ds_read2st64_b32 v[100:101], v188 offset0:144 offset1:148\n" \
  "ds_read2st64_b32 v[102:103], v188 offset0:152 offset1:156\n" \
  "ds_write_b32 v179, v112 offset:0\n" \
  "ds_write_b32 v191, v113 offset:0\n" \
  "ds_write_b32 v140, v116 offset:0\n" \
  "ds_write_b32 v141, v117 offset:0\n" \
  "ds_write_b32 v182, v120 offset:8192\n" \
  "ds_write_b32 v183, v121 offset:8192\n" \
  "ds_write_b32 v5, v124 offset:8192\n" \
  "ds_write_b32 v6, v125 offset:8192\n" \
  "s_waitcnt lgkmcnt(12)\n" \
  "v_pk_add_f32 v[104:105], v[104:105], v[108:109]\n" \
  "v_pk_add_f32 v[106:107], v[106:107], v[110:111]\n" \
  "v_mov_b32 v21, 0x3727c5ac\n" \
  "v_pk_add_f32 v[104:105], v[104:105], v[106:107]\n" \
  "s_waitcnt lgkmcnt(8)\n" \
  "v_pk_add_f32 v[96:97], v[96:97], v[98:99]\n" \
  "v_add_f32 v104, v104, v105\n" \
  "v_pk_add_f32 v[100:101], v[100:101], v[102:103]\n" \
  "v_fmamk_f32 v104, v104, 0x3b000000, v21\n" \
  "v_pk_add_f32 v[96:97], v[96:97], v[100:101]\n" \
  "v_rsq_f32 v104, v104\n" \
  "v_add_f32 v96, v96, v97\n" \
  "s_nop 0\n" \
  "v_mul_f32 v96, v96, v104\n" \
  "s_nop 1\n" \
  "v_mov_b32_dpp v97, v96 quad_perm:[1,0,3,2] row_mask:0xf bank_mask:0xf\n" \
  "v_cvt_pk_bf16_f32 v96, v96, v97\n" \
  "buffer_store_dword v96, v190, s[28:31], s50 offen sc1\n" \
  "120:\n" \
  ASTAMP(48828, 0) \
  "ds_write_b32 v179, v128 offset:16384\n" \
  "ds_write_b32 v191, v129 offset:16384\n" \
  "ds_write_b32 v140, v132 offset:16384\n" \
  "ds_write_b32 v141, v133 offset:16384\n" \
  "ds_read_b128 v[112:115], v185\n" \
  "ds_read_b128 v[116:119], v143\n" \
  "ds_read_b128 v[120:123], v185 offset:16384\n" \
  "ds_read_b128 v[124:127], v143 offset:16384\n" \
  "v_pk_add_f32 v[24:25], v[12:13], v[144:145] neg_lo:[0,1] neg_hi:[0,1]\n" \
  "v_pk_add_f32 v[26:27], v[14:15], v[146:147] neg_lo:[0,1] neg_hi:[0,1]\n" \
  "v_pk_add_f32 v[28:29], v[16:17], v[148:149] neg_lo:[0,1] neg_hi:[0,1]\n" \
  "v_pk_add_f32 v[30:31], v[18:19], v[150:151] neg_lo:[0,1] neg_hi:[0,1]\n" \
  "s_waitcnt lgkmcnt(2)\n" \
  "v_mfma_f32_16x16x32_bf16 v[96:99], v[112:115], v[112:115], 0\n" \
  "v_mfma_f32_16x16x32_bf16 v[96:99], v[116:119], v[116:119], v[96:99]\n" \
  "s_waitcnt lgkmcnt(0)\n" \
  "v_mfma_f32_16x16x32_bf16 v[100:103], v[120:123], v[120:123], 0\n" \
  "v_mfma_f32_16x16x32_bf16 v[100:103], v[124:127], v[124:127], v[100:103]\n" \
  "v_mov_b32 v21, 0\n" \
  "s_mov_b64 exec, 0xffffff\n" \
  "ds_write_b32 v4, v21 offset:4096\n" \
  "s_mov_b64 exec, -1\n" \
  "s_nop 1\n" \
  "v_cndmask_b32 v96, v96, v97, s[68:69]\n" \
  "v_cndmask_b32 v98, v98, v99, s[68:69]\n" \
  "v_cndmask_b32 v96, v96, v98, s[70:71]\n" \
  "v_cndmask_b32 v100, v100, v101, s[68:69]\n" \
  "v_cndmask_b32 v102, v102, v103, s[68:69]\n" \
  "v_cndmask_b32 v100, v100, v102, s[70:71]\n" \
  "s_mov_b32 exec_lo, 0x00f0000f\n" \
  "s_mov_b32 exec_hi, 0xf0000f00\n" \
  "ds_write_b32 v187, v96 offset:40960\n" \
  "s_mov_b32 exec_hi, 0\n" \
  "ds_write_b32 v187, v100 offset:41472\n" \
  "s_mov_b64 exec, -1\n" \
  "s_branch 29b\n" \
  "21:\n" \
  "v_cvt_pk_bf16_f32 v12, v24, v25\n" \
  "v_cvt_pk_bf16_f32 v13, v26, v27\n" \
  "v_cvt_pk_bf16_f32 v14, v28, v29\n" \
  "v_cvt_pk_bf16_f32 v15, v30, v31\n" \
  "ds_write_b32 v1, v12 offset:4\n" \
  "ds_write_b32 v1, v13 offset:1060\n" \
  "ds_write_b32 v1, v14 offset:68\n" \
  "ds_write_b32 v1, v15 offset:1124\n" \
  "s_cmp_lg_u32 s22, 7\n" \
  "s_cbranch_scc1 3f\n" \
  "s_mov_b64 exec, s[72:73]\n" \
  "v_lshlrev_b32 v8, 6, v9\n" \
  "ds_read_b64 v[16:17], v8 offset:48640\n" \
  "v_mul_f32 v18, 0x3d3504f3, v28\n" \
  "v_mul_f32 v19, 0x3d3504f3, v30\n" \
  "v_lshlrev_b32 v8, 3, v9\n" \
  "ds_write_b64 v8, v[18:19] offset:40960\n" \
  "v_mul_u32_u24 v8, 0x840, v9\n" \
  "s_waitcnt lgkmcnt(1)\n" \
  "v_add_f32 v16, v29, v16\n" \
  "v_add_f32 v17, v31, v17\n" \
  "v_mul_f32 v16, 0x3f3504f3, v16\n" \
  "v_mul_f32 v17, 0x3f3504f3, v17\n" \
  "v_cvt_pk_bf16_f32 v16, v16, 0\n" \
  "v_cvt_pk_bf16_f32 v17, v17, 0\n" \
  "ds_write_b32 v8, v16\n" \
  "ds_write_b32 v8, v17 offset:1056\n" \
  "s_mov_b64 exec, -1\n" \
  "3:\n" \
  "s_lshl_b32 s81, s22, 2\n" \
  "v_mov_b32 v8, s81\n" \
  "s_waitcnt vmcnt(0)\n" \
  "v_lshlrev_b32 v96, 16, v10\n" \
  "v_lshlrev_b32 v98, 16, v11\n" \
  "v_lshlrev_b32 v100, 16, v238\n" \
  "v_lshlrev_b32 v102, 16, v239\n" \
  "v_and_b32 v97, 0xffff0000, v10\n" \
  "v_and_b32 v99, 0xffff0000, v11\n" \
  "v_and_b32 v101, 0xffff0000, v238\n" \
  "v_and_b32 v103, 0xffff0000, v239\n" \
  "s_waitcnt lgkmcnt(0)\n" \
  "s_barrier\n" \
  "ds_read2_b32 v[12:13], v2 offset0:0 offset1:32\n" \
  "ds_read2_b32 v[14:15], v2 offset0:16 offset1:48\n" \
  "ds_read_b32 v8, v8 offset:40960\n" \
  "s_waitcnt lgkmcnt(2)\n" \
  "v_mfma_f32_16x16x16_bf16 v[104:107], v[12:13], v[224:225], 0\n" \
  "v_mfma_f32_16x16x16_bf16 v[108:111], v[12:13], v[226:227], 0\n" \
  "s_waitcnt lgkmcnt(1)\n" \
  "v_mfma_f32_16x16x16_bf16 v[112:115], v[14:15], v[224:225], 0\n" \
  "v_mfma_f32_16x16x16_bf16 v[116:119], v[14:15], v[226:227], 0\n" \
  "s_waitcnt lgkmcnt(0)\n" \
  "v_pk_add_f32 v[96:97], v[96:97], v[8:9] op_sel_hi:[1,0] neg_hi:[0,1]\n" \
  "v_pk_add_f32 v[98:99], v[98:99], v[8:9] op_sel_hi:[1,0] neg_hi:[0,1]\n" \
  "v_pk_add_f32 v[100:101], v[100:101], v[8:9] op_sel_hi:[1,0] neg_hi:[0,1]\n" \
  "v_pk_add_f32 v[102:103], v[102:103], v[8:9] op_sel_hi:[1,0] neg_hi:[0,1]\n" \
  "s_nop 0\n" \
  "v_pk_mul_f32 v[16:17], v[108:109], v[232:233]\n" \
  "v_pk_mul_f32 v[18:19], v[104:105], v[232:233]\n" \
  "v_pk_mul_f32 v[120:121], v[110:111], v[234:235]\n" \
  "v_pk_mul_f32 v[122:123], v[106:107], v[234:235]\n" \
  "v_pk_fma_f32 v[104:105], v[104:105], v[228:229], v[16:17] neg_lo:[0,0,1] neg_hi:[0,0,1]\n" \
  "v_pk_fma_f32 v[108:109], v[108:109], v[228:229], v[18:19]\n" \
  "v_pk_fma_f32 v[106:107], v[106:107], v[230:231], v[120:121] neg_lo:[0,0,1] neg_hi:[0,0,1]\n" \
  "v_pk_fma_f32 v[110:111], v[110:111], v[230:231], v[122:123]\n" \
  "v_cvt_pk_bf16_f32 v120, v104, v108\n" \
  "v_cvt_pk_bf16_f32 v121, v105, v109\n" \
  "v_cvt_pk_bf16_f32 v122, v106, v110\n" \
  "v_cvt_pk_bf16_f32 v123, v107, v111\n" \
  "v_pk_mul_f32 v[16:17], v[116:117], v[244:245]\n" \
  "v_pk_mul_f32 v[18:19], v[112:113], v[244:245]\n" \
  "v_mfma_f32_16x16x32_bf16 v[96:99], v[120:123], v[248:251], v[96:99]\n" \
  "v_mfma_f32_16x16x32_bf16 v[100:103], v[120:123], v[252:255], v[100:103]\n" \
  "v_pk_mul_f32 v[124:125], v[118:119], v[246:247]\n" \
  "v_pk_mul_f32 v[126:127], v[114:115], v[246:247]\n" \
  "v_pk_fma_f32 v[112:113], v[112:113], v[240:241], v[16:17] neg_lo:[0,0,1] neg_hi:[0,0,1]\n" \
  "v_pk_fma_f32 v[116:117], v[116:117], v[240:241], v[18:19]\n" \
  "v_pk_fma_f32 v[114:115], v[114:115], v[242:243], v[124:125] neg_lo:[0,0,1] neg_hi:[0,0,1]\n" \
  "v_pk_fma_f32 v[118:119], v[118:119], v[242:243], v[126:127]\n" \
  "v_cvt_pk_bf16_f32 v124, v112, v116\n" \
  "v_cvt_pk_bf16_f32 v125, v113, v117\n" \
  "v_cvt_pk_bf16_f32 v126, v114, v118\n" \
  "v_cvt_pk_bf16_f32 v127, v115, v119\n" \
  "s_nop 1\n" \
  "v_mfma_f32_16x16x32_bf16 v[96:99], v[124:127], v[192:195], v[96:99]\n" \
  "v_mfma_f32_16x16x32_bf16 v[100:103], v[124:127], v[196:199], v[100:103]\n" \
  "s_nop 6\n" \
  "v_cvt_pk_bf16_f32 v12, v96, v97\n" \
  "v_cvt_pk_bf16_f32 v13, v98, v99\n" \
  "global_store_dwordx2 v7, v[12:13], s[18:19]\n" \
  "v_cvt_pk_bf16_f32 v14, v100, v101\n" \
  "v_cvt_pk_bf16_f32 v15, v102, v103\n" \
  "global_store_dwordx2 v7, v[14:15], s[18:19] offset:512\n" \
  "s_or_b32 s81, s22, s33\n" \
  "s_cbranch_scc1 4f\n" \
  "v_mov_b64 v[16:17], s[36:37]\n" \
  "s_mov_b32 s81, 22489088\n" \
  "s_mov_b64 exec, 1\n" \
  "buffer_store_dwordx2 v[16:17], off, s[24:27], s81 sc1\n" \
  "s_mov_b64 exec, -1\n" \
  "4:\n" \
  ASTAMP(48840, 0) \
  ".Lk_pf1:\n" \
  "s_branch 9f\n" \
  "8:\n" \
  "v_subrev_u32 v7, 2048, v7\n" \
  "s_waitcnt lgkmcnt(0)\n" \
  "v_max3_u32 v20, v96, v97, v98\n" \
  "v_max3_u32 v20, v20, v99, v100\n" \
  "v_max3_u32 v20, v20, v101, v102\n" \
  "v_max3_u32 v20, v20, v103, v104\n" \
  "v_max3_u32 v20, v20, v105, v106\n" \
  "v_max3_u32 v20, v20, v107, v108\n" \
  "v_max3_u32 v20, v20, v109, v110\n" \
  "v_max3_u32 v20, v20, v111, v112\n" \
  "v_max3_u32 v20, v20, v113, v114\n" \
  "v_max3_u32 v20, v20, v115, v116\n" \
  "v_max3_u32 v20, v20, v117, v118\n" \
  "v_max3_u32 v20, v20, v119, v120\n" \
  "v_max3_u32 v20, v20, v121, v122\n" \
  "v_max3_u32 v20, v20, v123, v124\n" \
  "v_max3_u32 v20, v20, v125, v126\n" \
  "v_max3_u32 v20, v20, v127, v128\n" \
  "v_max3_u32 v20, v20, v129, v130\n" \
  "v_max3_u32 v20, v20, v131, v132\n" \
  "v_max3_u32 v20, v20, v133, v134\n" \
  "v_max3_u32 v20, v20, v135, v136\n" \
  "v_max3_u32 v20, v20, v137, v138\n" \
  "v_max3_u32 v20, v20, v139, v140\n" \
  "v_max3_u32 v20, v20, v141, v142\n" \
  "v_max3_u32 v20, v20, v143, v144\n" \
  "v_max3_u32 v20, v20, v145, v146\n" \
  "v_max3_u32 v20, v20, v147, v148\n" \
  "v_max3_u32 v20, v20, v149, v150\n" \
  "v_max3_u32 v20, v20, v151, v152\n" \
  "v_max3_u32 v20, v20, v153, v154\n" \
  "v_max3_u32 v20, v20, v155, v156\n" \
  "v_max3_u32 v20, v20, v157, v158\n" \
  "v_max3_u32 v20, v20, v159, v160\n" \
  "v_max3_u32 v20, v20, v161, v162\n" \
  "v_max3_u32 v20, v20, v163, v164\n" \
  "v_max3_u32 v20, v20, v165, v166\n" \
  "v_max3_u32 v20, v20, v167, v168\n" \
  "v_max3_u32 v20, v20, v169, v170\n" \
  "v_max3_u32 v20, v20, v171, v172\n" \
  "v_max3_u32 v20, v20, v173, v174\n" \
  "v_max3_u32 v20, v20, v175, v176\n" \
  "v_max3_u32 v20, v20, v177, v178\n" \
  "v_max3_u32 v20, v20, v179, v180\n" \
  "v_max3_u32 v20, v20, v181, v182\n" \
  "v_max3_u32 v20, v20, v183, v184\n" \
  "v_max3_u32 v20, v20, v185, v186\n" \
  "v_max3_u32 v20, v20, v187, v188\n" \
  "v_max3_u32 v20, v20, v189, v190\n" \
  "v_max3_u32 v20, v20, v191, v191\n" \
  "v_cmp_eq_u32 vcc, -1, v20\n" \
  "s_cmp_eq_u64 vcc, 0\n" \
  "s_cselect_b64 s[76:77], 0, s[76:77]\n" \
  "s_sub_u32 s78, s78, 1\n" \
  "s_cselect_b64 s[76:77], 0, s[76:77]\n" \
  "buffer_load_dwordx4 v[96:99], v7, s[28:31], s51 offen offset:0 sc1\n" \
  "buffer_load_dwordx4 v[100:103], v7, s[28:31], s51 offen offset:1024 sc1\n" \
  "s_mov_b64 exec, s[66:67]\n" \
  "buffer_load_dwordx4 v[128:131], v4, s[28:31], s53 offen offset:0 sc1\n" \
  "buffer_load_dwordx4 v[132:135], v4, s[28:31], s53 offen offset:256 sc1\n" \
  "s_mov_b64 exec, -1\n" \
  "buffer_load_dwordx4 v[104:107], v7, s[28:31], s51 offen offset:2048 sc1\n" \
  "s_mov_b64 exec, s[66:67]\n" \
  "buffer_load_dwordx4 v[136:139], v4, s[28:31], s53 offen offset:1024 sc1\n" \
  "s_mov_b64 exec, -1\n" \
  "s_branch 11b\n" \
  "12:\n" \
  "s_or_b32 s79, s79, 8\n" \
  "13:\n" \
  "s_waitcnt vmcnt(0) lgkmcnt(0)\n" \
  "s_and_b32 s81, s79, 15\n" \
  "s_cbranch_scc0 16f\n" \
  "v_mbcnt_lo_u32_b32 v8, -1, 0\n" \
  "v_mbcnt_hi_u32_b32 v8, -1, v8\n" \
  "v_lshrrev_b32 v9, 4, v8\n" \
  "v_mov_b64 v[10:11], 0\n" \
  "s_lshl_b32 s81, s22, 11\n" \
  "v_lshl_add_u32 v1, v8, 4, s81\n" \
  "s_lshl_b32 s81, s22, 2\n" \
  "s_add_u32 s81, s81, 41984\n" \
  "v_lshl_add_u32 v2, v8, 5, s81\n" \
  "v_and_b32 v9, 31, v8\n" \
  "v_lshlrev_b32 v9, 5, v9\n" \
  "v_lshrrev_b32 v8, 5, v8\n" \
  "v_lshl_add_u32 v8, v8, 4, v9\n" \
  "s_mov_b32 s80, 0\n" \
  "v_mov_b32 v128, v191\n" \
  "v_mov_b32 v129, v191\n" \
  "v_mov_b32 v130, v191\n" \
  "v_mov_b32 v131, v191\n" \
  "v_mov_b32 v132, v191\n" \
  "v_mov_b32 v133, v191\n" \
  "v_mov_b32 v134, v191\n" \
  "v_mov_b32 v135, v191\n" \
  "v_mov_b32 v136, v191\n" \
  "v_mov_b32 v137, v191\n" \
  "v_mov_b32 v138, v191\n" \
  "v_mov_b32 v139, v191\n" \
  "v_mov_b32 v140, v191\n" \
  "v_mov_b32 v141, v191\n" \
  "v_mov_b32 v142, v191\n" \
  "v_mov_b32 v143, v191\n" \
  "v_mov_b32 v144, v191\n" \
  "v_mov_b32 v145, v191\n" \
  "v_mov_b32 v146, v191\n" \
  "v_mov_b32 v147, v191\n" \
  "v_mov_b32 v148, v191\n" \
  "v_mov_b32 v149, v191\n" \
  "v_mov_b32 v150, v191\n" \
  "v_mov_b32 v151, v191\n" \
  "v_mov_b32 v152, v191\n" \
  "v_mov_b32 v153, v191\n" \
  "v_mov_b32 v154, v191\n" \
  "v_mov_b32 v155, v191\n" \
  "v_mov_b32 v156, v191\n" \
  "v_mov_b32 v157, v191\n" \
  "v_mov_b32 v158, v191\n" \
  "v_mov_b32 v159, v191\n" \
  "v_mov_b32 v160, v191\n" \
  "v_mov_b32 v161, v191\n" \
  "v_mov_b32 v162, v191\n" \
  "v_mov_b32 v163, v191\n" \
  "v_mov_b32 v164, v191\n" \
  "v_mov_b32 v165, v191\n" \
  "v_mov_b32 v166, v191\n" \
  "v_mov_b32 v167, v191\n" \
  "s_mov_b64 exec, s[66:67]\n" \
  "buffer_load_dwordx4 v[128:131], v4, s[28:31], s53 offen offset:0 sc1\n" \
  "buffer_load_dwordx4 v[132:135], v4, s[28:31], s53 offen offset:256 sc1\n" \
  "buffer_load_dwordx4 v[136:139], v4, s[28:31], s53 offen offset:1024 sc1\n" \
  "buffer_load_dwordx4 v[140:143], v4, s[28:31], s53 offen offset:1280 sc1\n" \
  "buffer_load_dwordx4 v[144:147], v4, s[28:31], s53 offen offset:2048 sc1\n" \
  "buffer_load_dwordx4 v[148:151], v4, s[28:31], s53 offen offset:2304 sc1\n" \
  "buffer_load_dwordx4 v[152:155], v4, s[28:31], s53 offen offset:3072 sc1\n" \
  "buffer_load_dwordx4 v[156:159], v4, s[28:31], s53 offen offset:3328 sc1\n" \
  "buffer_load_dwordx4 v[160:163], v4, s[28:31], s54 offen offset:0 sc1\n" \
  "buffer_load_dwordx4 v[164:167], v4, s[28:31], s54 offen offset:256 sc1\n" \
  "s_mov_b64 exec, -1\n" \
  "s_waitcnt vmcnt(0)\n" \
  "15:\n" \
  "s_bitcmp1_b32 s79, s80\n" \
  "s_cbranch_scc0 17f\n" \
  "s_lshl_b32 s81, s80, 11\n" \
  "v_add_u32 v7, s81, v8\n" \
  "ds_read_b128 v[216:219], v7 offset:32768\n" \
  "v_mov_b32 v220, 0xff7fffff\n" \
  "s_waitcnt lgkmcnt(0)\n" \
  "v_mfma_f32_32x32x16_bf16 v[192:207], v[96:99], v[216:219], 0\n" \
  "s_nop 11\n" \
  "v_max3_f32 v220, v220, v192, v193\n" \
  "v_max3_f32 v220, v220, v194, v195\n" \
  "v_max3_f32 v220, v220, v196, v197\n" \
  "v_max3_f32 v220, v220, v198, v199\n" \
  "v_max3_f32 v220, v220, v200, v201\n" \
  "v_max3_f32 v220, v220, v202, v203\n" \
  "v_max3_f32 v220, v220, v204, v205\n" \
  "v_max3_f32 v220, v220, v206, v207\n" \
  "v_mfma_f32_32x32x16_bf16 v[192:207], v[100:103], v[216:219], 0\n" \
  "s_nop 11\n" \
  "v_max3_f32 v220, v220, v192, v193\n" \
  "v_max3_f32 v220, v220, v194, v195\n" \
  "v_max3_f32 v220, v220, v196, v197\n" \
  "v_max3_f32 v220, v220, v198, v199\n" \
  "v_max3_f32 v220, v220, v200, v201\n" \
  "v_max3_f32 v220, v220, v202, v203\n" \
  "v_max3_f32 v220, v220, v204, v205\n" \
  "v_max3_f32 v220, v220, v206, v207\n" \
  "v_mfma_f32_32x32x16_bf16 v[192:207], v[104:107], v[216:219], 0\n" \
  "s_nop 11\n" \
  "v_max3_f32 v220, v220, v192, v193\n" \
  "v_max3_f32 v220, v220, v194, v195\n" \
  "v_max3_f32 v220, v220, v196, v197\n" \
  "v_max3_f32 v220, v220, v198, v199\n" \
  "v_max3_f32 v220, v220, v200, v201\n" \
  "v_max3_f32 v220, v220, v202, v203\n" \
  "v_max3_f32 v220, v220, v204, v205\n" \
  "v_max3_f32 v220, v220, v206, v207\n" \
  "v_mfma_f32_32x32x16_bf16 v[192:207], v[108:111], v[216:219], 0\n" \
  "s_nop 11\n" \
  "v_max3_f32 v220, v220, v192, v193\n" \
  "v_max3_f32 v220, v220, v194, v195\n" \
  "v_max3_f32 v220, v220, v196, v197\n" \
  "v_max3_f32 v220, v220, v198, v199\n" \
  "v_max3_f32 v220, v220, v200, v201\n" \
  "v_max3_f32 v220, v220, v202, v203\n" \
  "v_max3_f32 v220, v220, v204, v205\n" \
  "v_max3_f32 v220, v220, v206, v207\n" \
  "v_mfma_f32_32x32x16_bf16 v[192:207], v[112:115], v[216:219], 0\n" \
  "s_nop 11\n" \
  "v_max3_f32 v220, v220, v192, v193\n" \
  "v_max3_f32 v220, v220, v194, v195\n" \
  "v_max3_f32 v220, v220, v196, v197\n" \
  "v_max3_f32 v220, v220, v198, v199\n" \
  "v_max3_f32 v220, v220, v200, v201\n" \
  "v_max3_f32 v220, v220, v202, v203\n" \
  "v_max3_f32 v220, v220, v204, v205\n" \
  "v_max3_f32 v220, v220, v206, v207\n" \
  "v_mfma_f32_32x32x16_bf16 v[192:207], v[116:119], v[216:219], 0\n" \
  "s_nop 11\n" \
  "v_max3_f32 v220, v220, v192, v193\n" \
  "v_max3_f32 v220, v220, v194, v195\n" \
  "v_max3_f32 v220, v220, v196, v197\n" \
  "v_max3_f32 v220, v220, v198, v199\n" \
  "v_max3_f32 v220, v220, v200, v201\n" \
  "v_max3_f32 v220, v220, v202, v203\n" \
  "v_max3_f32 v220, v220, v204, v205\n" \
  "v_max3_f32 v220, v220, v206, v207\n" \
  "v_mfma_f32_32x32x16_bf16 v[192:207], v[120:123], v[216:219], 0\n" \
  "s_nop 11\n" \
  "v_max3_f32 v220, v220, v192, v193\n" \
  "v_max3_f32 v220, v220, v194, v195\n" \
  "v_max3_f32 v220, v220, v196, v197\n" \
  "v_max3_f32 v220, v220, v198, v199\n" \
  "v_max3_f32 v220, v220, v200, v201\n" \
  "v_max3_f32 v220, v220, v202, v203\n" \
  "v_max3_f32 v220, v220, v204, v205\n" \
  "v_max3_f32 v220, v220, v206, v207\n" \
  "v_mfma_f32_32x32x16_bf16 v[192:207], v[124:127], v[216:219], 0\n" \
  "s_nop 11\n" \
  "v_max3_f32 v220, v220, v192, v193\n" \
  "v_max3_f32 v220, v220, v194, v195\n" \
  "v_max3_f32 v220, v220, v196, v197\n" \
  "v_max3_f32 v220, v220, v198, v199\n" \
  "v_max3_f32 v220, v220, v200, v201\n" \
  "v_max3_f32 v220, v220, v202, v203\n" \
  "v_max3_f32 v220, v220, v204, v205\n" \
  "v_max3_f32 v220, v220, v206, v207\n" \
  "v_mov_b32 v221, v220\n" \
  "s_nop 1\n" \
  "v_permlane32_swap_b32 v220, v221\n" \
  "v_max_f32 v220, v220, v221\n" \
  "s_waitcnt lgkmcnt(0)\n" \
  "s_barrier\n" \
  "ds_write_b32 v2, v220 offset:0\n" \
  "s_waitcnt lgkmcnt(0)\n" \
  "s_barrier\n" \
  "ds_read_b128 v[192:195], v9 offset:41984\n" \
  "ds_read_b128 v[196:199], v9 offset:42000\n" \
  "v_mfma_f32_32x32x8_f16 v[240:255], v[10:11], v[10:11], 0\n" \
  "s_waitcnt lgkmcnt(0)\n" \
  "s_barrier\n" \
  "v_max3_f32 v220, v192, v193, v194\n" \
  "v_max3_f32 v220, v220, v195, v196\n" \
  "v_max3_f32 v220, v220, v197, v198\n" \
  "v_max3_f32 v220, v220, v199, v199\n" \
  "v_mfma_f32_32x32x16_bf16 v[192:207], v[96:99], v[216:219], 0\n" \
  "s_nop 11\n" \
  "v_sub_f32 v192, v192, v220\n" \
  "v_sub_f32 v193, v193, v220\n" \
  "v_sub_f32 v194, v194, v220\n" \
  "v_sub_f32 v195, v195, v220\n" \
  "v_sub_f32 v196, v196, v220\n" \
  "v_sub_f32 v197, v197, v220\n" \
  "v_sub_f32 v198, v198, v220\n" \
  "v_sub_f32 v199, v199, v220\n" \
  "v_sub_f32 v200, v200, v220\n" \
  "v_sub_f32 v201, v201, v220\n" \
  "v_sub_f32 v202, v202, v220\n" \
  "v_sub_f32 v203, v203, v220\n" \
  "v_sub_f32 v204, v204, v220\n" \
  "v_sub_f32 v205, v205, v220\n" \
  "v_sub_f32 v206, v206, v220\n" \
  "v_sub_f32 v207, v207, v220\n" \
  "v_exp_f32 v192, v192\n" \
  "v_exp_f32 v193, v193\n" \
  "v_exp_f32 v194, v194\n" \
  "v_exp_f32 v195, v195\n" \
  "v_exp_f32 v196, v196\n" \
  "v_exp_f32 v197, v197\n" \
  "v_exp_f32 v198, v198\n" \
  "v_exp_f32 v199, v199\n" \
  "v_exp_f32 v200, v200\n" \
  "v_exp_f32 v201, v201\n" \
  "v_exp_f32 v202, v202\n" \
  "v_exp_f32 v203, v203\n" \
  "v_exp_f32 v204, v204\n" \
  "v_exp_f32 v205, v205\n" \
  "v_exp_f32 v206, v206\n" \
  "v_exp_f32 v207, v207\n" \
  "s_nop 1\n" \
  "v_smfmac_f32_32x32x32_bf16 v[240:255], v[128:131], v[192:199], v0 abid:1\n" \
  "v_smfmac_f32_32x32x32_bf16 v[240:255], v[132:135], v[200:207], v0 abid:1\n" \
  "v_mfma_f32_32x32x16_bf16 v[192:207], v[100:103], v[216:219], 0\n" \
  "s_nop 11\n" \
  "v_sub_f32 v192, v192, v220\n" \
  "v_sub_f32 v193, v193, v220\n" \
  "v_sub_f32 v194, v194, v220\n" \
  "v_sub_f32 v195, v195, v220\n" \
  "v_sub_f32 v196, v196, v220\n" \
  "v_sub_f32 v197, v197, v220\n" \
  "v_sub_f32 v198, v198, v220\n" \
  "v_sub_f32 v199, v199, v220\n" \
  "v_sub_f32 v200, v200, v220\n" \
  "v_sub_f32 v201, v201, v220\n" \
  "v_sub_f32 v202, v202, v220\n" \
  "v_sub_f32 v203, v203, v220\n" \
  "v_sub_f32 v204, v204, v220\n" \
  "v_sub_f32 v205, v205, v220\n" \
  "v_sub_f32 v206, v206, v220\n" \
  "v_sub_f32 v207, v207, v220\n" \
  "v_exp_f32 v192, v192\n" \
  "v_exp_f32 v193, v193\n" \
  "v_exp_f32 v194, v194\n" \
  "v_exp_f32 v195, v195\n" \
  "v_exp_f32 v196, v196\n" \
  "v_exp_f32 v197, v197\n" \
  "v_exp_f32 v198, v198\n" \
  "v_exp_f32 v199, v199\n" \
  "v_exp_f32 v200, v200\n" \
  "v_exp_f32 v201, v201\n" \
  "v_exp_f32 v202, v202\n" \
  "v_exp_f32 v203, v203\n" \
  "v_exp_f32 v204, v204\n" \
  "v_exp_f32 v205, v205\n" \
  "v_exp_f32 v206, v206\n" \
  "v_exp_f32 v207, v207\n" \
  "s_nop 1\n" \
  "v_smfmac_f32_32x32x32_bf16 v[240:255], v[136:139], v[192:199], v0 abid:1\n" \
  "v_smfmac_f32_32x32x32_bf16 v[240:255], v[140:143], v[200:207], v0 abid:1\n" \
  "v_mfma_f32_32x32x16_bf16 v[192:207], v[104:107], v[216:219], 0\n" \
  "s_nop 11\n" \
  "v_sub_f32 v192, v192, v220\n" \
  "v_sub_f32 v193, v193, v220\n" \
  "v_sub_f32 v194, v194, v220\n" \
  "v_sub_f32 v195, v195, v220\n" \
  "v_sub_f32 v196, v196, v220\n" \
  "v_sub_f32 v197, v197, v220\n" \
  "v_sub_f32 v198, v198, v220\n" \
  "v_sub_f32 v199, v199, v220\n" \
  "v_sub_f32 v200, v200, v220\n" \
  "v_sub_f32 v201, v201, v220\n" \
  "v_sub_f32 v202, v202, v220\n" \
  "v_sub_f32 v203, v203, v220\n" \
  "v_sub_f32 v204, v204, v220\n" \
  "v_sub_f32 v205, v205, v220\n" \
  "v_sub_f32 v206, v206, v220\n" \
  "v_sub_f32 v207, v207, v220\n" \
  "v_exp_f32 v192, v192\n" \
  "v_exp_f32 v193, v193\n" \
  "v_exp_f32 v194, v194\n" \
  "v_exp_f32 v195, v195\n" \
  "v_exp_f32 v196, v196\n" \
  "v_exp_f32 v197, v197\n" \
  "v_exp_f32 v198, v198\n" \
  "v_exp_f32 v199, v199\n" \
  "v_exp_f32 v200, v200\n" \
  "v_exp_f32 v201, v201\n" \
  "v_exp_f32 v202, v202\n" \
  "v_exp_f32 v203, v203\n" \
  "v_exp_f32 v204, v204\n" \
  "v_exp_f32 v205, v205\n" \
  "v_exp_f32 v206, v206\n" \
  "v_exp_f32 v207, v207\n" \
  "s_nop 1\n" \
  "v_smfmac_f32_32x32x32_bf16 v[240:255], v[144:147], v[192:199], v0 abid:1\n" \
  "v_smfmac_f32_32x32x32_bf16 v[240:255], v[148:151], v[200:207], v0 abid:1\n" \
  "v_mfma_f32_32x32x16_bf16 v[192:207], v[108:111], v[216:219], 0\n" \
  "s_nop 11\n" \
  "v_sub_f32 v192, v192, v220\n" \
  "v_sub_f32 v193, v193, v220\n" \
  "v_sub_f32 v194, v194, v220\n" \
  "v_sub_f32 v195, v195, v220\n" \
  "v_sub_f32 v196, v196, v220\n" \
  "v_sub_f32 v197, v197, v220\n" \
  "v_sub_f32 v198, v198, v220\n" \
  "v_sub_f32 v199, v199, v220\n" \
  "v_sub_f32 v200, v200, v220\n" \
  "v_sub_f32 v201, v201, v220\n" \
  "v_sub_f32 v202, v202, v220\n" \
  "v_sub_f32 v203, v203, v220\n" \
  "v_sub_f32 v204, v204, v220\n" \
  "v_sub_f32 v205, v205, v220\n" \
  "v_sub_f32 v206, v206, v220\n" \
  "v_sub_f32 v207, v207, v220\n" \
  "v_exp_f32 v192, v192\n" \
  "v_exp_f32 v193, v193\n" \
  "v_exp_f32 v194, v194\n" \
  "v_exp_f32 v195, v195\n" \
  "v_exp_f32 v196, v196\n" \
  "v_exp_f32 v197, v197\n" \
  "v_exp_f32 v198, v198\n" \
  "v_exp_f32 v199, v199\n" \
  "v_exp_f32 v200, v200\n" \
  "v_exp_f32 v201, v201\n" \
  "v_exp_f32 v202, v202\n" \
  "v_exp_f32 v203, v203\n" \
  "v_exp_f32 v204, v204\n" \
  "v_exp_f32 v205, v205\n" \
  "v_exp_f32 v206, v206\n" \
  "v_exp_f32 v207, v207\n" \
  "s_nop 1\n" \
  "v_smfmac_f32_32x32x32_bf16 v[240:255], v[152:155], v[192:199], v0 abid:1\n" \
  "v_smfmac_f32_32x32x32_bf16 v[240:255], v[156:159], v[200:207], v0 abid:1\n" \
  "v_mfma_f32_32x32x16_bf16 v[192:207], v[112:115], v[216:219], 0\n" \
  "s_nop 11\n" \
  "v_sub_f32 v192, v192, v220\n" \
  "v_sub_f32 v193, v193, v220\n" \
  "v_sub_f32 v194, v194, v220\n" \
  "v_sub_f32 v195, v195, v220\n" \
  "v_sub_f32 v196, v196, v220\n" \
  "v_sub_f32 v197, v197, v220\n" \
  "v_sub_f32 v198, v198, v220\n" \
  "v_sub_f32 v199, v199, v220\n" \
  "v_sub_f32 v200, v200, v220\n" \
  "v_sub_f32 v201, v201, v220\n" \
  "v_sub_f32 v202, v202, v220\n" \
  "v_sub_f32 v203, v203, v220\n" \
  "v_sub_f32 v204, v204, v220\n" \
  "v_sub_f32 v205, v205, v220\n" \
  "v_sub_f32 v206, v206, v220\n" \
  "v_sub_f32 v207, v207, v220\n" \
  "v_exp_f32 v192, v192\n" \
  "v_exp_f32 v193, v193\n" \
  "v_exp_f32 v194, v194\n" \
  "v_exp_f32 v195, v195\n" \
  "v_exp_f32 v196, v196\n" \
  "v_exp_f32 v197, v197\n" \
  "v_exp_f32 v198, v198\n" \
  "v_exp_f32 v199, v199\n" \
  "v_exp_f32 v200, v200\n" \
  "v_exp_f32 v201, v201\n" \
  "v_exp_f32 v202, v202\n" \
  "v_exp_f32 v203, v203\n" \
  "v_exp_f32 v204, v204\n" \
  "v_exp_f32 v205, v205\n" \
  "v_exp_f32 v206, v206\n" \
  "v_exp_f32 v207, v207\n" \
  "s_nop 1\n" \
  "v_smfmac_f32_32x32x32_bf16 v[240:255], v[160:163], v[192:199], v0 abid:1\n" \
  "v_smfmac_f32_32x32x32_bf16 v[240:255], v[164:167], v[200:207], v0 abid:1\n" \
  "v_mfma_f32_32x32x16_bf16 v[192:207], v[116:119], v[216:219], 0\n" \
  "s_nop 11\n" \
  "v_sub_f32 v192, v192, v220\n" \
  "v_sub_f32 v193, v193, v220\n" \
  "v_sub_f32 v194, v194, v220\n" \
  "v_sub_f32 v195, v195, v220\n" \
  "v_sub_f32 v196, v196, v220\n" \
  "v_sub_f32 v197, v197, v220\n" \
  "v_sub_f32 v198, v198, v220\n" \
  "v_sub_f32 v199, v199, v220\n" \
  "v_sub_f32 v200, v200, v220\n" \
  "v_sub_f32 v201, v201, v220\n" \
  "v_sub_f32 v202, v202, v220\n" \
  "v_sub_f32 v203, v203, v220\n" \
  "v_sub_f32 v204, v204, v220\n" \
  "v_sub_f32 v205, v205, v220\n" \
  "v_sub_f32 v206, v206, v220\n" \
  "v_sub_f32 v207, v207, v220\n" \
  "v_exp_f32 v192, v192\n" \
  "v_exp_f32 v193, v193\n" \
  "v_exp_f32 v194, v194\n" \
  "v_exp_f32 v195, v195\n" \
  "v_exp_f32 v196, v196\n" \
  "v_exp_f32 v197, v197\n" \
  "v_exp_f32 v198, v198\n" \
  "v_exp_f32 v199, v199\n" \
  "v_exp_f32 v200, v200\n" \
  "v_exp_f32 v201, v201\n" \
  "v_exp_f32 v202, v202\n" \
  "v_exp_f32 v203, v203\n" \
  "v_exp_f32 v204, v204\n" \
  "v_exp_f32 v205, v205\n" \
  "v_exp_f32 v206, v206\n" \
  "v_exp_f32 v207, v207\n" \
  "s_nop 1\n" \
  "v_smfmac_f32_32x32x32_bf16 v[240:255], v[168:171], v[192:199], v0 abid:1\n" \
  "v_smfmac_f32_32x32x32_bf16 v[240:255], v[172:175], v[200:207], v0 abid:1\n" \
  "v_mfma_f32_32x32x16_bf16 v[192:207], v[120:123], v[216:219], 0\n" \
  "s_nop 11\n" \
  "v_sub_f32 v192, v192, v220\n" \
  "v_sub_f32 v193, v193, v220\n" \
  "v_sub_f32 v194, v194, v220\n" \
  "v_sub_f32 v195, v195, v220\n" \
  "v_sub_f32 v196, v196, v220\n" \
  "v_sub_f32 v197, v197, v220\n" \
  "v_sub_f32 v198, v198, v220\n" \
  "v_sub_f32 v199, v199, v220\n" \
  "v_sub_f32 v200, v200, v220\n" \
  "v_sub_f32 v201, v201, v220\n" \
  "v_sub_f32 v202, v202, v220\n" \
  "v_sub_f32 v203, v203, v220\n" \
  "v_sub_f32 v204, v204, v220\n" \
  "v_sub_f32 v205, v205, v220\n" \
  "v_sub_f32 v206, v206, v220\n" \
  "v_sub_f32 v207, v207, v220\n" \
  "v_exp_f32 v192, v192\n" \
  "v_exp_f32 v193, v193\n" \
  "v_exp_f32 v194, v194\n" \
  "v_exp_f32 v195, v195\n" \
  "v_exp_f32 v196, v196\n" \
  "v_exp_f32 v197, v197\n" \
  "v_exp_f32 v198, v198\n" \
  "v_exp_f32 v199, v199\n" \
  "v_exp_f32 v200, v200\n" \
  "v_exp_f32 v201, v201\n" \
  "v_exp_f32 v202, v202\n" \
  "v_exp_f32 v203, v203\n" \
  "v_exp_f32 v204, v204\n" \
  "v_exp_f32 v205, v205\n" \
  "v_exp_f32 v206, v206\n" \
  "v_exp_f32 v207, v207\n" \
  "s_nop 1\n" \
  "v_smfmac_f32_32x32x32_bf16 v[240:255], v[176:179], v[192:199], v0 abid:1\n" \
  "v_smfmac_f32_32x32x32_bf16 v[240:255], v[180:183], v[200:207], v0 abid:1\n" \
  "v_mfma_f32_32x32x16_bf16 v[192:207], v[124:127], v[216:219], 0\n" \
  "s_nop 11\n" \
  "v_sub_f32 v192, v192, v220\n" \
  "v_sub_f32 v193, v193, v220\n" \
  "v_sub_f32 v194, v194, v220\n" \
  "v_sub_f32 v195, v195, v220\n" \
  "v_sub_f32 v196, v196, v220\n" \
  "v_sub_f32 v197, v197, v220\n" \
  "v_sub_f32 v198, v198, v220\n" \
  "v_sub_f32 v199, v199, v220\n" \
  "v_sub_f32 v200, v200, v220\n" \
  "v_sub_f32 v201, v201, v220\n" \
  "v_sub_f32 v202, v202, v220\n" \
  "v_sub_f32 v203, v203, v220\n" \
  "v_sub_f32 v204, v204, v220\n" \
  "v_sub_f32 v205, v205, v220\n" \
  "v_sub_f32 v206, v206, v220\n" \
  "v_sub_f32 v207, v207, v220\n" \
  "v_exp_f32 v192, v192\n" \
  "v_exp_f32 v193, v193\n" \
  "v_exp_f32 v194, v194\n" \
  "v_exp_f32 v195, v195\n" \
  "v_exp_f32 v196, v196\n" \
  "v_exp_f32 v197, v197\n" \
  "v_exp_f32 v198, v198\n" \
  "v_exp_f32 v199, v199\n" \
  "v_exp_f32 v200, v200\n" \
  "v_exp_f32 v201, v201\n" \
  "v_exp_f32 v202, v202\n" \
  "v_exp_f32 v203, v203\n" \
  "v_exp_f32 v204, v204\n" \
  "v_exp_f32 v205, v205\n" \
  "v_exp_f32 v206, v206\n" \
  "v_exp_f32 v207, v207\n" \
  "s_nop 1\n" \
  "v_smfmac_f32_32x32x32_bf16 v[240:255], v[184:187], v[192:199], v0 abid:1\n" \
  "v_smfmac_f32_32x32x32_bf16 v[240:255], v[188:191], v[200:207], v0 abid:1\n" \
  "s_nop 11\n" \
  "ds_write_b128 v1, v[240:243] offset:0\n" \
  "ds_write_b128 v1, v[244:247] offset:1024\n" \
  "ds_write_b32 v2, v248 offset:0\n" \
  "s_waitcnt lgkmcnt(0)\n" \
  "s_barrier\n" \
  "s_cmp_lt_u32 s22, 4\n" \
  "s_cbranch_scc0 82f\n" \
  "ds_read_b128 v[20:23], v4 offset:0\n" \
  "ds_read_b64 v[192:193], v3 offset:0\n" \
  "ds_read_b64 v[194:195], v3 offset:2048\n" \
  "ds_read_b64 v[196:197], v3 offset:4096\n" \
  "ds_read_b64 v[198:199], v3 offset:6144\n" \
  "ds_read_b64 v[200:201], v3 offset:8192\n" \
  "ds_read_b64 v[202:203], v3 offset:10240\n" \
  "ds_read_b64 v[204:205], v3 offset:12288\n" \
  "ds_read_b64 v[206:207], v3 offset:14336\n" \
  "s_waitcnt lgkmcnt(0)\n" \
  "v_pk_add_f32 v[20:21], v[20:21], v[22:23]\n" \
  "v_pk_add_f32 v[192:193], v[192:193], v[200:201]\n" \
  "v_pk_add_f32 v[194:195], v[194:195], v[202:203]\n" \
  "v_add_f32 v20, v20, v21\n" \
  "s_nop 0\n" \
  "v_pk_add_f32 v[196:197], v[196:197], v[204:205]\n" \
  "v_pk_add_f32 v[198:199], v[198:199], v[206:207]\n" \
  "v_add_f32_dpp v20, v20, v20 quad_perm:[1,0,3,2] row_mask:0xf bank_mask:0xf\n" \
  "v_pk_add_f32 v[192:193], v[192:193], v[196:197]\n" \
  "v_pk_add_f32 v[194:195], v[194:195], v[198:199]\n" \
  "v_rcp_f32 v21, v20\n" \
  "v_pk_add_f32 v[192:193], v[192:193], v[194:195]\n" \
  "s_nop 0\n" \
  "v_mul_f32 v192, v192, v21\n" \
  "v_mul_f32 v193, v193, v21\n" \
  "v_cvt_pk_bf16_f32 v192, v192, v193\n" \
  "ds_write_b32 v5, v192 offset:0\n" \
  "82:\n" \
  "s_lshl_b32 s81, s80, 11\n" \
  "v_add_u32 v7, s81, v8\n" \
  "ds_read_b128 v[216:219], v7 offset:33792\n" \
  "v_mov_b32 v220, 0xff7fffff\n" \
  "s_waitcnt lgkmcnt(0)\n" \
  "v_mfma_f32_32x32x16_bf16 v[192:207], v[96:99], v[216:219], 0\n" \
  "s_nop 11\n" \
  "v_max3_f32 v220, v220, v192, v193\n" \
  "v_max3_f32 v220, v220, v194, v195\n" \
  "v_max3_f32 v220, v220, v196, v197\n" \
  "v_max3_f32 v220, v220, v198, v199\n" \
  "v_max3_f32 v220, v220, v200, v201\n" \
  "v_max3_f32 v220, v220, v202, v203\n" \
  "v_max3_f32 v220, v220, v204, v205\n" \
  "v_max3_f32 v220, v220, v206, v207\n" \
  "v_mfma_f32_32x32x16_bf16 v[192:207], v[100:103], v[216:219], 0\n" \
  "s_nop 11\n" \
  "v_max3_f32 v220, v220, v192, v193\n" \
  "v_max3_f32 v220, v220, v194, v195\n" \
  "v_max3_f32 v220, v220, v196, v197\n" \
  "v_max3_f32 v220, v220, v198, v199\n" \
  "v_max3_f32 v220, v220, v200, v201\n" \
  "v_max3_f32 v220, v220, v202, v203\n" \
  "v_max3_f32 v220, v220, v204, v205\n" \
  "v_max3_f32 v220, v220, v206, v207\n" \
  "v_mfma_f32_32x32x16_bf16 v[192:207], v[104:107], v[216:219], 0\n" \
  "s_nop 11\n" \
  "v_max3_f32 v220, v220, v192, v193\n" \
  "v_max3_f32 v220, v220, v194, v195\n" \
  "v_max3_f32 v220, v220, v196, v197\n" \
  "v_max3_f32 v220, v220, v198, v199\n" \
  "v_max3_f32 v220, v220, v200, v201\n" \
  "v_max3_f32 v220, v220, v202, v203\n" \
  "v_max3_f32 v220, v220, v204, v205\n" \
  "v_max3_f32 v220, v220, v206, v207\n" \
  "v_mfma_f32_32x32x16_bf16 v[192:207], v[108:111], v[216:219], 0\n" \
  "s_nop 11\n" \
  "v_max3_f32 v220, v220, v192, v193\n" \
  "v_max3_f32 v220, v220, v194, v195\n" \
  "v_max3_f32 v220, v220, v196, v197\n" \
  "v_max3_f32 v220, v220, v198, v199\n" \
  "v_max3_f32 v220, v220, v200, v201\n" \
  "v_max3_f32 v220, v220, v202, v203\n" \
  "v_max3_f32 v220, v220, v204, v205\n" \
  "v_max3_f32 v220, v220, v206, v207\n" \
  "v_mfma_f32_32x32x16_bf16 v[192:207], v[112:115], v[216:219], 0\n" \
  "s_nop 11\n" \
  "v_max3_f32 v220, v220, v192, v193\n" \
  "v_max3_f32 v220, v220, v194, v195\n" \
  "v_max3_f32 v220, v220, v196, v197\n" \
  "v_max3_f32 v220, v220, v198, v199\n" \
  "v_max3_f32 v220, v220, v200, v201\n" \
  "v_max3_f32 v220, v220, v202, v203\n" \
  "v_max3_f32 v220, v220, v204, v205\n" \
  "v_max3_f32 v220, v220, v206, v207\n" \
  "v_mfma_f32_32x32x16_bf16 v[192:207], v[116:119], v[216:219], 0\n" \
  "s_nop 11\n" \
  "v_max3_f32 v220, v220, v192, v193\n" \
  "v_max3_f32 v220, v220, v194, v195\n" \
  "v_max3_f32 v220, v220, v196, v197\n" \
  "v_max3_f32 v220, v220, v198, v199\n" \
  "v_max3_f32 v220, v220, v200, v201\n" \
  "v_max3_f32 v220, v220, v202, v203\n" \
  "v_max3_f32 v220, v220, v204, v205\n" \
  "v_max3_f32 v220, v220, v206, v207\n" \
  "v_mfma_f32_32x32x16_bf16 v[192:207], v[120:123], v[216:219], 0\n" \
  "s_nop 11\n" \
  "v_max3_f32 v220, v220, v192, v193\n" \
  "v_max3_f32 v220, v220, v194, v195\n" \
  "v_max3_f32 v220, v220, v196, v197\n" \
  "v_max3_f32 v220, v220, v198, v199\n" \
  "v_max3_f32 v220, v220, v200, v201\n" \
  "v_max3_f32 v220, v220, v202, v203\n" \
  "v_max3_f32 v220, v220, v204, v205\n" \
  "v_max3_f32 v220, v220, v206, v207\n" \
  "v_mfma_f32_32x32x16_bf16 v[192:207], v[124:127], v[216:219], 0\n" \
  "s_nop 11\n" \
  "v_max3_f32 v220, v220, v192, v193\n" \
  "v_max3_f32 v220, v220, v194, v195\n" \
  "v_max3_f32 v220, v220, v196, v197\n" \
  "v_max3_f32 v220, v220, v198, v199\n" \
  "v_max3_f32 v220, v220, v200, v201\n" \
  "v_max3_f32 v220, v220, v202, v203\n" \
  "v_max3_f32 v220, v220, v204, v205\n" \
  "v_max3_f32 v220, v220, v206, v207\n" \
  "v_mov_b32 v221, v220\n" \
  "s_nop 1\n" \
  "v_permlane32_swap_b32 v220, v221\n" \
  "v_max_f32 v220, v220, v221\n" \
  "s_waitcnt lgkmcnt(0)\n" \
  "s_barrier\n" \
  "ds_write_b32 v2, v220 offset:2048\n" \
  "s_waitcnt lgkmcnt(0)\n" \
  "s_barrier\n" \
  "ds_read_b128 v[192:195], v9 offset:44032\n" \
  "ds_read_b128 v[196:199], v9 offset:44048\n" \
  "v_mfma_f32_32x32x8_f16 v[240:255], v[10:11], v[10:11], 0\n" \
  "s_waitcnt lgkmcnt(0)\n" \
  "s_barrier\n" \
  "v_max3_f32 v220, v192, v193, v194\n" \
  "v_max3_f32 v220, v220, v195, v196\n" \
  "v_max3_f32 v220, v220, v197, v198\n" \
  "v_max3_f32 v220, v220, v199, v199\n" \
  "v_mfma_f32_32x32x16_bf16 v[192:207], v[96:99], v[216:219], 0\n" \
  "s_nop 11\n" \
  "v_sub_f32 v192, v192, v220\n" \
  "v_sub_f32 v193, v193, v220\n" \
  "v_sub_f32 v194, v194, v220\n" \
  "v_sub_f32 v195, v195, v220\n" \
  "v_sub_f32 v196, v196, v220\n" \
  "v_sub_f32 v197, v197, v220\n" \
  "v_sub_f32 v198, v198, v220\n" \
  "v_sub_f32 v199, v199, v220\n" \
  "v_sub_f32 v200, v200, v220\n" \
  "v_sub_f32 v201, v201, v220\n" \
  "v_sub_f32 v202, v202, v220\n" \
  "v_sub_f32 v203, v203, v220\n" \
  "v_sub_f32 v204, v204, v220\n" \
  "v_sub_f32 v205, v205, v220\n" \
  "v_sub_f32 v206, v206, v220\n" \
  "v_sub_f32 v207, v207, v220\n" \
  "v_exp_f32 v192, v192\n" \
  "v_exp_f32 v193, v193\n" \
  "v_exp_f32 v194, v194\n" \
  "v_exp_f32 v195, v195\n" \
  "v_exp_f32 v196, v196\n" \
  "v_exp_f32 v197, v197\n" \
  "v_exp_f32 v198, v198\n" \
  "v_exp_f32 v199, v199\n" \
  "v_exp_f32 v200, v200\n" \
  "v_exp_f32 v201, v201\n" \
  "v_exp_f32 v202, v202\n" \
  "v_exp_f32 v203, v203\n" \
  "v_exp_f32 v204, v204\n" \
  "v_exp_f32 v205, v205\n" \
  "v_exp_f32 v206, v206\n" \
  "v_exp_f32 v207, v207\n" \
  "s_nop 1\n" \
  "v_smfmac_f32_32x32x32_bf16 v[240:255], v[128:131], v[192:199], v0 abid:1\n" \
  "v_smfmac_f32_32x32x32_bf16 v[240:255], v[132:135], v[200:207], v0 abid:1\n" \
  "v_mfma_f32_32x32x16_bf16 v[192:207], v[100:103], v[216:219], 0\n" \
  "s_nop 11\n" \
  "v_sub_f32 v192, v192, v220\n" \
  "v_sub_f32 v193, v193, v220\n" \
  "v_sub_f32 v194, v194, v220\n" \
  "v_sub_f32 v195, v195, v220\n" \
  "v_sub_f32 v196, v196, v220\n" \
  "v_sub_f32 v197, v197, v220\n" \
  "v_sub_f32 v198, v198, v220\n" \
  "v_sub_f32 v199, v199, v220\n" \
  "v_sub_f32 v200, v200, v220\n" \
  "v_sub_f32 v201, v201, v220\n" \
  "v_sub_f32 v202, v202, v220\n" \
  "v_sub_f32 v203, v203, v220\n" \
  "v_sub_f32 v204, v204, v220\n" \
  "v_sub_f32 v205, v205, v220\n" \
  "v_sub_f32 v206, v206, v220\n" \
  "v_sub_f32 v207, v207, v220\n" \
  "v_exp_f32 v192, v192\n" \
  "v_exp_f32 v193, v193\n" \
  "v_exp_f32 v194, v194\n" \
  "v_exp_f32 v195, v195\n" \
  "v_exp_f32 v196, v196\n" \
  "v_exp_f32 v197, v197\n" \
  "v_exp_f32 v198, v198\n" \
  "v_exp_f32 v199, v199\n" \
  "v_exp_f32 v200, v200\n" \
  "v_exp_f32 v201, v201\n" \
  "v_exp_f32 v202, v202\n" \
  "v_exp_f32 v203, v203\n" \
  "v_exp_f32 v204, v204\n" \
  "v_exp_f32 v205, v205\n" \
  "v_exp_f32 v206, v206\n" \
  "v_exp_f32 v207, v207\n" \
  "s_nop 1\n" \
  "v_smfmac_f32_32x32x32_bf16 v[240:255], v[136:139], v[192:199], v0 abid:1\n" \
  "v_smfmac_f32_32x32x32_bf16 v[240:255], v[140:143], v[200:207], v0 abid:1\n" \
  "v_mfma_f32_32x32x16_bf16 v[192:207], v[104:107], v[216:219], 0\n" \
  "s_nop 11\n" \
  "v_sub_f32 v192, v192, v220\n" \
  "v_sub_f32 v193, v193, v220\n" \
  "v_sub_f32 v194, v194, v220\n" \
  "v_sub_f32 v195, v195, v220\n" \
  "v_sub_f32 v196, v196, v220\n" \
  "v_sub_f32 v197, v197, v220\n" \
  "v_sub_f32 v198, v198, v220\n" \
  "v_sub_f32 v199, v199, v220\n" \
  "v_sub_f32 v200, v200, v220\n" \
  "v_sub_f32 v201, v201, v220\n" \
  "v_sub_f32 v202, v202, v220\n" \
  "v_sub_f32 v203, v203, v220\n" \
  "v_sub_f32 v204, v204, v220\n" \
  "v_sub_f32 v205, v205, v220\n" \
  "v_sub_f32 v206, v206, v220\n" \
  "v_sub_f32 v207, v207, v220\n" \
  "v_exp_f32 v192, v192\n" \
  "v_exp_f32 v193, v193\n" \
  "v_exp_f32 v194, v194\n" \
  "v_exp_f32 v195, v195\n" \
  "v_exp_f32 v196, v196\n" \
  "v_exp_f32 v197, v197\n" \
  "v_exp_f32 v198, v198\n" \
  "v_exp_f32 v199, v199\n" \
  "v_exp_f32 v200, v200\n" \
  "v_exp_f32 v201, v201\n" \
  "v_exp_f32 v202, v202\n" \
  "v_exp_f32 v203, v203\n" \
  "v_exp_f32 v204, v204\n" \
  "v_exp_f32 v205, v205\n" \
  "v_exp_f32 v206, v206\n" \
  "v_exp_f32 v207, v207\n" \
  "s_nop 1\n" \
  "v_smfmac_f32_32x32x32_bf16 v[240:255], v[144:147], v[192:199], v0 abid:1\n" \
  "v_smfmac_f32_32x32x32_bf16 v[240:255], v[148:151], v[200:207], v0 abid:1\n" \
  "v_mfma_f32_32x32x16_bf16 v[192:207], v[108:111], v[216:219], 0\n" \
  "s_nop 11\n" \
  "v_sub_f32 v192, v192, v220\n" \
  "v_sub_f32 v193, v193, v220\n" \
  "v_sub_f32 v194, v194, v220\n" \
  "v_sub_f32 v195, v195, v220\n" \
  "v_sub_f32 v196, v196, v220\n" \
  "v_sub_f32 v197, v197, v220\n" \
  "v_sub_f32 v198, v198, v220\n" \
  "v_sub_f32 v199, v199, v220\n" \
  "v_sub_f32 v200, v200, v220\n" \
  "v_sub_f32 v201, v201, v220\n" \
  "v_sub_f32 v202, v202, v220\n" \
  "v_sub_f32 v203, v203, v220\n" \
  "v_sub_f32 v204, v204, v220\n" \
  "v_sub_f32 v205, v205, v220\n" \
  "v_sub_f32 v206, v206, v220\n" \
  "v_sub_f32 v207, v207, v220\n" \
  "v_exp_f32 v192, v192\n" \
  "v_exp_f32 v193, v193\n" \
  "v_exp_f32 v194, v194\n" \
  "v_exp_f32 v195, v195\n" \
  "v_exp_f32 v196, v196\n" \
  "v_exp_f32 v197, v197\n" \
  "v_exp_f32 v198, v198\n" \
  "v_exp_f32 v199, v199\n" \
  "v_exp_f32 v200, v200\n" \
  "v_exp_f32 v201, v201\n" \
  "v_exp_f32 v202, v202\n" \
  "v_exp_f32 v203, v203\n" \
  "v_exp_f32 v204, v204\n" \
  "v_exp_f32 v205, v205\n" \
  "v_exp_f32 v206, v206\n" \
  "v_exp_f32 v207, v207\n" \
  "s_nop 1\n" \
  "v_smfmac_f32_32x32x32_bf16 v[240:255], v[152:155], v[192:199], v0 abid:1\n" \
  "v_smfmac_f32_32x32x32_bf16 v[240:255], v[156:159], v[200:207], v0 abid:1\n" \
  "v_mfma_f32_32x32x16_bf16 v[192:207], v[112:115], v[216:219], 0\n" \
  "s_nop 11\n" \
  "v_sub_f32 v192, v192, v220\n" \
  "v_sub_f32 v193, v193, v220\n" \
  "v_sub_f32 v194, v194, v220\n" \
  "v_sub_f32 v195, v195, v220\n" \
  "v_sub_f32 v196, v196, v220\n" \
  "v_sub_f32 v197, v197, v220\n" \
  "v_sub_f32 v198, v198, v220\n" \
  "v_sub_f32 v199, v199, v220\n" \
  "v_sub_f32 v200, v200, v220\n" \
  "v_sub_f32 v201, v201, v220\n" \
  "v_sub_f32 v202, v202, v220\n" \
  "v_sub_f32 v203, v203, v220\n" \
  "v_sub_f32 v204, v204, v220\n" \
  "v_sub_f32 v205, v205, v220\n" \
  "v_sub_f32 v206, v206, v220\n" \
  "v_sub_f32 v207, v207, v220\n" \
  "v_exp_f32 v192, v192\n" \
  "v_exp_f32 v193, v193\n" \
  "v_exp_f32 v194, v194\n" \
  "v_exp_f32 v195, v195\n" \
  "v_exp_f32 v196, v196\n" \
  "v_exp_f32 v197, v197\n" \
  "v_exp_f32 v198, v198\n" \
  "v_exp_f32 v199, v199\n" \
  "v_exp_f32 v200, v200\n" \
  "v_exp_f32 v201, v201\n" \
  "v_exp_f32 v202, v202\n" \
  "v_exp_f32 v203, v203\n" \
  "v_exp_f32 v204, v204\n" \
  "v_exp_f32 v205, v205\n" \
  "v_exp_f32 v206, v206\n" \
  "v_exp_f32 v207, v207\n" \
  "s_nop 1\n" \
  "v_smfmac_f32_32x32x32_bf16 v[240:255], v[160:163], v[192:199], v0 abid:1\n" \
  "v_smfmac_f32_32x32x32_bf16 v[240:255], v[164:167], v[200:207], v0 abid:1\n" \
  "v_mfma_f32_32x32x16_bf16 v[192:207], v[116:119], v[216:219], 0\n" \
  "s_nop 11\n" \
  "v_sub_f32 v192, v192, v220\n" \
  "v_sub_f32 v193, v193, v220\n" \
  "v_sub_f32 v194, v194, v220\n" \
  "v_sub_f32 v195, v195, v220\n" \
  "v_sub_f32 v196, v196, v220\n" \
  "v_sub_f32 v197, v197, v220\n" \
  "v_sub_f32 v198, v198, v220\n" \
  "v_sub_f32 v199, v199, v220\n" \
  "v_sub_f32 v200, v200, v220\n" \
  "v_sub_f32 v201, v201, v220\n" \
  "v_sub_f32 v202, v202, v220\n" \
  "v_sub_f32 v203, v203, v220\n" \
  "v_sub_f32 v204, v204, v220\n" \
  "v_sub_f32 v205, v205, v220\n" \
  "v_sub_f32 v206, v206, v220\n" \
  "v_sub_f32 v207, v207, v220\n" \
  "v_exp_f32 v192, v192\n" \
  "v_exp_f32 v193, v193\n" \
  "v_exp_f32 v194, v194\n" \
  "v_exp_f32 v195, v195\n" \
  "v_exp_f32 v196, v196\n" \
  "v_exp_f32 v197, v197\n" \
  "v_exp_f32 v198, v198\n" \
  "v_exp_f32 v199, v199\n" \
  "v_exp_f32 v200, v200\n" \
  "v_exp_f32 v201, v201\n" \
  "v_exp_f32 v202, v202\n" \
  "v_exp_f32 v203, v203\n" \
  "v_exp_f32 v204, v204\n" \
  "v_exp_f32 v205, v205\n" \
  "v_exp_f32 v206, v206\n" \
  "v_exp_f32 v207, v207\n" \
  "s_nop 1\n" \
  "v_smfmac_f32_32x32x32_bf16 v[240:255], v[168:171], v[192:199], v0 abid:1\n" \
  "v_smfmac_f32_32x32x32_bf16 v[240:255], v[172:175], v[200:207], v0 abid:1\n" \
  "v_mfma_f32_32x32x16_bf16 v[192:207], v[120:123], v[216:219], 0\n" \
  "s_nop 11\n" \
  "v_sub_f32 v192, v192, v220\n" \
  "v_sub_f32 v193, v193, v220\n" \
  "v_sub_f32 v194, v194, v220\n" \
  "v_sub_f32 v195, v195, v220\n" \
  "v_sub_f32 v196, v196, v220\n" \
  "v_sub_f32 v197, v197, v220\n" \
  "v_sub_f32 v198, v198, v220\n" \
  "v_sub_f32 v199, v199, v220\n" \
  "v_sub_f32 v200, v200, v220\n" \
  "v_sub_f32 v201, v201, v220\n" \
  "v_sub_f32 v202, v202, v220\n" \
  "v_sub_f32 v203, v203, v220\n" \
  "v_sub_f32 v204, v204, v220\n" \
  "v_sub_f32 v205, v205, v220\n" \
  "v_sub_f32 v206, v206, v220\n" \
  "v_sub_f32 v207, v207, v220\n" \
  "v_exp_f32 v192, v192\n" \
  "v_exp_f32 v193, v193\n" \
  "v_exp_f32 v194, v194\n" \
  "v_exp_f32 v195, v195\n" \
  "v_exp_f32 v196, v196\n" \
  "v_exp_f32 v197, v197\n" \
  "v_exp_f32 v198, v198\n" \
  "v_exp_f32 v199, v199\n" \
  "v_exp_f32 v200, v200\n" \
  "v_exp_f32 v201, v201\n" \
  "v_exp_f32 v202, v202\n" \
  "v_exp_f32 v203, v203\n" \
  "v_exp_f32 v204, v204\n" \
  "v_exp_f32 v205, v205\n" \
  "v_exp_f32 v206, v206\n" \
  "v_exp_f32 v207, v207\n" \
  "s_nop 1\n" \
  "v_smfmac_f32_32x32x32_bf16 v[240:255], v[176:179], v[192:199], v0 abid:1\n" \
  "v_smfmac_f32_32x32x32_bf16 v[240:255], v[180:183], v[200:207], v0 abid:1\n" \
  "v_mfma_f32_32x32x16_bf16 v[192:207], v[124:127], v[216:219], 0\n" \
  "s_nop 11\n" \
  "v_sub_f32 v192, v192, v220\n" \
  "v_sub_f32 v193, v193, v220\n" \
  "v_sub_f32 v194, v194, v220\n" \
  "v_sub_f32 v195, v195, v220\n" \
  "v_sub_f32 v196, v196, v220\n" \
  "v_sub_f32 v197, v197, v220\n" \
  "v_sub_f32 v198, v198, v220\n" \
  "v_sub_f32 v199, v199, v220\n" \
  "v_sub_f32 v200, v200, v220\n" \
  "v_sub_f32 v201, v201, v220\n" \
  "v_sub_f32 v202, v202, v220\n" \
  "v_sub_f32 v203, v203, v220\n" \
  "v_sub_f32 v204, v204, v220\n" \
  "v_sub_f32 v205, v205, v220\n" \
  "v_sub_f32 v206, v206, v220\n" \
  "v_sub_f32 v207, v207, v220\n" \
  "v_exp_f32 v192, v192\n" \
  "v_exp_f32 v193, v193\n" \
  "v_exp_f32 v194, v194\n" \
  "v_exp_f32 v195, v195\n" \
  "v_exp_f32 v196, v196\n" \
  "v_exp_f32 v197, v197\n" \
  "v_exp_f32 v198, v198\n" \
  "v_exp_f32 v199, v199\n" \
  "v_exp_f32 v200, v200\n" \
  "v_exp_f32 v201, v201\n" \
  "v_exp_f32 v202, v202\n" \
  "v_exp_f32 v203, v203\n" \
  "v_exp_f32 v204, v204\n" \
  "v_exp_f32 v205, v205\n" \
  "v_exp_f32 v206, v206\n" \
  "v_exp_f32 v207, v207\n" \
  "s_nop 1\n" \
  "v_smfmac_f32_32x32x32_bf16 v[240:255], v[184:187], v[192:199], v0 abid:1\n" \
  "v_smfmac_f32_32x32x32_bf16 v[240:255], v[188:191], v[200:207], v0 abid:1\n" \
  "s_nop 11\n" \
  "ds_write_b128 v1, v[240:243] offset:16384\n" \
  "ds_write_b128 v1, v[244:247] offset:17408\n" \
  "ds_write_b32 v2, v248 offset:2048\n" \
  "s_waitcnt lgkmcnt(0)\n" \
  "s_barrier\n" \
  "s_cmp_lt_u32 s22, 4\n" \
  "s_cbranch_scc0 83f\n" \
  "ds_read_b128 v[20:23], v4 offset:2048\n" \
  "ds_read_b64 v[192:193], v3 offset:16384\n" \
  "ds_read_b64 v[194:195], v3 offset:18432\n" \
  "ds_read_b64 v[196:197], v3 offset:20480\n" \
  "ds_read_b64 v[198:199], v3 offset:22528\n" \
  "ds_read_b64 v[200:201], v3 offset:24576\n" \
  "ds_read_b64 v[202:203], v3 offset:26624\n" \
  "ds_read_b64 v[204:205], v3 offset:28672\n" \
  "ds_read_b64 v[206:207], v3 offset:30720\n" \
  "s_waitcnt lgkmcnt(0)\n" \
  "v_pk_add_f32 v[20:21], v[20:21], v[22:23]\n" \
  "v_pk_add_f32 v[192:193], v[192:193], v[200:201]\n" \
  "v_pk_add_f32 v[194:195], v[194:195], v[202:203]\n" \
  "v_add_f32 v20, v20, v21\n" \
  "s_nop 0\n" \
  "v_pk_add_f32 v[196:197], v[196:197], v[204:205]\n" \
  "v_pk_add_f32 v[198:199], v[198:199], v[206:207]\n" \
  "v_add_f32_dpp v20, v20, v20 quad_perm:[1,0,3,2] row_mask:0xf bank_mask:0xf\n" \
  "v_pk_add_f32 v[192:193], v[192:193], v[196:197]\n" \
  "v_pk_add_f32 v[194:195], v[194:195], v[198:199]\n" \
  "v_rcp_f32 v21, v20\n" \
  "v_pk_add_f32 v[192:193], v[192:193], v[194:195]\n" \
  "s_nop 0\n" \
  "v_mul_f32 v192, v192, v21\n" \
  "v_mul_f32 v193, v193, v21\n" \
  "v_cvt_pk_bf16_f32 v192, v192, v193\n" \
  "ds_write_b32 v5, v192 offset:128\n" \
  "83:\n" \
  "s_waitcnt lgkmcnt(0)\n" \
  "s_barrier\n" \
  "ds_read_b128 v[192:195], v6 offset:0\n" \
  "ds_read_b128 v[196:199], v6 offset:64\n" \
  "ds_read_b128 v[200:203], v6 offset:128\n" \
  "ds_read_b128 v[204:207], v6 offset:192\n" \
  "v_mfma_f32_16x16x16_f16 v[224:227], v[10:11], v[10:11], 0\n" \
  "v_mfma_f32_16x16x16_f16 v[228:231], v[10:11], v[10:11], 0\n" \
  "s_waitcnt lgkmcnt(0)\n" \
  "v_smfmac_f32_16x16x64_bf16 v[224:227], v[192:195], v[32:39], v0\n" \
  "v_smfmac_f32_16x16x64_bf16 v[228:231], v[192:195], v[64:71], v0\n" \
  "v_smfmac_f32_16x16x64_bf16 v[224:227], v[196:199], v[40:47], v0\n" \
  "v_smfmac_f32_16x16x64_bf16 v[228:231], v[196:199], v[72:79], v0\n" \
  "v_smfmac_f32_16x16x64_bf16 v[224:227], v[200:203], v[48:55], v0\n" \
  "v_smfmac_f32_16x16x64_bf16 v[228:231], v[200:203], v[80:87], v0\n" \
  "v_smfmac_f32_16x16x64_bf16 v[224:227], v[204:207], v[56:63], v0\n" \
  "v_smfmac_f32_16x16x64_bf16 v[228:231], v[204:207], v[88:95], v0\n" \
  "s_nop 7\n" \
  "s_cmp_eq_u32 s80, 3\n" \
  "s_cbranch_scc0 18f\n" \
  "v_mov_b64 v[12:13], v[224:225]\n" \
  "v_mov_b64 v[14:15], v[226:227]\n" \
  "v_mov_b64 v[16:17], v[228:229]\n" \
  "v_mov_b64 v[18:19], v[230:231]\n" \
  "s_branch 17f\n" \
  "18:\n" \
  "v_cvt_pk_bf16_f32 v20, v224, v225\n" \
  "v_cvt_pk_bf16_f32 v21, v226, v227\n" \
  "v_cvt_pk_bf16_f32 v22, v228, v229\n" \
  "v_cvt_pk_bf16_f32 v23, v230, v231\n" \
  "v_min_u32 v20, -2, v20\n" \
  "v_min_u32 v21, -2, v21\n" \
  "v_min_u32 v22, -2, v22\n" \
  "v_min_u32 v23, -2, v23\n" \
  "s_cmp_eq_u32 s80, 1\n" \
  "s_cselect_b32 s81, s56, s55\n" \
  "s_cmp_eq_u32 s80, 2\n" \
  "s_cselect_b32 s81, s57, s81\n" \
  "buffer_store_dwordx4 v[20:23], v4, s[28:31], s81 offen sc1\n" \
  "s_nop 1\n" \
  "17:\n" \
  "s_add_u32 s80, s80, 1\n" \
  "s_cmp_lt_u32 s80, 4\n" \
  "s_cbranch_scc1 15b\n" \
  "s_waitcnt lgkmcnt(0)\n" \
  "s_barrier\n" \
  "v_mbcnt_lo_u32_b32 v20, -1, 0\n" \
  "v_mbcnt_hi_u32_b32 v20, -1, v20\n" \
  "v_lshrrev_b32 v21, 4, v20\n" \
  "v_and_b32 v22, 12, v20\n" \
  "v_lshlrev_b32 v22, 10, v22\n" \
  "v_lshl_add_u32 v22, v21, 6, v22\n" \
  "v_and_b32 v21, 3, v20\n" \
  "v_lshl_add_u32 v22, v21, 4, v22\n" \
  "buffer_load_dwordx4 v[128:131], v22, s[24:27], s61 offen offset:2560 sc1\n" \
  "buffer_load_dwordx4 v[132:135], v22, s[24:27], s61 offen offset:2816 sc1\n" \
  "buffer_load_dwordx4 v[136:139], v22, s[24:27], s61 offen offset:3072 sc1\n" \
  "buffer_load_dwordx4 v[140:143], v22, s[24:27], s61 offen offset:3328 sc1\n" \
  "buffer_load_dwordx4 v[144:147], v22, s[24:27], s61 offen offset:3584 sc1\n" \
  "buffer_load_dwordx4 v[148:151], v22, s[24:27], s61 offen offset:3840 sc1\n" \
  "buffer_load_dwordx4 v[152:155], v22, s[24:27], s62 offen offset:0 sc1\n" \
  "buffer_load_dwordx4 v[156:159], v22, s[24:27], s62 offen offset:256 sc1\n" \
  "s_add_u32 s87, s62, 16384\n" \
  "buffer_load_dwordx4 v[160:163], v22, s[24:27], s87 offen offset:0 sc1\n" \
  "buffer_load_dwordx4 v[164:167], v22, s[24:27], s87 offen offset:256 sc1\n" \
  "16:\n" \
  "s_bitcmp1_b32 s79, 4\n" \
  "s_cbranch_scc0 19f\n" \
  "s_mov_b32 s78, 0x10000\n" \
  "20:\n" \
  "s_sub_u32 s78, s78, 1\n" \
  "s_cbranch_scc1 19f\n" \
  "buffer_load_dwordx2 v[96:97], v4, s[28:31], s59 offen sc1\n" \
  "buffer_load_dwordx2 v[98:99], v4, s[24:27], s60 offen sc1\n" \
  "s_waitcnt vmcnt(0)\n" \
  "v_cmp_ne_u64 vcc, s[36:37], v[96:97]\n" \
  "v_cmp_ne_u64 s[88:89], s[36:37], v[98:99]\n" \
  "s_and_b64 vcc, vcc, 15\n" \
  "s_or_b64 vcc, vcc, s[88:89]\n" \
  "s_cbranch_scc1 20b\n" \
  "buffer_load_dwordx4 v[96:99], v4, s[28:31], s58 offen sc1\n" \
  "buffer_load_dwordx4 v[100:103], v4, s[28:31], s85 offen sc1\n" \
  "buffer_load_dwordx4 v[104:107], v4, s[28:31], s86 offen sc1\n" \
  "s_waitcnt vmcnt(0)\n" \
  "v_mfma_f32_16x16x16_bf16 v[208:211], v[236:237], v[96:97], v[24:27]\n" \
  "v_mfma_f32_16x16x16_bf16 v[212:215], v[236:237], v[98:99], v[28:31]\n" \
  "v_mfma_f32_16x16x16_bf16 v[208:211], v[236:237], v[100:101], v[208:211]\n" \
  "v_mfma_f32_16x16x16_bf16 v[212:215], v[236:237], v[102:103], v[212:215]\n" \
  "v_mfma_f32_16x16x16_bf16 v[208:211], v[236:237], v[104:105], v[208:211]\n" \
  "v_mfma_f32_16x16x16_bf16 v[212:215], v[236:237], v[106:107], v[212:215]\n" \
  "v_max3_u32 v20, v96, v97, v98\n" \
  "v_max3_u32 v20, v20, v99, v100\n" \
  "v_max3_u32 v20, v20, v101, v102\n" \
  "v_max3_u32 v20, v20, v103, v104\n" \
  "v_max3_u32 v20, v20, v105, v106\n" \
  "v_max3_u32 v20, v20, v107, v107\n" \
  "v_cmp_eq_u32 vcc, -1, v20\n" \
  "s_nop 3\n" \
  "s_cbranch_vccnz 20b\n" \
  "19:\n" \
  "s_cmp_eq_u32 s35, 0\n" \
  "s_cbranch_scc0 84f\n" \
  "v_mbcnt_lo_u32_b32 v8, -1, 0\n" \
  "v_mbcnt_hi_u32_b32 v8, -1, v8\n" \
  "v_lshrrev_b32 v9, 4, v8\n" \
  "v_lshlrev_b32 v2, 6, v9\n" \
  "s_lshl_b32 s81, s22, 3\n" \
  "v_add_u32 v1, s81, v2\n" \
  "v_and_b32 v7, 7, v8\n" \
  "v_lshlrev_b32 v7, 10, v7\n" \
  "v_lshl_add_u32 v7, v9, 4, v7\n" \
  "v_and_b32 v8, 8, v8\n" \
  "v_lshl_add_u32 v7, v8, 3, v7\n" \
  "s_lshl_b32 s84, s22, 7\n" \
  "s_add_u32 s84, s84, s45\n" \
  "s_add_u32 s84, s84, 10687488\n" \
  "ds_read_b128 v[240:243], v4 offset:37760\n" \
  "ds_read_b128 v[244:247], v4 offset:38784\n" \
  "s_branch 85f\n" \
  "84:\n" \
  "v_mbcnt_lo_u32_b32 v8, -1, 0\n" \
  "v_mbcnt_hi_u32_b32 v8, -1, v8\n" \
  "v_lshrrev_b32 v9, 4, v8\n" \
  "v_and_b32 v8, 15, v8\n" \
  "v_lshl_add_u32 v7, v8, 5, s23\n" \
  "v_lshl_add_u32 v7, v9, 3, v7\n" \
  "global_load_dwordx2 v[10:11], v7, s[4:5]\n" \
  "global_load_dwordx2 v[238:239], v7, s[4:5] offset:512\n" \
  "v_lshlrev_b32 v8, 2, v8\n" \
  "v_mul_u32_u24 v1, 0x840, v9\n" \
  "s_lshl_b32 s81, s22, 7\n" \
  "v_add3_u32 v1, v1, v8, s81\n" \
  "s_mul_i32 s81, s22, 0x420\n" \
  "v_lshl_add_u32 v2, v9, 8, v8\n" \
  "v_add_u32 v2, s81, v2\n" \
  "ds_read_b128 v[224:227], v4 offset:28544\n" \
  "ds_read_b128 v[228:231], v4 offset:29568\n" \
  "ds_read_b128 v[232:235], v4 offset:30592\n" \
  "ds_read_b128 v[240:243], v4 offset:31616\n" \
  "ds_read_b128 v[244:247], v4 offset:32640\n" \
  "ds_read_b128 v[248:251], v4 offset:33664\n" \
  "ds_read_b128 v[252:255], v4 offset:34688\n" \
  "ds_read_b128 v[192:195], v4 offset:35712\n" \
  "ds_read_b128 v[196:199], v4 offset:36736\n" \
  "85:\n" \
  "s_branch 14b\n" \
  "9:\n"
#define NSTAMP 43
#define KERNEL_CLOB "v1", "v2", "v3", "v4", "v5", "v6", "v7", "v8", "v9", "v10", "v11", "v12", "v13", "v14", "v15", "v16", "v17", "v18", "v19", "v20", "v21", "v22", "v23", "v24", "v25", "v26", "v27", "v28", "v29", "v30", "v31", "v32", "v33", "v34", "v35", "v36", "v37", "v38", "v39", "v40", "v41", "v42", "v43", "v44", "v45", "v46", "v47", "v48", "v49", "v50", "v51", "v52", "v53", "v54", "v55", "v56", "v57", "v58", "v59", "v60", "v61", "v62", "v63", "v64", "v65", "v66", "v67", "v68", "v69", "v70", "v71", "v72", "v73", "v74", "v75", "v76", "v77", "v78", "v79", "v80", "v81", "v82", "v83", "v84", "v85", "v86", "v87", "v88", "v89", "v90", "v91", "v92", "v93", "v94", "v95", "v96", "v97", "v98", "v99", "v100", "v101", "v102", "v103", "v104", "v105", "v106", "v107", "v108", "v109", "v110", "v111", "v112", "v113", "v114", "v115", "v116", "v117", "v118", "v119", "v120", "v121", "v122", "v123", "v124", "v125", "v126", "v127", "v128", "v129", "v130", "v131", "v132", "v133", "v134", "v135", "v136", "v137", "v138", "v139", "v140", "v141", "v142", "v143", "v144", "v145", "v146", "v147", "v148", "v149", "v150", "v151", "v152", "v153", "v154", "v155", "v156", "v157", "v158", "v159", "v160", "v161", "v162", "v163", "v164", "v165", "v166", "v167", "v168", "v169", "v170", "v171", "v172", "v173", "v174", "v175", "v176", "v177", "v178", "v179", "v180", "v181", "v182", "v183", "v184", "v185", "v186", "v187", "v188", "v189", "v190", "v191", "v192", "v193", "v194", "v195", "v196", "v197", "v198", "v199", "v200", "v201", "v202", "v203", "v204", "v205", "v206", "v207", "v208", "v209", "v210", "v211", "v212", "v213", "v214", "v215", "v216", "v217", "v218", "v219", "v220", "v221", "v222", "v223", "v224", "v225", "v226", "v227", "v228", "v229", "v230", "v231", "v232", "v233", "v234", "v235", "v236", "v237", "v238", "v239", "v240", "v241", "v242", "v243", "v244", "v245", "v246", "v247", "v248", "v249", "v250", "v251", "v252", "v253", "v254", "v255", "s4", "s5", "s6", "s7", "s8", "s9", "s10", "s11", "s12", "s13", "s14", "s15", "s16", "s17", "s18", "s19", "s20", "s21", "s22", "s23", "s24", "s25", "s26", "s27", "s28", "s29", "s30", "s31", "s33", "s34", "s35", "s36", "s37", "s38", "s39", "s40", "s41", "s42", "s43", "s44", "s45", "s46", "s47", "s48", "s49", "s50", "s51", "s52", "s53", "s54", "s55", "s56", "s57", "s58", "s59", "s60", "s61", "s62", "s63", "s64", "s65", "s66", "s67", "s68", "s69", "s70", "s71", "s72", "s73", "s74", "s75", "s76", "s77", "s78", "s79", "s80", "s81", "s82", "s83", "s84", "s85", "s86", "s87", "s88", "s89", "s90", "s91", "s92", "s93", "s94", "s95", "s96", "s97"
#define L_LP 41984
#define L_MISC 48640
#define WS_USED 22489344
static __device__ const __attribute__((aligned(256))) unsigned TABLES[29952] = {
0x3400,0x3400,0x3400,0x3400,0x3400,0x2e1f3364,0x31a831a8,0x33642e1f,0x3400,0x31a831a8,0x34000000,0x31a8b1a8,0x3400,0x33642e1f,0x31a8b1a8,0xae1fb364,
0x3400,0x34000000,0xb400,0xb4008000,0x3400,0x3364ae1f,0xb1a8b1a8,0xae1f3364,0x3400,0x31a8b1a8,0xb4008000,0x31a831a8,0x3400,0x2e1fb364,0xb1a831a8,0x3364ae1f,
0x3400,0xb400,0x3400,0xb400,0x3400,0xae1fb364,0x31a831a8,0xb364ae1f,0x3400,0xb1a8b1a8,0x34000000,0xb1a831a8,0x3400,0xb364ae1f,0x31a8b1a8,0x2e1f3364,
0x3400,0xb4008000,0xb400,0x34000000,0x3400,0xb3642e1f,0xb1a8b1a8,0x2e1fb364,0x3400,0xb1a831a8,0xb4008000,0xb1a8b1a8,0x3400,0xae1f3364,0xb1a831a8,0xb3642e1f,
0x3400,0x3400,0x3400,0x3400,0x34000000,0x3364ae1f,0x31a8b1a8,0x2e1fb364,0xb400,0xb1a8b1a8,0xb4008000,0xb1a831a8,0xb4008000,0xae1f3364,0x31a831a8,0x3364ae1f,
0x3400,0x34000000,0xb400,0xb4008000,0x34000000,0xae1fb364,0xb1a831a8,0x33642e1f,0xb400,0xb1a831a8,0x34000000,0xb1a8b1a8,0xb4008000,0x33642e1f,0xb1a8b1a8,0x2e1f3364,
0x3400,0xb400,0x3400,0xb400,0x34000000,0xb3642e1f,0x31a8b1a8,0xae1f3364,0xb400,0x31a831a8,0xb4008000,0x31a8b1a8,0xb4008000,0x2e1fb364,0x31a831a8,0xb3642e1f,
0x3400,0xb4008000,0xb400,0x34000000,0x34000000,0x2e1f3364,0xb1a831a8,0xb364ae1f,0xb400,0x31a8b1a8,0x34000000,0x31a831a8,0xb4008000,0xb364ae1f,0xb1a8b1a8,0xae1fb364,
0x3400,0x3400,0x3400,0x3400,0xb400,0xae1fb364,0xb1a8b1a8,0xb364ae1f,0x3400,0x31a831a8,0x34000000,0x31a8b1a8,0xb400,0xb364ae1f,0xb1a831a8,0x2e1f3364,
0x3400,0x34000000,0xb400,0xb4008000,0xb400,0xb3642e1f,0x31a831a8,0x2e1fb364,0x3400,0x31a8b1a8,0xb4008000,0x31a831a8,0xb400,0xae1f3364,0x31a8b1a8,0xb3642e1f,
0x3400,0xb400,0x3400,0xb400,0xb400,0x2e1f3364,0xb1a8b1a8,0x33642e1f,0x3400,0xb1a8b1a8,0x34000000,0xb1a831a8,0xb400,0x33642e1f,0xb1a831a8,0xae1fb364,
0x3400,0xb4008000,0xb400,0x34000000,0xb400,0x3364ae1f,0x31a831a8,0xae1f3364,0x3400,0xb1a831a8,0xb4008000,0xb1a8b1a8,0xb400,0x2e1fb364,0x31a8b1a8,0x3364ae1f,
0x3400,0x3400,0x3400,0x3400,0xb4008000,0xb3642e1f,0xb1a831a8,0xae1f3364,0xb400,0xb1a8b1a8,0xb4008000,0xb1a831a8,0x34000000,0x2e1fb364,0xb1a8b1a8,0xb3642e1f,
0x3400,0x34000000,0xb400,0xb4008000,0xb4008000,0x2e1f3364,0x31a8b1a8,0xb364ae1f,0xb400,0xb1a831a8,0x34000000,0xb1a8b1a8,0x34000000,0xb364ae1f,0x31a831a8,0xae1fb364,
0x3400,0xb400,0x3400,0xb400,0xb4008000,0x3364ae1f,0xb1a831a8,0x2e1fb364,0xb400,0x31a831a8,0xb4008000,0x31a8b1a8,0x34000000,0xae1f3364,0xb1a8b1a8,0x3364ae1f,
0x3400,0xb4008000,0xb400,0x34000000,0xb4008000,0xae1fb364,0x31a8b1a8,0x33642e1f,0xb400,0x31a8b1a8,0x34000000,0x31a831a8,0x34000000,0x33642e1f,0x31a831a8,0x2e1f3364,
0x34000000,0x34000000,0x34000000,0x34000000,0x34000000,0x3364ae1f,0x31a8b1a8,0x2e1fb364,0x34000000,0x31a8b1a8,0xb400,0xb1a8b1a8,0x34000000,0x2e1fb364,0xb1a8b1a8,0xb3642e1f,
0x34000000,0xb400,0xb4008000,0x80003400,0x34000000,0xae1fb364,0xb1a831a8,0x33642e1f,0x34000000,0xb1a8b1a8,0x80003400,0x31a8b1a8,0x34000000,0xb364ae1f,0x31a831a8,0xae1fb364,
0x34000000,0xb4008000,0x34000000,0xb4008000,0x34000000,0xb3642e1f,0x31a8b1a8,0xae1f3364,0x34000000,0xb1a831a8,0xb400,0x31a831a8,0x34000000,0xae1f3364,0xb1a8b1a8,0x3364ae1f,
0x34000000,0x80003400,0xb4008000,0xb400,0x34000000,0x2e1f3364,0xb1a831a8,0xb364ae1f,0x34000000,0x31a831a8,0x80003400,0xb1a831a8,0x34000000,0x33642e1f,0x31a831a8,0x2e1f3364,
0x34000000,0x34000000,0x34000000,0x34000000,0xb400,0xae1fb364,0xb1a8b1a8,0xb364ae1f,0xb4008000,0xb1a831a8,0x80003400,0x31a831a8,0x80003400,0x33642e1f,0x31a8b1a8,0xae1fb364,
0x34000000,0xb400,0xb4008000,0x80003400,0xb400,0xb3642e1f,0x31a831a8,0x2e1fb364,0xb4008000,0x31a831a8,0xb400,0xb1a831a8,0x80003400,0x2e1fb364,0xb1a831a8,0x3364ae1f,
0x34000000,0xb4008000,0x34000000,0xb4008000,0xb400,0x2e1f3364,0xb1a8b1a8,0x33642e1f,0xb4008000,0x31a8b1a8,0x80003400,0xb1a8b1a8,0x80003400,0xb364ae1f,0x31a8b1a8,0x2e1f3364,
0x34000000,0x80003400,0xb4008000,0xb400,0xb400,0x3364ae1f,0x31a831a8,0xae1f3364,0xb4008000,0xb1a8b1a8,0xb400,0x31a8b1a8,0x80003400,0xae1f3364,0xb1a831a8,0xb3642e1f,
0x34000000,0x34000000,0x34000000,0x34000000,0xb4008000,0xb3642e1f,0xb1a831a8,0xae1f3364,0x34000000,0x31a8b1a8,0xb400,0xb1a8b1a8,0xb4008000,0xae1f3364,0x31a831a8,0x3364ae1f,
0x34000000,0xb400,0xb4008000,0x80003400,0xb4008000,0x2e1f3364,0x31a8b1a8,0xb364ae1f,0x34000000,0xb1a8b1a8,0x80003400,0x31a8b1a8,0xb4008000,0x33642e1f,0xb1a8b1a8,0x2e1f3364,
0x34000000,0xb4008000,0x34000000,0xb4008000,0xb4008000,0x3364ae1f,0xb1a831a8,0x2e1fb364,0x34000000,0xb1a831a8,0xb400,0x31a831a8,0xb4008000,0x2e1fb364,0x31a831a8,0xb3642e1f,
0x34000000,0x80003400,0xb4008000,0xb400,0xb4008000,0xae1fb364,0x31a8b1a8,0x33642e1f,0x34000000,0x31a831a8,0x80003400,0xb1a831a8,0xb4008000,0xb364ae1f,0xb1a8b1a8,0xae1fb364,
0x34000000,0x34000000,0x34000000,0x34000000,0x80003400,0x2e1f3364,0x31a831a8,0x33642e1f,0xb4008000,0xb1a831a8,0x80003400,0x31a831a8,0xb400,0xb364ae1f,0xb1a831a8,0x2e1f3364,
0x34000000,0xb400,0xb4008000,0x80003400,0x80003400,0x3364ae1f,0xb1a8b1a8,0xae1f3364,0xb4008000,0x31a831a8,0xb400,0xb1a831a8,0xb400,0xae1f3364,0x31a8b1a8,0xb3642e1f,
0x34000000,0xb4008000,0x34000000,0xb4008000,0x80003400,0xae1fb364,0x31a831a8,0xb364ae1f,0xb4008000,0x31a8b1a8,0x80003400,0xb1a8b1a8,0xb400,0x33642e1f,0xb1a831a8,0xae1fb364,
0x34000000,0x80003400,0xb4008000,0xb400,0x80003400,0xb3642e1f,0xb1a8b1a8,0x2e1fb364,0xb4008000,0xb1a8b1a8,0xb400,0x31a8b1a8,0xb400,0x2e1fb364,0x31a8b1a8,0x3364ae1f,
0x3400,0x3400,0x3400,0x3400,0x3400,0x2e1f3364,0x31a831a8,0x33642e1f,0x3400,0x31a831a8,0x34000000,0x31a8b1a8,0x3400,0x33642e1f,0x31a8b1a8,0xae1fb364,
0x3400,0x34000000,0xb400,0xb4008000,0x3400,0x3364ae1f,0xb1a8b1a8,0xae1f3364,0x3400,0x31a8b1a8,0xb4008000,0x31a831a8,0x3400,0x2e1fb364,0xb1a831a8,0x3364ae1f,
0x3400,0xb400,0x3400,0xb400,0x3400,0xae1fb364,0x31a831a8,0xb364ae1f,0x3400,0xb1a8b1a8,0x34000000,0xb1a831a8,0x3400,0xb364ae1f,0x31a8b1a8,0x2e1f3364,
0x3400,0xb4008000,0xb400,0x34000000,0x3400,0xb3642e1f,0xb1a8b1a8,0x2e1fb364,0x3400,0xb1a831a8,0xb4008000,0xb1a8b1a8,0x3400,0xae1f3364,0xb1a831a8,0xb3642e1f,
0x3400,0x3400,0x3400,0x3400,0x34000000,0x3364ae1f,0x31a8b1a8,0x2e1fb364,0xb400,0xb1a8b1a8,0xb4008000,0xb1a831a8,0xb4008000,0xae1f3364,0x31a831a8,0x3364ae1f,
0x3400,0x34000000,0xb400,0xb4008000,0x34000000,0xae1fb364,0xb1a831a8,0x33642e1f,0xb400,0xb1a831a8,0x34000000,0xb1a8b1a8,0xb4008000,0x33642e1f,0xb1a8b1a8,0x2e1f3364,
0x3400,0xb400,0x3400,0xb400,0x34000000,0xb3642e1f,0x31a8b1a8,0xae1f3364,0xb400,0x31a831a8,0xb4008000,0x31a8b1a8,0xb4008000,0x2e1fb364,0x31a831a8,0xb3642e1f,
0x3400,0xb4008000,0xb400,0x34000000,0x34000000,0x2e1f3364,0xb1a831a8,0xb364ae1f,0xb400,0x31a8b1a8,0x34000000,0x31a831a8,0xb4008000,0xb364ae1f,0xb1a8b1a8,0xae1fb364,
0x3400,0x3400,0x3400,0x3400,0xb400,0xae1fb364,0xb1a8b1a8,0xb364ae1f,0x3400,0x31a831a8,0x34000000,0x31a8b1a8,0xb400,0xb364ae1f,0xb1a831a8,0x2e1f3364,
0x3400,0x34000000,0xb400,0xb4008000,0xb400,0xb3642e1f,0x31a831a8,0x2e1fb364,0x3400,0x31a8b1a8,0xb4008000,0x31a831a8,0xb400,0xae1f3364,0x31a8b1a8,0xb3642e1f,
0x3400,0xb400,0x3400,0xb400,0xb400,0x2e1f3364,0xb1a8b1a8,0x33642e1f,0x3400,0xb1a8b1a8,0x34000000,0xb1a831a8,0xb400,0x33642e1f,0xb1a831a8,0xae1fb364,
0x3400,0xb4008000,0xb400,0x34000000,0xb400,0x3364ae1f,0x31a831a8,0xae1f3364,0x3400,0xb1a831a8,0xb4008000,0xb1a8b1a8,0xb400,0x2e1fb364,0x31a8b1a8,0x3364ae1f,
0x3400,0x3400,0x3400,0x3400,0xb4008000,0xb3642e1f,0xb1a831a8,0xae1f3364,0xb400,0xb1a8b1a8,0xb4008000,0xb1a831a8,0x34000000,0x2e1fb364,0xb1a8b1a8,0xb3642e1f,
0x3400,0x34000000,0xb400,0xb4008000,0xb4008000,0x2e1f3364,0x31a8b1a8,0xb364ae1f,0xb400,0xb1a831a8,0x34000000,0xb1a8b1a8,0x34000000,0xb364ae1f,0x31a831a8,0xae1fb364,
0x3400,0xb400,0x3400,0xb400,0xb4008000,0x3364ae1f,0xb1a831a8,0x2e1fb364,0xb400,0x31a831a8,0xb4008000,0x31a8b1a8,0x34000000,0xae1f3364,0xb1a8b1a8,0x3364ae1f,
0x3400,0xb4008000,0xb400,0x34000000,0xb4008000,0xae1fb364,0x31a8b1a8,0x33642e1f,0xb400,0x31a8b1a8,0x34000000,0x31a831a8,0x34000000,0x33642e1f,0x31a831a8,0x2e1f3364,
0x34000000,0x34000000,0x34000000,0x34000000,0x34000000,0x3364ae1f,0x31a8b1a8,0x2e1fb364,0x34000000,0x31a8b1a8,0xb400,0xb1a8b1a8,0x34000000,0x2e1fb364,0xb1a8b1a8,0xb3642e1f,
0x34000000,0xb400,0xb4008000,0x80003400,0x34000000,0xae1fb364,0xb1a831a8,0x33642e1f,0x34000000,0xb1a8b1a8,0x80003400,0x31a8b1a8,0x34000000,0xb364ae1f,0x31a831a8,0xae1fb364,
0x34000000,0xb4008000,0x34000000,0xb4008000,0x34000000,0xb3642e1f,0x31a8b1a8,0xae1f3364,0x34000000,0xb1a831a8,0xb400,0x31a831a8,0x34000000,0xae1f3364,0xb1a8b1a8,0x3364ae1f,
0x34000000,0x80003400,0xb4008000,0xb400,0x34000000,0x2e1f3364,0xb1a831a8,0xb364ae1f,0x34000000,0x31a831a8,0x80003400,0xb1a831a8,0x34000000,0x33642e1f,0x31a831a8,0x2e1f3364,
0x34000000,0x34000000,0x34000000,0x34000000,0xb400,0xae1fb364,0xb1a8b1a8,0xb364ae1f,0xb4008000,0xb1a831a8,0x80003400,0x31a831a8,0x80003400,0x33642e1f,0x31a8b1a8,0xae1fb364,
0x34000000,0xb400,0xb4008000,0x80003400,0xb400,0xb3642e1f,0x31a831a8,0x2e1fb364,0xb4008000,0x31a831a8,0xb400,0xb1a831a8,0x80003400,0x2e1fb364,0xb1a831a8,0x3364ae1f,
0x34000000,0xb4008000,0x34000000,0xb4008000,0xb400,0x2e1f3364,0xb1a8b1a8,0x33642e1f,0xb4008000,0x31a8b1a8,0x80003400,0xb1a8b1a8,0x80003400,0xb364ae1f,0x31a8b1a8,0x2e1f3364,
0x34000000,0x80003400,0xb4008000,0xb400,0xb400,0x3364ae1f,0x31a831a8,0xae1f3364,0xb4008000,0xb1a8b1a8,0xb400,0x31a8b1a8,0x80003400,0xae1f3364,0xb1a831a8,0xb3642e1f,
0x34000000,0x34000000,0x34000000,0x34000000,0xb4008000,0xb3642e1f,0xb1a831a8,0xae1f3364,0x34000000,0x31a8b1a8,0xb400,0xb1a8b1a8,0xb4008000,0xae1f3364,0x31a831a8,0x3364ae1f,
0x34000000,0xb400,0xb4008000,0x80003400,0xb4008000,0x2e1f3364,0x31a8b1a8,0xb364ae1f,0x34000000,0xb1a8b1a8,0x80003400,0x31a8b1a8,0xb4008000,0x33642e1f,0xb1a8b1a8,0x2e1f3364,
0x34000000,0xb4008000,0x34000000,0xb4008000,0xb4008000,0x3364ae1f,0xb1a831a8,0x2e1fb364,0x34000000,0xb1a831a8,0xb400,0x31a831a8,0xb4008000,0x2e1fb364,0x31a831a8,0xb3642e1f,
0x34000000,0x80003400,0xb4008000,0xb400,0xb4008000,0xae1fb364,0x31a8b1a8,0x33642e1f,0x34000000,0x31a831a8,0x80003400,0xb1a831a8,0xb4008000,0xb364ae1f,0xb1a8b1a8,0xae1fb364,
0x34000000,0x34000000,0x34000000,0x34000000,0x80003400,0x2e1f3364,0x31a831a8,0x33642e1f,0xb4008000,0xb1a831a8,0x80003400,0x31a831a8,0xb400,0xb364ae1f,0xb1a831a8,0x2e1f3364,
0x34000000,0xb400,0xb4008000,0x80003400,0x80003400,0x3364ae1f,0xb1a8b1a8,0xae1f3364,0xb4008000,0x31a831a8,0xb400,0xb1a831a8,0xb400,0xae1f3364,0x31a8b1a8,0xb3642e1f,
0x34000000,0xb4008000,0x34000000,0xb4008000,0x80003400,0xae1fb364,0x31a831a8,0xb364ae1f,0xb4008000,0x31a8b1a8,0x80003400,0xb1a8b1a8,0xb400,0x33642e1f,0xb1a831a8,0xae1fb364,
0x34000000,0x80003400,0xb4008000,0xb400,0x80003400,0xb3642e1f,0xb1a8b1a8,0x2e1fb364,0xb4008000,0xb1a8b1a8,0xb400,0x31a8b1a8,0xb400,0x2e1fb364,0x31a8b1a8,0x3364ae1f,
0x3400,0x3400,0x3400,0x3400,0x3400,0xae1f3364,0xb1a831a8,0xb3642e1f,0x3400,0xb1a831a8,0xb4000000,0xb1a8b1a8,0x3400,0xb3642e1f,0xb1a8b1a8,0x2e1fb364,
0x3400,0xb4000000,0x8000b400,0x34008000,0x3400,0xb364ae1f,0x31a8b1a8,0x2e1f3364,0x3400,0xb1a8b1a8,0x34008000,0xb1a831a8,0x3400,0xae1fb364,0x31a831a8,0xb364ae1f,
0x3400,0x8000b400,0x3400,0x8000b400,0x3400,0x2e1fb364,0xb1a831a8,0x3364ae1f,0x3400,0x31a8b1a8,0xb4000000,0x31a831a8,0x3400,0x3364ae1f,0xb1a8b1a8,0xae1f3364,
0x3400,0x34008000,0x8000b400,0xb4000000,0x3400,0x33642e1f,0x31a8b1a8,0xae1fb364,0x3400,0x31a831a8,0x34008000,0x31a8b1a8,0x3400,0x2e1f3364,0x31a831a8,0x33642e1f,
0x3400,0x3400,0x3400,0x3400,0xb4000000,0xb364ae1f,0xb1a8b1a8,0xae1fb364,0x8000b400,0x31a8b1a8,0x34008000,0x31a831a8,0x34008000,0x2e1f3364,0xb1a831a8,0xb364ae1f,
0x3400,0xb4000000,0x8000b400,0x34008000,0xb4000000,0x2e1fb364,0x31a831a8,0xb3642e1f,0x8000b400,0x31a831a8,0xb4000000,0x31a8b1a8,0x34008000,0xb3642e1f,0x31a8b1a8,0xae1f3364,
0x3400,0x8000b400,0x3400,0x8000b400,0xb4000000,0x33642e1f,0xb1a8b1a8,0x2e1f3364,0x8000b400,0xb1a831a8,0x34008000,0xb1a8b1a8,0x34008000,0xae1fb364,0xb1a831a8,0x33642e1f,
0x3400,0x34008000,0x8000b400,0xb4000000,0xb4000000,0xae1f3364,0x31a831a8,0x3364ae1f,0x8000b400,0xb1a8b1a8,0xb4000000,0xb1a831a8,0x34008000,0x3364ae1f,0x31a8b1a8,0x2e1fb364,
0x3400,0x3400,0x3400,0x3400,0x8000b400,0x2e1fb364,0x31a8b1a8,0x3364ae1f,0x3400,0xb1a831a8,0xb4000000,0xb1a8b1a8,0x8000b400,0x3364ae1f,0x31a831a8,0xae1f3364,
0x3400,0xb4000000,0x8000b400,0x34008000,0x8000b400,0x33642e1f,0xb1a831a8,0xae1fb364,0x3400,0xb1a8b1a8,0x34008000,0xb1a831a8,0x8000b400,0x2e1f3364,0xb1a8b1a8,0x33642e1f,
0x3400,0x8000b400,0x3400,0x8000b400,0x8000b400,0xae1f3364,0x31a8b1a8,0xb3642e1f,0x3400,0x31a8b1a8,0xb4000000,0x31a831a8,0x8000b400,0xb3642e1f,0x31a831a8,0x2e1fb364,
0x3400,0x34008000,0x8000b400,0xb4000000,0x8000b400,0xb364ae1f,0xb1a831a8,0x2e1f3364,0x3400,0x31a831a8,0x34008000,0x31a8b1a8,0x8000b400,0xae1fb364,0xb1a8b1a8,0xb364ae1f,
0x3400,0x3400,0x3400,0x3400,0x34008000,0x33642e1f,0x31a831a8,0x2e1f3364,0x8000b400,0x31a8b1a8,0x34008000,0x31a831a8,0xb4000000,0xae1fb364,0x31a8b1a8,0x33642e1f,
0x3400,0xb4000000,0x8000b400,0x34008000,0x34008000,0xae1f3364,0xb1a8b1a8,0x3364ae1f,0x8000b400,0x31a831a8,0xb4000000,0x31a8b1a8,0xb4000000,0x3364ae1f,0xb1a831a8,0x2e1fb364,
0x3400,0x8000b400,0x3400,0x8000b400,0x34008000,0xb364ae1f,0x31a831a8,0xae1fb364,0x8000b400,0xb1a831a8,0x34008000,0xb1a8b1a8,0xb4000000,0x2e1f3364,0x31a8b1a8,0xb364ae1f,
0x3400,0x34008000,0x8000b400,0xb4000000,0x34008000,0x2e1fb364,0xb1a8b1a8,0xb3642e1f,0x8000b400,0xb1a8b1a8,0xb4000000,0xb1a831a8,0xb4000000,0xb3642e1f,0xb1a831a8,0xae1f3364,
0x34000000,0x34000000,0x34000000,0x34000000,0x34000000,0x33642e1f,0x31a831a8,0x2e1f3364,0x34000000,0x31a831a8,0x3400,0xb1a831a8,0x34000000,0x2e1f3364,0xb1a831a8,0xb364ae1f,
0x34000000,0x3400,0xb4000000,0x8000b400,0x34000000,0xae1f3364,0xb1a8b1a8,0x3364ae1f,0x34000000,0xb1a831a8,0x8000b400,0x31a831a8,0x34000000,0xb3642e1f,0x31a8b1a8,0xae1f3364,
0x34000000,0xb4000000,0x34000000,0xb4000000,0x34000000,0xb364ae1f,0x31a831a8,0xae1fb364,0x34000000,0xb1a8b1a8,0x3400,0x31a8b1a8,0x34000000,0xae1fb364,0xb1a831a8,0x33642e1f,
0x34000000,0x8000b400,0xb4000000,0x3400,0x34000000,0x2e1fb364,0xb1a8b1a8,0xb3642e1f,0x34000000,0x31a8b1a8,0x8000b400,0xb1a8b1a8,0x34000000,0x3364ae1f,0x31a8b1a8,0x2e1fb364,
0x34000000,0x34000000,0x34000000,0x34000000,0x3400,0xae1f3364,0xb1a831a8,0xb3642e1f,0xb4000000,0xb1a8b1a8,0x8000b400,0x31a8b1a8,0x8000b400,0x3364ae1f,0x31a831a8,0xae1f3364,
0x34000000,0x3400,0xb4000000,0x8000b400,0x3400,0xb364ae1f,0x31a8b1a8,0x2e1f3364,0xb4000000,0x31a8b1a8,0x3400,0xb1a8b1a8,0x8000b400,0x2e1f3364,0xb1a8b1a8,0x33642e1f,
0x34000000,0xb4000000,0x34000000,0xb4000000,0x3400,0x2e1fb364,0xb1a831a8,0x3364ae1f,0xb4000000,0x31a831a8,0x8000b400,0xb1a831a8,0x8000b400,0xb3642e1f,0x31a831a8,0x2e1fb364,
0x34000000,0x8000b400,0xb4000000,0x3400,0x3400,0x33642e1f,0x31a8b1a8,0xae1fb364,0xb4000000,0xb1a831a8,0x3400,0x31a831a8,0x8000b400,0xae1fb364,0xb1a8b1a8,0xb364ae1f,
0x34000000,0x34000000,0x34000000,0x34000000,0xb4000000,0xb364ae1f,0xb1a8b1a8,0xae1fb364,0x34000000,0x31a831a8,0x3400,0xb1a831a8,0xb4000000,0xae1fb364,0x31a8b1a8,0x33642e1f,
0x34000000,0x3400,0xb4000000,0x8000b400,0xb4000000,0x2e1fb364,0x31a831a8,0xb3642e1f,0x34000000,0xb1a831a8,0x8000b400,0x31a831a8,0xb4000000,0x3364ae1f,0xb1a831a8,0x2e1fb364,
0x34000000,0xb4000000,0x34000000,0xb4000000,0xb4000000,0x33642e1f,0xb1a8b1a8,0x2e1f3364,0x34000000,0xb1a8b1a8,0x3400,0x31a8b1a8,0xb4000000,0x2e1f3364,0x31a8b1a8,0xb364ae1f,
0x34000000,0x8000b400,0xb4000000,0x3400,0xb4000000,0xae1f3364,0x31a831a8,0x3364ae1f,0x34000000,0x31a8b1a8,0x8000b400,0xb1a8b1a8,0xb4000000,0xb3642e1f,0xb1a831a8,0xae1f3364,
0x34000000,0x34000000,0x34000000,0x34000000,0x8000b400,0x2e1fb364,0x31a8b1a8,0x3364ae1f,0xb4000000,0xb1a8b1a8,0x8000b400,0x31a8b1a8,0x3400,0xb3642e1f,0xb1a8b1a8,0x2e1fb364,
0x34000000,0x3400,0xb4000000,0x8000b400,0x8000b400,0x33642e1f,0xb1a831a8,0xae1fb364,0xb4000000,0x31a8b1a8,0x3400,0xb1a8b1a8,0x3400,0xae1fb364,0x31a831a8,0xb364ae1f,
0x34000000,0xb4000000,0x34000000,0xb4000000,0x8000b400,0xae1f3364,0x31a8b1a8,0xb3642e1f,0xb4000000,0x31a831a8,0x8000b400,0xb1a831a8,0x3400,0x3364ae1f,0xb1a8b1a8,0xae1f3364,
0x34000000,0x8000b400,0xb4000000,0x3400,0x8000b400,0xb364ae1f,0xb1a831a8,0x2e1f3364,0xb4000000,0xb1a831a8,0x3400,0x31a831a8,0x3400,0x2e1f3364,0x31a831a8,0x33642e1f,
0x3400,0x3400,0x3400,0x3400,0x3400,0xae1f3364,0xb1a831a8,0xb3642e1f,0x3400,0xb1a831a8,0xb4000000,0xb1a8b1a8,0x3400,0xb3642e1f,0xb1a8b1a8,0x2e1fb364,
0x3400,0xb4000000,0x8000b400,0x34008000,0x3400,0xb364ae1f,0x31a8b1a8,0x2e1f3364,0x3400,0xb1a8b1a8,0x34008000,0xb1a831a8,0x3400,0xae1fb364,0x31a831a8,0xb364ae1f,
0x3400,0x8000b400,0x3400,0x8000b400,0x3400,0x2e1fb364,0xb1a831a8,0x3364ae1f,0x3400,0x31a8b1a8,0xb4000000,0x31a831a8,0x3400,0x3364ae1f,0xb1a8b1a8,0xae1f3364,
0x3400,0x34008000,0x8000b400,0xb4000000,0x3400,0x33642e1f,0x31a8b1a8,0xae1fb364,0x3400,0x31a831a8,0x34008000,0x31a8b1a8,0x3400,0x2e1f3364,0x31a831a8,0x33642e1f,
0x3400,0x3400,0x3400,0x3400,0xb4000000,0xb364ae1f,0xb1a8b1a8,0xae1fb364,0x8000b400,0x31a8b1a8,0x34008000,0x31a831a8,0x34008000,0x2e1f3364,0xb1a831a8,0xb364ae1f,
0x3400,0xb4000000,0x8000b400,0x34008000,0xb4000000,0x2e1fb364,0x31a831a8,0xb3642e1f,0x8000b400,0x31a831a8,0xb4000000,0x31a8b1a8,0x34008000,0xb3642e1f,0x31a8b1a8,0xae1f3364,
0x3400,0x8000b400,0x3400,0x8000b400,0xb4000000,0x33642e1f,0xb1a8b1a8,0x2e1f3364,0x8000b400,0xb1a831a8,0x34008000,0xb1a8b1a8,0x34008000,0xae1fb364,0xb1a831a8,0x33642e1f,
0x3400,0x34008000,0x8000b400,0xb4000000,0xb4000000,0xae1f3364,0x31a831a8,0x3364ae1f,0x8000b400,0xb1a8b1a8,0xb4000000,0xb1a831a8,0x34008000,0x3364ae1f,0x31a8b1a8,0x2e1fb364,
0x3400,0x3400,0x3400,0x3400,0x8000b400,0x2e1fb364,0x31a8b1a8,0x3364ae1f,0x3400,0xb1a831a8,0xb4000000,0xb1a8b1a8,0x8000b400,0x3364ae1f,0x31a831a8,0xae1f3364,
0x3400,0xb4000000,0x8000b400,0x34008000,0x8000b400,0x33642e1f,0xb1a831a8,0xae1fb364,0x3400,0xb1a8b1a8,0x34008000,0xb1a831a8,0x8000b400,0x2e1f3364,0xb1a8b1a8,0x33642e1f,
0x3400,0x8000b400,0x3400,0x8000b400,0x8000b400,0xae1f3364,0x31a8b1a8,0xb3642e1f,0x3400,0x31a8b1a8,0xb4000000,0x31a831a8,0x8000b400,0xb3642e1f,0x31a831a8,0x2e1fb364,
0x3400,0x34008000,0x8000b400,0xb4000000,0x8000b400,0xb364ae1f,0xb1a831a8,0x2e1f3364,0x3400,0x31a831a8,0x34008000,0x31a8b1a8,0x8000b400,0xae1fb364,0xb1a8b1a8,0xb364ae1f,
0x3400,0x3400,0x3400,0x3400,0x34008000,0x33642e1f,0x31a831a8,0x2e1f3364,0x8000b400,0x31a8b1a8,0x34008000,0x31a831a8,0xb4000000,0xae1fb364,0x31a8b1a8,0x33642e1f,
0x3400,0xb4000000,0x8000b400,0x34008000,0x34008000,0xae1f3364,0xb1a8b1a8,0x3364ae1f,0x8000b400,0x31a831a8,0xb4000000,0x31a8b1a8,0xb4000000,0x3364ae1f,0xb1a831a8,0x2e1fb364,
0x3400,0x8000b400,0x3400,0x8000b400,0x34008000,0xb364ae1f,0x31a831a8,0xae1fb364,0x8000b400,0xb1a831a8,0x34008000,0xb1a8b1a8,0xb4000000,0x2e1f3364,0x31a8b1a8,0xb364ae1f,
0x3400,0x34008000,0x8000b400,0xb4000000,0x34008000,0x2e1fb364,0xb1a8b1a8,0xb3642e1f,0x8000b400,0xb1a8b1a8,0xb4000000,0xb1a831a8,0xb4000000,0xb3642e1f,0xb1a831a8,0xae1f3364,
0x34000000,0x34000000,0x34000000,0x34000000,0x34000000,0x33642e1f,0x31a831a8,0x2e1f3364,0x34000000,0x31a831a8,0x3400,0xb1a831a8,0x34000000,0x2e1f3364,0xb1a831a8,0xb364ae1f,
0x34000000,0x3400,0xb4000000,0x8000b400,0x34000000,0xae1f3364,0xb1a8b1a8,0x3364ae1f,0x34000000,0xb1a831a8,0x8000b400,0x31a831a8,0x34000000,0xb3642e1f,0x31a8b1a8,0xae1f3364,
0x34000000,0xb4000000,0x34000000,0xb4000000,0x34000000,0xb364ae1f,0x31a831a8,0xae1fb364,0x34000000,0xb1a8b1a8,0x3400,0x31a8b1a8,0x34000000,0xae1fb364,0xb1a831a8,0x33642e1f,
0x34000000,0x8000b400,0xb4000000,0x3400,0x34000000,0x2e1fb364,0xb1a8b1a8,0xb3642e1f,0x34000000,0x31a8b1a8,0x8000b400,0xb1a8b1a8,0x34000000,0x3364ae1f,0x31a8b1a8,0x2e1fb364,
0x34000000,0x34000000,0x34000000,0x34000000,0x3400,0xae1f3364,0xb1a831a8,0xb3642e1f,0xb4000000,0xb1a8b1a8,0x8000b400,0x31a8b1a8,0x8000b400,0x3364ae1f,0x31a831a8,0xae1f3364,
0x34000000,0x3400,0xb4000000,0x8000b400,0x3400,0xb364ae1f,0x31a8b1a8,0x2e1f3364,0xb4000000,0x31a8b1a8,0x3400,0xb1a8b1a8,0x8000b400,0x2e1f3364,0xb1a8b1a8,0x33642e1f,
0x34000000,0xb4000000,0x34000000,0xb4000000,0x3400,0x2e1fb364,0xb1a831a8,0x3364ae1f,0xb4000000,0x31a831a8,0x8000b400,0xb1a831a8,0x8000b400,0xb3642e1f,0x31a831a8,0x2e1fb364,
0x34000000,0x8000b400,0xb4000000,0x3400,0x3400,0x33642e1f,0x31a8b1a8,0xae1fb364,0xb4000000,0xb1a831a8,0x3400,0x31a831a8,0x8000b400,0xae1fb364,0xb1a8b1a8,0xb364ae1f,
0x34000000,0x34000000,0x34000000,0x34000000,0xb4000000,0xb364ae1f,0xb1a8b1a8,0xae1fb364,0x34000000,0x31a831a8,0x3400,0xb1a831a8,0xb4000000,0xae1fb364,0x31a8b1a8,0x33642e1f,
0x34000000,0x3400,0xb4000000,0x8000b400,0xb4000000,0x2e1fb364,0x31a831a8,0xb3642e1f,0x34000000,0xb1a831a8,0x8000b400,0x31a831a8,0xb4000000,0x3364ae1f,0xb1a831a8,0x2e1fb364,
0x34000000,0xb4000000,0x34000000,0xb4000000,0xb4000000,0x33642e1f,0xb1a8b1a8,0x2e1f3364,0x34000000,0xb1a8b1a8,0x3400,0x31a8b1a8,0xb4000000,0x2e1f3364,0x31a8b1a8,0xb364ae1f,
0x34000000,0x8000b400,0xb4000000,0x3400,0xb4000000,0xae1f3364,0x31a831a8,0x3364ae1f,0x34000000,0x31a8b1a8,0x8000b400,0xb1a8b1a8,0xb4000000,0xb3642e1f,0xb1a831a8,0xae1f3364,
0x34000000,0x34000000,0x34000000,0x34000000,0x8000b400,0x2e1fb364,0x31a8b1a8,0x3364ae1f,0xb4000000,0xb1a8b1a8,0x8000b400,0x31a8b1a8,0x3400,0xb3642e1f,0xb1a8b1a8,0x2e1fb364,
0x34000000,0x3400,0xb4000000,0x8000b400,0x8000b400,0x33642e1f,0xb1a831a8,0xae1fb364,0xb4000000,0x31a8b1a8,0x3400,0xb1a8b1a8,0x3400,0xae1fb364,0x31a831a8,0xb364ae1f,
0x34000000,0xb4000000,0x34000000,0xb4000000,0x8000b400,0xae1f3364,0x31a8b1a8,0xb3642e1f,0xb4000000,0x31a831a8,0x8000b400,0xb1a831a8,0x3400,0x3364ae1f,0xb1a8b1a8,0xae1f3364,
0x34000000,0x8000b400,0xb4000000,0x3400,0x8000b400,0xb364ae1f,0xb1a831a8,0x2e1f3364,0xb4000000,0xb1a831a8,0x3400,0x31a831a8,0x3400,0x2e1f3364,0x31a831a8,0x33642e1f,
0x3f800000,0x3f800000,0x3f800000,0x3f800000,0x3f800000,0x3f7fec43,0x3f7fb10f,0x3f7f4e6d,0x3f800000,0x3f7fb10f,0x3f7ec46d,0x3f7d3aac,0x3f800000,0x3f7f4e6d,0x3f7d3aac,0x3f79c79d,
0x3f800000,0x3f7ec46d,0x3f7b14be,0x3f74fa0b,0x3f800000,0x3f7e1324,0x3f7853f8,0x3f6ed89e,0x3f800000,0x3f7d3aac,0x3f74fa0b,0x3f676bd8,0x3f800000,0x3f7c3b28,0x3f710908,0x3f5ebe05,
0x3f800000,0x3f7b14be,0x3f6c835e,0x3f54db31,0x3f800000,0x3f79c79d,0x3f676bd8,0x3f49d112,0x3f800000,0x3f7853f8,0x3f61c598,0x3f3daef9,0x3f800000,0x3f76ba07,0x3f5b941a,0x3f3085bb,
0x3f800000,0x3f74fa0b,0x3f54db31,0x3f226799,0x3f800000,0x3f731447,0x3f4d9f02,0x3f13682a,0x3f800000,0x3f710908,0x3f45e403,0x3f039c3d,0x3f800000,0x3f6ed89e,0x3f3daef9,0x3ee63375,
0x3f800000,0x3f800000,0x3f800000,0x3f800000,0x3f7ec46d,0x3f7e1324,0x3f7d3aac,0x3f7c3b28,0x3f7b14be,0x3f7853f8,0x3f74fa0b,0x3f710908,0x3f74fa0b,0x3f6ed89e,0x3f676bd8,0x3f5ebe05,
0x3f6c835e,0x3f61c598,0x3f54db31,0x3f45e403,0x3f61c598,0x3f514d3d,0x3f3daef9,0x3f273656,0x3f54db31,0x3f3daef9,0x3f226799,0x3f039c3d,0x3f45e403,0x3f273656,0x3f039c3d,0x3eb8442a,
0x3f3504f3,0x3f0e39da,0x3ec3ef15,0x3e47c5c2,0x3f226799,0x3ee63375,0x3e78cfcc,0x3cc90ab0,0x3f0e39da,0x3eac7cd4,0x3dc8bd36,0xbe164083,0x3ef15aea,0x3e605c13,0xbd48fb30,0xbea09ae5,
0x3ec3ef15,0x3dc8bd36,0xbe47c5c2,0xbef15aea,0x3e94a031,0xbcc90ab0,0xbeac7cd4,0xbf1d7fd1,0x3e47c5c2,0xbe164083,0xbef15aea,0xbf3daef9,0x3dc8bd36,0xbe888e93,0xbf187fc0,0xbf584853,
0x3f800000,0x3f800000,0x3f800000,0x3f800000,0x3f7b14be,0x3f79c79d,0x3f7853f8,0x3f76ba07,0x3f6c835e,0x3f676bd8,0x3f61c598,0x3f5b941a,0x3f54db31,0x3f49d112,0x3f3daef9,0x3f3085bb,
0x3f3504f3,0x3f226799,0x3f0e39da,0x3ef15aea,0x3f0e39da,0x3ee63375,0x3eac7cd4,0x3e605c13,0x3ec3ef15,0x3e78cfcc,0x3dc8bd36,0xbd48fb30,0x3e47c5c2,0x3cc90ab0,0xbe164083,0xbea09ae5,
0x248d3132,0xbe47c5c2,0xbec3ef15,0xbf0e39da,0xbe47c5c2,0xbecf7bca,0xbf187fc0,0xbf41d870,0xbec3ef15,0xbf187fc0,0xbf45e403,0xbf676bd8,0xbf0e39da,0xbf41d870,0xbf676bd8,0xbf7c3b28,
0xbf3504f3,0xbf61c598,0xbf7b14be,0xbf7ec46d,0xbf54db31,0xbf76ba07,0xbf7fb10f,0xbf6ed89e,0xbf6c835e,0xbf7fb10f,0xbf74fa0b,0xbf4d9f02,0xbf7b14be,0xbf7c3b28,0xbf5b941a,0xbf1d7fd1,
0x3f800000,0x3f800000,0x3f800000,0x3f800000,0x3f74fa0b,0x3f731447,0x3f710908,0x3f6ed89e,0x3f54db31,0x3f4d9f02,0x3f45e403,0x3f3daef9,0x3f226799,0x3f13682a,0x3f039c3d,0x3ee63375,
0x3ec3ef15,0x3e94a031,0x3e47c5c2,0x3dc8bd36,0x3dc8bd36,0xbcc90ab0,0xbe164083,0xbe888e93,0xbe47c5c2,0xbeac7cd4,0xbef15aea,0xbf187fc0,0xbef15aea,0xbf1d7fd1,0xbf3daef9,0xbf584853,
0xbf3504f3,0xbf54db31,0xbf6c835e,0xbf7b14be,0xbf61c598,0xbf76ba07,0xbf7fb10f,0xbf7c3b28,0xbf7b14be,0xbf7fb10f,0xbf74fa0b,0xbf5b941a,0xbf7ec46d,0xbf6ed89e,0xbf4d9f02,0xbf1d7fd1,
0xbf6c835e,0xbf45e403,0xbf0e39da,0xbe94a031,0xbf45e403,0xbf08f59b,0xbe78cfcc,0x3d96a905,0xbf0e39da,0xbe78cfcc,0x3dc8bd36,0x3edae880,0xbe94a031,0x3d96a905,0x3edae880,0x3f396842,
0x0,0x0,0x0,0x0,0x0,0x3cc90ab0,0x3d48fb30,0x3d96a905,0x0,0x3d48fb30,0x3dc8bd36,0x3e164083,0x0,0x3d96a905,0x3e164083,0x3e605c13,
0x0,0x3dc8bd36,0x3e47c5c2,0x3e94a031,0x0,0x3dfab273,0x3e78cfcc,0x3eb8442a,0x0,0x3e164083,0x3e94a031,0x3edae880,0x0,0x3e2f10a2,0x3eac7cd4,0x3efc5d27,
0x0,0x3e47c5c2,0x3ec3ef15,0x3f0e39da,0x0,0x3e605c13,0x3edae880,0x3f1d7fd1,0x0,0x3e78cfcc,0x3ef15aea,0x3f2beb4a,0x0,0x3e888e93,0x3f039c3d,0x3f396842,
0x0,0x3e94a031,0x3f0e39da,0x3f45e403,0x0,0x3ea09ae5,0x3f187fc0,0x3f514d3d,0x0,0x3eac7cd4,0x3f226799,0x3f5b941a,0x0,0x3eb8442a,0x3f2beb4a,0x3f64aa59,
0x0,0x0,0x0,0x0,0x3dc8bd36,0x3dfab273,0x3e164083,0x3e2f10a2,0x3e47c5c2,0x3e78cfcc,0x3e94a031,0x3eac7cd4,0x3e94a031,0x3eb8442a,0x3edae880,0x3efc5d27,
0x3ec3ef15,0x3ef15aea,0x3f0e39da,0x3f226799,0x3ef15aea,0x3f13682a,0x3f2beb4a,0x3f41d870,0x3f0e39da,0x3f2beb4a,0x3f45e403,0x3f5b941a,0x3f226799,0x3f41d870,0x3f5b941a,0x3f6ed89e,
0x3f3504f3,0x3f54db31,0x3f6c835e,0x3f7b14be,0x3f45e403,0x3f64aa59,0x3f7853f8,0x3f7fec43,0x3f54db31,0x3f710908,0x3f7ec46d,0x3f7d3aac,0x3f61c598,0x3f79c79d,0x3f7fb10f,0x3f731447,
0x3f6c835e,0x3f7ec46d,0x3f7b14be,0x3f61c598,0x3f74fa0b,0x3f7fec43,0x3f710908,0x3f49d112,0x3f7b14be,0x3f7d3aac,0x3f61c598,0x3f2beb4a,0x3f7ec46d,0x3f76ba07,0x3f4d9f02,0x3f08f59b,
0x0,0x0,0x0,0x0,0x3e47c5c2,0x3e605c13,0x3e78cfcc,0x3e888e93,0x3ec3ef15,0x3edae880,0x3ef15aea,0x3f039c3d,0x3f0e39da,0x3f1d7fd1,0x3f2beb4a,0x3f396842,
0x3f3504f3,0x3f45e403,0x3f54db31,0x3f61c598,0x3f54db31,0x3f64aa59,0x3f710908,0x3f79c79d,0x3f6c835e,0x3f7853f8,0x3f7ec46d,0x3f7fb10f,0x3f7b14be,0x3f7fec43,0x3f7d3aac,0x3f731447,
0x3f800000,0x3f7b14be,0x3f6c835e,0x3f54db31,0x3f7b14be,0x3f6a09a7,0x3f4d9f02,0x3f273656,0x3f6c835e,0x3f4d9f02,0x3f226799,0x3edae880,0x3f54db31,0x3f273656,0x3edae880,0x3e2f10a2,
0x3f3504f3,0x3ef15aea,0x3e47c5c2,0xbdc8bd36,0x3f0e39da,0x3e888e93,0xbd48fb30,0xbeb8442a,0x3ec3ef15,0x3d48fb30,0xbe94a031,0xbf187fc0,0x3e47c5c2,0xbe2f10a2,0xbf039c3d,0xbf49d112,
0x0,0x0,0x0,0x0,0x3e94a031,0x3ea09ae5,0x3eac7cd4,0x3eb8442a,0x3f0e39da,0x3f187fc0,0x3f226799,0x3f2beb4a,0x3f45e403,0x3f514d3d,0x3f5b941a,0x3f64aa59,
0x3f6c835e,0x3f74fa0b,0x3f7b14be,0x3f7ec46d,0x3f7ec46d,0x3f7fec43,0x3f7d3aac,0x3f76ba07,0x3f7b14be,0x3f710908,0x3f61c598,0x3f4d9f02,0x3f61c598,0x3f49d112,0x3f2beb4a,0x3f08f59b,
0x3f3504f3,0x3f0e39da,0x3ec3ef15,0x3e47c5c2,0x3ef15aea,0x3e888e93,0x3d48fb30,0xbe2f10a2,0x3e47c5c2,0xbd48fb30,0xbe94a031,0xbf039c3d,0xbdc8bd36,0xbeb8442a,0xbf187fc0,0xbf49d112,
0xbec3ef15,0xbf226799,0xbf54db31,0xbf74fa0b,0xbf226799,0xbf584853,0xbf7853f8,0xbf7f4e6d,0xbf54db31,0xbf7853f8,0xbf7ec46d,0xbf676bd8,0xbf74fa0b,0xbf7f4e6d,0xbf676bd8,0xbf3085bb,
0x3f800000,0x3f800000,0x3f800000,0x3f800000,0x3f800000,0x3f7fec43,0x3f7fb10f,0x3f7f4e6d,0x3f800000,0x3f7fb10f,0x3f7ec46d,0x3f7d3aac,0x3f800000,0x3f7f4e6d,0x3f7d3aac,0x3f79c79d,
0x3f800000,0x3f7ec46d,0x3f7b14be,0x3f74fa0b,0x3f800000,0x3f7e1324,0x3f7853f8,0x3f6ed89e,0x3f800000,0x3f7d3aac,0x3f74fa0b,0x3f676bd8,0x3f800000,0x3f7c3b28,0x3f710908,0x3f5ebe05,
0x3f800000,0x3f7b14be,0x3f6c835e,0x3f54db31,0x3f800000,0x3f79c79d,0x3f676bd8,0x3f49d112,0x3f800000,0x3f7853f8,0x3f61c598,0x3f3daef9,0x3f800000,0x3f76ba07,0x3f5b941a,0x3f3085bb,
0x3f800000,0x3f74fa0b,0x3f54db31,0x3f226799,0x3f800000,0x3f731447,0x3f4d9f02,0x3f13682a,0x3f800000,0x3f710908,0x3f45e403,0x3f039c3d,0x3f800000,0x3f6ed89e,0x3f3daef9,0x3ee63375,
0x3f800000,0x3f800000,0x3f800000,0x3f800000,0x3f7ec46d,0x3f7e1324,0x3f7d3aac,0x3f7c3b28,0x3f7b14be,0x3f7853f8,0x3f74fa0b,0x3f710908,0x3f74fa0b,0x3f6ed89e,0x3f676bd8,0x3f5ebe05,
0x3f6c835e,0x3f61c598,0x3f54db31,0x3f45e403,0x3f61c598,0x3f514d3d,0x3f3daef9,0x3f273656,0x3f54db31,0x3f3daef9,0x3f226799,0x3f039c3d,0x3f45e403,0x3f273656,0x3f039c3d,0x3eb8442a,
0x3f3504f3,0x3f0e39da,0x3ec3ef15,0x3e47c5c2,0x3f226799,0x3ee63375,0x3e78cfcc,0x3cc90ab0,0x3f0e39da,0x3eac7cd4,0x3dc8bd36,0xbe164083,0x3ef15aea,0x3e605c13,0xbd48fb30,0xbea09ae5,
0x3ec3ef15,0x3dc8bd36,0xbe47c5c2,0xbef15aea,0x3e94a031,0xbcc90ab0,0xbeac7cd4,0xbf1d7fd1,0x3e47c5c2,0xbe164083,0xbef15aea,0xbf3daef9,0x3dc8bd36,0xbe888e93,0xbf187fc0,0xbf584853,
0x3f800000,0x3f800000,0x3f800000,0x3f800000,0x3f7b14be,0x3f79c79d,0x3f7853f8,0x3f76ba07,0x3f6c835e,0x3f676bd8,0x3f61c598,0x3f5b941a,0x3f54db31,0x3f49d112,0x3f3daef9,0x3f3085bb,
0x3f3504f3,0x3f226799,0x3f0e39da,0x3ef15aea,0x3f0e39da,0x3ee63375,0x3eac7cd4,0x3e605c13,0x3ec3ef15,0x3e78cfcc,0x3dc8bd36,0xbd48fb30,0x3e47c5c2,0x3cc90ab0,0xbe164083,0xbea09ae5,
0x248d3132,0xbe47c5c2,0xbec3ef15,0xbf0e39da,0xbe47c5c2,0xbecf7bca,0xbf187fc0,0xbf41d870,0xbec3ef15,0xbf187fc0,0xbf45e403,0xbf676bd8,0xbf0e39da,0xbf41d870,0xbf676bd8,0xbf7c3b28,
0xbf3504f3,0xbf61c598,0xbf7b14be,0xbf7ec46d,0xbf54db31,0xbf76ba07,0xbf7fb10f,0xbf6ed89e,0xbf6c835e,0xbf7fb10f,0xbf74fa0b,0xbf4d9f02,0xbf7b14be,0xbf7c3b28,0xbf5b941a,0xbf1d7fd1,
0x3f800000,0x3f800000,0x3f800000,0x3f800000,0x3f74fa0b,0x3f731447,0x3f710908,0x3f6ed89e,0x3f54db31,0x3f4d9f02,0x3f45e403,0x3f3daef9,0x3f226799,0x3f13682a,0x3f039c3d,0x3ee63375,
0x3ec3ef15,0x3e94a031,0x3e47c5c2,0x3dc8bd36,0x3dc8bd36,0xbcc90ab0,0xbe164083,0xbe888e93,0xbe47c5c2,0xbeac7cd4,0xbef15aea,0xbf187fc0,0xbef15aea,0xbf1d7fd1,0xbf3daef9,0xbf584853,
0xbf3504f3,0xbf54db31,0xbf6c835e,0xbf7b14be,0xbf61c598,0xbf76ba07,0xbf7fb10f,0xbf7c3b28,0xbf7b14be,0xbf7fb10f,0xbf74fa0b,0xbf5b941a,0xbf7ec46d,0xbf6ed89e,0xbf4d9f02,0xbf1d7fd1,
0xbf6c835e,0xbf45e403,0xbf0e39da,0xbe94a031,0xbf45e403,0xbf08f59b,0xbe78cfcc,0x3d96a905,0xbf0e39da,0xbe78cfcc,0x3dc8bd36,0x3edae880,0xbe94a031,0x3d96a905,0x3edae880,0x3f396842,
0x0,0x0,0x0,0x0,0x0,0x3cc90ab0,0x3d48fb30,0x3d96a905,0x0,0x3d48fb30,0x3dc8bd36,0x3e164083,0x0,0x3d96a905,0x3e164083,0x3e605c13,
0x0,0x3dc8bd36,0x3e47c5c2,0x3e94a031,0x0,0x3dfab273,0x3e78cfcc,0x3eb8442a,0x0,0x3e164083,0x3e94a031,0x3edae880,0x0,0x3e2f10a2,0x3eac7cd4,0x3efc5d27,
0x0,0x3e47c5c2,0x3ec3ef15,0x3f0e39da,0x0,0x3e605c13,0x3edae880,0x3f1d7fd1,0x0,0x3e78cfcc,0x3ef15aea,0x3f2beb4a,0x0,0x3e888e93,0x3f039c3d,0x3f396842,
0x0,0x3e94a031,0x3f0e39da,0x3f45e403,0x0,0x3ea09ae5,0x3f187fc0,0x3f514d3d,0x0,0x3eac7cd4,0x3f226799,0x3f5b941a,0x0,0x3eb8442a,0x3f2beb4a,0x3f64aa59,
0x0,0x0,0x0,0x0,0x3dc8bd36,0x3dfab273,0x3e164083,0x3e2f10a2,0x3e47c5c2,0x3e78cfcc,0x3e94a031,0x3eac7cd4,0x3e94a031,0x3eb8442a,0x3edae880,0x3efc5d27,
0x3ec3ef15,0x3ef15aea,0x3f0e39da,0x3f226799,0x3ef15aea,0x3f13682a,0x3f2beb4a,0x3f41d870,0x3f0e39da,0x3f2beb4a,0x3f45e403,0x3f5b941a,0x3f226799,0x3f41d870,0x3f5b941a,0x3f6ed89e,
0x3f3504f3,0x3f54db31,0x3f6c835e,0x3f7b14be,0x3f45e403,0x3f64aa59,0x3f7853f8,0x3f7fec43,0x3f54db31,0x3f710908,0x3f7ec46d,0x3f7d3aac,0x3f61c598,0x3f79c79d,0x3f7fb10f,0x3f731447,
0x3f6c835e,0x3f7ec46d,0x3f7b14be,0x3f61c598,0x3f74fa0b,0x3f7fec43,0x3f710908,0x3f49d112,0x3f7b14be,0x3f7d3aac,0x3f61c598,0x3f2beb4a,0x3f7ec46d,0x3f76ba07,0x3f4d9f02,0x3f08f59b,
0x0,0x0,0x0,0x0,0x3e47c5c2,0x3e605c13,0x3e78cfcc,0x3e888e93,0x3ec3ef15,0x3edae880,0x3ef15aea,0x3f039c3d,0x3f0e39da,0x3f1d7fd1,0x3f2beb4a,0x3f396842,
0x3f3504f3,0x3f45e403,0x3f54db31,0x3f61c598,0x3f54db31,0x3f64aa59,0x3f710908,0x3f79c79d,0x3f6c835e,0x3f7853f8,0x3f7ec46d,0x3f7fb10f,0x3f7b14be,0x3f7fec43,0x3f7d3aac,0x3f731447,
0x3f800000,0x3f7b14be,0x3f6c835e,0x3f54db31,0x3f7b14be,0x3f6a09a7,0x3f4d9f02,0x3f273656,0x3f6c835e,0x3f4d9f02,0x3f226799,0x3edae880,0x3f54db31,0x3f273656,0x3edae880,0x3e2f10a2,
0x3f3504f3,0x3ef15aea,0x3e47c5c2,0xbdc8bd36,0x3f0e39da,0x3e888e93,0xbd48fb30,0xbeb8442a,0x3ec3ef15,0x3d48fb30,0xbe94a031,0xbf187fc0,0x3e47c5c2,0xbe2f10a2,0xbf039c3d,0xbf49d112,
0x0,0x0,0x0,0x0,0x3e94a031,0x3ea09ae5,0x3eac7cd4,0x3eb8442a,0x3f0e39da,0x3f187fc0,0x3f226799,0x3f2beb4a,0x3f45e403,0x3f514d3d,0x3f5b941a,0x3f64aa59,
0x3f6c835e,0x3f74fa0b,0x3f7b14be,0x3f7ec46d,0x3f7ec46d,0x3f7fec43,0x3f7d3aac,0x3f76ba07,0x3f7b14be,0x3f710908,0x3f61c598,0x3f4d9f02,0x3f61c598,0x3f49d112,0x3f2beb4a,0x3f08f59b,
0x3f3504f3,0x3f0e39da,0x3ec3ef15,0x3e47c5c2,0x3ef15aea,0x3e888e93,0x3d48fb30,0xbe2f10a2,0x3e47c5c2,0xbd48fb30,0xbe94a031,0xbf039c3d,0xbdc8bd36,0xbeb8442a,0xbf187fc0,0xbf49d112,
0xbec3ef15,0xbf226799,0xbf54db31,0xbf74fa0b,0xbf226799,0xbf584853,0xbf7853f8,0xbf7f4e6d,0xbf54db31,0xbf7853f8,0xbf7ec46d,0xbf676bd8,0xbf74fa0b,0xbf7f4e6d,0xbf676bd8,0xbf3085bb,
0x3400,0x224833fe,0x264633f6,0x28b233ea,0x3400,0x2ed7333b,0x322f3113,0x33c32bc6,0x3400,0x31ed315f,0x33f6a646,0x30c4b26d,0x3400,0x33882d64,0x3113b22f,0xb01db2dd,
0x3400,0x33fea248,0xa646b3f6,0xb3ea28b2,0x3400,0x333baed7,0xb22fb113,0xabc633c3,0x3400,0x315fb1ed,0xb3f62646,0x326d30c4,0x3400,0x2d64b388,0xb113322f,0x32ddb01d,
0x3400,0xa248b3fe,0x264633f6,0xa8b2b3ea,0x3400,0xaed7b33b,0x322f3113,0xb3c3abc6,0x3400,0xb1edb15f,0x33f6a646,0xb0c4326d,0x3400,0xb388ad64,0x3113b22f,0x301d32dd,
0x3400,0xb3fe2248,0xa646b3f6,0x33eaa8b2,0x3400,0xb33b2ed7,0xb22fb113,0x2bc6b3c3,0x3400,0xb15f31ed,0xb3f62646,0xb26db0c4,0x3400,0xad643388,0xb113322f,0xb2dd301d,
0x2a3e33d9,0x2bc633c3,0x2ca533a8,0x2d643388,0x33d9aa3e,0x326db0c4,0x2f8bb30e,0x2248b3fe,0xaa3eb3d9,0xb2ddb01d,0xb3a82ca5,0xaed7333b,0xb3d92a3e,0xa8b233ea,0x330e2f8b,0x31edb15f,
0x2a3e33d9,0x33c3abc6,0xaca5b3a8,0xb3882d64,0x33d9aa3e,0xb0c4b26d,0xaf8b330e,0x33fe2248,0xaa3eb3d9,0xb01d32dd,0x33a8aca5,0xb33baed7,0xb3d92a3e,0x33ea28b2,0xb30eaf8b,0x315f31ed,
0x2a3e33d9,0xabc6b3c3,0x2ca533a8,0xad64b388,0x33d9aa3e,0xb26d30c4,0x2f8bb30e,0xa24833fe,0xaa3eb3d9,0x32dd301d,0xb3a82ca5,0x2ed7b33b,0xb3d92a3e,0x28b2b3ea,0x330e2f8b,0xb1ed315f,
0x2a3e33d9,0xb3c32bc6,0xaca5b3a8,0x3388ad64,0x33d9aa3e,0x30c4326d,0xaf8b330e,0xb3fea248,0xaa3eb3d9,0x301db2dd,0x33a8aca5,0x333b2ed7,0xb3d92a3e,0xb3eaa8b2,0xb30eaf8b,0xb15fb1ed,
0x2e1f3364,0x2ed7333b,0x2f8b330e,0x301d32dd,0xae1fb364,0xb1edb15f,0xb3a8aca5,0xb3ea28b2,0x2e1f3364,0x33882d64,0x330eaf8b,0x2bc6b3c3,0xae1fb364,0xb3fe2248,0xaca533a8,0x326d30c4,
0x2e1f3364,0x333baed7,0xaf8bb30e,0xb2dd301d,0xae1fb364,0xb15f31ed,0x33a82ca5,0xa8b2b3ea,0x2e1f3364,0x2d64b388,0xb30e2f8b,0x33c32bc6,0xae1fb364,0x224833fe,0x2ca5b3a8,0xb0c4326d,
0x2e1f3364,0xaed7b33b,0x2f8b330e,0xb01db2dd,0xae1fb364,0x31ed315f,0xb3a8aca5,0x33eaa8b2,0x2e1f3364,0xb388ad64,0x330eaf8b,0xabc633c3,0xae1fb364,0x33fea248,0xaca533a8,0xb26db0c4,
0x2e1f3364,0xb33b2ed7,0xaf8bb30e,0x32ddb01d,0xae1fb364,0x315fb1ed,0x33a82ca5,0x28b233ea,0x2e1f3364,0xad643388,0xb30e2f8b,0xb3c3abc6,0xae1fb364,0xa248b3fe,0x2ca5b3a8,0x30c4b26d,
0x307232a7,0x30c4326d,0x3113322f,0x315f31ed,0xb2a73072,0xb01d32dd,0xa64633f6,0x2d643388,0xb072b2a7,0xb3eaa8b2,0xb22f3113,0xa24833fe,0x32a7b072,0xabc6b3c3,0xb3f6a646,0xaed7333b,
0x307232a7,0x326db0c4,0xb113b22f,0xb1ed315f,0xb2a73072,0x32dd301d,0x2646b3f6,0xb3882d64,0xb072b2a7,0xa8b233ea,0x322fb113,0xb3fea248,0x32a7b072,0xb3c32bc6,0x33f62646,0xb33baed7,
0x307232a7,0xb0c4b26d,0x3113322f,0xb15fb1ed,0xb2a73072,0x301db2dd,0xa64633f6,0xad64b388,0xb072b2a7,0x33ea28b2,0xb22f3113,0x2248b3fe,0x32a7b072,0x2bc633c3,0xb3f6a646,0x2ed7b33b,
0x307232a7,0xb26d30c4,0xb113b22f,0x31edb15f,0xb2a73072,0xb2ddb01d,0x2646b3f6,0x3388ad64,0xb072b2a7,0x28b2b3ea,0x322fb113,0x33fe2248,0x32a7b072,0x33c3abc6,0x33f62646,0x333b2ed7,
0x34000000,0x33fea248,0x33f6a646,0x33eaa8b2,0x34000000,0x333baed7,0x3113b22f,0x2bc6b3c3,0x34000000,0x315fb1ed,0xa646b3f6,0xb26db0c4,0x34000000,0x2d64b388,0xb22fb113,0xb2dd301d,
0x34000000,0xa248b3fe,0xb3f62646,0x28b233ea,0x34000000,0xaed7b33b,0xb113322f,0x33c32bc6,0x34000000,0xb1edb15f,0x264633f6,0x30c4b26d,0x34000000,0xb388ad64,0x322f3113,0xb01db2dd,
0x34000000,0xb3fe2248,0x33f6a646,0xb3ea28b2,0x34000000,0xb33b2ed7,0x3113b22f,0xabc633c3,0x34000000,0xb15f31ed,0xa646b3f6,0x326d30c4,0x34000000,0xad643388,0xb22fb113,0x32ddb01d,
0x34000000,0x224833fe,0xb3f62646,0xa8b2b3ea,0x34000000,0x2ed7333b,0xb113322f,0xb3c3abc6,0x34000000,0x31ed315f,0x264633f6,0xb0c4326d,0x34000000,0x33882d64,0x322f3113,0x301d32dd,
0x33d9aa3e,0x33c3abc6,0x33a8aca5,0x3388ad64,0xaa3eb3d9,0xb0c4b26d,0xb30eaf8b,0xb3fea248,0xb3d92a3e,0xb01d32dd,0x2ca533a8,0x333b2ed7,0x2a3e33d9,0x33ea28b2,0x2f8bb30e,0xb15fb1ed,
0x33d9aa3e,0xabc6b3c3,0xb3a82ca5,0x2d643388,0xaa3eb3d9,0xb26d30c4,0x330e2f8b,0x2248b3fe,0xb3d92a3e,0x32dd301d,0xaca5b3a8,0xaed7333b,0x2a3e33d9,0x28b2b3ea,0xaf8b330e,0x31edb15f,
0x33d9aa3e,0xb3c32bc6,0x33a8aca5,0xb3882d64,0xaa3eb3d9,0x30c4326d,0xb30eaf8b,0x33fe2248,0xb3d92a3e,0x301db2dd,0x2ca533a8,0xb33baed7,0x2a3e33d9,0xb3eaa8b2,0x2f8bb30e,0x315f31ed,
0x33d9aa3e,0x2bc633c3,0xb3a82ca5,0xad64b388,0xaa3eb3d9,0x326db0c4,0x330e2f8b,0xa24833fe,0xb3d92a3e,0xb2ddb01d,0xaca5b3a8,0x2ed7b33b,0x2a3e33d9,0xa8b233ea,0xaf8b330e,0xb1ed315f,
0x3364ae1f,0x333baed7,0x330eaf8b,0x32ddb01d,0xb3642e1f,0xb15f31ed,0xaca533a8,0x28b233ea,0x3364ae1f,0x2d64b388,0xaf8bb30e,0xb3c3abc6,0xb3642e1f,0x224833fe,0x33a82ca5,0x30c4b26d,
0x3364ae1f,0xaed7b33b,0xb30e2f8b,0x301d32dd,0xb3642e1f,0x31ed315f,0x2ca5b3a8,0xb3ea28b2,0x3364ae1f,0xb388ad64,0x2f8b330e,0x2bc6b3c3,0xb3642e1f,0x33fea248,0xb3a8aca5,0x326d30c4,
0x3364ae1f,0xb33b2ed7,0x330eaf8b,0xb2dd301d,0xb3642e1f,0x315fb1ed,0xaca533a8,0xa8b2b3ea,0x3364ae1f,0xad643388,0xaf8bb30e,0x33c32bc6,0xb3642e1f,0xa248b3fe,0x33a82ca5,0xb0c4326d,
0x3364ae1f,0x2ed7333b,0xb30e2f8b,0xb01db2dd,0xb3642e1f,0xb1edb15f,0x2ca5b3a8,0x33eaa8b2,0x3364ae1f,0x33882d64,0x2f8b330e,0xabc633c3,0xb3642e1f,0xb3fe2248,0xb3a8aca5,0xb26db0c4,
0x32a7b072,0x326db0c4,0x322fb113,0x31edb15f,0x307232a7,0x32dd301d,0x33f62646,0x3388ad64,0xb2a73072,0xa8b233ea,0x3113322f,0x33fe2248,0xb072b2a7,0xb3c32bc6,0xa64633f6,0x333b2ed7,
0x32a7b072,0xb0c4b26d,0xb22f3113,0x315f31ed,0x307232a7,0x301db2dd,0xb3f6a646,0x2d643388,0xb2a73072,0x33ea28b2,0xb113b22f,0xa24833fe,0xb072b2a7,0x2bc633c3,0x2646b3f6,0xaed7333b,
0x32a7b072,0xb26d30c4,0x322fb113,0xb1ed315f,0x307232a7,0xb2ddb01d,0x33f62646,0xb3882d64,0xb2a73072,0x28b2b3ea,0x3113322f,0xb3fea248,0xb072b2a7,0x33c3abc6,0xa64633f6,0xb33baed7,
0x32a7b072,0x30c4326d,0xb22f3113,0xb15fb1ed,0x307232a7,0xb01d32dd,0xb3f6a646,0xad64b388,0xb2a73072,0xb3eaa8b2,0xb113b22f,0x2248b3fe,0xb072b2a7,0xabc6b3c3,0x2646b3f6,0x2ed7b33b,
0x3400,0x3400,0x3400,0x3400,0x3400,0x2e1f3364,0x31a831a8,0x33642e1f,0x3400,0x31a831a8,0x34000000,0x31a8b1a8,0x3400,0x33642e1f,0x31a8b1a8,0xae1fb364,
0x3400,0x34000000,0xb400,0xb4008000,0x3400,0x3364ae1f,0xb1a8b1a8,0xae1f3364,0x3400,0x31a8b1a8,0xb4008000,0x31a831a8,0x3400,0x2e1fb364,0xb1a831a8,0x3364ae1f,
0x3400,0xb400,0x3400,0xb400,0x3400,0xae1fb364,0x31a831a8,0xb364ae1f,0x3400,0xb1a8b1a8,0x34000000,0xb1a831a8,0x3400,0xb364ae1f,0x31a8b1a8,0x2e1f3364,
0x3400,0xb4008000,0xb400,0x34000000,0x3400,0xb3642e1f,0xb1a8b1a8,0x2e1fb364,0x3400,0xb1a831a8,0xb4008000,0xb1a8b1a8,0x3400,0xae1f3364,0xb1a831a8,0xb3642e1f,
0x3400,0x3400,0x3400,0x3400,0x34000000,0x3364ae1f,0x31a8b1a8,0x2e1fb364,0xb400,0xb1a8b1a8,0xb4008000,0xb1a831a8,0xb4008000,0xae1f3364,0x31a831a8,0x3364ae1f,
0x3400,0x34000000,0xb400,0xb4008000,0x34000000,0xae1fb364,0xb1a831a8,0x33642e1f,0xb400,0xb1a831a8,0x34000000,0xb1a8b1a8,0xb4008000,0x33642e1f,0xb1a8b1a8,0x2e1f3364,
0x3400,0xb400,0x3400,0xb400,0x34000000,0xb3642e1f,0x31a8b1a8,0xae1f3364,0xb400,0x31a831a8,0xb4008000,0x31a8b1a8,0xb4008000,0x2e1fb364,0x31a831a8,0xb3642e1f,
0x3400,0xb4008000,0xb400,0x34000000,0x34000000,0x2e1f3364,0xb1a831a8,0xb364ae1f,0xb400,0x31a8b1a8,0x34000000,0x31a831a8,0xb4008000,0xb364ae1f,0xb1a8b1a8,0xae1fb364,
0x3400,0x3400,0x3400,0x3400,0xb400,0xae1fb364,0xb1a8b1a8,0xb364ae1f,0x3400,0x31a831a8,0x34000000,0x31a8b1a8,0xb400,0xb364ae1f,0xb1a831a8,0x2e1f3364,
0x3400,0x34000000,0xb400,0xb4008000,0xb400,0xb3642e1f,0x31a831a8,0x2e1fb364,0x3400,0x31a8b1a8,0xb4008000,0x31a831a8,0xb400,0xae1f3364,0x31a8b1a8,0xb3642e1f,
0x3400,0xb400,0x3400,0xb400,0xb400,0x2e1f3364,0xb1a8b1a8,0x33642e1f,0x3400,0xb1a8b1a8,0x34000000,0xb1a831a8,0xb400,0x33642e1f,0xb1a831a8,0xae1fb364,
0x3400,0xb4008000,0xb400,0x34000000,0xb400,0x3364ae1f,0x31a831a8,0xae1f3364,0x3400,0xb1a831a8,0xb4008000,0xb1a8b1a8,0xb400,0x2e1fb364,0x31a8b1a8,0x3364ae1f,
0x3400,0x3400,0x3400,0x3400,0xb4008000,0xb3642e1f,0xb1a831a8,0xae1f3364,0xb400,0xb1a8b1a8,0xb4008000,0xb1a831a8,0x34000000,0x2e1fb364,0xb1a8b1a8,0xb3642e1f,
0x3400,0x34000000,0xb400,0xb4008000,0xb4008000,0x2e1f3364,0x31a8b1a8,0xb364ae1f,0xb400,0xb1a831a8,0x34000000,0xb1a8b1a8,0x34000000,0xb364ae1f,0x31a831a8,0xae1fb364,
0x3400,0xb400,0x3400,0xb400,0xb4008000,0x3364ae1f,0xb1a831a8,0x2e1fb364,0xb400,0x31a831a8,0xb4008000,0x31a8b1a8,0x34000000,0xae1f3364,0xb1a8b1a8,0x3364ae1f,
0x3400,0xb4008000,0xb400,0x34000000,0xb4008000,0xae1fb364,0x31a8b1a8,0x33642e1f,0xb400,0x31a8b1a8,0x34000000,0x31a831a8,0x34000000,0x33642e1f,0x31a831a8,0x2e1f3364,
0x34000000,0x34000000,0x34000000,0x34000000,0x34000000,0x3364ae1f,0x31a8b1a8,0x2e1fb364,0x34000000,0x31a8b1a8,0xb400,0xb1a8b1a8,0x34000000,0x2e1fb364,0xb1a8b1a8,0xb3642e1f,
0x34000000,0xb400,0xb4008000,0x80003400,0x34000000,0xae1fb364,0xb1a831a8,0x33642e1f,0x34000000,0xb1a8b1a8,0x80003400,0x31a8b1a8,0x34000000,0xb364ae1f,0x31a831a8,0xae1fb364,
0x34000000,0xb4008000,0x34000000,0xb4008000,0x34000000,0xb3642e1f,0x31a8b1a8,0xae1f3364,0x34000000,0xb1a831a8,0xb400,0x31a831a8,0x34000000,0xae1f3364,0xb1a8b1a8,0x3364ae1f,
0x34000000,0x80003400,0xb4008000,0xb400,0x34000000,0x2e1f3364,0xb1a831a8,0xb364ae1f,0x34000000,0x31a831a8,0x80003400,0xb1a831a8,0x34000000,0x33642e1f,0x31a831a8,0x2e1f3364,
0x34000000,0x34000000,0x34000000,0x34000000,0xb400,0xae1fb364,0xb1a8b1a8,0xb364ae1f,0xb4008000,0xb1a831a8,0x80003400,0x31a831a8,0x80003400,0x33642e1f,0x31a8b1a8,0xae1fb364,
0x34000000,0xb400,0xb4008000,0x80003400,0xb400,0xb3642e1f,0x31a831a8,0x2e1fb364,0xb4008000,0x31a831a8,0xb400,0xb1a831a8,0x80003400,0x2e1fb364,0xb1a831a8,0x3364ae1f,
0x34000000,0xb4008000,0x34000000,0xb4008000,0xb400,0x2e1f3364,0xb1a8b1a8,0x33642e1f,0xb4008000,0x31a8b1a8,0x80003400,0xb1a8b1a8,0x80003400,0xb364ae1f,0x31a8b1a8,0x2e1f3364,
0x34000000,0x80003400,0xb4008000,0xb400,0xb400,0x3364ae1f,0x31a831a8,0xae1f3364,0xb4008000,0xb1a8b1a8,0xb400,0x31a8b1a8,0x80003400,0xae1f3364,0xb1a831a8,0xb3642e1f,
0x34000000,0x34000000,0x34000000,0x34000000,0xb4008000,0xb3642e1f,0xb1a831a8,0xae1f3364,0x34000000,0x31a8b1a8,0xb400,0xb1a8b1a8,0xb4008000,0xae1f3364,0x31a831a8,0x3364ae1f,
0x34000000,0xb400,0xb4008000,0x80003400,0xb4008000,0x2e1f3364,0x31a8b1a8,0xb364ae1f,0x34000000,0xb1a8b1a8,0x80003400,0x31a8b1a8,0xb4008000,0x33642e1f,0xb1a8b1a8,0x2e1f3364,
0x34000000,0xb4008000,0x34000000,0xb4008000,0xb4008000,0x3364ae1f,0xb1a831a8,0x2e1fb364,0x34000000,0xb1a831a8,0xb400,0x31a831a8,0xb4008000,0x2e1fb364,0x31a831a8,0xb3642e1f,
0x34000000,0x80003400,0xb4008000,0xb400,0xb4008000,0xae1fb364,0x31a8b1a8,0x33642e1f,0x34000000,0x31a831a8,0x80003400,0xb1a831a8,0xb4008000,0xb364ae1f,0xb1a8b1a8,0xae1fb364,
0x34000000,0x34000000,0x34000000,0x34000000,0x80003400,0x2e1f3364,0x31a831a8,0x33642e1f,0xb4008000,0xb1a831a8,0x80003400,0x31a831a8,0xb400,0xb364ae1f,0xb1a831a8,0x2e1f3364,
0x34000000,0xb400,0xb4008000,0x80003400,0x80003400,0x3364ae1f,0xb1a8b1a8,0xae1f3364,0xb4008000,0x31a831a8,0xb400,0xb1a831a8,0xb400,0xae1f3364,0x31a8b1a8,0xb3642e1f,
0x34000000,0xb4008000,0x34000000,0xb4008000,0x80003400,0xae1fb364,0x31a831a8,0xb364ae1f,0xb4008000,0x31a8b1a8,0x80003400,0xb1a8b1a8,0xb400,0x33642e1f,0xb1a831a8,0xae1fb364,
0x34000000,0x80003400,0xb4008000,0xb400,0x80003400,0xb3642e1f,0xb1a8b1a8,0x2e1fb364,0xb4008000,0xb1a8b1a8,0xb400,0x31a8b1a8,0xb400,0x2e1fb364,0x31a8b1a8,0x3364ae1f,
0x3400,0x3400,0x3400,0x3400,0x3400,0xae1f3364,0xb1a831a8,0xb3642e1f,0x3400,0xb1a831a8,0xb4000000,0xb1a8b1a8,0x3400,0xb3642e1f,0xb1a8b1a8,0x2e1fb364,
0x3400,0xb4000000,0x8000b400,0x34008000,0x3400,0xb364ae1f,0x31a8b1a8,0x2e1f3364,0x3400,0xb1a8b1a8,0x34008000,0xb1a831a8,0x3400,0xae1fb364,0x31a831a8,0xb364ae1f,
0x3400,0x8000b400,0x3400,0x8000b400,0x3400,0x2e1fb364,0xb1a831a8,0x3364ae1f,0x3400,0x31a8b1a8,0xb4000000,0x31a831a8,0x3400,0x3364ae1f,0xb1a8b1a8,0xae1f3364,
0x3400,0x34008000,0x8000b400,0xb4000000,0x3400,0x33642e1f,0x31a8b1a8,0xae1fb364,0x3400,0x31a831a8,0x34008000,0x31a8b1a8,0x3400,0x2e1f3364,0x31a831a8,0x33642e1f,
0x3400,0x3400,0x3400,0x3400,0xb4000000,0xb364ae1f,0xb1a8b1a8,0xae1fb364,0x8000b400,0x31a8b1a8,0x34008000,0x31a831a8,0x34008000,0x2e1f3364,0xb1a831a8,0xb364ae1f,
0x3400,0xb4000000,0x8000b400,0x34008000,0xb4000000,0x2e1fb364,0x31a831a8,0xb3642e1f,0x8000b400,0x31a831a8,0xb4000000,0x31a8b1a8,0x34008000,0xb3642e1f,0x31a8b1a8,0xae1f3364,
0x3400,0x8000b400,0x3400,0x8000b400,0xb4000000,0x33642e1f,0xb1a8b1a8,0x2e1f3364,0x8000b400,0xb1a831a8,0x34008000,0xb1a8b1a8,0x34008000,0xae1fb364,0xb1a831a8,0x33642e1f,
0x3400,0x34008000,0x8000b400,0xb4000000,0xb4000000,0xae1f3364,0x31a831a8,0x3364ae1f,0x8000b400,0xb1a8b1a8,0xb4000000,0xb1a831a8,0x34008000,0x3364ae1f,0x31a8b1a8,0x2e1fb364,
0x3400,0x3400,0x3400,0x3400,0x8000b400,0x2e1fb364,0x31a8b1a8,0x3364ae1f,0x3400,0xb1a831a8,0xb4000000,0xb1a8b1a8,0x8000b400,0x3364ae1f,0x31a831a8,0xae1f3364,
0x3400,0xb4000000,0x8000b400,0x34008000,0x8000b400,0x33642e1f,0xb1a831a8,0xae1fb364,0x3400,0xb1a8b1a8,0x34008000,0xb1a831a8,0x8000b400,0x2e1f3364,0xb1a8b1a8,0x33642e1f,
0x3400,0x8000b400,0x3400,0x8000b400,0x8000b400,0xae1f3364,0x31a8b1a8,0xb3642e1f,0x3400,0x31a8b1a8,0xb4000000,0x31a831a8,0x8000b400,0xb3642e1f,0x31a831a8,0x2e1fb364,
0x3400,0x34008000,0x8000b400,0xb4000000,0x8000b400,0xb364ae1f,0xb1a831a8,0x2e1f3364,0x3400,0x31a831a8,0x34008000,0x31a8b1a8,0x8000b400,0xae1fb364,0xb1a8b1a8,0xb364ae1f,
0x3400,0x3400,0x3400,0x3400,0x34008000,0x33642e1f,0x31a831a8,0x2e1f3364,0x8000b400,0x31a8b1a8,0x34008000,0x31a831a8,0xb4000000,0xae1fb364,0x31a8b1a8,0x33642e1f,
0x3400,0xb4000000,0x8000b400,0x34008000,0x34008000,0xae1f3364,0xb1a8b1a8,0x3364ae1f,0x8000b400,0x31a831a8,0xb4000000,0x31a8b1a8,0xb4000000,0x3364ae1f,0xb1a831a8,0x2e1fb364,
0x3400,0x8000b400,0x3400,0x8000b400,0x34008000,0xb364ae1f,0x31a831a8,0xae1fb364,0x8000b400,0xb1a831a8,0x34008000,0xb1a8b1a8,0xb4000000,0x2e1f3364,0x31a8b1a8,0xb364ae1f,
0x3400,0x34008000,0x8000b400,0xb4000000,0x34008000,0x2e1fb364,0xb1a8b1a8,0xb3642e1f,0x8000b400,0xb1a8b1a8,0xb4000000,0xb1a831a8,0xb4000000,0xb3642e1f,0xb1a831a8,0xae1f3364,
0x34000000,0x34000000,0x34000000,0x34000000,0x34000000,0x33642e1f,0x31a831a8,0x2e1f3364,0x34000000,0x31a831a8,0x3400,0xb1a831a8,0x34000000,0x2e1f3364,0xb1a831a8,0xb364ae1f,
0x34000000,0x3400,0xb4000000,0x8000b400,0x34000000,0xae1f3364,0xb1a8b1a8,0x3364ae1f,0x34000000,0xb1a831a8,0x8000b400,0x31a831a8,0x34000000,0xb3642e1f,0x31a8b1a8,0xae1f3364,
0x34000000,0xb4000000,0x34000000,0xb4000000,0x34000000,0xb364ae1f,0x31a831a8,0xae1fb364,0x34000000,0xb1a8b1a8,0x3400,0x31a8b1a8,0x34000000,0xae1fb364,0xb1a831a8,0x33642e1f,
0x34000000,0x8000b400,0xb4000000,0x3400,0x34000000,0x2e1fb364,0xb1a8b1a8,0xb3642e1f,0x34000000,0x31a8b1a8,0x8000b400,0xb1a8b1a8,0x34000000,0x3364ae1f,0x31a8b1a8,0x2e1fb364,
0x34000000,0x34000000,0x34000000,0x34000000,0x3400,0xae1f3364,0xb1a831a8,0xb3642e1f,0xb4000000,0xb1a8b1a8,0x8000b400,0x31a8b1a8,0x8000b400,0x3364ae1f,0x31a831a8,0xae1f3364,
0x34000000,0x3400,0xb4000000,0x8000b400,0x3400,0xb364ae1f,0x31a8b1a8,0x2e1f3364,0xb4000000,0x31a8b1a8,0x3400,0xb1a8b1a8,0x8000b400,0x2e1f3364,0xb1a8b1a8,0x33642e1f,
0x34000000,0xb4000000,0x34000000,0xb4000000,0x3400,0x2e1fb364,0xb1a831a8,0x3364ae1f,0xb4000000,0x31a831a8,0x8000b400,0xb1a831a8,0x8000b400,0xb3642e1f,0x31a831a8,0x2e1fb364,
0x34000000,0x8000b400,0xb4000000,0x3400,0x3400,0x33642e1f,0x31a8b1a8,0xae1fb364,0xb4000000,0xb1a831a8,0x3400,0x31a831a8,0x8000b400,0xae1fb364,0xb1a8b1a8,0xb364ae1f,
0x34000000,0x34000000,0x34000000,0x34000000,0xb4000000,0xb364ae1f,0xb1a8b1a8,0xae1fb364,0x34000000,0x31a831a8,0x3400,0xb1a831a8,0xb4000000,0xae1fb364,0x31a8b1a8,0x33642e1f,
0x34000000,0x3400,0xb4000000,0x8000b400,0xb4000000,0x2e1fb364,0x31a831a8,0xb3642e1f,0x34000000,0xb1a831a8,0x8000b400,0x31a831a8,0xb4000000,0x3364ae1f,0xb1a831a8,0x2e1fb364,
0x34000000,0xb4000000,0x34000000,0xb4000000,0xb4000000,0x33642e1f,0xb1a8b1a8,0x2e1f3364,0x34000000,0xb1a8b1a8,0x3400,0x31a8b1a8,0xb4000000,0x2e1f3364,0x31a8b1a8,0xb364ae1f,
0x34000000,0x8000b400,0xb4000000,0x3400,0xb4000000,0xae1f3364,0x31a831a8,0x3364ae1f,0x34000000,0x31a8b1a8,0x8000b400,0xb1a8b1a8,0xb4000000,0xb3642e1f,0xb1a831a8,0xae1f3364,
0x34000000,0x34000000,0x34000000,0x34000000,0x8000b400,0x2e1fb364,0x31a8b1a8,0x3364ae1f,0xb4000000,0xb1a8b1a8,0x8000b400,0x31a8b1a8,0x3400,0xb3642e1f,0xb1a8b1a8,0x2e1fb364,
0x34000000,0x3400,0xb4000000,0x8000b400,0x8000b400,0x33642e1f,0xb1a831a8,0xae1fb364,0xb4000000,0x31a8b1a8,0x3400,0xb1a8b1a8,0x3400,0xae1fb364,0x31a831a8,0xb364ae1f,
0x34000000,0xb4000000,0x34000000,0xb4000000,0x8000b400,0xae1f3364,0x31a8b1a8,0xb3642e1f,0xb4000000,0x31a831a8,0x8000b400,0xb1a831a8,0x3400,0x3364ae1f,0xb1a8b1a8,0xae1f3364,
0x34000000,0x8000b400,0xb4000000,0x3400,0x8000b400,0xb364ae1f,0xb1a831a8,0x2e1f3364,0xb4000000,0xb1a831a8,0x3400,0x31a831a8,0x3400,0x2e1f3364,0x31a831a8,0x33642e1f,
0x3400,0x3400,0x3400,0x3400,0xa24833fe,0xaed7333b,0xb1ed315f,0xb3882d64,0xa64633f6,0xb22f3113,0xb3f6a646,0xb113b22f,0xa8b233ea,0xb3c32bc6,0xb0c4b26d,0x301db2dd,
0xaa3e33d9,0xb3d9aa3e,0x2a3eb3d9,0x33d92a3e,0xabc633c3,0xb26db0c4,0x32ddb01d,0x28b233ea,0xaca533a8,0xaf8bb30e,0x33a82ca5,0xb30e2f8b,0xad643388,0xa248b3fe,0x2ed7333b,0xb1edb15f,
0xae1f3364,0x2e1fb364,0xae1f3364,0x2e1fb364,0xaed7333b,0x31edb15f,0xb3882d64,0x33fe2248,0xaf8b330e,0x33a8aca5,0xb30eaf8b,0x2ca533a8,0xb01d32dd,0x33ea28b2,0xabc6b3c3,0xb26d30c4,
0xb07232a7,0x32a73072,0x3072b2a7,0xb2a7b072,0xb0c4326d,0x301d32dd,0x33eaa8b2,0x2bc6b3c3,0xb113322f,0x264633f6,0x322f3113,0x33f6a646,0xb15f31ed,0xad643388,0x224833fe,0x2ed7333b,
0x3400,0x3400,0x3400,0x3400,0xb3fea248,0xb33baed7,0xb15fb1ed,0xad64b388,0x2646b3f6,0x322fb113,0x33f62646,0x3113322f,0x33ea28b2,0x2bc633c3,0xb26d30c4,0xb2ddb01d,
0xaa3e33d9,0xb3d9aa3e,0x2a3eb3d9,0x33d92a3e,0xb3c3abc6,0x30c4b26d,0x301d32dd,0xb3ea28b2,0x2ca5b3a8,0x2f8b330e,0xb3a8aca5,0x330eaf8b,0x33882d64,0xb3fe2248,0x333baed7,0xb15f31ed,
0xae1f3364,0x2e1fb364,0xae1f3364,0x2e1fb364,0xb33baed7,0x315f31ed,0xad64b388,0xa24833fe,0x2f8bb30e,0xb3a82ca5,0x330e2f8b,0xaca5b3a8,0x32dd301d,0x28b2b3ea,0xb3c32bc6,0x30c4326d,
0xb07232a7,0x32a73072,0x3072b2a7,0xb2a7b072,0xb26db0c4,0xb2dd301d,0x28b233ea,0x33c32bc6,0x3113b22f,0xa646b3f6,0xb22fb113,0xb3f62646,0x31ed315f,0x33882d64,0x33fea248,0x333baed7,
0x3400,0x3400,0x3400,0x3400,0x2248b3fe,0x2ed7b33b,0x31edb15f,0x3388ad64,0xa64633f6,0xb22f3113,0xb3f6a646,0xb113b22f,0x28b2b3ea,0x33c3abc6,0x30c4326d,0xb01d32dd,
0xaa3e33d9,0xb3d9aa3e,0x2a3eb3d9,0x33d92a3e,0x2bc6b3c3,0x326d30c4,0xb2dd301d,0xa8b2b3ea,0xaca533a8,0xaf8bb30e,0x33a82ca5,0xb30e2f8b,0x2d64b388,0x224833fe,0xaed7b33b,0x31ed315f,
0xae1f3364,0x2e1fb364,0xae1f3364,0x2e1fb364,0x2ed7b33b,0xb1ed315f,0x3388ad64,0xb3fea248,0xaf8b330e,0x33a8aca5,0xb30eaf8b,0x2ca533a8,0x301db2dd,0xb3eaa8b2,0x2bc633c3,0x326db0c4,
0xb07232a7,0x32a73072,0x3072b2a7,0xb2a7b072,0x30c4b26d,0xb01db2dd,0xb3ea28b2,0xabc633c3,0xb113322f,0x264633f6,0x322f3113,0x33f6a646,0x315fb1ed,0x2d64b388,0xa248b3fe,0xaed7b33b,
0x3400,0x3400,0x3400,0x3400,0x33fe2248,0x333b2ed7,0x315f31ed,0x2d643388,0x2646b3f6,0x322fb113,0x33f62646,0x3113322f,0xb3eaa8b2,0xabc6b3c3,0x326db0c4,0x32dd301d,
0xaa3e33d9,0xb3d9aa3e,0x2a3eb3d9,0x33d92a3e,0x33c32bc6,0xb0c4326d,0xb01db2dd,0x33eaa8b2,0x2ca5b3a8,0x2f8b330e,0xb3a8aca5,0x330eaf8b,0xb388ad64,0x33fea248,0xb33b2ed7,0x315fb1ed,
0xae1f3364,0x2e1fb364,0xae1f3364,0x2e1fb364,0x333b2ed7,0xb15fb1ed,0x2d643388,0x2248b3fe,0x2f8bb30e,0xb3a82ca5,0x330e2f8b,0xaca5b3a8,0xb2ddb01d,0xa8b233ea,0x33c3abc6,0xb0c4b26d,
0xb07232a7,0x32a73072,0x3072b2a7,0xb2a7b072,0x326d30c4,0x32ddb01d,0xa8b2b3ea,0xb3c3abc6,0x3113b22f,0xa646b3f6,0xb22fb113,0xb3f62646,0xb1edb15f,0xb388ad64,0xb3fe2248,0xb33b2ed7,
0x34000000,0x34000000,0x34000000,0x34000000,0x33fe2248,0x333b2ed7,0x315f31ed,0x2d643388,0x33f62646,0x3113322f,0xa64633f6,0xb22f3113,0x33ea28b2,0x2bc633c3,0xb26d30c4,0xb2ddb01d,
0x33d92a3e,0xaa3e33d9,0xb3d9aa3e,0x2a3eb3d9,0x33c32bc6,0xb0c4326d,0xb01db2dd,0x33eaa8b2,0x33a82ca5,0xb30e2f8b,0x2ca5b3a8,0x2f8b330e,0x33882d64,0xb3fe2248,0x333baed7,0xb15f31ed,
0x33642e1f,0xb364ae1f,0x33642e1f,0xb364ae1f,0x333b2ed7,0xb15fb1ed,0x2d643388,0x2248b3fe,0x330e2f8b,0xaca5b3a8,0xaf8b330e,0x33a8aca5,0x32dd301d,0x28b2b3ea,0xb3c32bc6,0x30c4326d,
0x32a73072,0x3072b2a7,0xb2a7b072,0xb07232a7,0x326d30c4,0x32ddb01d,0xa8b2b3ea,0xb3c3abc6,0x322f3113,0x33f6a646,0x3113b22f,0xa646b3f6,0x31ed315f,0x33882d64,0x33fea248,0x333baed7,
0x34000000,0x34000000,0x34000000,0x34000000,0xa24833fe,0xaed7333b,0xb1ed315f,0xb3882d64,0xb3f6a646,0xb113b22f,0x2646b3f6,0x322fb113,0x28b2b3ea,0x33c3abc6,0x30c4326d,0xb01d32dd,
0x33d92a3e,0xaa3e33d9,0xb3d9aa3e,0x2a3eb3d9,0xabc633c3,0xb26db0c4,0x32ddb01d,0x28b233ea,0xb3a8aca5,0x330eaf8b,0xaca533a8,0xaf8bb30e,0x2d64b388,0x224833fe,0xaed7b33b,0x31ed315f,
0x33642e1f,0xb364ae1f,0x33642e1f,0xb364ae1f,0xaed7333b,0x31edb15f,0xb3882d64,0x33fe2248,0xb30eaf8b,0x2ca533a8,0x2f8bb30e,0xb3a82ca5,0x301db2dd,0xb3eaa8b2,0x2bc633c3,0x326db0c4,
0x32a73072,0x3072b2a7,0xb2a7b072,0xb07232a7,0xb0c4326d,0x301d32dd,0x33eaa8b2,0x2bc6b3c3,0xb22fb113,0xb3f62646,0xb113322f,0x264633f6,0x315fb1ed,0x2d64b388,0xa248b3fe,0xaed7b33b,
0x34000000,0x34000000,0x34000000,0x34000000,0xb3fea248,0xb33baed7,0xb15fb1ed,0xad64b388,0x33f62646,0x3113322f,0xa64633f6,0xb22f3113,0xb3eaa8b2,0xabc6b3c3,0x326db0c4,0x32dd301d,
0x33d92a3e,0xaa3e33d9,0xb3d9aa3e,0x2a3eb3d9,0xb3c3abc6,0x30c4b26d,0x301d32dd,0xb3ea28b2,0x33a82ca5,0xb30e2f8b,0x2ca5b3a8,0x2f8b330e,0xb388ad64,0x33fea248,0xb33b2ed7,0x315fb1ed,
0x33642e1f,0xb364ae1f,0x33642e1f,0xb364ae1f,0xb33baed7,0x315f31ed,0xad64b388,0xa24833fe,0x330e2f8b,0xaca5b3a8,0xaf8b330e,0x33a8aca5,0xb2ddb01d,0xa8b233ea,0x33c3abc6,0xb0c4b26d,
0x32a73072,0x3072b2a7,0xb2a7b072,0xb07232a7,0xb26db0c4,0xb2dd301d,0x28b233ea,0x33c32bc6,0x322f3113,0x33f6a646,0x3113b22f,0xa646b3f6,0xb1edb15f,0xb388ad64,0xb3fe2248,0xb33b2ed7,
0x34000000,0x34000000,0x34000000,0x34000000,0x2248b3fe,0x2ed7b33b,0x31edb15f,0x3388ad64,0xb3f6a646,0xb113b22f,0x2646b3f6,0x322fb113,0xa8b233ea,0xb3c32bc6,0xb0c4b26d,0x301db2dd,
0x33d92a3e,0xaa3e33d9,0xb3d9aa3e,0x2a3eb3d9,0x2bc6b3c3,0x326d30c4,0xb2dd301d,0xa8b2b3ea,0xb3a8aca5,0x330eaf8b,0xaca533a8,0xaf8bb30e,0xad643388,0xa248b3fe,0x2ed7333b,0xb1edb15f,
0x33642e1f,0xb364ae1f,0x33642e1f,0xb364ae1f,0x2ed7b33b,0xb1ed315f,0x3388ad64,0xb3fea248,0xb30eaf8b,0x2ca533a8,0x2f8bb30e,0xb3a82ca5,0xb01d32dd,0x33ea28b2,0xabc6b3c3,0xb26d30c4,
0x32a73072,0x3072b2a7,0xb2a7b072,0xb07232a7,0x30c4b26d,0xb01db2dd,0xb3ea28b2,0xabc633c3,0xb22fb113,0xb3f62646,0xb113322f,0x264633f6,0xb15f31ed,0xad643388,0x224833fe,0x2ed7333b,
0x3f800000,0x3f7fffb1,0x3f7ffec4,0x3f7ffd39,0x3f800000,0x3f7fe705,0x3f7f9c18,0x3f7f1f49,0x3f800000,0x3f7fa6e3,0x3f7e9bc9,0x3f7cdf6c,0x3f800000,0x3f7f3f54,0x3f7cfe73,0x3f7940c0,
0x3f800000,0x3f7eb069,0x3f7ac516,0x3f74484b,0x3f800000,0x3f7dfa38,0x3f77f110,0x3f6dfcf2,0x3f800000,0x3f7d1cdd,0x3f748422,0x3f666771,0x3f800000,0x3f7c187a,0x3f708066,0x3f5d924d,
0x3f800000,0x3f7aed37,0x3f6be858,0x3f5389c7,0x3f800000,0x3f799b43,0x3f66becc,0x3f485bca,0x3f800000,0x3f7822d1,0x3f6106f2,0x3f3c17d9,0x3f800000,0x3f76841b,0x3f5ac450,0x3f2ecef7,
0x3f800000,0x3f74bf62,0x3f53fac3,0x3f209393,0x3f800000,0x3f72d4eb,0x3f4cae79,0x3f11796b,0x3f800000,0x3f70c501,0x3f44e3f5,0x3f019573,0x3f800000,0x3f6e8ff8,0x3f3ca003,0x3ee1fb6a,
0x3f7ffb11,0x3f7ff84a,0x3f7ff4e6,0x3f7ff0e3,0x3f7e70b0,0x3f7d906e,0x3f7c7eb0,0x3f7b3bab,0x3f7a7302,0x3f77583a,0x3f73913f,0x3f6f20b0,0x3f740bdd,0x3f6d67a1,0x3f655e0b,0x3f5bfb34,
0x3f6b4b0c,0x3f5fe4e9,0x3f5233c6,0x3f425b89,0x3f604621,0x3f4f0417,0x3f3a7ca4,0x3f2302d3,0x3f531849,0x3f3b0629,0x3f1ebc12,0x3efdbabb,0x3f43e200,0x3f24381a,0x3eff17b2,0x3eadf73c,
0x3f32c8c9,0x3f0af1b7,0x3ebb31a0,0x3e3228d4,0x3f1ff6cb,0x3edf28a9,0x3e667c66,0x3b490fc6,0x3f0b9a6b,0x3ea5129f,0x3da3308c,0xbe2bf804,0x3eebcbbb,0x3e5101f1,0xbd8a200a,0xbeab0201,
0x3ebe1d4a,0x3da973ba,0xbe5a3997,0xbefafef7,0x3e8e9a22,0xbd2351cb,0xbeb554ec,0xbf21cbfb,0x3e3b6ecf,0xbe25c58c,0xbef9a02d,0xbf4154e1,0x3dafb680,0xbe901c2c,0xbf1c420c,0xbf5b2c79,
0x3f7fec43,0x3f7fe705,0x3f7fe129,0x3f7fdaaf,0x3f79c79d,0x3f7822d1,0x3f764d97,0x3f74484b,0x3f6a09a7,0x3f644fac,0x3f5df6be,0x3f570348,0x3f514d3d,0x3f456439,0x3f385216,0x3f2a2a80,
0x3f3085bb,0x3f1ce11f,0x3f07a136,0x3ee1fb6a,0x3f08f59b,0x3ed97cb9,0x3e9d9e78,0x3e3e853e,0x3eb8442a,0x3e5d4b1a,0x3d8a200a,0xbda973ba,0x3e2f10a2,0xbb490fc6,0xbe354098,0xbeb0eacb,
0xbcc90ab0,0xbe636c83,0xbed25a09,0xbf1553b7,0xbe605c13,0xbedc53c1,0xbf1ebc12,0xbf476085,0xbecf7bca,0xbf1e1e22,0xbf4ac77f,0xbf6afb8c,0xbf13682a,0xbf466354,0xbf6aab7b,0xbf7d906e,
0xbf396842,0xbf650479,0xbf7c7eb0,0xbf7dc68c,0xbf584853,0xbf788486,0xbf7f2f9d,0xbf6b99fb,0xbf6ed89e,0xbf7ff0e3,0xbf7294f8,0xbf485bca,0xbf7c3b28,0xbf7aed37,0xbf577026,0xbf16999f,
0x3f7fd397,0x3f7fcbe2,0x3f7fc38f,0x3f7fba9e,0x3f721352,0x3f6faf1b,0x3f6d1c1d,0x3f6a5ad9,0x3f4f7a1f,0x3f476085,0x3f3ebc1b,0x3f3592e7,0x3f1b02c6,0x3f0af1b7,0x3ef41f07,0x3ed0eb2a,
0x3eb263ef,0x3e80f8c0,0x3e1c76de,0x3d55880e,0x3d7b2b74,0xbd83db0a,0xbe419b37,0xbe9f1cdf,0xbe6c9a7f,0xbebf926f,0xbf0242b1,0xbf21cbfb,0xbf00e7e4,0xbf256bcb,0xbf44e3f5,0xbf5e5aa6,
0xbf3b8f3b,0xbf5a5ba0,0xbf708066,0xbf7d1cdd,0xbf660f88,0xbf7940c0,0xbf7ffec4,0xbf79f35e,0xbf7cbfc9,0xbf7efcc9,0xbf718f57,0xbf554aa4,0xbf7dabcc,0xbf6afb8c,0xbf46e22a,0xbf140c5f,
0xbf68bf3c,0xbf3f41fc,0xbf04f484,0xbe7bdba4,0xbf3fc767,0xbf003a06,0xbe4dee60,0x3df475c0,0xbf064b82,0xbe5101f1,0x3e1008b7,0x3eeff7fb,0xbe827dc0,0x3de7fa9a,0x3eee9479,0x3f4154e1,
0x0,0x3b490fc6,0x3bc90f88,0x3c16cb58,0x0,0x3ce22a7a,0x3d621469,0x3da973ba,0x0,0x3d55880e,0x3dd53db9,0x3e1f917b,0x0,0x3d9cecf9,0x3e1c76de,0x3e698bba,
0x0,0x3dcefdb7,0x3e4dee60,0x3e9920fe,0x0,0x3e007745,0x3e7ee6e1,0x3ebca7af,0x0,0x3e195be0,0x3e97a117,0x3edf28a9,0x0,0x3e3228d4,0x3eaf713a,0x3f003a06,
0x0,0x3e4ada4f,0x3ec6d529,0x3f102dd5,0x0,0x3e636c83,0x3eddbe79,0x3f1f599f,0x0,0x3e7bdba4,0x3ef41f07,0x3f2da859,0x0,0x3e8a11f7,0x3f04f484,0x3f3b0629,
0x0,0x3e9620d2,0x3f0f8784,0x3f476085,0x0,0x3ea21887,0x3f19c200,0x3f52a649,0x0,0x3eadf73c,0x3f239da9,0x3f5cc7d1,0x0,0x3eb9bb1e,0x3f2d1469,0x3f65b710,
0x3c490e90,0x3c7b514b,0x3c96c9b6,0x3cafea69,0x3de1bc2e,0x3e0cec4a,0x3e28defc,0x3e44b0b9,0x3e541501,0x3e840270,0x3e9d9e78,0x3eb6ccc3,0x3e9aa086,0x3ebf926f,0x3ee363fa,0x3f02ef9f,
0x3ec9b953,0x3ef840c8,0x3f121eb0,0x3f269de3,0x3ef6e0cb,0x3f16999f,0x3f2f61a5,0x3f456439,0x3f10d3cd,0x3f2ecef7,0x3f48d8b3,0x3f5e5aa6,0x3f24d225,0x3f446337,0x3f5df6be,0x3f70c501,
0x3f373a23,0x3f570348,0x3f6e46be,0x3f7c187a,0x3f47de65,0x3f666771,0x3f796e4e,0x3f7fffb1,0x3f5695e5,0x3f725470,0x3f7f2f9d,0x3f7c5d3a,0x3f633c5a,0x3f7a9c59,0x3f7f6ac7,0x3f714c7a,
0x3f6db293,0x3f7f1f49,0x3f7a1e84,0x3f5f20db,0x3f75dec6,0x3f7fcbe2,0x3f6f6830,0x3f466354,0x3f7baccd,0x3f7c9f8a,0x3f5f8327,0x3f27ce61,0x3f7f0e58,0x3f75a67b,0x3f4ac77f,0x3f044889,
0x3cc90ab0,0x3ce22a7a,0x3cfb49ba,0x3d0a342f,0x3e605c13,0x3e7bdba4,0x3e8b9507,0x3e9920fe,0x3ecf7bca,0x3ee79a5d,0x3eff17b2,0x3f0af1b7,0x3f13682a,0x3f2302d3,0x3f31a81d,0x3f3f41fc,
0x3f396842,0x3f4a4c87,0x3f591e6a,0x3f65b710,0x3f584853,0x3f67c187,0x3f73913f,0x3f7b87b2,0x3f6ed89e,0x3f79f35e,0x3f7f6ac7,0x3f7f1f49,0x3f7c3b28,0x3f7fffb1,0x3f7bf531,0x3f703b37,
0x3f7fec43,0x3f799b43,0x3f696591,0x3f4fefa8,0x3f79c79d,0x3f671599,0x3f48d8b3,0x3f209393,0x3f6a09a7,0x3f495521,0x3f1c420c,0x3ecb2aae,0x3f514d3d,0x3f21cbfb,0x3ecc9b8b,0x3e0cec4a,
0x3f3085bb,0x3ee4cbfe,0x3e28defc,0xbe06b26e,0x3f08f59b,0x3e75c35a,0xbda3308c,0xbec8477c,0x3eb8442a,0x3cafea69,0xbea395c5,0xbf1f599f,0x3e2f10a2,0xbe4ada4f,0xbf0a48ad,0xbf4f0417,
0x3d16c32c,0x3d2351cb,0x3d2fe007,0x3d3c6dd5,0x3ea68f12,0x3eb3dca5,0x3ec1071e,0x3ece0bea,0x3f15f6d9,0x3f209393,0x3f2ac082,0x3f34768f,0x3f4bbbf8,0x3f570348,0x3f6106f2,0x3f69b7e4,
0x3f6ff573,0x3f77beb7,0x3f7cfe73,0x3f7fa6e3,0x3f7f84ab,0x3f7f7808,0x3f7b61fc,0x3f73530e,0x3f791298,0x3f6d67a1,0x3f5c61c7,0x3f466354,0x3f5d2d53,0x3f436051,0x3f239da9,0x3efdbabb,
0x3f2e3bde,0x3f05a02c,0x3eaf713a,0x3e195be0,0x3ee0924f,0x3e698bba,0x3bc90f88,0xbe5d4b1a,0x3e22abb6,0xbdb5f8da,0xbea986c4,0xbf0d9281,0xbe09cf86,0xbecb2aae,0xbf212ff9,0xbf50d937,
0xbed53641,0xbf2a2a80,0xbf5ac450,0xbf7822d1,0xbf299415,0xbf5d924d,0xbf7ac516,0xbf7e2b72,0xbf59f26a,0xbf7a9c59,0xbf7d7474,0xbf62241a,0xbf778bc5,0xbf7e5a38,0xbf628210,0xbf27ce61,
0x3f800000,0x3f800000,0x3f800000,0x3f800000,0x3f7fffb1,0x3f7fe705,0x3f7fa6e3,0x3f7f3f54,0x3f7ffec4,0x3f7f9c18,0x3f7e9bc9,0x3f7cfe73,0x3f7ffd39,0x3f7f1f49,0x3f7cdf6c,0x3f7940c0,
0x3f7ffb11,0x3f7e70b0,0x3f7a7302,0x3f740bdd,0x3f7ff84a,0x3f7d906e,0x3f77583a,0x3f6d67a1,0x3f7ff4e6,0x3f7c7eb0,0x3f73913f,0x3f655e0b,0x3f7ff0e3,0x3f7b3bab,0x3f6f20b0,0x3f5bfb34,
0x3f7fec43,0x3f79c79d,0x3f6a09a7,0x3f514d3d,0x3f7fe705,0x3f7822d1,0x3f644fac,0x3f456439,0x3f7fe129,0x3f764d97,0x3f5df6be,0x3f385216,0x3f7fdaaf,0x3f74484b,0x3f570348,0x3f2a2a80,
0x3f7fd397,0x3f721352,0x3f4f7a1f,0x3f1b02c6,0x3f7fcbe2,0x3f6faf1b,0x3f476085,0x3f0af1b7,0x3f7fc38f,0x3f6d1c1d,0x3f3ebc1b,0x3ef41f07,0x3f7fba9e,0x3f6a5ad9,0x3f3592e7,0x3ed0eb2a,
0x3f800000,0x3f800000,0x3f800000,0x3f800000,0x3f7eb069,0x3f7dfa38,0x3f7d1cdd,0x3f7c187a,0x3f7ac516,0x3f77f110,0x3f748422,0x3f708066,0x3f74484b,0x3f6dfcf2,0x3f666771,0x3f5d924d,
0x3f6b4b0c,0x3f604621,0x3f531849,0x3f43e200,0x3f5fe4e9,0x3f4f0417,0x3f3b0629,0x3f24381a,0x3f5233c6,0x3f3a7ca4,0x3f1ebc12,0x3eff17b2,0x3f425b89,0x3f2302d3,0x3efdbabb,0x3eadf73c,
0x3f3085bb,0x3f08f59b,0x3eb8442a,0x3e2f10a2,0x3f1ce11f,0x3ed97cb9,0x3e5d4b1a,0xbb490fc6,0x3f07a136,0x3e9d9e78,0x3d8a200a,0xbe354098,0x3ee1fb6a,0x3e3e853e,0xbda973ba,0xbeb0eacb,
0x3eb263ef,0x3d7b2b74,0xbe6c9a7f,0xbf00e7e4,0x3e80f8c0,0xbd83db0a,0xbebf926f,0xbf256bcb,0x3e1c76de,0xbe419b37,0xbf0242b1,0xbf44e3f5,0x3d55880e,0xbe9f1cdf,0xbf21cbfb,0xbf5e5aa6,
0x3f800000,0x3f800000,0x3f800000,0x3f800000,0x3f7aed37,0x3f799b43,0x3f7822d1,0x3f76841b,0x3f6be858,0x3f66becc,0x3f6106f2,0x3f5ac450,0x3f5389c7,0x3f485bca,0x3f3c17d9,0x3f2ecef7,
0x3f32c8c9,0x3f1ff6cb,0x3f0b9a6b,0x3eebcbbb,0x3f0af1b7,0x3edf28a9,0x3ea5129f,0x3e5101f1,0x3ebb31a0,0x3e667c66,0x3da3308c,0xbd8a200a,0x3e3228d4,0x3b490fc6,0xbe2bf804,0xbeab0201,
0xbcc90ab0,0xbe605c13,0xbecf7bca,0xbf13682a,0xbe636c83,0xbedc53c1,0xbf1e1e22,0xbf466354,0xbed25a09,0xbf1ebc12,0xbf4ac77f,0xbf6aab7b,0xbf1553b7,0xbf476085,0xbf6afb8c,0xbf7d906e,
0xbf3b8f3b,0xbf660f88,0xbf7cbfc9,0xbf7dabcc,0xbf5a5ba0,0xbf7940c0,0xbf7efcc9,0xbf6afb8c,0xbf708066,0xbf7ffec4,0xbf718f57,0xbf46e22a,0xbf7d1cdd,0xbf79f35e,0xbf554aa4,0xbf140c5f,
0x3f800000,0x3f800000,0x3f800000,0x3f800000,0x3f74bf62,0x3f72d4eb,0x3f70c501,0x3f6e8ff8,0x3f53fac3,0x3f4cae79,0x3f44e3f5,0x3f3ca003,0x3f209393,0x3f11796b,0x3f019573,0x3ee1fb6a,
0x3ebe1d4a,0x3e8e9a22,0x3e3b6ecf,0x3dafb680,0x3da973ba,0xbd2351cb,0xbe25c58c,0xbe901c2c,0xbe5a3997,0xbeb554ec,0xbef9a02d,0xbf1c420c,0xbefafef7,0xbf21cbfb,0xbf4154e1,0xbf5b2c79,
0xbf396842,0xbf584853,0xbf6ed89e,0xbf7c3b28,0xbf650479,0xbf788486,0xbf7ff0e3,0xbf7aed37,0xbf7c7eb0,0xbf7f2f9d,0xbf7294f8,0xbf577026,0xbf7dc68c,0xbf6b99fb,0xbf485bca,0xbf16999f,
0xbf68bf3c,0xbf3fc767,0xbf064b82,0xbe827dc0,0xbf3f41fc,0xbf003a06,0xbe5101f1,0x3de7fa9a,0xbf04f484,0xbe4dee60,0x3e1008b7,0x3eee9479,0xbe7bdba4,0x3df475c0,0x3eeff7fb,0x3f4154e1,
0x0,0x0,0x0,0x0,0x3b490fc6,0x3ce22a7a,0x3d55880e,0x3d9cecf9,0x3bc90f88,0x3d621469,0x3dd53db9,0x3e1c76de,0x3c16cb58,0x3da973ba,0x3e1f917b,0x3e698bba,
0x3c490e90,0x3de1bc2e,0x3e541501,0x3e9aa086,0x3c7b514b,0x3e0cec4a,0x3e840270,0x3ebf926f,0x3c96c9b6,0x3e28defc,0x3e9d9e78,0x3ee363fa,0x3cafea69,0x3e44b0b9,0x3eb6ccc3,0x3f02ef9f,
0x3cc90ab0,0x3e605c13,0x3ecf7bca,0x3f13682a,0x3ce22a7a,0x3e7bdba4,0x3ee79a5d,0x3f2302d3,0x3cfb49ba,0x3e8b9507,0x3eff17b2,0x3f31a81d,0x3d0a342f,0x3e9920fe,0x3f0af1b7,0x3f3f41fc,
0x3d16c32c,0x3ea68f12,0x3f15f6d9,0x3f4bbbf8,0x3d2351cb,0x3eb3dca5,0x3f209393,0x3f570348,0x3d2fe007,0x3ec1071e,0x3f2ac082,0x3f6106f2,0x3d3c6dd5,0x3ece0bea,0x3f34768f,0x3f69b7e4,
0x0,0x0,0x0,0x0,0x3dcefdb7,0x3e007745,0x3e195be0,0x3e3228d4,0x3e4dee60,0x3e7ee6e1,0x3e97a117,0x3eaf713a,0x3e9920fe,0x3ebca7af,0x3edf28a9,0x3f003a06,
0x3ec9b953,0x3ef6e0cb,0x3f10d3cd,0x3f24d225,0x3ef840c8,0x3f16999f,0x3f2ecef7,0x3f446337,0x3f121eb0,0x3f2f61a5,0x3f48d8b3,0x3f5df6be,0x3f269de3,0x3f456439,0x3f5e5aa6,0x3f70c501,
0x3f396842,0x3f584853,0x3f6ed89e,0x3f7c3b28,0x3f4a4c87,0x3f67c187,0x3f79f35e,0x3f7fffb1,0x3f591e6a,0x3f73913f,0x3f7f6ac7,0x3f7bf531,0x3f65b710,0x3f7b87b2,0x3f7f1f49,0x3f703b37,
0x3f6ff573,0x3f7f84ab,0x3f791298,0x3f5d2d53,0x3f77beb7,0x3f7f7808,0x3f6d67a1,0x3f436051,0x3f7cfe73,0x3f7b61fc,0x3f5c61c7,0x3f239da9,0x3f7fa6e3,0x3f73530e,0x3f466354,0x3efdbabb,
0x0,0x0,0x0,0x0,0x3e4ada4f,0x3e636c83,0x3e7bdba4,0x3e8a11f7,0x3ec6d529,0x3eddbe79,0x3ef41f07,0x3f04f484,0x3f102dd5,0x3f1f599f,0x3f2da859,0x3f3b0629,
0x3f373a23,0x3f47de65,0x3f5695e5,0x3f633c5a,0x3f570348,0x3f666771,0x3f725470,0x3f7a9c59,0x3f6e46be,0x3f796e4e,0x3f7f2f9d,0x3f7f6ac7,0x3f7c187a,0x3f7fffb1,0x3f7c5d3a,0x3f714c7a,
0x3f7fec43,0x3f79c79d,0x3f6a09a7,0x3f514d3d,0x3f799b43,0x3f671599,0x3f495521,0x3f21cbfb,0x3f696591,0x3f48d8b3,0x3f1c420c,0x3ecc9b8b,0x3f4fefa8,0x3f209393,0x3ecb2aae,0x3e0cec4a,
0x3f2e3bde,0x3ee0924f,0x3e22abb6,0xbe09cf86,0x3f05a02c,0x3e698bba,0xbdb5f8da,0xbecb2aae,0x3eaf713a,0x3bc90f88,0xbea986c4,0xbf212ff9,0x3e195be0,0xbe5d4b1a,0xbf0d9281,0xbf50d937,
0x0,0x0,0x0,0x0,0x3e9620d2,0x3ea21887,0x3eadf73c,0x3eb9bb1e,0x3f0f8784,0x3f19c200,0x3f239da9,0x3f2d1469,0x3f476085,0x3f52a649,0x3f5cc7d1,0x3f65b710,
0x3f6db293,0x3f75dec6,0x3f7baccd,0x3f7f0e58,0x3f7f1f49,0x3f7fcbe2,0x3f7c9f8a,0x3f75a67b,0x3f7a1e84,0x3f6f6830,0x3f5f8327,0x3f4ac77f,0x3f5f20db,0x3f466354,0x3f27ce61,0x3f044889,
0x3f3085bb,0x3f08f59b,0x3eb8442a,0x3e2f10a2,0x3ee4cbfe,0x3e75c35a,0x3cafea69,0xbe4ada4f,0x3e28defc,0xbda3308c,0xbea395c5,0xbf0a48ad,0xbe06b26e,0xbec8477c,0xbf1f599f,0xbf4f0417,
0xbed53641,0xbf299415,0xbf59f26a,0xbf778bc5,0xbf2a2a80,0xbf5d924d,0xbf7a9c59,0xbf7e5a38,0xbf5ac450,0xbf7ac516,0xbf7d7474,0xbf628210,0xbf7822d1,0xbf7e2b72,0xbf62241a,0xbf27ce61,
0x3400,0x264633f6,0x2a3e33d9,0x2ca533a8,0x3400,0x2f8b330e,0x32a73072,0x33f62646,0x3400,0x322f3113,0x33d9aa3e,0x2f8bb30e,0x3400,0x33a82ca5,0x3072b2a7,0xb113b22f,
0x3400,0x33f6a646,0xaa3eb3d9,0xb3a82ca5,0x3400,0x330eaf8b,0xb2a7b072,0xa64633f6,0x3400,0x3113b22f,0xb3d92a3e,0x330e2f8b,0x3400,0x2ca5b3a8,0xb07232a7,0x322fb113,
0x3400,0xa646b3f6,0x2a3e33d9,0xaca5b3a8,0x3400,0xaf8bb30e,0x32a73072,0xb3f6a646,0x3400,0xb22fb113,0x33d9aa3e,0xaf8b330e,0x3400,0xb3a8aca5,0x3072b2a7,0x3113322f,
0x3400,0xb3f62646,0xaa3eb3d9,0x33a8aca5,0x3400,0xb30e2f8b,0xb2a7b072,0x2646b3f6,0x3400,0xb113322f,0xb3d92a3e,0xb30eaf8b,0x3400,0xaca533a8,0xb07232a7,0xb22f3113,
0x2e1f3364,0x2f8b330e,0x307232a7,0x3113322f,0x3364ae1f,0x3113b22f,0x2a3eb3d9,0xaca5b3a8,0xae1fb364,0xb3a8aca5,0xb2a73072,0xa64633f6,0xb3642e1f,0x264633f6,0x33d92a3e,0x2f8bb30e,
0x2e1f3364,0x330eaf8b,0xb072b2a7,0xb22f3113,0x3364ae1f,0xb22fb113,0xaa3e33d9,0x33a8aca5,0xae1fb364,0xaca533a8,0x32a7b072,0xb3f6a646,0xb3642e1f,0x33f6a646,0xb3d9aa3e,0x330e2f8b,
0x2e1f3364,0xaf8bb30e,0x307232a7,0xb113b22f,0x3364ae1f,0xb113322f,0x2a3eb3d9,0x2ca533a8,0xae1fb364,0x33a82ca5,0xb2a73072,0x2646b3f6,0xb3642e1f,0xa646b3f6,0x33d92a3e,0xaf8b330e,
0x2e1f3364,0xb30e2f8b,0xb072b2a7,0x322fb113,0x3364ae1f,0x322f3113,0xaa3e33d9,0xb3a82ca5,0xae1fb364,0x2ca5b3a8,0x32a7b072,0x33f62646,0xb3642e1f,0xb3f62646,0xb3d9aa3e,0xb30eaf8b,
0x31a831a8,0x322f3113,0x32a73072,0x330e2f8b,0xb1a8b1a8,0xb3a8aca5,0xb3d92a3e,0xb22f3113,0x31a831a8,0x33f6a646,0x3072b2a7,0xaca5b3a8,0xb1a8b1a8,0xb30e2f8b,0x2a3e33d9,0x33f62646,
0x31a831a8,0x3113b22f,0xb2a7b072,0xaf8b330e,0xb1a8b1a8,0xaca533a8,0x33d9aa3e,0xb113b22f,0x31a831a8,0xa646b3f6,0xb07232a7,0x33a8aca5,0xb1a8b1a8,0x2f8b330e,0xaa3eb3d9,0xa64633f6,
0x31a831a8,0xb22fb113,0x32a73072,0xb30eaf8b,0xb1a8b1a8,0x33a82ca5,0xb3d92a3e,0x322fb113,0x31a831a8,0xb3f62646,0x3072b2a7,0x2ca533a8,0xb1a8b1a8,0x330eaf8b,0x2a3e33d9,0xb3f6a646,
0x31a831a8,0xb113322f,0xb2a7b072,0x2f8bb30e,0xb1a8b1a8,0x2ca5b3a8,0x33d9aa3e,0x3113322f,0x31a831a8,0x264633f6,0xb07232a7,0xb3a82ca5,0xb1a8b1a8,0xaf8bb30e,0xaa3eb3d9,0x2646b3f6,
0x33642e1f,0x33a82ca5,0x33d92a3e,0x33f62646,0xae1f3364,0x264633f6,0x307232a7,0x330e2f8b,0xb364ae1f,0xb30e2f8b,0xaa3e33d9,0x3113322f,0x2e1fb364,0xb22fb113,0xb2a73072,0x2ca533a8,
0x33642e1f,0x2ca5b3a8,0xb3d9aa3e,0xa64633f6,0xae1f3364,0x33f6a646,0xb072b2a7,0xaf8b330e,0xb364ae1f,0x2f8b330e,0x2a3eb3d9,0xb22f3113,0x2e1fb364,0xb113322f,0x32a7b072,0xb3a82ca5,
0x33642e1f,0xb3a8aca5,0x33d92a3e,0xb3f6a646,0xae1f3364,0xa646b3f6,0x307232a7,0xb30eaf8b,0xb364ae1f,0x330eaf8b,0xaa3e33d9,0xb113b22f,0x2e1fb364,0x322f3113,0xb2a73072,0xaca5b3a8,
0x33642e1f,0xaca533a8,0xb3d9aa3e,0x2646b3f6,0xae1f3364,0xb3f62646,0xb072b2a7,0x2f8bb30e,0xb364ae1f,0xaf8bb30e,0x2a3eb3d9,0x322fb113,0x2e1fb364,0x3113b22f,0x32a7b072,0x33a8aca5,
0x34000000,0x33f6a646,0x33d9aa3e,0x33a8aca5,0x34000000,0x330eaf8b,0x3072b2a7,0x2646b3f6,0x34000000,0x3113b22f,0xaa3eb3d9,0xb30eaf8b,0x34000000,0x2ca5b3a8,0xb2a7b072,0xb22f3113,
0x34000000,0xa646b3f6,0xb3d92a3e,0x2ca533a8,0x34000000,0xaf8bb30e,0xb07232a7,0x33f62646,0x34000000,0xb22fb113,0x2a3e33d9,0x2f8bb30e,0x34000000,0xb3a8aca5,0x32a73072,0xb113b22f,
0x34000000,0xb3f62646,0x33d9aa3e,0xb3a82ca5,0x34000000,0xb30e2f8b,0x3072b2a7,0xa64633f6,0x34000000,0xb113322f,0xaa3eb3d9,0x330e2f8b,0x34000000,0xaca533a8,0xb2a7b072,0x322fb113,
0x34000000,0x264633f6,0xb3d92a3e,0xaca5b3a8,0x34000000,0x2f8b330e,0xb07232a7,0xb3f6a646,0x34000000,0x322f3113,0x2a3e33d9,0xaf8b330e,0x34000000,0x33a82ca5,0x32a73072,0x3113322f,
0x3364ae1f,0x330eaf8b,0x32a7b072,0x322fb113,0xae1fb364,0xb22fb113,0xb3d9aa3e,0xb3a82ca5,0xb3642e1f,0xaca533a8,0x307232a7,0x33f62646,0x2e1f3364,0x33f6a646,0x2a3eb3d9,0xb30eaf8b,
0x3364ae1f,0xaf8bb30e,0xb2a73072,0x3113322f,0xae1fb364,0xb113322f,0x33d92a3e,0xaca5b3a8,0xb3642e1f,0x33a82ca5,0xb072b2a7,0xa64633f6,0x2e1f3364,0xa646b3f6,0xaa3e33d9,0x2f8bb30e,
0x3364ae1f,0xb30e2f8b,0x32a7b072,0xb22f3113,0xae1fb364,0x322f3113,0xb3d9aa3e,0x33a8aca5,0xb3642e1f,0x2ca5b3a8,0x307232a7,0xb3f6a646,0x2e1f3364,0xb3f62646,0x2a3eb3d9,0x330e2f8b,
0x3364ae1f,0x2f8b330e,0xb2a73072,0xb113b22f,0xae1fb364,0x3113b22f,0x33d92a3e,0x2ca533a8,0xb3642e1f,0xb3a8aca5,0xb072b2a7,0x2646b3f6,0x2e1f3364,0x264633f6,0xaa3e33d9,0xaf8b330e,
0x31a8b1a8,0x3113b22f,0x3072b2a7,0x2f8bb30e,0xb1a831a8,0xaca533a8,0x2a3e33d9,0x3113322f,0x31a8b1a8,0xa646b3f6,0xb2a7b072,0xb3a82ca5,0xb1a831a8,0x2f8b330e,0x33d9aa3e,0x2646b3f6,
0x31a8b1a8,0xb22fb113,0xb07232a7,0x330e2f8b,0xb1a831a8,0x33a82ca5,0xaa3eb3d9,0xb22f3113,0x31a8b1a8,0xb3f62646,0x32a73072,0xaca5b3a8,0xb1a831a8,0x330eaf8b,0xb3d92a3e,0x33f62646,
0x31a8b1a8,0xb113322f,0x3072b2a7,0xaf8b330e,0xb1a831a8,0x2ca5b3a8,0x2a3e33d9,0xb113b22f,0x31a8b1a8,0x264633f6,0xb2a7b072,0x33a8aca5,0xb1a831a8,0xaf8bb30e,0x33d9aa3e,0xa64633f6,
0x31a8b1a8,0x322f3113,0xb07232a7,0xb30eaf8b,0xb1a831a8,0xb3a8aca5,0xaa3eb3d9,0x322fb113,0x31a8b1a8,0x33f6a646,0x32a73072,0x2ca533a8,0xb1a831a8,0xb30e2f8b,0xb3d92a3e,0xb3f6a646,
0x2e1fb364,0x2ca5b3a8,0x2a3eb3d9,0x2646b3f6,0x33642e1f,0x33f6a646,0x32a7b072,0x2f8bb30e,0xae1f3364,0x2f8b330e,0x33d92a3e,0x322fb113,0xb364ae1f,0xb113322f,0x307232a7,0x33a8aca5,
0x2e1fb364,0xb3a8aca5,0xaa3e33d9,0x33f62646,0x33642e1f,0xa646b3f6,0xb2a73072,0x330e2f8b,0xae1f3364,0x330eaf8b,0xb3d9aa3e,0x3113322f,0xb364ae1f,0x322f3113,0xb072b2a7,0x2ca533a8,
0x2e1fb364,0xaca533a8,0x2a3eb3d9,0xa64633f6,0x33642e1f,0xb3f62646,0x32a7b072,0xaf8b330e,0xae1f3364,0xaf8bb30e,0x33d92a3e,0xb22f3113,0xb364ae1f,0x3113b22f,0x307232a7,0xb3a82ca5,
0x2e1fb364,0x33a82ca5,0xaa3e33d9,0xb3f6a646,0x33642e1f,0x264633f6,0xb2a73072,0xb30eaf8b,0xae1f3364,0xb30e2f8b,0xb3d9aa3e,0xb113b22f,0xb364ae1f,0xb22fb113,0xb072b2a7,0xaca5b3a8,
0x3400,0x3400,0x3400,0x3400,0x3400,0x2e1f3364,0x31a831a8,0x33642e1f,0x3400,0x31a831a8,0x34000000,0x31a8b1a8,0x3400,0x33642e1f,0x31a8b1a8,0xae1fb364,
0x3400,0x34000000,0xb400,0xb4008000,0x3400,0x3364ae1f,0xb1a8b1a8,0xae1f3364,0x3400,0x31a8b1a8,0xb4008000,0x31a831a8,0x3400,0x2e1fb364,0xb1a831a8,0x3364ae1f,
0x3400,0xb400,0x3400,0xb400,0x3400,0xae1fb364,0x31a831a8,0xb364ae1f,0x3400,0xb1a8b1a8,0x34000000,0xb1a831a8,0x3400,0xb364ae1f,0x31a8b1a8,0x2e1f3364,
0x3400,0xb4008000,0xb400,0x34000000,0x3400,0xb3642e1f,0xb1a8b1a8,0x2e1fb364,0x3400,0xb1a831a8,0xb4008000,0xb1a8b1a8,0x3400,0xae1f3364,0xb1a831a8,0xb3642e1f,
0x3400,0x3400,0x3400,0x3400,0x34000000,0x3364ae1f,0x31a8b1a8,0x2e1fb364,0xb400,0xb1a8b1a8,0xb4008000,0xb1a831a8,0xb4008000,0xae1f3364,0x31a831a8,0x3364ae1f,
0x3400,0x34000000,0xb400,0xb4008000,0x34000000,0xae1fb364,0xb1a831a8,0x33642e1f,0xb400,0xb1a831a8,0x34000000,0xb1a8b1a8,0xb4008000,0x33642e1f,0xb1a8b1a8,0x2e1f3364,
0x3400,0xb400,0x3400,0xb400,0x34000000,0xb3642e1f,0x31a8b1a8,0xae1f3364,0xb400,0x31a831a8,0xb4008000,0x31a8b1a8,0xb4008000,0x2e1fb364,0x31a831a8,0xb3642e1f,
0x3400,0xb4008000,0xb400,0x34000000,0x34000000,0x2e1f3364,0xb1a831a8,0xb364ae1f,0xb400,0x31a8b1a8,0x34000000,0x31a831a8,0xb4008000,0xb364ae1f,0xb1a8b1a8,0xae1fb364,
0x3400,0x3400,0x3400,0x3400,0xb400,0xae1fb364,0xb1a8b1a8,0xb364ae1f,0x3400,0x31a831a8,0x34000000,0x31a8b1a8,0xb400,0xb364ae1f,0xb1a831a8,0x2e1f3364,
0x3400,0x34000000,0xb400,0xb4008000,0xb400,0xb3642e1f,0x31a831a8,0x2e1fb364,0x3400,0x31a8b1a8,0xb4008000,0x31a831a8,0xb400,0xae1f3364,0x31a8b1a8,0xb3642e1f,
0x3400,0xb400,0x3400,0xb400,0xb400,0x2e1f3364,0xb1a8b1a8,0x33642e1f,0x3400,0xb1a8b1a8,0x34000000,0xb1a831a8,0xb400,0x33642e1f,0xb1a831a8,0xae1fb364,
0x3400,0xb4008000,0xb400,0x34000000,0xb400,0x3364ae1f,0x31a831a8,0xae1f3364,0x3400,0xb1a831a8,0xb4008000,0xb1a8b1a8,0xb400,0x2e1fb364,0x31a8b1a8,0x3364ae1f,
0x3400,0x3400,0x3400,0x3400,0xb4008000,0xb3642e1f,0xb1a831a8,0xae1f3364,0xb400,0xb1a8b1a8,0xb4008000,0xb1a831a8,0x34000000,0x2e1fb364,0xb1a8b1a8,0xb3642e1f,
0x3400,0x34000000,0xb400,0xb4008000,0xb4008000,0x2e1f3364,0x31a8b1a8,0xb364ae1f,0xb400,0xb1a831a8,0x34000000,0xb1a8b1a8,0x34000000,0xb364ae1f,0x31a831a8,0xae1fb364,
0x3400,0xb400,0x3400,0xb400,0xb4008000,0x3364ae1f,0xb1a831a8,0x2e1fb364,0xb400,0x31a831a8,0xb4008000,0x31a8b1a8,0x34000000,0xae1f3364,0xb1a8b1a8,0x3364ae1f,
0x3400,0xb4008000,0xb400,0x34000000,0xb4008000,0xae1fb364,0x31a8b1a8,0x33642e1f,0xb400,0x31a8b1a8,0x34000000,0x31a831a8,0x34000000,0x33642e1f,0x31a831a8,0x2e1f3364,
0x34000000,0x34000000,0x34000000,0x34000000,0x34000000,0x3364ae1f,0x31a8b1a8,0x2e1fb364,0x34000000,0x31a8b1a8,0xb400,0xb1a8b1a8,0x34000000,0x2e1fb364,0xb1a8b1a8,0xb3642e1f,
0x34000000,0xb400,0xb4008000,0x80003400,0x34000000,0xae1fb364,0xb1a831a8,0x33642e1f,0x34000000,0xb1a8b1a8,0x80003400,0x31a8b1a8,0x34000000,0xb364ae1f,0x31a831a8,0xae1fb364,
0x34000000,0xb4008000,0x34000000,0xb4008000,0x34000000,0xb3642e1f,0x31a8b1a8,0xae1f3364,0x34000000,0xb1a831a8,0xb400,0x31a831a8,0x34000000,0xae1f3364,0xb1a8b1a8,0x3364ae1f,
0x34000000,0x80003400,0xb4008000,0xb400,0x34000000,0x2e1f3364,0xb1a831a8,0xb364ae1f,0x34000000,0x31a831a8,0x80003400,0xb1a831a8,0x34000000,0x33642e1f,0x31a831a8,0x2e1f3364,
0x34000000,0x34000000,0x34000000,0x34000000,0xb400,0xae1fb364,0xb1a8b1a8,0xb364ae1f,0xb4008000,0xb1a831a8,0x80003400,0x31a831a8,0x80003400,0x33642e1f,0x31a8b1a8,0xae1fb364,
0x34000000,0xb400,0xb4008000,0x80003400,0xb400,0xb3642e1f,0x31a831a8,0x2e1fb364,0xb4008000,0x31a831a8,0xb400,0xb1a831a8,0x80003400,0x2e1fb364,0xb1a831a8,0x3364ae1f,
0x34000000,0xb4008000,0x34000000,0xb4008000,0xb400,0x2e1f3364,0xb1a8b1a8,0x33642e1f,0xb4008000,0x31a8b1a8,0x80003400,0xb1a8b1a8,0x80003400,0xb364ae1f,0x31a8b1a8,0x2e1f3364,
0x34000000,0x80003400,0xb4008000,0xb400,0xb400,0x3364ae1f,0x31a831a8,0xae1f3364,0xb4008000,0xb1a8b1a8,0xb400,0x31a8b1a8,0x80003400,0xae1f3364,0xb1a831a8,0xb3642e1f,
0x34000000,0x34000000,0x34000000,0x34000000,0xb4008000,0xb3642e1f,0xb1a831a8,0xae1f3364,0x34000000,0x31a8b1a8,0xb400,0xb1a8b1a8,0xb4008000,0xae1f3364,0x31a831a8,0x3364ae1f,
0x34000000,0xb400,0xb4008000,0x80003400,0xb4008000,0x2e1f3364,0x31a8b1a8,0xb364ae1f,0x34000000,0xb1a8b1a8,0x80003400,0x31a8b1a8,0xb4008000,0x33642e1f,0xb1a8b1a8,0x2e1f3364,
0x34000000,0xb4008000,0x34000000,0xb4008000,0xb4008000,0x3364ae1f,0xb1a831a8,0x2e1fb364,0x34000000,0xb1a831a8,0xb400,0x31a831a8,0xb4008000,0x2e1fb364,0x31a831a8,0xb3642e1f,
0x34000000,0x80003400,0xb4008000,0xb400,0xb4008000,0xae1fb364,0x31a8b1a8,0x33642e1f,0x34000000,0x31a831a8,0x80003400,0xb1a831a8,0xb4008000,0xb364ae1f,0xb1a8b1a8,0xae1fb364,
0x34000000,0x34000000,0x34000000,0x34000000,0x80003400,0x2e1f3364,0x31a831a8,0x33642e1f,0xb4008000,0xb1a831a8,0x80003400,0x31a831a8,0xb400,0xb364ae1f,0xb1a831a8,0x2e1f3364,
0x34000000,0xb400,0xb4008000,0x80003400,0x80003400,0x3364ae1f,0xb1a8b1a8,0xae1f3364,0xb4008000,0x31a831a8,0xb400,0xb1a831a8,0xb400,0xae1f3364,0x31a8b1a8,0xb3642e1f,
0x34000000,0xb4008000,0x34000000,0xb4008000,0x80003400,0xae1fb364,0x31a831a8,0xb364ae1f,0xb4008000,0x31a8b1a8,0x80003400,0xb1a8b1a8,0xb400,0x33642e1f,0xb1a831a8,0xae1fb364,
0x34000000,0x80003400,0xb4008000,0xb400,0x80003400,0xb3642e1f,0xb1a8b1a8,0x2e1fb364,0xb4008000,0xb1a8b1a8,0xb400,0x31a8b1a8,0xb400,0x2e1fb364,0x31a8b1a8,0x3364ae1f,
0x3400,0x3400,0x3400,0x3400,0x3400,0xae1f3364,0xb1a831a8,0xb3642e1f,0x3400,0xb1a831a8,0xb4000000,0xb1a8b1a8,0x3400,0xb3642e1f,0xb1a8b1a8,0x2e1fb364,
0x3400,0xb4000000,0x8000b400,0x34008000,0x3400,0xb364ae1f,0x31a8b1a8,0x2e1f3364,0x3400,0xb1a8b1a8,0x34008000,0xb1a831a8,0x3400,0xae1fb364,0x31a831a8,0xb364ae1f,
0x3400,0x8000b400,0x3400,0x8000b400,0x3400,0x2e1fb364,0xb1a831a8,0x3364ae1f,0x3400,0x31a8b1a8,0xb4000000,0x31a831a8,0x3400,0x3364ae1f,0xb1a8b1a8,0xae1f3364,
0x3400,0x34008000,0x8000b400,0xb4000000,0x3400,0x33642e1f,0x31a8b1a8,0xae1fb364,0x3400,0x31a831a8,0x34008000,0x31a8b1a8,0x3400,0x2e1f3364,0x31a831a8,0x33642e1f,
0x3400,0x3400,0x3400,0x3400,0xb4000000,0xb364ae1f,0xb1a8b1a8,0xae1fb364,0x8000b400,0x31a8b1a8,0x34008000,0x31a831a8,0x34008000,0x2e1f3364,0xb1a831a8,0xb364ae1f,
0x3400,0xb4000000,0x8000b400,0x34008000,0xb4000000,0x2e1fb364,0x31a831a8,0xb3642e1f,0x8000b400,0x31a831a8,0xb4000000,0x31a8b1a8,0x34008000,0xb3642e1f,0x31a8b1a8,0xae1f3364,
0x3400,0x8000b400,0x3400,0x8000b400,0xb4000000,0x33642e1f,0xb1a8b1a8,0x2e1f3364,0x8000b400,0xb1a831a8,0x34008000,0xb1a8b1a8,0x34008000,0xae1fb364,0xb1a831a8,0x33642e1f,
0x3400,0x34008000,0x8000b400,0xb4000000,0xb4000000,0xae1f3364,0x31a831a8,0x3364ae1f,0x8000b400,0xb1a8b1a8,0xb4000000,0xb1a831a8,0x34008000,0x3364ae1f,0x31a8b1a8,0x2e1fb364,
0x3400,0x3400,0x3400,0x3400,0x8000b400,0x2e1fb364,0x31a8b1a8,0x3364ae1f,0x3400,0xb1a831a8,0xb4000000,0xb1a8b1a8,0x8000b400,0x3364ae1f,0x31a831a8,0xae1f3364,
0x3400,0xb4000000,0x8000b400,0x34008000,0x8000b400,0x33642e1f,0xb1a831a8,0xae1fb364,0x3400,0xb1a8b1a8,0x34008000,0xb1a831a8,0x8000b400,0x2e1f3364,0xb1a8b1a8,0x33642e1f,
0x3400,0x8000b400,0x3400,0x8000b400,0x8000b400,0xae1f3364,0x31a8b1a8,0xb3642e1f,0x3400,0x31a8b1a8,0xb4000000,0x31a831a8,0x8000b400,0xb3642e1f,0x31a831a8,0x2e1fb364,
0x3400,0x34008000,0x8000b400,0xb4000000,0x8000b400,0xb364ae1f,0xb1a831a8,0x2e1f3364,0x3400,0x31a831a8,0x34008000,0x31a8b1a8,0x8000b400,0xae1fb364,0xb1a8b1a8,0xb364ae1f,
0x3400,0x3400,0x3400,0x3400,0x34008000,0x33642e1f,0x31a831a8,0x2e1f3364,0x8000b400,0x31a8b1a8,0x34008000,0x31a831a8,0xb4000000,0xae1fb364,0x31a8b1a8,0x33642e1f,
0x3400,0xb4000000,0x8000b400,0x34008000,0x34008000,0xae1f3364,0xb1a8b1a8,0x3364ae1f,0x8000b400,0x31a831a8,0xb4000000,0x31a8b1a8,0xb4000000,0x3364ae1f,0xb1a831a8,0x2e1fb364,
0x3400,0x8000b400,0x3400,0x8000b400,0x34008000,0xb364ae1f,0x31a831a8,0xae1fb364,0x8000b400,0xb1a831a8,0x34008000,0xb1a8b1a8,0xb4000000,0x2e1f3364,0x31a8b1a8,0xb364ae1f,
0x3400,0x34008000,0x8000b400,0xb4000000,0x34008000,0x2e1fb364,0xb1a8b1a8,0xb3642e1f,0x8000b400,0xb1a8b1a8,0xb4000000,0xb1a831a8,0xb4000000,0xb3642e1f,0xb1a831a8,0xae1f3364,
0x34000000,0x34000000,0x34000000,0x34000000,0x34000000,0x33642e1f,0x31a831a8,0x2e1f3364,0x34000000,0x31a831a8,0x3400,0xb1a831a8,0x34000000,0x2e1f3364,0xb1a831a8,0xb364ae1f,
0x34000000,0x3400,0xb4000000,0x8000b400,0x34000000,0xae1f3364,0xb1a8b1a8,0x3364ae1f,0x34000000,0xb1a831a8,0x8000b400,0x31a831a8,0x34000000,0xb3642e1f,0x31a8b1a8,0xae1f3364,
0x34000000,0xb4000000,0x34000000,0xb4000000,0x34000000,0xb364ae1f,0x31a831a8,0xae1fb364,0x34000000,0xb1a8b1a8,0x3400,0x31a8b1a8,0x34000000,0xae1fb364,0xb1a831a8,0x33642e1f,
0x34000000,0x8000b400,0xb4000000,0x3400,0x34000000,0x2e1fb364,0xb1a8b1a8,0xb3642e1f,0x34000000,0x31a8b1a8,0x8000b400,0xb1a8b1a8,0x34000000,0x3364ae1f,0x31a8b1a8,0x2e1fb364,
0x34000000,0x34000000,0x34000000,0x34000000,0x3400,0xae1f3364,0xb1a831a8,0xb3642e1f,0xb4000000,0xb1a8b1a8,0x8000b400,0x31a8b1a8,0x8000b400,0x3364ae1f,0x31a831a8,0xae1f3364,
0x34000000,0x3400,0xb4000000,0x8000b400,0x3400,0xb364ae1f,0x31a8b1a8,0x2e1f3364,0xb4000000,0x31a8b1a8,0x3400,0xb1a8b1a8,0x8000b400,0x2e1f3364,0xb1a8b1a8,0x33642e1f,
0x34000000,0xb4000000,0x34000000,0xb4000000,0x3400,0x2e1fb364,0xb1a831a8,0x3364ae1f,0xb4000000,0x31a831a8,0x8000b400,0xb1a831a8,0x8000b400,0xb3642e1f,0x31a831a8,0x2e1fb364,
0x34000000,0x8000b400,0xb4000000,0x3400,0x3400,0x33642e1f,0x31a8b1a8,0xae1fb364,0xb4000000,0xb1a831a8,0x3400,0x31a831a8,0x8000b400,0xae1fb364,0xb1a8b1a8,0xb364ae1f,
0x34000000,0x34000000,0x34000000,0x34000000,0xb4000000,0xb364ae1f,0xb1a8b1a8,0xae1fb364,0x34000000,0x31a831a8,0x3400,0xb1a831a8,0xb4000000,0xae1fb364,0x31a8b1a8,0x33642e1f,
0x34000000,0x3400,0xb4000000,0x8000b400,0xb4000000,0x2e1fb364,0x31a831a8,0xb3642e1f,0x34000000,0xb1a831a8,0x8000b400,0x31a831a8,0xb4000000,0x3364ae1f,0xb1a831a8,0x2e1fb364,
0x34000000,0xb4000000,0x34000000,0xb4000000,0xb4000000,0x33642e1f,0xb1a8b1a8,0x2e1f3364,0x34000000,0xb1a8b1a8,0x3400,0x31a8b1a8,0xb4000000,0x2e1f3364,0x31a8b1a8,0xb364ae1f,
0x34000000,0x8000b400,0xb4000000,0x3400,0xb4000000,0xae1f3364,0x31a831a8,0x3364ae1f,0x34000000,0x31a8b1a8,0x8000b400,0xb1a8b1a8,0xb4000000,0xb3642e1f,0xb1a831a8,0xae1f3364,
0x34000000,0x34000000,0x34000000,0x34000000,0x8000b400,0x2e1fb364,0x31a8b1a8,0x3364ae1f,0xb4000000,0xb1a8b1a8,0x8000b400,0x31a8b1a8,0x3400,0xb3642e1f,0xb1a8b1a8,0x2e1fb364,
0x34000000,0x3400,0xb4000000,0x8000b400,0x8000b400,0x33642e1f,0xb1a831a8,0xae1fb364,0xb4000000,0x31a8b1a8,0x3400,0xb1a8b1a8,0x3400,0xae1fb364,0x31a831a8,0xb364ae1f,
0x34000000,0xb4000000,0x34000000,0xb4000000,0x8000b400,0xae1f3364,0x31a8b1a8,0xb3642e1f,0xb4000000,0x31a831a8,0x8000b400,0xb1a831a8,0x3400,0x3364ae1f,0xb1a8b1a8,0xae1f3364,
0x34000000,0x8000b400,0xb4000000,0x3400,0x8000b400,0xb364ae1f,0xb1a831a8,0x2e1f3364,0xb4000000,0xb1a831a8,0x3400,0x31a831a8,0x3400,0x2e1f3364,0x31a831a8,0x33642e1f,
0x3400,0x3400,0x3400,0x3400,0xa64633f6,0xaf8b330e,0xb22f3113,0xb3a82ca5,0xaa3e33d9,0xb2a73072,0xb3d9aa3e,0xb072b2a7,0xaca533a8,0xb3f62646,0xaf8bb30e,0x3113b22f,
0xae1f3364,0xb364ae1f,0x2e1fb364,0x33642e1f,0xaf8b330e,0xb113b22f,0x33a8aca5,0xa64633f6,0xb07232a7,0xaa3eb3d9,0x32a73072,0xb3d92a3e,0xb113322f,0x2ca5b3a8,0x264633f6,0xaf8bb30e,
0xb1a831a8,0x31a8b1a8,0xb1a831a8,0x31a8b1a8,0xb22f3113,0x33a8aca5,0xb3f6a646,0x330e2f8b,0xb2a73072,0x33d92a3e,0xb072b2a7,0xaa3e33d9,0xb30e2f8b,0x322f3113,0x2ca5b3a8,0xb3f62646,
0xb3642e1f,0x2e1f3364,0x3364ae1f,0xae1fb364,0xb3a82ca5,0xa64633f6,0x330e2f8b,0x322fb113,0xb3d92a3e,0xb07232a7,0x2a3e33d9,0x32a73072,0xb3f62646,0xb30e2f8b,0xb113322f,0xaca533a8,
0x3400,0x3400,0x3400,0x3400,0xb3f6a646,0xb30eaf8b,0xb113b22f,0xaca5b3a8,0x2a3eb3d9,0x32a7b072,0x33d92a3e,0x307232a7,0x33a82ca5,0x264633f6,0xb30e2f8b,0xb22fb113,
0xae1f3364,0xb364ae1f,0x2e1fb364,0x33642e1f,0xb30eaf8b,0x322fb113,0x2ca533a8,0xb3f6a646,0x3072b2a7,0x2a3e33d9,0xb2a7b072,0x33d9aa3e,0x322f3113,0xb3a8aca5,0x33f6a646,0xb30e2f8b,
0xb1a831a8,0x31a8b1a8,0xb1a831a8,0x31a8b1a8,0xb113b22f,0x2ca533a8,0x2646b3f6,0xaf8b330e,0x32a7b072,0xb3d9aa3e,0x307232a7,0x2a3eb3d9,0x2f8b330e,0x3113b22f,0xb3a8aca5,0x264633f6,
0xb3642e1f,0x2e1f3364,0x3364ae1f,0xae1fb364,0xaca5b3a8,0xb3f6a646,0xaf8b330e,0x3113322f,0x33d9aa3e,0x3072b2a7,0xaa3eb3d9,0xb2a7b072,0x264633f6,0x2f8b330e,0x322f3113,0x33a82ca5,
0x3400,0x3400,0x3400,0x3400,0x2646b3f6,0x2f8bb30e,0x322fb113,0x33a8aca5,0xaa3e33d9,0xb2a73072,0xb3d9aa3e,0xb072b2a7,0x2ca5b3a8,0x33f6a646,0x2f8b330e,0xb113322f,
0xae1f3364,0xb364ae1f,0x2e1fb364,0x33642e1f,0x2f8bb30e,0x3113322f,0xb3a82ca5,0x2646b3f6,0xb07232a7,0xaa3eb3d9,0x32a73072,0xb3d92a3e,0x3113b22f,0xaca533a8,0xa646b3f6,0x2f8b330e,
0xb1a831a8,0x31a8b1a8,0xb1a831a8,0x31a8b1a8,0x322fb113,0xb3a82ca5,0x33f62646,0xb30eaf8b,0xb2a73072,0x33d92a3e,0xb072b2a7,0xaa3e33d9,0x330eaf8b,0xb22fb113,0xaca533a8,0x33f6a646,
0xb3642e1f,0x2e1f3364,0x3364ae1f,0xae1fb364,0x33a8aca5,0x2646b3f6,0xb30eaf8b,0xb22f3113,0xb3d92a3e,0xb07232a7,0x2a3e33d9,0x32a73072,0x33f6a646,0x330eaf8b,0x3113b22f,0x2ca5b3a8,
0x3400,0x3400,0x3400,0x3400,0x33f62646,0x330e2f8b,0x3113322f,0x2ca533a8,0x2a3eb3d9,0x32a7b072,0x33d92a3e,0x307232a7,0xb3a8aca5,0xa646b3f6,0x330eaf8b,0x322f3113,
0xae1f3364,0xb364ae1f,0x2e1fb364,0x33642e1f,0x330e2f8b,0xb22f3113,0xaca5b3a8,0x33f62646,0x3072b2a7,0x2a3e33d9,0xb2a7b072,0x33d9aa3e,0xb22fb113,0x33a82ca5,0xb3f62646,0x330eaf8b,
0xb1a831a8,0x31a8b1a8,0xb1a831a8,0x31a8b1a8,0x3113322f,0xaca5b3a8,0xa64633f6,0x2f8bb30e,0x32a7b072,0xb3d9aa3e,0x307232a7,0x2a3eb3d9,0xaf8bb30e,0xb113322f,0x33a82ca5,0xa646b3f6,
0xb3642e1f,0x2e1f3364,0x3364ae1f,0xae1fb364,0x2ca533a8,0x33f62646,0x2f8bb30e,0xb113b22f,0x33d9aa3e,0x3072b2a7,0xaa3eb3d9,0xb2a7b072,0xa646b3f6,0xaf8bb30e,0xb22fb113,0xb3a8aca5,
0x34000000,0x34000000,0x34000000,0x34000000,0x33f62646,0x330e2f8b,0x3113322f,0x2ca533a8,0x33d92a3e,0x307232a7,0xaa3e33d9,0xb2a73072,0x33a82ca5,0x264633f6,0xb30e2f8b,0xb22fb113,
0x33642e1f,0xae1f3364,0xb364ae1f,0x2e1fb364,0x330e2f8b,0xb22f3113,0xaca5b3a8,0x33f62646,0x32a73072,0xb3d92a3e,0x3072b2a7,0x2a3e33d9,0x322f3113,0xb3a8aca5,0x33f6a646,0xb30e2f8b,
0x31a831a8,0xb1a8b1a8,0x31a831a8,0xb1a8b1a8,0x3113322f,0xaca5b3a8,0xa64633f6,0x2f8bb30e,0x307232a7,0x2a3eb3d9,0xb2a73072,0x33d92a3e,0x2f8b330e,0x3113b22f,0xb3a8aca5,0x264633f6,
0x2e1f3364,0x3364ae1f,0xae1fb364,0xb3642e1f,0x2ca533a8,0x33f62646,0x2f8bb30e,0xb113b22f,0x2a3e33d9,0x32a73072,0x33d9aa3e,0x3072b2a7,0x264633f6,0x2f8b330e,0x322f3113,0x33a82ca5,
0x34000000,0x34000000,0x34000000,0x34000000,0xa64633f6,0xaf8b330e,0xb22f3113,0xb3a82ca5,0xb3d9aa3e,0xb072b2a7,0x2a3eb3d9,0x32a7b072,0x2ca5b3a8,0x33f6a646,0x2f8b330e,0xb113322f,
0x33642e1f,0xae1f3364,0xb364ae1f,0x2e1fb364,0xaf8b330e,0xb113b22f,0x33a8aca5,0xa64633f6,0xb2a7b072,0x33d9aa3e,0xb07232a7,0xaa3eb3d9,0x3113b22f,0xaca533a8,0xa646b3f6,0x2f8b330e,
0x31a831a8,0xb1a8b1a8,0x31a831a8,0xb1a8b1a8,0xb22f3113,0x33a8aca5,0xb3f6a646,0x330e2f8b,0xb072b2a7,0xaa3e33d9,0x32a7b072,0xb3d9aa3e,0x330eaf8b,0xb22fb113,0xaca533a8,0x33f6a646,
0x2e1f3364,0x3364ae1f,0xae1fb364,0xb3642e1f,0xb3a82ca5,0xa64633f6,0x330e2f8b,0x322fb113,0xaa3eb3d9,0xb2a7b072,0xb3d92a3e,0xb07232a7,0x33f6a646,0x330eaf8b,0x3113b22f,0x2ca5b3a8,
0x34000000,0x34000000,0x34000000,0x34000000,0xb3f6a646,0xb30eaf8b,0xb113b22f,0xaca5b3a8,0x33d92a3e,0x307232a7,0xaa3e33d9,0xb2a73072,0xb3a8aca5,0xa646b3f6,0x330eaf8b,0x322f3113,
0x33642e1f,0xae1f3364,0xb364ae1f,0x2e1fb364,0xb30eaf8b,0x322fb113,0x2ca533a8,0xb3f6a646,0x32a73072,0xb3d92a3e,0x3072b2a7,0x2a3e33d9,0xb22fb113,0x33a82ca5,0xb3f62646,0x330eaf8b,
0x31a831a8,0xb1a8b1a8,0x31a831a8,0xb1a8b1a8,0xb113b22f,0x2ca533a8,0x2646b3f6,0xaf8b330e,0x307232a7,0x2a3eb3d9,0xb2a73072,0x33d92a3e,0xaf8bb30e,0xb113322f,0x33a82ca5,0xa646b3f6,
0x2e1f3364,0x3364ae1f,0xae1fb364,0xb3642e1f,0xaca5b3a8,0xb3f6a646,0xaf8b330e,0x3113322f,0x2a3e33d9,0x32a73072,0x33d9aa3e,0x3072b2a7,0xa646b3f6,0xaf8bb30e,0xb22fb113,0xb3a8aca5,
0x34000000,0x34000000,0x34000000,0x34000000,0x2646b3f6,0x2f8bb30e,0x322fb113,0x33a8aca5,0xb3d9aa3e,0xb072b2a7,0x2a3eb3d9,0x32a7b072,0xaca533a8,0xb3f62646,0xaf8bb30e,0x3113b22f,
0x33642e1f,0xae1f3364,0xb364ae1f,0x2e1fb364,0x2f8bb30e,0x3113322f,0xb3a82ca5,0x2646b3f6,0xb2a7b072,0x33d9aa3e,0xb07232a7,0xaa3eb3d9,0xb113322f,0x2ca5b3a8,0x264633f6,0xaf8bb30e,
0x31a831a8,0xb1a8b1a8,0x31a831a8,0xb1a8b1a8,0x322fb113,0xb3a82ca5,0x33f62646,0xb30eaf8b,0xb072b2a7,0xaa3e33d9,0x32a7b072,0xb3d9aa3e,0xb30e2f8b,0x322f3113,0x2ca5b3a8,0xb3f62646,
0x2e1f3364,0x3364ae1f,0xae1fb364,0xb3642e1f,0x33a8aca5,0x2646b3f6,0xb30eaf8b,0xb22f3113,0xaa3eb3d9,0xb2a7b072,0xb3d92a3e,0xb07232a7,0xb3f62646,0xb30e2f8b,0xb113322f,0xaca533a8,
0x3f800000,0x3f7ffec4,0x3f7ffb11,0x3f7ff4e6,0x3f800000,0x3f7fe129,0x3f7f84ab,0x3f7eea9d,0x3f800000,0x3f7f9c18,0x3f7e70b0,0x3f7c7eb0,0x3f800000,0x3f7f2f9d,0x3f7cbfc9,0x3f78b47b,
0x3f800000,0x3f7e9bc9,0x3f7a7302,0x3f73913f,0x3f800000,0x3f7de0b1,0x3f778bc5,0x3f6d1c1d,0x3f800000,0x3f7cfe73,0x3f740bdd,0x3f655e0b,0x3f800000,0x3f7bf531,0x3f6ff573,0x3f5c61c7,
0x3f800000,0x3f7ac516,0x3f6b4b0c,0x3f5233c6,0x3f800000,0x3f796e4e,0x3f660f88,0x3f46e22a,0x3f800000,0x3f77f110,0x3f604621,0x3f3a7ca4,0x3f800000,0x3f764d97,0x3f59f26a,0x3f2d1469,
0x3f800000,0x3f748422,0x3f531849,0x3f1ebc12,0x3f800000,0x3f7294f8,0x3f4bbbf8,0x3f0f8784,0x3f800000,0x3f708066,0x3f43e200,0x3eff17b2,0x3f800000,0x3f6e46be,0x3f3b8f3b,0x3eddbe79,
0x3f7fec43,0x3f7fe129,0x3f7fd397,0x3f7fc38f,0x3f7e1324,0x3f7cfe73,0x3f7baccd,0x3f7a1e84,0x3f79c79d,0x3f764d97,0x3f721352,0x3f6d1c1d,0x3f731447,0x3f6be858,0x3f633c5a,0x3f591e6a,
0x3f6a09a7,0x3f5df6be,0x3f4f7a1f,0x3f3ebc1b,0x3f5ebe05,0x3f4cae79,0x3f373a23,0x3f1ebc12,0x3f514d3d,0x3f385216,0x3f1b02c6,0x3ef41f07,0x3f41d870,0x3f212ff9,0x3ef6e0cb,0x3ea395c5,
0x3f3085bb,0x3f07a136,0x3eb263ef,0x3e1c76de,0x3f1d7fd1,0x3ed8106b,0x3e541501,0xbc96c9b6,0x3f08f59b,0x3e9d9e78,0x3d7b2b74,0xbe419b37,0x3ee63375,0x3e419b37,0xbdafb680,0xbeb554ec,
0x3eb8442a,0x3d8a200a,0xbe6c9a7f,0xbf0242b1,0x3e888e93,0xbd621469,0xbebe1d4a,0xbf26050a,0x3e2f10a2,0xbe354098,0xbf00e7e4,0xbf44e3f5,0x3d96a905,0xbe97a117,0xbf1ff6cb,0xbf5df6be,
0x3f7fb10f,0x3f7f9c18,0x3f7f84ab,0x3f7f6ac7,0x3f7853f8,0x3f764d97,0x3f740bdd,0x3f718f57,0x3f676bd8,0x3f6106f2,0x3f59f26a,0x3f5233c6,0x3f4d9f02,0x3f40d0da,0x3f32c8c9,0x3f239da9,
0x3f2beb4a,0x3f173c07,0x3f00e7e4,0x3ed25a09,0x3f039c3d,0x3ecc9b8b,0x3e8e9a22,0x3e1c76de,0x3eac7cd4,0x3e419b37,0x3d16c32c,0xbdee3876,0x3e164083,0xbcfb49ba,0xbe541501,0xbec1071e,
0xbd48fb30,0xbe7ee6e1,0xbee0924f,0xbf1c420c,0xbe78cfcc,0xbee900b7,0xbf24d225,0xbf4cae79,0xbedae880,0xbf239da9,0xbf4f7a1f,0xbf6e46be,0xbf187fc0,0xbf4ac77f,0xbf6db293,0xbf7e9bc9,
0xbf3daef9,0xbf6816a8,0xbf7dabcc,0xbf7c7eb0,0xbf5b941a,0xbf7a1e84,0xbf7e70b0,0xbf6816a8,0xbf710908,0xbf7ffec4,0xbf6ff573,0xbf42de29,0xbf7d3aac,0xbf796e4e,0xbf531849,0xbf0f8784,
0x3f7f4e6d,0x3f7f2f9d,0x3f7f0e58,0x3f7eea9d,0x3f6ed89e,0x3f6be858,0x3f68bf3c,0x3f655e0b,0x3f49d112,0x3f40d0da,0x3f373a23,0x3f2d1469,0x3f13682a,0x3f0242b1,0x3ee0924f,0x3ebb31a0,
0x3ea09ae5,0x3e5a3997,0x3de1bc2e,0x3bc90f88,0x3cc90ab0,0xbdd53db9,0xbe6c9a7f,0xbeb554ec,0xbe888e93,0xbed25a09,0xbf0b9a6b,0xbf2ac082,0xbf08f59b,0xbf2d1469,0xbf4bbbf8,0xbf63f473,
0xbf41d870,0xbf5f8327,0xbf740bdd,0xbf7e9bc9,0xbf6a09a7,0xbf7b61fc,0xbf7fd397,0xbf772417,0xbf7e1324,0xbf7de0b1,0xbf6db293,0xbf4e8d90,0xbf7c3b28,0xbf66becc,0xbf3fc767,0xbf0a48ad,
0xbf64aa59,0xbf385216,0xbef6e0cb,0xbe4dee60,0xbf396842,0xbeee9479,0xbe22abb6,0x3e28defc,0xbefc5d27,0xbe28defc,0x3e3b6ecf,0x3f0242b1,0xbe605c13,0x3e1c76de,0x3f00e7e4,0x3f48d8b3,
0x0,0x3bc90f88,0x3c490e90,0x3c96c9b6,0x0,0x3cfb49ba,0x3d7b2b74,0x3dbc3ac3,0x0,0x3d621469,0x3de1bc2e,0x3e28defc,0x0,0x3da3308c,0x3e22abb6,0x3e72b651,
0x0,0x3dd53db9,0x3e541501,0x3e9d9e78,0x0,0x3e039502,0x3e827dc0,0x3ec1071e,0x0,0x3e1c76de,0x3e9aa086,0x3ee363fa,0x0,0x3e354098,0x3eb263ef,0x3f0242b1,
0x0,0x3e4dee60,0x3ec9b953,0x3f121eb0,0x0,0x3e667c66,0x3ee0924f,0x3f212ff9,0x0,0x3e7ee6e1,0x3ef6e0cb,0x3f2f61a5,0x0,0x3e8b9507,0x3f064b82,0x3f3ca003,
0x0,0x3e97a117,0x3f10d3cd,0x3f48d8b3,0x0,0x3ea395c5,0x3f1b02c6,0x3f53fac3,0x0,0x3eaf713a,0x3f24d225,0x3f5df6be,0x0,0x3ebb31a0,0x3f2e3bde,0x3f66becc,
0x3cc90ab0,0x3cfb49ba,0x3d16c32c,0x3d2fe007,0x3dfab273,0x3e1c76de,0x3e3b6ecf,0x3e5a3997,0x3e605c13,0x3e8b9507,0x3ea68f12,0x3ec1071e,0x3ea09ae5,0x3ec6d529,0x3eebcbbb,0x3f07a136,
0x3ecf7bca,0x3eff17b2,0x3f15f6d9,0x3f2ac082,0x3efc5d27,0x3f19c200,0x3f32c8c9,0x3f48d8b3,0x3f13682a,0x3f31a81d,0x3f4bbbf8,0x3f6106f2,0x3f273656,0x3f46e22a,0x3f604621,0x3f7294f8,
0x3f396842,0x3f591e6a,0x3f6ff573,0x3f7cfe73,0x3f49d112,0x3f6816a8,0x3f7a7302,0x3f7ff4e6,0x3f584853,0x3f73913f,0x3f7f84ab,0x3f7b61fc,0x3f64aa59,0x3f7b61fc,0x3f7f0e58,0x3f6f6830,
0x3f6ed89e,0x3f7f6ac7,0x3f791298,0x3f5c61c7,0x3f76ba07,0x3f7f9c18,0x3f6db293,0x3f42de29,0x3f7c3b28,0x3f7bf531,0x3f5d2d53,0x3f239da9,0x3f7f4e6d,0x3f748422,0x3f47de65,0x3eff17b2,
0x3d48fb30,0x3d621469,0x3d7b2b74,0x3d8a200a,0x3e78cfcc,0x3e8b9507,0x3e9aa086,0x3ea986c4,0x3edae880,0x3ef41f07,0x3f064b82,0x3f121eb0,0x3f187fc0,0x3f286605,0x3f373a23,0x3f44e3f5,
0x3f3daef9,0x3f4e8d90,0x3f5d2d53,0x3f696591,0x3f5b941a,0x3f6aab7b,0x3f75dec6,0x3f7cfe73,0x3f710908,0x3f7b61fc,0x3f7fd397,0x3f7e4323,0x3f7d3aac,0x3f7fe129,0x3f7a7302,0x3f6d1c1d,
0x3f7fb10f,0x3f77f110,0x3f660f88,0x3f4ac77f,0x3f7853f8,0x3f63f473,0x3f43e200,0x3f19c200,0x3f676bd8,0x3f44e3f5,0x3f15f6d9,0x3ebb31a0,0x3f4d9f02,0x3f1c420c,0x3ebe1d4a,0x3dd53db9,
0x3f2beb4a,0x3ed8106b,0x3e09cf86,0xbe28defc,0x3f039c3d,0x3e5a3997,0xbde1bc2e,0xbed8106b,0x3eac7cd4,0xbbc90f88,0xbeb263ef,0xbf26050a,0x3e164083,0xbe667c66,0xbf10d3cd,0xbf53fac3,
0x3d96a905,0x3da3308c,0x3dafb680,0x3dbc3ac3,0x3eb8442a,0x3ec6d529,0x3ed53641,0x3ee363fa,0x3f1d7fd1,0x3f286605,0x3f32c8c9,0x3f3ca003,0x3f514d3d,0x3f5c61c7,0x3f660f88,0x3f6e46be,
0x3f731447,0x3f7a1e84,0x3f7e70b0,0x3f7ffec4,0x3f7fec43,0x3f7e9bc9,0x3f791298,0x3f6f6830,0x3f76ba07,0x3f696591,0x3f5695e5,0x3f3ebc1b,0x3f584853,0x3f3ca003,0x3f1b02c6,0x3ee900b7,
0x3f273656,0x3ef9a02d,0x3e9aa086,0x3dd53db9,0x3ecf7bca,0x3e419b37,0xbd16c32c,0xbe8586ce,0x3dfab273,0xbe039502,0xbebe1d4a,0xbf173c07,0xbe2f10a2,0xbeddbe79,0xbf299415,0xbf577026,
0xbee63375,0xbf31a81d,0xbf604621,0xbf7ac516,0xbf3085bb,0xbf628210,0xbf7cbfc9,0xbf7c7eb0,0xbf5ebe05,0xbf7c7eb0,0xbf7baccd,0xbf5c61c7,0xbf79c79d,0xbf7cfe73,0xbf5d2d53,0xbf1ebc12,
0x3f800000,0x3f800000,0x3f800000,0x3f800000,0x3f7ffec4,0x3f7fe129,0x3f7f9c18,0x3f7f2f9d,0x3f7ffb11,0x3f7f84ab,0x3f7e70b0,0x3f7cbfc9,0x3f7ff4e6,0x3f7eea9d,0x3f7c7eb0,0x3f78b47b,
0x3f7fec43,0x3f7e1324,0x3f79c79d,0x3f731447,0x3f7fe129,0x3f7cfe73,0x3f764d97,0x3f6be858,0x3f7fd397,0x3f7baccd,0x3f721352,0x3f633c5a,0x3f7fc38f,0x3f7a1e84,0x3f6d1c1d,0x3f591e6a,
0x3f7fb10f,0x3f7853f8,0x3f676bd8,0x3f4d9f02,0x3f7f9c18,0x3f764d97,0x3f6106f2,0x3f40d0da,0x3f7f84ab,0x3f740bdd,0x3f59f26a,0x3f32c8c9,0x3f7f6ac7,0x3f718f57,0x3f5233c6,0x3f239da9,
0x3f7f4e6d,0x3f6ed89e,0x3f49d112,0x3f13682a,0x3f7f2f9d,0x3f6be858,0x3f40d0da,0x3f0242b1,0x3f7f0e58,0x3f68bf3c,0x3f373a23,0x3ee0924f,0x3f7eea9d,0x3f655e0b,0x3f2d1469,0x3ebb31a0,
0x3f800000,0x3f800000,0x3f800000,0x3f800000,0x3f7e9bc9,0x3f7de0b1,0x3f7cfe73,0x3f7bf531,0x3f7a7302,0x3f778bc5,0x3f740bdd,0x3f6ff573,0x3f73913f,0x3f6d1c1d,0x3f655e0b,0x3f5c61c7,
0x3f6a09a7,0x3f5ebe05,0x3f514d3d,0x3f41d870,0x3f5df6be,0x3f4cae79,0x3f385216,0x3f212ff9,0x3f4f7a1f,0x3f373a23,0x3f1b02c6,0x3ef6e0cb,0x3f3ebc1b,0x3f1ebc12,0x3ef41f07,0x3ea395c5,
0x3f2beb4a,0x3f039c3d,0x3eac7cd4,0x3e164083,0x3f173c07,0x3ecc9b8b,0x3e419b37,0xbcfb49ba,0x3f00e7e4,0x3e8e9a22,0x3d16c32c,0xbe541501,0x3ed25a09,0x3e1c76de,0xbdee3876,0xbec1071e,
0x3ea09ae5,0x3cc90ab0,0xbe888e93,0xbf08f59b,0x3e5a3997,0xbdd53db9,0xbed25a09,0xbf2d1469,0x3de1bc2e,0xbe6c9a7f,0xbf0b9a6b,0xbf4bbbf8,0x3bc90f88,0xbeb554ec,0xbf2ac082,0xbf63f473,
0x3f800000,0x3f800000,0x3f800000,0x3f800000,0x3f7ac516,0x3f796e4e,0x3f77f110,0x3f764d97,0x3f6b4b0c,0x3f660f88,0x3f604621,0x3f59f26a,0x3f5233c6,0x3f46e22a,0x3f3a7ca4,0x3f2d1469,
0x3f3085bb,0x3f1d7fd1,0x3f08f59b,0x3ee63375,0x3f07a136,0x3ed8106b,0x3e9d9e78,0x3e419b37,0x3eb263ef,0x3e541501,0x3d7b2b74,0xbdafb680,0x3e1c76de,0xbc96c9b6,0xbe419b37,0xbeb554ec,
0xbd48fb30,0xbe78cfcc,0xbedae880,0xbf187fc0,0xbe7ee6e1,0xbee900b7,0xbf239da9,0xbf4ac77f,0xbee0924f,0xbf24d225,0xbf4f7a1f,0xbf6db293,0xbf1c420c,0xbf4cae79,0xbf6e46be,0xbf7e9bc9,
0xbf41d870,0xbf6a09a7,0xbf7e1324,0xbf7c3b28,0xbf5f8327,0xbf7b61fc,0xbf7de0b1,0xbf66becc,0xbf740bdd,0xbf7fd397,0xbf6db293,0xbf3fc767,0xbf7e9bc9,0xbf772417,0xbf4e8d90,0xbf0a48ad,
0x3f800000,0x3f800000,0x3f800000,0x3f800000,0x3f748422,0x3f7294f8,0x3f708066,0x3f6e46be,0x3f531849,0x3f4bbbf8,0x3f43e200,0x3f3b8f3b,0x3f1ebc12,0x3f0f8784,0x3eff17b2,0x3eddbe79,
0x3eb8442a,0x3e888e93,0x3e2f10a2,0x3d96a905,0x3d8a200a,0xbd621469,0xbe354098,0xbe97a117,0xbe6c9a7f,0xbebe1d4a,0xbf00e7e4,0xbf1ff6cb,0xbf0242b1,0xbf26050a,0xbf44e3f5,0xbf5df6be,
0xbf3daef9,0xbf5b941a,0xbf710908,0xbf7d3aac,0xbf6816a8,0xbf7a1e84,0xbf7ffec4,0xbf796e4e,0xbf7dabcc,0xbf7e70b0,0xbf6ff573,0xbf531849,0xbf7c7eb0,0xbf6816a8,0xbf42de29,0xbf0f8784,
0xbf64aa59,0xbf396842,0xbefc5d27,0xbe605c13,0xbf385216,0xbeee9479,0xbe28defc,0x3e1c76de,0xbef6e0cb,0xbe22abb6,0x3e3b6ecf,0x3f00e7e4,0xbe4dee60,0x3e28defc,0x3f0242b1,0x3f48d8b3,
0x0,0x0,0x0,0x0,0x3bc90f88,0x3cfb49ba,0x3d621469,0x3da3308c,0x3c490e90,0x3d7b2b74,0x3de1bc2e,0x3e22abb6,0x3c96c9b6,0x3dbc3ac3,0x3e28defc,0x3e72b651,
0x3cc90ab0,0x3dfab273,0x3e605c13,0x3ea09ae5,0x3cfb49ba,0x3e1c76de,0x3e8b9507,0x3ec6d529,0x3d16c32c,0x3e3b6ecf,0x3ea68f12,0x3eebcbbb,0x3d2fe007,0x3e5a3997,0x3ec1071e,0x3f07a136,
0x3d48fb30,0x3e78cfcc,0x3edae880,0x3f187fc0,0x3d621469,0x3e8b9507,0x3ef41f07,0x3f286605,0x3d7b2b74,0x3e9aa086,0x3f064b82,0x3f373a23,0x3d8a200a,0x3ea986c4,0x3f121eb0,0x3f44e3f5,
0x3d96a905,0x3eb8442a,0x3f1d7fd1,0x3f514d3d,0x3da3308c,0x3ec6d529,0x3f286605,0x3f5c61c7,0x3dafb680,0x3ed53641,0x3f32c8c9,0x3f660f88,0x3dbc3ac3,0x3ee363fa,0x3f3ca003,0x3f6e46be,
0x0,0x0,0x0,0x0,0x3dd53db9,0x3e039502,0x3e1c76de,0x3e354098,0x3e541501,0x3e827dc0,0x3e9aa086,0x3eb263ef,0x3e9d9e78,0x3ec1071e,0x3ee363fa,0x3f0242b1,
0x3ecf7bca,0x3efc5d27,0x3f13682a,0x3f273656,0x3eff17b2,0x3f19c200,0x3f31a81d,0x3f46e22a,0x3f15f6d9,0x3f32c8c9,0x3f4bbbf8,0x3f604621,0x3f2ac082,0x3f48d8b3,0x3f6106f2,0x3f7294f8,
0x3f3daef9,0x3f5b941a,0x3f710908,0x3f7d3aac,0x3f4e8d90,0x3f6aab7b,0x3f7b61fc,0x3f7fe129,0x3f5d2d53,0x3f75dec6,0x3f7fd397,0x3f7a7302,0x3f696591,0x3f7cfe73,0x3f7e4323,0x3f6d1c1d,
0x3f731447,0x3f7fec43,0x3f76ba07,0x3f584853,0x3f7a1e84,0x3f7e9bc9,0x3f696591,0x3f3ca003,0x3f7e70b0,0x3f791298,0x3f5695e5,0x3f1b02c6,0x3f7ffec4,0x3f6f6830,0x3f3ebc1b,0x3ee900b7,
0x0,0x0,0x0,0x0,0x3e4dee60,0x3e667c66,0x3e7ee6e1,0x3e8b9507,0x3ec9b953,0x3ee0924f,0x3ef6e0cb,0x3f064b82,0x3f121eb0,0x3f212ff9,0x3f2f61a5,0x3f3ca003,
0x3f396842,0x3f49d112,0x3f584853,0x3f64aa59,0x3f591e6a,0x3f6816a8,0x3f73913f,0x3f7b61fc,0x3f6ff573,0x3f7a7302,0x3f7f84ab,0x3f7f0e58,0x3f7cfe73,0x3f7ff4e6,0x3f7b61fc,0x3f6f6830,
0x3f7fb10f,0x3f7853f8,0x3f676bd8,0x3f4d9f02,0x3f77f110,0x3f63f473,0x3f44e3f5,0x3f1c420c,0x3f660f88,0x3f43e200,0x3f15f6d9,0x3ebe1d4a,0x3f4ac77f,0x3f19c200,0x3ebb31a0,0x3dd53db9,
0x3f273656,0x3ecf7bca,0x3dfab273,0xbe2f10a2,0x3ef9a02d,0x3e419b37,0xbe039502,0xbeddbe79,0x3e9aa086,0xbd16c32c,0xbebe1d4a,0xbf299415,0x3dd53db9,0xbe8586ce,0xbf173c07,0xbf577026,
0x0,0x0,0x0,0x0,0x3e97a117,0x3ea395c5,0x3eaf713a,0x3ebb31a0,0x3f10d3cd,0x3f1b02c6,0x3f24d225,0x3f2e3bde,0x3f48d8b3,0x3f53fac3,0x3f5df6be,0x3f66becc,
0x3f6ed89e,0x3f76ba07,0x3f7c3b28,0x3f7f4e6d,0x3f7f6ac7,0x3f7f9c18,0x3f7bf531,0x3f748422,0x3f791298,0x3f6db293,0x3f5d2d53,0x3f47de65,0x3f5c61c7,0x3f42de29,0x3f239da9,0x3eff17b2,
0x3f2beb4a,0x3f039c3d,0x3eac7cd4,0x3e164083,0x3ed8106b,0x3e5a3997,0xbbc90f88,0xbe667c66,0x3e09cf86,0xbde1bc2e,0xbeb263ef,0xbf10d3cd,0xbe28defc,0xbed8106b,0xbf26050a,0xbf53fac3,
0xbee63375,0xbf3085bb,0xbf5ebe05,0xbf79c79d,0xbf31a81d,0xbf628210,0xbf7c7eb0,0xbf7cfe73,0xbf604621,0xbf7cbfc9,0xbf7baccd,0xbf5d2d53,0xbf7ac516,0xbf7c7eb0,0xbf5c61c7,0xbf1ebc12,
0x3400,0x28b233ea,0x2ca533a8,0x2ed7333b,0x3400,0x301d32dd,0x330e2f8b,0x33fea248,0x3400,0x326d30c4,0x33a8aca5,0x2d64b388,0x3400,0x33c32bc6,0x2f8bb30e,0xb1edb15f,
0x3400,0x33eaa8b2,0xaca5b3a8,0xb33b2ed7,0x3400,0x32ddb01d,0xb30eaf8b,0x224833fe,0x3400,0x30c4b26d,0xb3a82ca5,0x33882d64,0x3400,0x2bc6b3c3,0xaf8b330e,0x315fb1ed,
0x3400,0xa8b2b3ea,0x2ca533a8,0xaed7b33b,0x3400,0xb01db2dd,0x330e2f8b,0xb3fe2248,0x3400,0xb26db0c4,0x33a8aca5,0xad643388,0x3400,0xb3c3abc6,0x2f8bb30e,0x31ed315f,
0x3400,0xb3ea28b2,0xaca5b3a8,0x333baed7,0x3400,0xb2dd301d,0xb30eaf8b,0xa248b3fe,0x3400,0xb0c4326d,0xb3a82ca5,0xb388ad64,0x3400,0xabc633c3,0xaf8b330e,0xb15f31ed,
0x307232a7,0x315f31ed,0x322f3113,0x32dd301d,0x32a7b072,0x2ed7b33b,0xa646b3f6,0xb0c4b26d,0xb072b2a7,0xb3fea248,0xb113322f,0x2bc633c3,0xb2a73072,0x2d643388,0x33f6a646,0x28b2b3ea,
0x307232a7,0x31edb15f,0xb22fb113,0xb01d32dd,0x32a7b072,0xb33baed7,0x264633f6,0x326db0c4,0xb072b2a7,0xa24833fe,0x3113b22f,0xb3c32bc6,0xb2a73072,0x3388ad64,0xb3f62646,0x33ea28b2,
0x307232a7,0xb15fb1ed,0x322f3113,0xb2ddb01d,0x32a7b072,0xaed7333b,0xa646b3f6,0x30c4326d,0xb072b2a7,0x33fe2248,0xb113322f,0xabc6b3c3,0xb2a73072,0xad64b388,0x33f6a646,0xa8b233ea,
0x307232a7,0xb1ed315f,0xb22fb113,0x301db2dd,0x32a7b072,0x333b2ed7,0x264633f6,0xb26d30c4,0xb072b2a7,0x2248b3fe,0x3113b22f,0x33c3abc6,0xb2a73072,0xb3882d64,0xb3f62646,0xb3eaa8b2,
0x33642e1f,0x33c32bc6,0x33f62646,0x33fea248,0xb364ae1f,0xb3ea28b2,0xb22f3113,0xad643388,0x33642e1f,0x32ddb01d,0x2646b3f6,0xb1edb15f,0xb364ae1f,0xb0c4326d,0x3113322f,0x333baed7,
0x33642e1f,0x2bc6b3c3,0xb3f6a646,0x224833fe,0xb364ae1f,0x28b233ea,0x322fb113,0xb388ad64,0x33642e1f,0xb01db2dd,0xa64633f6,0x315fb1ed,0xb364ae1f,0x326d30c4,0xb113b22f,0x2ed7333b,
0x33642e1f,0xb3c3abc6,0x33f62646,0xb3fe2248,0xb364ae1f,0x33eaa8b2,0xb22f3113,0x2d64b388,0x33642e1f,0xb2dd301d,0x2646b3f6,0x31ed315f,0xb364ae1f,0x30c4b26d,0x3113322f,0xb33b2ed7,
0x33642e1f,0xabc633c3,0xb3f6a646,0xa248b3fe,0xb364ae1f,0xa8b2b3ea,0x322fb113,0x33882d64,0x33642e1f,0x301d32dd,0xa64633f6,0xb15f31ed,0xb364ae1f,0xb26db0c4,0xb113b22f,0xaed7b33b,
0x33d9aa3e,0x3388ad64,0x330eaf8b,0x326db0c4,0x2a3e33d9,0x315f31ed,0x33a82ca5,0x33c3abc6,0xb3d92a3e,0xaed7333b,0x2f8b330e,0x33ea28b2,0xaa3eb3d9,0xb3fea248,0xaca533a8,0x32dd301d,
0x33d9aa3e,0xad64b388,0xb30e2f8b,0x30c4326d,0x2a3e33d9,0x31edb15f,0xb3a8aca5,0x2bc633c3,0xb3d92a3e,0x333b2ed7,0xaf8bb30e,0xa8b233ea,0xaa3eb3d9,0xa24833fe,0x2ca5b3a8,0xb01d32dd,
0x33d9aa3e,0xb3882d64,0x330eaf8b,0xb26d30c4,0x2a3e33d9,0xb15fb1ed,0x33a82ca5,0xb3c32bc6,0xb3d92a3e,0x2ed7b33b,0x2f8b330e,0xb3eaa8b2,0xaa3eb3d9,0x33fe2248,0xaca533a8,0xb2ddb01d,
0x33d9aa3e,0x2d643388,0xb30e2f8b,0xb0c4b26d,0x2a3e33d9,0xb1ed315f,0xb3a8aca5,0xabc6b3c3,0xb3d92a3e,0xb33baed7,0xaf8bb30e,0x28b2b3ea,0xaa3eb3d9,0x2248b3fe,0x2ca5b3a8,0x301db2dd,
0x34000000,0x33eaa8b2,0x33a8aca5,0x333baed7,0x34000000,0x32ddb01d,0x2f8bb30e,0xa248b3fe,0x34000000,0x30c4b26d,0xaca5b3a8,0xb388ad64,0x34000000,0x2bc6b3c3,0xb30eaf8b,0xb15f31ed,
0x34000000,0xa8b2b3ea,0xb3a82ca5,0x2ed7333b,0x34000000,0xb01db2dd,0xaf8b330e,0x33fea248,0x34000000,0xb26db0c4,0x2ca533a8,0x2d64b388,0x34000000,0xb3c3abc6,0x330e2f8b,0xb1edb15f,
0x34000000,0xb3ea28b2,0x33a8aca5,0xb33b2ed7,0x34000000,0xb2dd301d,0x2f8bb30e,0x224833fe,0x34000000,0xb0c4326d,0xaca5b3a8,0x33882d64,0x34000000,0xabc633c3,0xb30eaf8b,0x315fb1ed,
0x34000000,0x28b233ea,0xb3a82ca5,0xaed7b33b,0x34000000,0x301d32dd,0xaf8b330e,0xb3fe2248,0x34000000,0x326d30c4,0x2ca533a8,0xad643388,0x34000000,0x33c32bc6,0x330e2f8b,0x31ed315f,
0x32a7b072,0x31edb15f,0x3113b22f,0x301db2dd,0xb072b2a7,0xb33baed7,0xb3f62646,0xb26d30c4,0xb2a73072,0xa24833fe,0x322f3113,0x33c3abc6,0x307232a7,0x3388ad64,0xa646b3f6,0xb3eaa8b2,
0x32a7b072,0xb15fb1ed,0xb113322f,0x32dd301d,0xb072b2a7,0xaed7333b,0x33f6a646,0xb0c4b26d,0xb2a73072,0x33fe2248,0xb22fb113,0x2bc633c3,0x307232a7,0xad64b388,0x264633f6,0x28b2b3ea,
0x32a7b072,0xb1ed315f,0x3113b22f,0xb01d32dd,0xb072b2a7,0x333b2ed7,0xb3f62646,0x326db0c4,0xb2a73072,0x2248b3fe,0x322f3113,0xb3c32bc6,0x307232a7,0xb3882d64,0xa646b3f6,0x33ea28b2,
0x32a7b072,0x315f31ed,0xb113322f,0xb2ddb01d,0xb072b2a7,0x2ed7b33b,0x33f6a646,0x30c4326d,0xb2a73072,0xb3fea248,0xb22fb113,0xabc6b3c3,0x307232a7,0x2d643388,0x264633f6,0xa8b233ea,
0x2e1fb364,0x2bc6b3c3,0x2646b3f6,0xa248b3fe,0xae1f3364,0x28b233ea,0x3113322f,0x33882d64,0x2e1fb364,0xb01db2dd,0xb3f6a646,0xb15f31ed,0xae1f3364,0x326d30c4,0x322fb113,0xaed7b33b,
0x2e1fb364,0xb3c3abc6,0xa64633f6,0x33fea248,0xae1f3364,0x33eaa8b2,0xb113b22f,0xad643388,0x2e1fb364,0xb2dd301d,0x33f62646,0xb1edb15f,0xae1f3364,0x30c4b26d,0xb22f3113,0x333baed7,
0x2e1fb364,0xabc633c3,0x2646b3f6,0x224833fe,0xae1f3364,0xa8b2b3ea,0x3113322f,0xb388ad64,0x2e1fb364,0x301d32dd,0xb3f6a646,0x315fb1ed,0xae1f3364,0xb26db0c4,0x322fb113,0x2ed7333b,
0x2e1fb364,0x33c32bc6,0xa64633f6,0xb3fe2248,0xae1f3364,0xb3ea28b2,0xb113b22f,0x2d64b388,0x2e1fb364,0x32ddb01d,0x33f62646,0x31ed315f,0xae1f3364,0xb0c4326d,0xb22f3113,0xb33b2ed7,
0xaa3eb3d9,0xad64b388,0xaf8bb30e,0xb0c4b26d,0x33d9aa3e,0x31edb15f,0x2ca5b3a8,0xabc6b3c3,0x2a3e33d9,0x333b2ed7,0x330eaf8b,0x28b2b3ea,0xb3d92a3e,0xa24833fe,0x33a82ca5,0x301db2dd,
0xaa3eb3d9,0xb3882d64,0x2f8b330e,0x326db0c4,0x33d9aa3e,0xb15fb1ed,0xaca533a8,0x33c3abc6,0x2a3e33d9,0x2ed7b33b,0xb30e2f8b,0x33ea28b2,0xb3d92a3e,0x33fe2248,0xb3a8aca5,0x32dd301d,
0xaa3eb3d9,0x2d643388,0xaf8bb30e,0x30c4326d,0x33d9aa3e,0xb1ed315f,0x2ca5b3a8,0x2bc633c3,0x2a3e33d9,0xb33baed7,0x330eaf8b,0xa8b233ea,0xb3d92a3e,0x2248b3fe,0x33a82ca5,0xb01d32dd,
0xaa3eb3d9,0x3388ad64,0x2f8b330e,0xb26d30c4,0x33d9aa3e,0x315f31ed,0xaca533a8,0xb3c32bc6,0x2a3e33d9,0xaed7333b,0xb30e2f8b,0xb3eaa8b2,0xb3d92a3e,0xb3fea248,0xb3a8aca5,0xb2ddb01d,
0x3400,0x3400,0x3400,0x3400,0x3400,0x2e1f3364,0x31a831a8,0x33642e1f,0x3400,0x31a831a8,0x34000000,0x31a8b1a8,0x3400,0x33642e1f,0x31a8b1a8,0xae1fb364,
0x3400,0x34000000,0xb400,0xb4008000,0x3400,0x3364ae1f,0xb1a8b1a8,0xae1f3364,0x3400,0x31a8b1a8,0xb4008000,0x31a831a8,0x3400,0x2e1fb364,0xb1a831a8,0x3364ae1f,
0x3400,0xb400,0x3400,0xb400,0x3400,0xae1fb364,0x31a831a8,0xb364ae1f,0x3400,0xb1a8b1a8,0x34000000,0xb1a831a8,0x3400,0xb364ae1f,0x31a8b1a8,0x2e1f3364,
0x3400,0xb4008000,0xb400,0x34000000,0x3400,0xb3642e1f,0xb1a8b1a8,0x2e1fb364,0x3400,0xb1a831a8,0xb4008000,0xb1a8b1a8,0x3400,0xae1f3364,0xb1a831a8,0xb3642e1f,
0x3400,0x3400,0x3400,0x3400,0x34000000,0x3364ae1f,0x31a8b1a8,0x2e1fb364,0xb400,0xb1a8b1a8,0xb4008000,0xb1a831a8,0xb4008000,0xae1f3364,0x31a831a8,0x3364ae1f,
0x3400,0x34000000,0xb400,0xb4008000,0x34000000,0xae1fb364,0xb1a831a8,0x33642e1f,0xb400,0xb1a831a8,0x34000000,0xb1a8b1a8,0xb4008000,0x33642e1f,0xb1a8b1a8,0x2e1f3364,
0x3400,0xb400,0x3400,0xb400,0x34000000,0xb3642e1f,0x31a8b1a8,0xae1f3364,0xb400,0x31a831a8,0xb4008000,0x31a8b1a8,0xb4008000,0x2e1fb364,0x31a831a8,0xb3642e1f,
0x3400,0xb4008000,0xb400,0x34000000,0x34000000,0x2e1f3364,0xb1a831a8,0xb364ae1f,0xb400,0x31a8b1a8,0x34000000,0x31a831a8,0xb4008000,0xb364ae1f,0xb1a8b1a8,0xae1fb364,
0x3400,0x3400,0x3400,0x3400,0xb400,0xae1fb364,0xb1a8b1a8,0xb364ae1f,0x3400,0x31a831a8,0x34000000,0x31a8b1a8,0xb400,0xb364ae1f,0xb1a831a8,0x2e1f3364,
0x3400,0x34000000,0xb400,0xb4008000,0xb400,0xb3642e1f,0x31a831a8,0x2e1fb364,0x3400,0x31a8b1a8,0xb4008000,0x31a831a8,0xb400,0xae1f3364,0x31a8b1a8,0xb3642e1f,
0x3400,0xb400,0x3400,0xb400,0xb400,0x2e1f3364,0xb1a8b1a8,0x33642e1f,0x3400,0xb1a8b1a8,0x34000000,0xb1a831a8,0xb400,0x33642e1f,0xb1a831a8,0xae1fb364,
0x3400,0xb4008000,0xb400,0x34000000,0xb400,0x3364ae1f,0x31a831a8,0xae1f3364,0x3400,0xb1a831a8,0xb4008000,0xb1a8b1a8,0xb400,0x2e1fb364,0x31a8b1a8,0x3364ae1f,
0x3400,0x3400,0x3400,0x3400,0xb4008000,0xb3642e1f,0xb1a831a8,0xae1f3364,0xb400,0xb1a8b1a8,0xb4008000,0xb1a831a8,0x34000000,0x2e1fb364,0xb1a8b1a8,0xb3642e1f,
0x3400,0x34000000,0xb400,0xb4008000,0xb4008000,0x2e1f3364,0x31a8b1a8,0xb364ae1f,0xb400,0xb1a831a8,0x34000000,0xb1a8b1a8,0x34000000,0xb364ae1f,0x31a831a8,0xae1fb364,
0x3400,0xb400,0x3400,0xb400,0xb4008000,0x3364ae1f,0xb1a831a8,0x2e1fb364,0xb400,0x31a831a8,0xb4008000,0x31a8b1a8,0x34000000,0xae1f3364,0xb1a8b1a8,0x3364ae1f,
0x3400,0xb4008000,0xb400,0x34000000,0xb4008000,0xae1fb364,0x31a8b1a8,0x33642e1f,0xb400,0x31a8b1a8,0x34000000,0x31a831a8,0x34000000,0x33642e1f,0x31a831a8,0x2e1f3364,
0x34000000,0x34000000,0x34000000,0x34000000,0x34000000,0x3364ae1f,0x31a8b1a8,0x2e1fb364,0x34000000,0x31a8b1a8,0xb400,0xb1a8b1a8,0x34000000,0x2e1fb364,0xb1a8b1a8,0xb3642e1f,
0x34000000,0xb400,0xb4008000,0x80003400,0x34000000,0xae1fb364,0xb1a831a8,0x33642e1f,0x34000000,0xb1a8b1a8,0x80003400,0x31a8b1a8,0x34000000,0xb364ae1f,0x31a831a8,0xae1fb364,
0x34000000,0xb4008000,0x34000000,0xb4008000,0x34000000,0xb3642e1f,0x31a8b1a8,0xae1f3364,0x34000000,0xb1a831a8,0xb400,0x31a831a8,0x34000000,0xae1f3364,0xb1a8b1a8,0x3364ae1f,
0x34000000,0x80003400,0xb4008000,0xb400,0x34000000,0x2e1f3364,0xb1a831a8,0xb364ae1f,0x34000000,0x31a831a8,0x80003400,0xb1a831a8,0x34000000,0x33642e1f,0x31a831a8,0x2e1f3364,
0x34000000,0x34000000,0x34000000,0x34000000,0xb400,0xae1fb364,0xb1a8b1a8,0xb364ae1f,0xb4008000,0xb1a831a8,0x80003400,0x31a831a8,0x80003400,0x33642e1f,0x31a8b1a8,0xae1fb364,
0x34000000,0xb400,0xb4008000,0x80003400,0xb400,0xb3642e1f,0x31a831a8,0x2e1fb364,0xb4008000,0x31a831a8,0xb400,0xb1a831a8,0x80003400,0x2e1fb364,0xb1a831a8,0x3364ae1f,
0x34000000,0xb4008000,0x34000000,0xb4008000,0xb400,0x2e1f3364,0xb1a8b1a8,0x33642e1f,0xb4008000,0x31a8b1a8,0x80003400,0xb1a8b1a8,0x80003400,0xb364ae1f,0x31a8b1a8,0x2e1f3364,
0x34000000,0x80003400,0xb4008000,0xb400,0xb400,0x3364ae1f,0x31a831a8,0xae1f3364,0xb4008000,0xb1a8b1a8,0xb400,0x31a8b1a8,0x80003400,0xae1f3364,0xb1a831a8,0xb3642e1f,
0x34000000,0x34000000,0x34000000,0x34000000,0xb4008000,0xb3642e1f,0xb1a831a8,0xae1f3364,0x34000000,0x31a8b1a8,0xb400,0xb1a8b1a8,0xb4008000,0xae1f3364,0x31a831a8,0x3364ae1f,
0x34000000,0xb400,0xb4008000,0x80003400,0xb4008000,0x2e1f3364,0x31a8b1a8,0xb364ae1f,0x34000000,0xb1a8b1a8,0x80003400,0x31a8b1a8,0xb4008000,0x33642e1f,0xb1a8b1a8,0x2e1f3364,
0x34000000,0xb4008000,0x34000000,0xb4008000,0xb4008000,0x3364ae1f,0xb1a831a8,0x2e1fb364,0x34000000,0xb1a831a8,0xb400,0x31a831a8,0xb4008000,0x2e1fb364,0x31a831a8,0xb3642e1f,
0x34000000,0x80003400,0xb4008000,0xb400,0xb4008000,0xae1fb364,0x31a8b1a8,0x33642e1f,0x34000000,0x31a831a8,0x80003400,0xb1a831a8,0xb4008000,0xb364ae1f,0xb1a8b1a8,0xae1fb364,
0x34000000,0x34000000,0x34000000,0x34000000,0x80003400,0x2e1f3364,0x31a831a8,0x33642e1f,0xb4008000,0xb1a831a8,0x80003400,0x31a831a8,0xb400,0xb364ae1f,0xb1a831a8,0x2e1f3364,
0x34000000,0xb400,0xb4008000,0x80003400,0x80003400,0x3364ae1f,0xb1a8b1a8,0xae1f3364,0xb4008000,0x31a831a8,0xb400,0xb1a831a8,0xb400,0xae1f3364,0x31a8b1a8,0xb3642e1f,
0x34000000,0xb4008000,0x34000000,0xb4008000,0x80003400,0xae1fb364,0x31a831a8,0xb364ae1f,0xb4008000,0x31a8b1a8,0x80003400,0xb1a8b1a8,0xb400,0x33642e1f,0xb1a831a8,0xae1fb364,
0x34000000,0x80003400,0xb4008000,0xb400,0x80003400,0xb3642e1f,0xb1a8b1a8,0x2e1fb364,0xb4008000,0xb1a8b1a8,0xb400,0x31a8b1a8,0xb400,0x2e1fb364,0x31a8b1a8,0x3364ae1f,
0x3400,0x3400,0x3400,0x3400,0x3400,0xae1f3364,0xb1a831a8,0xb3642e1f,0x3400,0xb1a831a8,0xb4000000,0xb1a8b1a8,0x3400,0xb3642e1f,0xb1a8b1a8,0x2e1fb364,
0x3400,0xb4000000,0x8000b400,0x34008000,0x3400,0xb364ae1f,0x31a8b1a8,0x2e1f3364,0x3400,0xb1a8b1a8,0x34008000,0xb1a831a8,0x3400,0xae1fb364,0x31a831a8,0xb364ae1f,
0x3400,0x8000b400,0x3400,0x8000b400,0x3400,0x2e1fb364,0xb1a831a8,0x3364ae1f,0x3400,0x31a8b1a8,0xb4000000,0x31a831a8,0x3400,0x3364ae1f,0xb1a8b1a8,0xae1f3364,
0x3400,0x34008000,0x8000b400,0xb4000000,0x3400,0x33642e1f,0x31a8b1a8,0xae1fb364,0x3400,0x31a831a8,0x34008000,0x31a8b1a8,0x3400,0x2e1f3364,0x31a831a8,0x33642e1f,
0x3400,0x3400,0x3400,0x3400,0xb4000000,0xb364ae1f,0xb1a8b1a8,0xae1fb364,0x8000b400,0x31a8b1a8,0x34008000,0x31a831a8,0x34008000,0x2e1f3364,0xb1a831a8,0xb364ae1f,
0x3400,0xb4000000,0x8000b400,0x34008000,0xb4000000,0x2e1fb364,0x31a831a8,0xb3642e1f,0x8000b400,0x31a831a8,0xb4000000,0x31a8b1a8,0x34008000,0xb3642e1f,0x31a8b1a8,0xae1f3364,
0x3400,0x8000b400,0x3400,0x8000b400,0xb4000000,0x33642e1f,0xb1a8b1a8,0x2e1f3364,0x8000b400,0xb1a831a8,0x34008000,0xb1a8b1a8,0x34008000,0xae1fb364,0xb1a831a8,0x33642e1f,
0x3400,0x34008000,0x8000b400,0xb4000000,0xb4000000,0xae1f3364,0x31a831a8,0x3364ae1f,0x8000b400,0xb1a8b1a8,0xb4000000,0xb1a831a8,0x34008000,0x3364ae1f,0x31a8b1a8,0x2e1fb364,
0x3400,0x3400,0x3400,0x3400,0x8000b400,0x2e1fb364,0x31a8b1a8,0x3364ae1f,0x3400,0xb1a831a8,0xb4000000,0xb1a8b1a8,0x8000b400,0x3364ae1f,0x31a831a8,0xae1f3364,
0x3400,0xb4000000,0x8000b400,0x34008000,0x8000b400,0x33642e1f,0xb1a831a8,0xae1fb364,0x3400,0xb1a8b1a8,0x34008000,0xb1a831a8,0x8000b400,0x2e1f3364,0xb1a8b1a8,0x33642e1f,
0x3400,0x8000b400,0x3400,0x8000b400,0x8000b400,0xae1f3364,0x31a8b1a8,0xb3642e1f,0x3400,0x31a8b1a8,0xb4000000,0x31a831a8,0x8000b400,0xb3642e1f,0x31a831a8,0x2e1fb364,
0x3400,0x34008000,0x8000b400,0xb4000000,0x8000b400,0xb364ae1f,0xb1a831a8,0x2e1f3364,0x3400,0x31a831a8,0x34008000,0x31a8b1a8,0x8000b400,0xae1fb364,0xb1a8b1a8,0xb364ae1f,
0x3400,0x3400,0x3400,0x3400,0x34008000,0x33642e1f,0x31a831a8,0x2e1f3364,0x8000b400,0x31a8b1a8,0x34008000,0x31a831a8,0xb4000000,0xae1fb364,0x31a8b1a8,0x33642e1f,
0x3400,0xb4000000,0x8000b400,0x34008000,0x34008000,0xae1f3364,0xb1a8b1a8,0x3364ae1f,0x8000b400,0x31a831a8,0xb4000000,0x31a8b1a8,0xb4000000,0x3364ae1f,0xb1a831a8,0x2e1fb364,
0x3400,0x8000b400,0x3400,0x8000b400,0x34008000,0xb364ae1f,0x31a831a8,0xae1fb364,0x8000b400,0xb1a831a8,0x34008000,0xb1a8b1a8,0xb4000000,0x2e1f3364,0x31a8b1a8,0xb364ae1f,
0x3400,0x34008000,0x8000b400,0xb4000000,0x34008000,0x2e1fb364,0xb1a8b1a8,0xb3642e1f,0x8000b400,0xb1a8b1a8,0xb4000000,0xb1a831a8,0xb4000000,0xb3642e1f,0xb1a831a8,0xae1f3364,
0x34000000,0x34000000,0x34000000,0x34000000,0x34000000,0x33642e1f,0x31a831a8,0x2e1f3364,0x34000000,0x31a831a8,0x3400,0xb1a831a8,0x34000000,0x2e1f3364,0xb1a831a8,0xb364ae1f,
0x34000000,0x3400,0xb4000000,0x8000b400,0x34000000,0xae1f3364,0xb1a8b1a8,0x3364ae1f,0x34000000,0xb1a831a8,0x8000b400,0x31a831a8,0x34000000,0xb3642e1f,0x31a8b1a8,0xae1f3364,
0x34000000,0xb4000000,0x34000000,0xb4000000,0x34000000,0xb364ae1f,0x31a831a8,0xae1fb364,0x34000000,0xb1a8b1a8,0x3400,0x31a8b1a8,0x34000000,0xae1fb364,0xb1a831a8,0x33642e1f,
0x34000000,0x8000b400,0xb4000000,0x3400,0x34000000,0x2e1fb364,0xb1a8b1a8,0xb3642e1f,0x34000000,0x31a8b1a8,0x8000b400,0xb1a8b1a8,0x34000000,0x3364ae1f,0x31a8b1a8,0x2e1fb364,
0x34000000,0x34000000,0x34000000,0x34000000,0x3400,0xae1f3364,0xb1a831a8,0xb3642e1f,0xb4000000,0xb1a8b1a8,0x8000b400,0x31a8b1a8,0x8000b400,0x3364ae1f,0x31a831a8,0xae1f3364,
0x34000000,0x3400,0xb4000000,0x8000b400,0x3400,0xb364ae1f,0x31a8b1a8,0x2e1f3364,0xb4000000,0x31a8b1a8,0x3400,0xb1a8b1a8,0x8000b400,0x2e1f3364,0xb1a8b1a8,0x33642e1f,
0x34000000,0xb4000000,0x34000000,0xb4000000,0x3400,0x2e1fb364,0xb1a831a8,0x3364ae1f,0xb4000000,0x31a831a8,0x8000b400,0xb1a831a8,0x8000b400,0xb3642e1f,0x31a831a8,0x2e1fb364,
0x34000000,0x8000b400,0xb4000000,0x3400,0x3400,0x33642e1f,0x31a8b1a8,0xae1fb364,0xb4000000,0xb1a831a8,0x3400,0x31a831a8,0x8000b400,0xae1fb364,0xb1a8b1a8,0xb364ae1f,
0x34000000,0x34000000,0x34000000,0x34000000,0xb4000000,0xb364ae1f,0xb1a8b1a8,0xae1fb364,0x34000000,0x31a831a8,0x3400,0xb1a831a8,0xb4000000,0xae1fb364,0x31a8b1a8,0x33642e1f,
0x34000000,0x3400,0xb4000000,0x8000b400,0xb4000000,0x2e1fb364,0x31a831a8,0xb3642e1f,0x34000000,0xb1a831a8,0x8000b400,0x31a831a8,0xb4000000,0x3364ae1f,0xb1a831a8,0x2e1fb364,
0x34000000,0xb4000000,0x34000000,0xb4000000,0xb4000000,0x33642e1f,0xb1a8b1a8,0x2e1f3364,0x34000000,0xb1a8b1a8,0x3400,0x31a8b1a8,0xb4000000,0x2e1f3364,0x31a8b1a8,0xb364ae1f,
0x34000000,0x8000b400,0xb4000000,0x3400,0xb4000000,0xae1f3364,0x31a831a8,0x3364ae1f,0x34000000,0x31a8b1a8,0x8000b400,0xb1a8b1a8,0xb4000000,0xb3642e1f,0xb1a831a8,0xae1f3364,
0x34000000,0x34000000,0x34000000,0x34000000,0x8000b400,0x2e1fb364,0x31a8b1a8,0x3364ae1f,0xb4000000,0xb1a8b1a8,0x8000b400,0x31a8b1a8,0x3400,0xb3642e1f,0xb1a8b1a8,0x2e1fb364,
0x34000000,0x3400,0xb4000000,0x8000b400,0x8000b400,0x33642e1f,0xb1a831a8,0xae1fb364,0xb4000000,0x31a8b1a8,0x3400,0xb1a8b1a8,0x3400,0xae1fb364,0x31a831a8,0xb364ae1f,
0x34000000,0xb4000000,0x34000000,0xb4000000,0x8000b400,0xae1f3364,0x31a8b1a8,0xb3642e1f,0xb4000000,0x31a831a8,0x8000b400,0xb1a831a8,0x3400,0x3364ae1f,0xb1a8b1a8,0xae1f3364,
0x34000000,0x8000b400,0xb4000000,0x3400,0x8000b400,0xb364ae1f,0xb1a831a8,0x2e1f3364,0xb4000000,0xb1a831a8,0x3400,0x31a831a8,0x3400,0x2e1f3364,0x31a831a8,0x33642e1f,
0x3400,0x3400,0x3400,0x3400,0xa8b233ea,0xb01d32dd,0xb26d30c4,0xb3c32bc6,0xaca533a8,0xb30e2f8b,0xb3a8aca5,0xaf8bb30e,0xaed7333b,0xb3fea248,0xad64b388,0x31edb15f,
0xb07232a7,0xb2a7b072,0x3072b2a7,0x32a73072,0xb15f31ed,0xaed7b33b,0x33fea248,0xad643388,0xb22f3113,0x2646b3f6,0x3113322f,0xb3f6a646,0xb2dd301d,0x30c4b26d,0xabc633c3,0xa8b2b3ea,
0xb3642e1f,0x3364ae1f,0xb3642e1f,0x3364ae1f,0xb3c32bc6,0x33ea28b2,0xb2ddb01d,0x30c4326d,0xb3f62646,0x322f3113,0xa646b3f6,0xb113322f,0xb3fea248,0x2d643388,0x31edb15f,0xb33baed7,
0xb3d9aa3e,0xaa3e33d9,0x33d92a3e,0x2a3eb3d9,0xb388ad64,0xb15f31ed,0x2ed7333b,0x33fea248,0xb30eaf8b,0xb3a82ca5,0xaf8b330e,0x2ca533a8,0xb26db0c4,0xb3c3abc6,0xb3ea28b2,0xb2dd301d,
0x3400,0x3400,0x3400,0x3400,0xb3eaa8b2,0xb2ddb01d,0xb0c4b26d,0xabc6b3c3,0x2ca5b3a8,0x330eaf8b,0x33a82ca5,0x2f8b330e,0x333b2ed7,0xa24833fe,0xb3882d64,0xb15fb1ed,
0xb07232a7,0xb2a7b072,0x3072b2a7,0x32a73072,0xb1edb15f,0x333baed7,0x224833fe,0xb388ad64,0x322fb113,0xa64633f6,0xb113b22f,0x33f62646,0x301d32dd,0xb26db0c4,0x33c32bc6,0xb3ea28b2,
0xb3642e1f,0x3364ae1f,0xb3642e1f,0x3364ae1f,0xabc6b3c3,0xa8b233ea,0x301db2dd,0xb26d30c4,0x33f6a646,0xb22fb113,0x264633f6,0x3113b22f,0xa24833fe,0x3388ad64,0xb15fb1ed,0xaed7333b,
0xb3d9aa3e,0xaa3e33d9,0x33d92a3e,0x2a3eb3d9,0x2d64b388,0xb1edb15f,0xb33b2ed7,0x224833fe,0x330e2f8b,0x33a8aca5,0x2f8bb30e,0xaca5b3a8,0xb0c4326d,0xabc633c3,0x28b233ea,0x301d32dd,
0x3400,0x3400,0x3400,0x3400,0x28b2b3ea,0x301db2dd,0x326db0c4,0x33c3abc6,0xaca533a8,0xb30e2f8b,0xb3a8aca5,0xaf8bb30e,0x2ed7b33b,0x33fe2248,0x2d643388,0xb1ed315f,
0xb07232a7,0xb2a7b072,0x3072b2a7,0x32a73072,0x315fb1ed,0x2ed7333b,0xb3fe2248,0x2d64b388,0xb22f3113,0x2646b3f6,0x3113322f,0xb3f6a646,0x32ddb01d,0xb0c4326d,0x2bc6b3c3,0x28b233ea,
0xb3642e1f,0x3364ae1f,0xb3642e1f,0x3364ae1f,0x33c3abc6,0xb3eaa8b2,0x32dd301d,0xb0c4b26d,0xb3f62646,0x322f3113,0xa646b3f6,0xb113322f,0x33fe2248,0xad64b388,0xb1ed315f,0x333b2ed7,
0xb3d9aa3e,0xaa3e33d9,0x33d92a3e,0x2a3eb3d9,0x33882d64,0x315fb1ed,0xaed7b33b,0xb3fe2248,0xb30eaf8b,0xb3a82ca5,0xaf8b330e,0x2ca533a8,0x326d30c4,0x33c32bc6,0x33eaa8b2,0x32ddb01d,
0x3400,0x3400,0x3400,0x3400,0x33ea28b2,0x32dd301d,0x30c4326d,0x2bc633c3,0x2ca5b3a8,0x330eaf8b,0x33a82ca5,0x2f8b330e,0xb33baed7,0x2248b3fe,0x3388ad64,0x315f31ed,
0xb07232a7,0xb2a7b072,0x3072b2a7,0x32a73072,0x31ed315f,0xb33b2ed7,0xa248b3fe,0x33882d64,0x322fb113,0xa64633f6,0xb113b22f,0x33f62646,0xb01db2dd,0x326d30c4,0xb3c3abc6,0x33eaa8b2,
0xb3642e1f,0x3364ae1f,0xb3642e1f,0x3364ae1f,0x2bc633c3,0x28b2b3ea,0xb01d32dd,0x326db0c4,0x33f6a646,0xb22fb113,0x264633f6,0x3113b22f,0x2248b3fe,0xb3882d64,0x315f31ed,0x2ed7b33b,
0xb3d9aa3e,0xaa3e33d9,0x33d92a3e,0x2a3eb3d9,0xad643388,0x31ed315f,0x333baed7,0xa248b3fe,0x330e2f8b,0x33a8aca5,0x2f8bb30e,0xaca5b3a8,0x30c4b26d,0x2bc6b3c3,0xa8b2b3ea,0xb01db2dd,
0x34000000,0x34000000,0x34000000,0x34000000,0x33ea28b2,0x32dd301d,0x30c4326d,0x2bc633c3,0x33a82ca5,0x2f8b330e,0xaca533a8,0xb30e2f8b,0x333b2ed7,0xa24833fe,0xb3882d64,0xb15fb1ed,
0x32a73072,0xb07232a7,0xb2a7b072,0x3072b2a7,0x31ed315f,0xb33b2ed7,0xa248b3fe,0x33882d64,0x3113322f,0xb3f6a646,0x322fb113,0xa64633f6,0x301d32dd,0xb26db0c4,0x33c32bc6,0xb3ea28b2,
0x2e1f3364,0xae1fb364,0x2e1f3364,0xae1fb364,0x2bc633c3,0x28b2b3ea,0xb01d32dd,0x326db0c4,0x264633f6,0x3113b22f,0xb3f62646,0x322f3113,0xa24833fe,0x3388ad64,0xb15fb1ed,0xaed7333b,
0xaa3e33d9,0x33d92a3e,0x2a3eb3d9,0xb3d9aa3e,0xad643388,0x31ed315f,0x333baed7,0xa248b3fe,0xaf8b330e,0x2ca533a8,0x330e2f8b,0x33a8aca5,0xb0c4326d,0xabc633c3,0x28b233ea,0x301d32dd,
0x34000000,0x34000000,0x34000000,0x34000000,0xa8b233ea,0xb01d32dd,0xb26d30c4,0xb3c32bc6,0xb3a8aca5,0xaf8bb30e,0x2ca5b3a8,0x330eaf8b,0x2ed7b33b,0x33fe2248,0x2d643388,0xb1ed315f,
0x32a73072,0xb07232a7,0xb2a7b072,0x3072b2a7,0xb15f31ed,0xaed7b33b,0x33fea248,0xad643388,0xb113b22f,0x33f62646,0xb22f3113,0x2646b3f6,0x32ddb01d,0xb0c4326d,0x2bc6b3c3,0x28b233ea,
0x2e1f3364,0xae1fb364,0x2e1f3364,0xae1fb364,0xb3c32bc6,0x33ea28b2,0xb2ddb01d,0x30c4326d,0xa646b3f6,0xb113322f,0x33f6a646,0xb22fb113,0x33fe2248,0xad64b388,0xb1ed315f,0x333b2ed7,
0xaa3e33d9,0x33d92a3e,0x2a3eb3d9,0xb3d9aa3e,0xb388ad64,0xb15f31ed,0x2ed7333b,0x33fea248,0x2f8bb30e,0xaca5b3a8,0xb30eaf8b,0xb3a82ca5,0x326d30c4,0x33c32bc6,0x33eaa8b2,0x32ddb01d,
0x34000000,0x34000000,0x34000000,0x34000000,0xb3eaa8b2,0xb2ddb01d,0xb0c4b26d,0xabc6b3c3,0x33a82ca5,0x2f8b330e,0xaca533a8,0xb30e2f8b,0xb33baed7,0x2248b3fe,0x3388ad64,0x315f31ed,
0x32a73072,0xb07232a7,0xb2a7b072,0x3072b2a7,0xb1edb15f,0x333baed7,0x224833fe,0xb388ad64,0x3113322f,0xb3f6a646,0x322fb113,0xa64633f6,0xb01db2dd,0x326d30c4,0xb3c3abc6,0x33eaa8b2,
0x2e1f3364,0xae1fb364,0x2e1f3364,0xae1fb364,0xabc6b3c3,0xa8b233ea,0x301db2dd,0xb26d30c4,0x264633f6,0x3113b22f,0xb3f62646,0x322f3113,0x2248b3fe,0xb3882d64,0x315f31ed,0x2ed7b33b,
0xaa3e33d9,0x33d92a3e,0x2a3eb3d9,0xb3d9aa3e,0x2d64b388,0xb1edb15f,0xb33b2ed7,0x224833fe,0xaf8b330e,0x2ca533a8,0x330e2f8b,0x33a8aca5,0x30c4b26d,0x2bc6b3c3,0xa8b2b3ea,0xb01db2dd,
0x34000000,0x34000000,0x34000000,0x34000000,0x28b2b3ea,0x301db2dd,0x326db0c4,0x33c3abc6,0xb3a8aca5,0xaf8bb30e,0x2ca5b3a8,0x330eaf8b,0xaed7333b,0xb3fea248,0xad64b388,0x31edb15f,
0x32a73072,0xb07232a7,0xb2a7b072,0x3072b2a7,0x315fb1ed,0x2ed7333b,0xb3fe2248,0x2d64b388,0xb113b22f,0x33f62646,0xb22f3113,0x2646b3f6,0xb2dd301d,0x30c4b26d,0xabc633c3,0xa8b2b3ea,
0x2e1f3364,0xae1fb364,0x2e1f3364,0xae1fb364,0x33c3abc6,0xb3eaa8b2,0x32dd301d,0xb0c4b26d,0xa646b3f6,0xb113322f,0x33f6a646,0xb22fb113,0xb3fea248,0x2d643388,0x31edb15f,0xb33baed7,
0xaa3e33d9,0x33d92a3e,0x2a3eb3d9,0xb3d9aa3e,0x33882d64,0x315fb1ed,0xaed7b33b,0xb3fe2248,0x2f8bb30e,0xaca5b3a8,0xb30eaf8b,0xb3a82ca5,0xb26db0c4,0xb3c3abc6,0xb3ea28b2,0xb2dd301d,
0x3f800000,0x3f7ffd39,0x3f7ff4e6,0x3f7fe705,0x3f800000,0x3f7fdaaf,0x3f7f6ac7,0x3f7eb069,0x3f800000,0x3f7f90b1,0x3f7e4323,0x3f7c187a,0x3f800000,0x3f7f1f49,0x3f7c7eb0,0x3f7822d1,
0x3f800000,0x3f7e868b,0x3f7a1e84,0x3f72d4eb,0x3f800000,0x3f7dc68c,0x3f772417,0x3f6c3624,0x3f800000,0x3f7cdf6c,0x3f73913f,0x3f644fac,0x3f800000,0x3f7bd14d,0x3f6f6830,0x3f5b2c79,
0x3f800000,0x3f7a9c59,0x3f6aab7b,0x3f50d937,0x3f800000,0x3f7940c0,0x3f655e0b,0x3f456439,0x3f800000,0x3f77beb7,0x3f5f8327,0x3f38dd65,0x3f800000,0x3f76167a,0x3f591e6a,0x3f2b561b,
0x3f800000,0x3f74484b,0x3f5233c6,0x3f1ce11f,0x3f800000,0x3f725470,0x3f4ac77f,0x3f0d9281,0x3f800000,0x3f703b37,0x3f42de29,0x3efafef7,0x3f800000,0x3f6dfcf2,0x3f3a7ca4,0x3ed97cb9,
0x3f7fd397,0x3f7fba9e,0x3f7f9c18,0x3f7f7808,0x3f7dabcc,0x3f7c5d3a,0x3f7ac516,0x3f78e3d6,0x3f791298,0x3f75341d,0x3f708066,0x3f6afb8c,0x3f721352,0x3f6a5ad9,0x3f6106f2,0x3f5627fe,
0x3f68bf3c,0x3f5bfb34,0x3f4cae79,0x3f3b0629,0x3f5d2d53,0x3f4a4c87,0x3f33e7bc,0x3f1a6293,0x3f4f7a1f,0x3f3592e7,0x3f173c07,0x3eea6681,0x3f3fc767,0x3f1e1e22,0x3eee9479,0x3e9920fe,
0x3f2e3bde,0x3f044889,0x3ea986c4,0x3e06b26e,0x3f1b02c6,0x3ed0eb2a,0x3e419b37,0xbd2351cb,0x3f064b82,0x3e9620d2,0x3d2fe007,0xbe57278f,0x3ee0924f,0x3e3228d4,0xbdd53db9,0xbebf926f,
0x3eb263ef,0x3d55880e,0xbe7ee6e1,0xbf06f686,0x3e827dc0,0xbd9064b4,0xbec6d529,0xbf2a2a80,0x3e22abb6,0xbe44b0b9,0xbf04f484,0xbf485bca,0x3d7b2b74,0xbe9f1cdf,0xbf239da9,0xbf60a6cf,
0x3f7f4e6d,0x3f7f1f49,0x3f7eea9d,0x3f7eb069,0x3f76ba07,0x3f74484b,0x3f718f57,0x3f6e8ff8,0x3f64aa59,0x3f5d924d,0x3f55b993,0x3f4d26fd,0x3f49d112,0x3f3c17d9,0x3f2d1469,0x3f1ce11f,
0x3f273656,0x3f11796b,0x3ef41f07,0x3ec27b55,0x3efc5d27,0x3ebf926f,0x3e7ee6e1,0x3df475c0,0x3ea09ae5,0x3e25c58c,0x3bc90f88,0xbe195be0,0x3dfab273,0xbd6ea038,0xbe72b651,0xbed0eb2a,
0xbd96a905,0xbe8d17c0,0xbeee9479,0xbf2302d3,0xbe888e93,0xbef58035,0xbf2ac082,0xbf51c0c2,0xbee63375,0xbf28fd41,0xbf53fac3,0xbf714c7a,0xbf1d7fd1,0xbf4f0417,0xbf708066,0xbf7f5ce9,
0xbf41d870,0xbf6afb8c,0xbf7e9bc9,0xbf7aed37,0xbf5ebe05,0xbf7b87b2,0xbf7d7474,0xbf644fac,0xbf731447,0xbf7fdaaf,0xbf6d1c1d,0xbf3d27b8,0xbf7e1324,0xbf77beb7,0xbf4e8d90,0xbf084b92,
0x3f7e70b0,0x3f7e2b72,0x3f7de0b1,0x3f7d906e,0x3f6b4b0c,0x3f67c187,0x3f63f473,0x3f5fe4e9,0x3f43e200,0x3f39f2ac,0x3f2f61a5,0x3f24381a,0x3f0b9a6b,0x3ef2bd43,0x3ecc9b8b,0x3ea5129f,
0x3e8e9a22,0x3e3228d4,0x3d8a200a,0xbd2351cb,0xbc490e90,0xbe1324ca,0xbe8b9507,0xbecb2aae,0xbe9aa086,0xbee4cbfe,0xbf14b039,0xbf33587a,0xbf10d3cd,0xbf34768f,0xbf5233c6,0xbf6912ae,
0xbf47de65,0xbf644fac,0xbf772417,0xbf7f90b1,0xbf6db293,0xbf7d1cdd,0xbf7f2f9d,0xbf73ced9,0xbf7f0e58,0xbf7c5d3a,0xbf696591,0xbf476085,0xbf7a7302,0xbf62241a,0xbf385216,0xbf003a06,
0xbf604621,0xbf311722,0xbee363fa,0xbe1f917b,0xbf32c8c9,0xbedc53c1,0xbdee3876,0x3e57278f,0xbeebcbbb,0xbe007745,0x3e667c66,0x3f0c42c9,0xbe3b6ecf,0x3e44b0b9,0x3f0a48ad,0x3f4fefa8,
0x0,0x3c16cb58,0x3c96c9b6,0x3ce22a7a,0x0,0x3d0a342f,0x3d8a200a,0x3dcefdb7,0x0,0x3d6ea038,0x3dee3876,0x3e3228d4,0x0,0x3da973ba,0x3e28defc,0x3e7bdba4,
0x0,0x3ddb7d37,0x3e5a3997,0x3ea21887,0x0,0x3e06b26e,0x3e8586ce,0x3ec5625c,0x0,0x3e1f917b,0x3e9d9e78,0x3ee79a5d,0x0,0x3e3857ec,0x3eb554ec,0x3f044889,
0x0,0x3e5101f1,0x3ecc9b8b,0x3f140c5f,0x0,0x3e698bba,0x3ee363fa,0x3f2302d3,0x0,0x3e80f8c0,0x3ef9a02d,0x3f311722,0x0,0x3e8d17c0,0x3f07a136,0x3f3e35c5,
0x0,0x3e9920fe,0x3f121eb0,0x3f4a4c87,0x0,0x3ea5129f,0x3f1c420c,0x3f554aa4,0x0,0x3eb0eacb,0x3f26050a,0x3f5f20db,0x0,0x3ebca7af,0x3f2f61a5,0x3f67c187,
0x3d16c32c,0x3d3c6dd5,0x3d621469,0x3d83db0a,0x3e09cf86,0x3e2bf804,0x3e4dee60,0x3e6fa8b2,0x3e6c9a7f,0x3e931f35,0x3eaf713a,0x3ecb2aae,0x3ea68f12,0x3ece0bea,0x3ef41f07,0x3f0c42c9,
0x3ed53641,0x3f02ef9f,0x3f19c200,0x3f2ecef7,0x3f00e7e4,0x3f1ce11f,0x3f36206c,0x3f4c3578,0x3f15f6d9,0x3f34768f,0x3f4e8d90,0x3f6398ac,0x3f299415,0x3f495521,0x3f628210,0x3f74484b,
0x3f3b8f3b,0x3f5b2c79,0x3f718f57,0x3f7dc68c,0x3f4bbbf8,0x3f69b7e4,0x3f7b61fc,0x3f7fcbe2,0x3f59f26a,0x3f74bf62,0x3f7fc38f,0x3f7a4910,0x3f660f88,0x3f7c187a,0x3f7e9bc9,0x3f6d67a1,
0x3f6ff573,0x3f7fa6e3,0x3f77f110,0x3f5988ad,0x3f778bc5,0x3f7f5ce9,0x3f6be858,0x3f3f41fc,0x3f7cbfc9,0x3f7b3bab,0x3f5ac450,0x3f1f599f,0x3f7f84ab,0x3f73530e,0x3f44e3f5,0x3ef58035,
0x3d96a905,0x3da973ba,0x3dbc3ac3,0x3dcefdb7,0x3e888e93,0x3e9920fe,0x3ea986c4,0x3eb9bb1e,0x3ee63375,0x3f003a06,0x3f0cead0,0x3f19210f,0x3f1d7fd1,0x3f2da859,0x3f3ca003,0x3f4a4c87,
0x3f41d870,0x3f52a649,0x3f6106f2,0x3f6cd007,0x3f5ebe05,0x3f6d67a1,0x3f77f110,0x3f7e2b72,0x3f731447,0x3f7c9f8a,0x3f7ffec4,0x3f7d1cdd,0x3f7e1324,0x3f7f90b1,0x3f78b47b,0x3f69b7e4,
0x3f7f4e6d,0x3f76167a,0x3f628210,0x3f456439,0x3f76ba07,0x3f60a6cf,0x3f3ebc1b,0x3f12c39a,0x3f64aa59,0x3f404c5c,0x3f0f8784,0x3eab0201,0x3f49d112,0x3f16999f,0x3eaf713a,0x3d9064b4,
0x3f273656,0x3ecb2aae,0x3dd53db9,0xbe4ada4f,0x3efc5d27,0x3e3e853e,0xbe1008b7,0xbee79a5d,0x3ea09ae5,0xbd0a342f,0xbec1071e,0xbf2c800f,0x3dfab273,0xbe80f8c0,0xbf173c07,0xbf58b3a1,
0x3de1bc2e,0x3df475c0,0x3e039502,0x3e0cec4a,0x3ec9b953,0x3ed97cb9,0x3ee900b7,0x3ef840c8,0x3f24d225,0x3f2ff3e6,0x3f3a7ca4,0x3f446337,0x3f5695e5,0x3f61668a,0x3f6aab7b,0x3f725470,
0x3f75dec6,0x3f7c187a,0x3f7f6ac7,0x3f7fcbe2,0x3f7ffb11,0x3f7d57de,0x3f764d97,0x3f6afb8c,0x3f740bdd,0x3f650479,0x3f5064af,0x3f36ad7f,0x3f531849,0x3f3592e7,0x3f121eb0,0x3ed3c867,
0x3f1ff6cb,0x3ee79a5d,0x3e8586ce,0x3d6ea038,0x3ebe1d4a,0x3e195be0,0xbda3308c,0xbe9c1faf,0x3dafb680,0xbe2bf804,0xbed25a09,0xbf209393,0xbe541501,0xbeeff7fb,0xbf31a81d,0xbf5d924d,
0xbef6e0cb,0xbf38dd65,0xbf655e0b,0xbf7cdf6c,0xbf373a23,0xbf671599,0xbf7e4323,0xbf7a4910,0xbf633c5a,0xbf7dfa38,0xbf796e4e,0xbf5627fe,0xbf7baccd,0xbf7b3bab,0xbf577026,0xbf1553b7,
0x3f800000,0x3f800000,0x3f800000,0x3f800000,0x3f7ffd39,0x3f7fdaaf,0x3f7f90b1,0x3f7f1f49,0x3f7ff4e6,0x3f7f6ac7,0x3f7e4323,0x3f7c7eb0,0x3f7fe705,0x3f7eb069,0x3f7c187a,0x3f7822d1,
0x3f7fd397,0x3f7dabcc,0x3f791298,0x3f721352,0x3f7fba9e,0x3f7c5d3a,0x3f75341d,0x3f6a5ad9,0x3f7f9c18,0x3f7ac516,0x3f708066,0x3f6106f2,0x3f7f7808,0x3f78e3d6,0x3f6afb8c,0x3f5627fe,
0x3f7f4e6d,0x3f76ba07,0x3f64aa59,0x3f49d112,0x3f7f1f49,0x3f74484b,0x3f5d924d,0x3f3c17d9,0x3f7eea9d,0x3f718f57,0x3f55b993,0x3f2d1469,0x3f7eb069,0x3f6e8ff8,0x3f4d26fd,0x3f1ce11f,
0x3f7e70b0,0x3f6b4b0c,0x3f43e200,0x3f0b9a6b,0x3f7e2b72,0x3f67c187,0x3f39f2ac,0x3ef2bd43,0x3f7de0b1,0x3f63f473,0x3f2f61a5,0x3ecc9b8b,0x3f7d906e,0x3f5fe4e9,0x3f24381a,0x3ea5129f,
0x3f800000,0x3f800000,0x3f800000,0x3f800000,0x3f7e868b,0x3f7dc68c,0x3f7cdf6c,0x3f7bd14d,0x3f7a1e84,0x3f772417,0x3f73913f,0x3f6f6830,0x3f72d4eb,0x3f6c3624,0x3f644fac,0x3f5b2c79,
0x3f68bf3c,0x3f5d2d53,0x3f4f7a1f,0x3f3fc767,0x3f5bfb34,0x3f4a4c87,0x3f3592e7,0x3f1e1e22,0x3f4cae79,0x3f33e7bc,0x3f173c07,0x3eee9479,0x3f3b0629,0x3f1a6293,0x3eea6681,0x3e9920fe,
0x3f273656,0x3efc5d27,0x3ea09ae5,0x3dfab273,0x3f11796b,0x3ebf926f,0x3e25c58c,0xbd6ea038,0x3ef41f07,0x3e7ee6e1,0x3bc90f88,0xbe72b651,0x3ec27b55,0x3df475c0,0xbe195be0,0xbed0eb2a,
0x3e8e9a22,0xbc490e90,0xbe9aa086,0xbf10d3cd,0x3e3228d4,0xbe1324ca,0xbee4cbfe,0xbf34768f,0x3d8a200a,0xbe8b9507,0xbf14b039,0xbf5233c6,0xbd2351cb,0xbecb2aae,0xbf33587a,0xbf6912ae,
0x3f800000,0x3f800000,0x3f800000,0x3f800000,0x3f7a9c59,0x3f7940c0,0x3f77beb7,0x3f76167a,0x3f6aab7b,0x3f655e0b,0x3f5f8327,0x3f591e6a,0x3f50d937,0x3f456439,0x3f38dd65,0x3f2b561b,
0x3f2e3bde,0x3f1b02c6,0x3f064b82,0x3ee0924f,0x3f044889,0x3ed0eb2a,0x3e9620d2,0x3e3228d4,0x3ea986c4,0x3e419b37,0x3d2fe007,0xbdd53db9,0x3e06b26e,0xbd2351cb,0xbe57278f,0xbebf926f,
0xbd96a905,0xbe888e93,0xbee63375,0xbf1d7fd1,0xbe8d17c0,0xbef58035,0xbf28fd41,0xbf4f0417,0xbeee9479,0xbf2ac082,0xbf53fac3,0xbf708066,0xbf2302d3,0xbf51c0c2,0xbf714c7a,0xbf7f5ce9,
0xbf47de65,0xbf6db293,0xbf7f0e58,0xbf7a7302,0xbf644fac,0xbf7d1cdd,0xbf7c5d3a,0xbf62241a,0xbf772417,0xbf7f2f9d,0xbf696591,0xbf385216,0xbf7f90b1,0xbf73ced9,0xbf476085,0xbf003a06,
0x3f800000,0x3f800000,0x3f800000,0x3f800000,0x3f74484b,0x3f725470,0x3f703b37,0x3f6dfcf2,0x3f5233c6,0x3f4ac77f,0x3f42de29,0x3f3a7ca4,0x3f1ce11f,0x3f0d9281,0x3efafef7,0x3ed97cb9,
0x3eb263ef,0x3e827dc0,0x3e22abb6,0x3d7b2b74,0x3d55880e,0xbd9064b4,0xbe44b0b9,0xbe9f1cdf,0xbe7ee6e1,0xbec6d529,0xbf04f484,0xbf239da9,0xbf06f686,0xbf2a2a80,0xbf485bca,0xbf60a6cf,
0xbf41d870,0xbf5ebe05,0xbf731447,0xbf7e1324,0xbf6afb8c,0xbf7b87b2,0xbf7fdaaf,0xbf77beb7,0xbf7e9bc9,0xbf7d7474,0xbf6d1c1d,0xbf4e8d90,0xbf7aed37,0xbf644fac,0xbf3d27b8,0xbf084b92,
0xbf604621,0xbf32c8c9,0xbeebcbbb,0xbe3b6ecf,0xbf311722,0xbedc53c1,0xbe007745,0x3e44b0b9,0xbee363fa,0xbdee3876,0x3e667c66,0x3f0a48ad,0xbe1f917b,0x3e57278f,0x3f0c42c9,0x3f4fefa8,
0x0,0x0,0x0,0x0,0x3c16cb58,0x3d0a342f,0x3d6ea038,0x3da973ba,0x3c96c9b6,0x3d8a200a,0x3dee3876,0x3e28defc,0x3ce22a7a,0x3dcefdb7,0x3e3228d4,0x3e7bdba4,
0x3d16c32c,0x3e09cf86,0x3e6c9a7f,0x3ea68f12,0x3d3c6dd5,0x3e2bf804,0x3e931f35,0x3ece0bea,0x3d621469,0x3e4dee60,0x3eaf713a,0x3ef41f07,0x3d83db0a,0x3e6fa8b2,0x3ecb2aae,0x3f0c42c9,
0x3d96a905,0x3e888e93,0x3ee63375,0x3f1d7fd1,0x3da973ba,0x3e9920fe,0x3f003a06,0x3f2da859,0x3dbc3ac3,0x3ea986c4,0x3f0cead0,0x3f3ca003,0x3dcefdb7,0x3eb9bb1e,0x3f19210f,0x3f4a4c87,
0x3de1bc2e,0x3ec9b953,0x3f24d225,0x3f5695e5,0x3df475c0,0x3ed97cb9,0x3f2ff3e6,0x3f61668a,0x3e039502,0x3ee900b7,0x3f3a7ca4,0x3f6aab7b,0x3e0cec4a,0x3ef840c8,0x3f446337,0x3f725470,
0x0,0x0,0x0,0x0,0x3ddb7d37,0x3e06b26e,0x3e1f917b,0x3e3857ec,0x3e5a3997,0x3e8586ce,0x3e9d9e78,0x3eb554ec,0x3ea21887,0x3ec5625c,0x3ee79a5d,0x3f044889,
0x3ed53641,0x3f00e7e4,0x3f15f6d9,0x3f299415,0x3f02ef9f,0x3f1ce11f,0x3f34768f,0x3f495521,0x3f19c200,0x3f36206c,0x3f4e8d90,0x3f628210,0x3f2ecef7,0x3f4c3578,0x3f6398ac,0x3f74484b,
0x3f41d870,0x3f5ebe05,0x3f731447,0x3f7e1324,0x3f52a649,0x3f6d67a1,0x3f7c9f8a,0x3f7f90b1,0x3f6106f2,0x3f77f110,0x3f7ffec4,0x3f78b47b,0x3f6cd007,0x3f7e2b72,0x3f7d1cdd,0x3f69b7e4,
0x3f75dec6,0x3f7ffb11,0x3f740bdd,0x3f531849,0x3f7c187a,0x3f7d57de,0x3f650479,0x3f3592e7,0x3f7f6ac7,0x3f764d97,0x3f5064af,0x3f121eb0,0x3f7fcbe2,0x3f6afb8c,0x3f36ad7f,0x3ed3c867,
0x0,0x0,0x0,0x0,0x3e5101f1,0x3e698bba,0x3e80f8c0,0x3e8d17c0,0x3ecc9b8b,0x3ee363fa,0x3ef9a02d,0x3f07a136,0x3f140c5f,0x3f2302d3,0x3f311722,0x3f3e35c5,
0x3f3b8f3b,0x3f4bbbf8,0x3f59f26a,0x3f660f88,0x3f5b2c79,0x3f69b7e4,0x3f74bf62,0x3f7c187a,0x3f718f57,0x3f7b61fc,0x3f7fc38f,0x3f7e9bc9,0x3f7dc68c,0x3f7fcbe2,0x3f7a4910,0x3f6d67a1,
0x3f7f4e6d,0x3f76ba07,0x3f64aa59,0x3f49d112,0x3f76167a,0x3f60a6cf,0x3f404c5c,0x3f16999f,0x3f628210,0x3f3ebc1b,0x3f0f8784,0x3eaf713a,0x3f456439,0x3f12c39a,0x3eab0201,0x3d9064b4,
0x3f1ff6cb,0x3ebe1d4a,0x3dafb680,0xbe541501,0x3ee79a5d,0x3e195be0,0xbe2bf804,0xbeeff7fb,0x3e8586ce,0xbda3308c,0xbed25a09,0xbf31a81d,0x3d6ea038,0xbe9c1faf,0xbf209393,0xbf5d924d,
0x0,0x0,0x0,0x0,0x3e9920fe,0x3ea5129f,0x3eb0eacb,0x3ebca7af,0x3f121eb0,0x3f1c420c,0x3f26050a,0x3f2f61a5,0x3f4a4c87,0x3f554aa4,0x3f5f20db,0x3f67c187,
0x3f6ff573,0x3f778bc5,0x3f7cbfc9,0x3f7f84ab,0x3f7fa6e3,0x3f7f5ce9,0x3f7b3bab,0x3f73530e,0x3f77f110,0x3f6be858,0x3f5ac450,0x3f44e3f5,0x3f5988ad,0x3f3f41fc,0x3f1f599f,0x3ef58035,
0x3f273656,0x3efc5d27,0x3ea09ae5,0x3dfab273,0x3ecb2aae,0x3e3e853e,0xbd0a342f,0xbe80f8c0,0x3dd53db9,0xbe1008b7,0xbec1071e,0xbf173c07,0xbe4ada4f,0xbee79a5d,0xbf2c800f,0xbf58b3a1,
0xbef6e0cb,0xbf373a23,0xbf633c5a,0xbf7baccd,0xbf38dd65,0xbf671599,0xbf7dfa38,0xbf7b3bab,0xbf655e0b,0xbf7e4323,0xbf796e4e,0xbf577026,0xbf7cdf6c,0xbf7a4910,0xbf5627fe,0xbf1553b7,
0x3400,0x2a3e33d9,0x2e1f3364,0x307232a7,0x3400,0x307232a7,0x33642e1f,0x33d9aa3e,0x3400,0x32a73072,0x3364ae1f,0x2a3eb3d9,0x3400,0x33d92a3e,0x2e1fb364,0xb2a7b072,
0x3400,0x33d9aa3e,0xae1fb364,0xb2a73072,0x3400,0x32a7b072,0xb364ae1f,0x2a3e33d9,0x3400,0x3072b2a7,0xb3642e1f,0x33d92a3e,0x3400,0x2a3eb3d9,0xae1f3364,0x3072b2a7,
0x3400,0xaa3eb3d9,0x2e1f3364,0xb072b2a7,0x3400,0xb072b2a7,0x33642e1f,0xb3d92a3e,0x3400,0xb2a7b072,0x3364ae1f,0xaa3e33d9,0x3400,0xb3d9aa3e,0x2e1fb364,0x32a73072,
0x3400,0xb3d92a3e,0xae1fb364,0x32a7b072,0x3400,0xb2a73072,0xb364ae1f,0xaa3eb3d9,0x3400,0xb07232a7,0xb3642e1f,0xb3d9aa3e,0x3400,0xaa3e33d9,0xae1f3364,0xb07232a7,
0x31a831a8,0x32a73072,0x33642e1f,0x33d92a3e,0x31a8b1a8,0x2a3eb3d9,0xae1fb364,0xb2a7b072,0xb1a8b1a8,0xb3d92a3e,0xae1f3364,0x307232a7,0xb1a831a8,0x307232a7,0x3364ae1f,0xaa3eb3d9,
0x31a831a8,0x3072b2a7,0xb364ae1f,0xaa3e33d9,0x31a8b1a8,0xb3d9aa3e,0x2e1f3364,0x3072b2a7,0xb1a8b1a8,0x2a3e33d9,0x2e1fb364,0xb2a73072,0xb1a831a8,0x32a7b072,0xb3642e1f,0x33d9aa3e,
0x31a831a8,0xb2a7b072,0x33642e1f,0xb3d9aa3e,0x31a8b1a8,0xaa3e33d9,0xae1fb364,0x32a73072,0xb1a8b1a8,0x33d9aa3e,0xae1f3364,0xb072b2a7,0xb1a831a8,0xb072b2a7,0x3364ae1f,0x2a3e33d9,
0x31a831a8,0xb07232a7,0xb364ae1f,0x2a3eb3d9,0x31a8b1a8,0x33d92a3e,0x2e1f3364,0xb07232a7,0xb1a8b1a8,0xaa3eb3d9,0x2e1fb364,0x32a7b072,0xb1a831a8,0xb2a73072,0xb3642e1f,0xb3d92a3e,
0x34000000,0x33d9aa3e,0x3364ae1f,0x32a7b072,0xb4008000,0xb2a73072,0xae1f3364,0x2a3e33d9,0x34000000,0x3072b2a7,0xae1fb364,0xb3d9aa3e,0xb4008000,0xaa3e33d9,0x33642e1f,0x3072b2a7,
0x34000000,0xaa3eb3d9,0xb3642e1f,0x307232a7,0xb4008000,0x307232a7,0x2e1fb364,0xb3d92a3e,0x34000000,0xb2a7b072,0x2e1f3364,0x2a3eb3d9,0xb4008000,0x33d92a3e,0xb364ae1f,0x32a73072,
0x34000000,0xb3d92a3e,0x3364ae1f,0xb2a73072,0xb4008000,0x32a7b072,0xae1f3364,0xaa3eb3d9,0x34000000,0xb07232a7,0xae1fb364,0x33d92a3e,0xb4008000,0x2a3eb3d9,0x33642e1f,0xb07232a7,
0x34000000,0x2a3e33d9,0xb3642e1f,0xb072b2a7,0xb4008000,0xb072b2a7,0x2e1fb364,0x33d9aa3e,0x34000000,0x32a73072,0x2e1f3364,0xaa3e33d9,0xb4008000,0xb3d9aa3e,0xb364ae1f,0xb2a7b072,
0x31a8b1a8,0x3072b2a7,0x2e1fb364,0x2a3eb3d9,0x31a831a8,0x33d92a3e,0x3364ae1f,0x3072b2a7,0xb1a831a8,0x2a3e33d9,0x33642e1f,0x32a7b072,0xb1a8b1a8,0xb2a73072,0x2e1f3364,0x33d9aa3e,
0x31a8b1a8,0xb2a7b072,0xae1f3364,0x33d92a3e,0x31a831a8,0x2a3eb3d9,0xb3642e1f,0x32a73072,0xb1a831a8,0x33d9aa3e,0xb364ae1f,0x307232a7,0xb1a8b1a8,0x307232a7,0xae1fb364,0x2a3e33d9,
0x31a8b1a8,0xb07232a7,0x2e1fb364,0xaa3e33d9,0x31a831a8,0xb3d9aa3e,0x3364ae1f,0xb07232a7,0xb1a831a8,0xaa3eb3d9,0x33642e1f,0xb2a73072,0xb1a8b1a8,0x32a7b072,0x2e1f3364,0xb3d92a3e,
0x31a8b1a8,0x32a73072,0xae1f3364,0xb3d9aa3e,0x31a831a8,0xaa3e33d9,0xb3642e1f,0xb2a7b072,0xb1a831a8,0xb3d92a3e,0xb364ae1f,0xb072b2a7,0xb1a8b1a8,0xb072b2a7,0xae1fb364,0xaa3eb3d9,
0x34000000,0x33d9aa3e,0x3364ae1f,0x32a7b072,0x34000000,0x32a7b072,0x2e1fb364,0xaa3eb3d9,0x34000000,0x3072b2a7,0xae1fb364,0xb3d9aa3e,0x34000000,0x2a3eb3d9,0xb364ae1f,0xb07232a7,
0x34000000,0xaa3eb3d9,0xb3642e1f,0x307232a7,0x34000000,0xb072b2a7,0xae1f3364,0x33d9aa3e,0x34000000,0xb2a7b072,0x2e1f3364,0x2a3eb3d9,0x34000000,0xb3d9aa3e,0x33642e1f,0xb2a7b072,
0x34000000,0xb3d92a3e,0x3364ae1f,0xb2a73072,0x34000000,0xb2a73072,0x2e1fb364,0x2a3e33d9,0x34000000,0xb07232a7,0xae1fb364,0x33d92a3e,0x34000000,0xaa3e33d9,0xb364ae1f,0x3072b2a7,
0x34000000,0x2a3e33d9,0xb3642e1f,0xb072b2a7,0x34000000,0x307232a7,0xae1f3364,0xb3d92a3e,0x34000000,0x32a73072,0x2e1f3364,0xaa3e33d9,0x34000000,0x33d92a3e,0x33642e1f,0x32a73072,
0x31a8b1a8,0x3072b2a7,0x2e1fb364,0x2a3eb3d9,0xb1a8b1a8,0xb3d9aa3e,0xb3642e1f,0xb07232a7,0xb1a831a8,0x2a3e33d9,0x33642e1f,0x32a7b072,0x31a831a8,0x32a7b072,0xae1fb364,0xb3d92a3e,
0x31a8b1a8,0xb2a7b072,0xae1f3364,0x33d92a3e,0xb1a8b1a8,0xaa3e33d9,0x3364ae1f,0xb2a7b072,0xb1a831a8,0x33d9aa3e,0xb364ae1f,0x307232a7,0x31a831a8,0xb072b2a7,0x2e1f3364,0xaa3eb3d9,
0x31a8b1a8,0xb07232a7,0x2e1fb364,0xaa3e33d9,0xb1a8b1a8,0x33d92a3e,0xb3642e1f,0x3072b2a7,0xb1a831a8,0xaa3eb3d9,0x33642e1f,0xb2a73072,0x31a831a8,0xb2a73072,0xae1fb364,0x33d9aa3e,
0x31a8b1a8,0x32a73072,0xae1f3364,0xb3d9aa3e,0xb1a8b1a8,0x2a3eb3d9,0x3364ae1f,0x32a73072,0xb1a831a8,0xb3d92a3e,0xb364ae1f,0xb072b2a7,0x31a831a8,0x307232a7,0x2e1f3364,0x2a3e33d9,
0xb400,0xaa3eb3d9,0xae1fb364,0xb072b2a7,0x80003400,0x307232a7,0x33642e1f,0x33d9aa3e,0xb400,0xb2a7b072,0xb3642e1f,0xaa3e33d9,0x80003400,0x33d92a3e,0x2e1fb364,0xb2a7b072,
0xb400,0xb3d92a3e,0x2e1f3364,0x32a7b072,0x80003400,0x32a7b072,0xb364ae1f,0x2a3e33d9,0xb400,0xb07232a7,0x3364ae1f,0xb3d9aa3e,0x80003400,0x2a3eb3d9,0xae1f3364,0x3072b2a7,
0xb400,0x2a3e33d9,0xae1fb364,0x307232a7,0x80003400,0xb072b2a7,0x33642e1f,0xb3d92a3e,0xb400,0x32a73072,0xb3642e1f,0x2a3eb3d9,0x80003400,0xb3d9aa3e,0x2e1fb364,0x32a73072,
0xb400,0x33d9aa3e,0x2e1f3364,0xb2a73072,0x80003400,0xb2a73072,0xb364ae1f,0xaa3eb3d9,0xb400,0x3072b2a7,0x3364ae1f,0x33d92a3e,0x80003400,0xaa3e33d9,0xae1f3364,0xb07232a7,
0xb1a8b1a8,0xb2a7b072,0xb364ae1f,0xb3d9aa3e,0x31a8b1a8,0x2a3eb3d9,0xae1fb364,0xb2a7b072,0x31a831a8,0x33d9aa3e,0x2e1fb364,0xb072b2a7,0xb1a831a8,0x307232a7,0x3364ae1f,0xaa3eb3d9,
0xb1a8b1a8,0xb07232a7,0x33642e1f,0x2a3eb3d9,0x31a8b1a8,0xb3d9aa3e,0x2e1f3364,0x3072b2a7,0x31a831a8,0xaa3eb3d9,0xae1f3364,0x32a7b072,0xb1a831a8,0x32a7b072,0xb3642e1f,0x33d9aa3e,
0xb1a8b1a8,0x32a73072,0xb364ae1f,0x33d92a3e,0x31a8b1a8,0xaa3e33d9,0xae1fb364,0x32a73072,0x31a831a8,0xb3d92a3e,0x2e1fb364,0x307232a7,0xb1a831a8,0xb072b2a7,0x3364ae1f,0x2a3e33d9,
0xb1a8b1a8,0x3072b2a7,0x33642e1f,0xaa3e33d9,0x31a8b1a8,0x33d92a3e,0x2e1f3364,0xb07232a7,0x31a831a8,0x2a3e33d9,0xae1f3364,0xb2a73072,0xb1a831a8,0xb2a73072,0xb3642e1f,0xb3d92a3e,
0x3400,0x3400,0x3400,0x3400,0x3400,0x2e1f3364,0x31a831a8,0x33642e1f,0x3400,0x31a831a8,0x34000000,0x31a8b1a8,0x3400,0x33642e1f,0x31a8b1a8,0xae1fb364,
0x3400,0x34000000,0xb400,0xb4008000,0x3400,0x3364ae1f,0xb1a8b1a8,0xae1f3364,0x3400,0x31a8b1a8,0xb4008000,0x31a831a8,0x3400,0x2e1fb364,0xb1a831a8,0x3364ae1f,
0x3400,0xb400,0x3400,0xb400,0x3400,0xae1fb364,0x31a831a8,0xb364ae1f,0x3400,0xb1a8b1a8,0x34000000,0xb1a831a8,0x3400,0xb364ae1f,0x31a8b1a8,0x2e1f3364,
0x3400,0xb4008000,0xb400,0x34000000,0x3400,0xb3642e1f,0xb1a8b1a8,0x2e1fb364,0x3400,0xb1a831a8,0xb4008000,0xb1a8b1a8,0x3400,0xae1f3364,0xb1a831a8,0xb3642e1f,
0x3400,0x3400,0x3400,0x3400,0x34000000,0x3364ae1f,0x31a8b1a8,0x2e1fb364,0xb400,0xb1a8b1a8,0xb4008000,0xb1a831a8,0xb4008000,0xae1f3364,0x31a831a8,0x3364ae1f,
0x3400,0x34000000,0xb400,0xb4008000,0x34000000,0xae1fb364,0xb1a831a8,0x33642e1f,0xb400,0xb1a831a8,0x34000000,0xb1a8b1a8,0xb4008000,0x33642e1f,0xb1a8b1a8,0x2e1f3364,
0x3400,0xb400,0x3400,0xb400,0x34000000,0xb3642e1f,0x31a8b1a8,0xae1f3364,0xb400,0x31a831a8,0xb4008000,0x31a8b1a8,0xb4008000,0x2e1fb364,0x31a831a8,0xb3642e1f,
0x3400,0xb4008000,0xb400,0x34000000,0x34000000,0x2e1f3364,0xb1a831a8,0xb364ae1f,0xb400,0x31a8b1a8,0x34000000,0x31a831a8,0xb4008000,0xb364ae1f,0xb1a8b1a8,0xae1fb364,
0x3400,0x3400,0x3400,0x3400,0xb400,0xae1fb364,0xb1a8b1a8,0xb364ae1f,0x3400,0x31a831a8,0x34000000,0x31a8b1a8,0xb400,0xb364ae1f,0xb1a831a8,0x2e1f3364,
0x3400,0x34000000,0xb400,0xb4008000,0xb400,0xb3642e1f,0x31a831a8,0x2e1fb364,0x3400,0x31a8b1a8,0xb4008000,0x31a831a8,0xb400,0xae1f3364,0x31a8b1a8,0xb3642e1f,
0x3400,0xb400,0x3400,0xb400,0xb400,0x2e1f3364,0xb1a8b1a8,0x33642e1f,0x3400,0xb1a8b1a8,0x34000000,0xb1a831a8,0xb400,0x33642e1f,0xb1a831a8,0xae1fb364,
0x3400,0xb4008000,0xb400,0x34000000,0xb400,0x3364ae1f,0x31a831a8,0xae1f3364,0x3400,0xb1a831a8,0xb4008000,0xb1a8b1a8,0xb400,0x2e1fb364,0x31a8b1a8,0x3364ae1f,
0x3400,0x3400,0x3400,0x3400,0xb4008000,0xb3642e1f,0xb1a831a8,0xae1f3364,0xb400,0xb1a8b1a8,0xb4008000,0xb1a831a8,0x34000000,0x2e1fb364,0xb1a8b1a8,0xb3642e1f,
0x3400,0x34000000,0xb400,0xb4008000,0xb4008000,0x2e1f3364,0x31a8b1a8,0xb364ae1f,0xb400,0xb1a831a8,0x34000000,0xb1a8b1a8,0x34000000,0xb364ae1f,0x31a831a8,0xae1fb364,
0x3400,0xb400,0x3400,0xb400,0xb4008000,0x3364ae1f,0xb1a831a8,0x2e1fb364,0xb400,0x31a831a8,0xb4008000,0x31a8b1a8,0x34000000,0xae1f3364,0xb1a8b1a8,0x3364ae1f,
0x3400,0xb4008000,0xb400,0x34000000,0xb4008000,0xae1fb364,0x31a8b1a8,0x33642e1f,0xb400,0x31a8b1a8,0x34000000,0x31a831a8,0x34000000,0x33642e1f,0x31a831a8,0x2e1f3364,
0x34000000,0x34000000,0x34000000,0x34000000,0x34000000,0x3364ae1f,0x31a8b1a8,0x2e1fb364,0x34000000,0x31a8b1a8,0xb400,0xb1a8b1a8,0x34000000,0x2e1fb364,0xb1a8b1a8,0xb3642e1f,
0x34000000,0xb400,0xb4008000,0x80003400,0x34000000,0xae1fb364,0xb1a831a8,0x33642e1f,0x34000000,0xb1a8b1a8,0x80003400,0x31a8b1a8,0x34000000,0xb364ae1f,0x31a831a8,0xae1fb364,
0x34000000,0xb4008000,0x34000000,0xb4008000,0x34000000,0xb3642e1f,0x31a8b1a8,0xae1f3364,0x34000000,0xb1a831a8,0xb400,0x31a831a8,0x34000000,0xae1f3364,0xb1a8b1a8,0x3364ae1f,
0x34000000,0x80003400,0xb4008000,0xb400,0x34000000,0x2e1f3364,0xb1a831a8,0xb364ae1f,0x34000000,0x31a831a8,0x80003400,0xb1a831a8,0x34000000,0x33642e1f,0x31a831a8,0x2e1f3364,
0x34000000,0x34000000,0x34000000,0x34000000,0xb400,0xae1fb364,0xb1a8b1a8,0xb364ae1f,0xb4008000,0xb1a831a8,0x80003400,0x31a831a8,0x80003400,0x33642e1f,0x31a8b1a8,0xae1fb364,
0x34000000,0xb400,0xb4008000,0x80003400,0xb400,0xb3642e1f,0x31a831a8,0x2e1fb364,0xb4008000,0x31a831a8,0xb400,0xb1a831a8,0x80003400,0x2e1fb364,0xb1a831a8,0x3364ae1f,
0x34000000,0xb4008000,0x34000000,0xb4008000,0xb400,0x2e1f3364,0xb1a8b1a8,0x33642e1f,0xb4008000,0x31a8b1a8,0x80003400,0xb1a8b1a8,0x80003400,0xb364ae1f,0x31a8b1a8,0x2e1f3364,
0x34000000,0x80003400,0xb4008000,0xb400,0xb400,0x3364ae1f,0x31a831a8,0xae1f3364,0xb4008000,0xb1a8b1a8,0xb400,0x31a8b1a8,0x80003400,0xae1f3364,0xb1a831a8,0xb3642e1f,
0x34000000,0x34000000,0x34000000,0x34000000,0xb4008000,0xb3642e1f,0xb1a831a8,0xae1f3364,0x34000000,0x31a8b1a8,0xb400,0xb1a8b1a8,0xb4008000,0xae1f3364,0x31a831a8,0x3364ae1f,
0x34000000,0xb400,0xb4008000,0x80003400,0xb4008000,0x2e1f3364,0x31a8b1a8,0xb364ae1f,0x34000000,0xb1a8b1a8,0x80003400,0x31a8b1a8,0xb4008000,0x33642e1f,0xb1a8b1a8,0x2e1f3364,
0x34000000,0xb4008000,0x34000000,0xb4008000,0xb4008000,0x3364ae1f,0xb1a831a8,0x2e1fb364,0x34000000,0xb1a831a8,0xb400,0x31a831a8,0xb4008000,0x2e1fb364,0x31a831a8,0xb3642e1f,
0x34000000,0x80003400,0xb4008000,0xb400,0xb4008000,0xae1fb364,0x31a8b1a8,0x33642e1f,0x34000000,0x31a831a8,0x80003400,0xb1a831a8,0xb4008000,0xb364ae1f,0xb1a8b1a8,0xae1fb364,
0x34000000,0x34000000,0x34000000,0x34000000,0x80003400,0x2e1f3364,0x31a831a8,0x33642e1f,0xb4008000,0xb1a831a8,0x80003400,0x31a831a8,0xb400,0xb364ae1f,0xb1a831a8,0x2e1f3364,
0x34000000,0xb400,0xb4008000,0x80003400,0x80003400,0x3364ae1f,0xb1a8b1a8,0xae1f3364,0xb4008000,0x31a831a8,0xb400,0xb1a831a8,0xb400,0xae1f3364,0x31a8b1a8,0xb3642e1f,
0x34000000,0xb4008000,0x34000000,0xb4008000,0x80003400,0xae1fb364,0x31a831a8,0xb364ae1f,0xb4008000,0x31a8b1a8,0x80003400,0xb1a8b1a8,0xb400,0x33642e1f,0xb1a831a8,0xae1fb364,
0x34000000,0x80003400,0xb4008000,0xb400,0x80003400,0xb3642e1f,0xb1a8b1a8,0x2e1fb364,0xb4008000,0xb1a8b1a8,0xb400,0x31a8b1a8,0xb400,0x2e1fb364,0x31a8b1a8,0x3364ae1f,
0x3400,0x3400,0x3400,0x3400,0x3400,0xae1f3364,0xb1a831a8,0xb3642e1f,0x3400,0xb1a831a8,0xb4000000,0xb1a8b1a8,0x3400,0xb3642e1f,0xb1a8b1a8,0x2e1fb364,
0x3400,0xb4000000,0x8000b400,0x34008000,0x3400,0xb364ae1f,0x31a8b1a8,0x2e1f3364,0x3400,0xb1a8b1a8,0x34008000,0xb1a831a8,0x3400,0xae1fb364,0x31a831a8,0xb364ae1f,
0x3400,0x8000b400,0x3400,0x8000b400,0x3400,0x2e1fb364,0xb1a831a8,0x3364ae1f,0x3400,0x31a8b1a8,0xb4000000,0x31a831a8,0x3400,0x3364ae1f,0xb1a8b1a8,0xae1f3364,
0x3400,0x34008000,0x8000b400,0xb4000000,0x3400,0x33642e1f,0x31a8b1a8,0xae1fb364,0x3400,0x31a831a8,0x34008000,0x31a8b1a8,0x3400,0x2e1f3364,0x31a831a8,0x33642e1f,
0x3400,0x3400,0x3400,0x3400,0xb4000000,0xb364ae1f,0xb1a8b1a8,0xae1fb364,0x8000b400,0x31a8b1a8,0x34008000,0x31a831a8,0x34008000,0x2e1f3364,0xb1a831a8,0xb364ae1f,
0x3400,0xb4000000,0x8000b400,0x34008000,0xb4000000,0x2e1fb364,0x31a831a8,0xb3642e1f,0x8000b400,0x31a831a8,0xb4000000,0x31a8b1a8,0x34008000,0xb3642e1f,0x31a8b1a8,0xae1f3364,
0x3400,0x8000b400,0x3400,0x8000b400,0xb4000000,0x33642e1f,0xb1a8b1a8,0x2e1f3364,0x8000b400,0xb1a831a8,0x34008000,0xb1a8b1a8,0x34008000,0xae1fb364,0xb1a831a8,0x33642e1f,
0x3400,0x34008000,0x8000b400,0xb4000000,0xb4000000,0xae1f3364,0x31a831a8,0x3364ae1f,0x8000b400,0xb1a8b1a8,0xb4000000,0xb1a831a8,0x34008000,0x3364ae1f,0x31a8b1a8,0x2e1fb364,
0x3400,0x3400,0x3400,0x3400,0x8000b400,0x2e1fb364,0x31a8b1a8,0x3364ae1f,0x3400,0xb1a831a8,0xb4000000,0xb1a8b1a8,0x8000b400,0x3364ae1f,0x31a831a8,0xae1f3364,
0x3400,0xb4000000,0x8000b400,0x34008000,0x8000b400,0x33642e1f,0xb1a831a8,0xae1fb364,0x3400,0xb1a8b1a8,0x34008000,0xb1a831a8,0x8000b400,0x2e1f3364,0xb1a8b1a8,0x33642e1f,
0x3400,0x8000b400,0x3400,0x8000b400,0x8000b400,0xae1f3364,0x31a8b1a8,0xb3642e1f,0x3400,0x31a8b1a8,0xb4000000,0x31a831a8,0x8000b400,0xb3642e1f,0x31a831a8,0x2e1fb364,
0x3400,0x34008000,0x8000b400,0xb4000000,0x8000b400,0xb364ae1f,0xb1a831a8,0x2e1f3364,0x3400,0x31a831a8,0x34008000,0x31a8b1a8,0x8000b400,0xae1fb364,0xb1a8b1a8,0xb364ae1f,
0x3400,0x3400,0x3400,0x3400,0x34008000,0x33642e1f,0x31a831a8,0x2e1f3364,0x8000b400,0x31a8b1a8,0x34008000,0x31a831a8,0xb4000000,0xae1fb364,0x31a8b1a8,0x33642e1f,
0x3400,0xb4000000,0x8000b400,0x34008000,0x34008000,0xae1f3364,0xb1a8b1a8,0x3364ae1f,0x8000b400,0x31a831a8,0xb4000000,0x31a8b1a8,0xb4000000,0x3364ae1f,0xb1a831a8,0x2e1fb364,
0x3400,0x8000b400,0x3400,0x8000b400,0x34008000,0xb364ae1f,0x31a831a8,0xae1fb364,0x8000b400,0xb1a831a8,0x34008000,0xb1a8b1a8,0xb4000000,0x2e1f3364,0x31a8b1a8,0xb364ae1f,
0x3400,0x34008000,0x8000b400,0xb4000000,0x34008000,0x2e1fb364,0xb1a8b1a8,0xb3642e1f,0x8000b400,0xb1a8b1a8,0xb4000000,0xb1a831a8,0xb4000000,0xb3642e1f,0xb1a831a8,0xae1f3364,
0x34000000,0x34000000,0x34000000,0x34000000,0x34000000,0x33642e1f,0x31a831a8,0x2e1f3364,0x34000000,0x31a831a8,0x3400,0xb1a831a8,0x34000000,0x2e1f3364,0xb1a831a8,0xb364ae1f,
0x34000000,0x3400,0xb4000000,0x8000b400,0x34000000,0xae1f3364,0xb1a8b1a8,0x3364ae1f,0x34000000,0xb1a831a8,0x8000b400,0x31a831a8,0x34000000,0xb3642e1f,0x31a8b1a8,0xae1f3364,
0x34000000,0xb4000000,0x34000000,0xb4000000,0x34000000,0xb364ae1f,0x31a831a8,0xae1fb364,0x34000000,0xb1a8b1a8,0x3400,0x31a8b1a8,0x34000000,0xae1fb364,0xb1a831a8,0x33642e1f,
0x34000000,0x8000b400,0xb4000000,0x3400,0x34000000,0x2e1fb364,0xb1a8b1a8,0xb3642e1f,0x34000000,0x31a8b1a8,0x8000b400,0xb1a8b1a8,0x34000000,0x3364ae1f,0x31a8b1a8,0x2e1fb364,
0x34000000,0x34000000,0x34000000,0x34000000,0x3400,0xae1f3364,0xb1a831a8,0xb3642e1f,0xb4000000,0xb1a8b1a8,0x8000b400,0x31a8b1a8,0x8000b400,0x3364ae1f,0x31a831a8,0xae1f3364,
0x34000000,0x3400,0xb4000000,0x8000b400,0x3400,0xb364ae1f,0x31a8b1a8,0x2e1f3364,0xb4000000,0x31a8b1a8,0x3400,0xb1a8b1a8,0x8000b400,0x2e1f3364,0xb1a8b1a8,0x33642e1f,
0x34000000,0xb4000000,0x34000000,0xb4000000,0x3400,0x2e1fb364,0xb1a831a8,0x3364ae1f,0xb4000000,0x31a831a8,0x8000b400,0xb1a831a8,0x8000b400,0xb3642e1f,0x31a831a8,0x2e1fb364,
0x34000000,0x8000b400,0xb4000000,0x3400,0x3400,0x33642e1f,0x31a8b1a8,0xae1fb364,0xb4000000,0xb1a831a8,0x3400,0x31a831a8,0x8000b400,0xae1fb364,0xb1a8b1a8,0xb364ae1f,
0x34000000,0x34000000,0x34000000,0x34000000,0xb4000000,0xb364ae1f,0xb1a8b1a8,0xae1fb364,0x34000000,0x31a831a8,0x3400,0xb1a831a8,0xb4000000,0xae1fb364,0x31a8b1a8,0x33642e1f,
0x34000000,0x3400,0xb4000000,0x8000b400,0xb4000000,0x2e1fb364,0x31a831a8,0xb3642e1f,0x34000000,0xb1a831a8,0x8000b400,0x31a831a8,0xb4000000,0x3364ae1f,0xb1a831a8,0x2e1fb364,
0x34000000,0xb4000000,0x34000000,0xb4000000,0xb4000000,0x33642e1f,0xb1a8b1a8,0x2e1f3364,0x34000000,0xb1a8b1a8,0x3400,0x31a8b1a8,0xb4000000,0x2e1f3364,0x31a8b1a8,0xb364ae1f,
0x34000000,0x8000b400,0xb4000000,0x3400,0xb4000000,0xae1f3364,0x31a831a8,0x3364ae1f,0x34000000,0x31a8b1a8,0x8000b400,0xb1a8b1a8,0xb4000000,0xb3642e1f,0xb1a831a8,0xae1f3364,
0x34000000,0x34000000,0x34000000,0x34000000,0x8000b400,0x2e1fb364,0x31a8b1a8,0x3364ae1f,0xb4000000,0xb1a8b1a8,0x8000b400,0x31a8b1a8,0x3400,0xb3642e1f,0xb1a8b1a8,0x2e1fb364,
0x34000000,0x3400,0xb4000000,0x8000b400,0x8000b400,0x33642e1f,0xb1a831a8,0xae1fb364,0xb4000000,0x31a8b1a8,0x3400,0xb1a8b1a8,0x3400,0xae1fb364,0x31a831a8,0xb364ae1f,
0x34000000,0xb4000000,0x34000000,0xb4000000,0x8000b400,0xae1f3364,0x31a8b1a8,0xb3642e1f,0xb4000000,0x31a831a8,0x8000b400,0xb1a831a8,0x3400,0x3364ae1f,0xb1a8b1a8,0xae1f3364,
0x34000000,0x8000b400,0xb4000000,0x3400,0x8000b400,0xb364ae1f,0xb1a831a8,0x2e1f3364,0xb4000000,0xb1a831a8,0x3400,0x31a831a8,0x3400,0x2e1f3364,0x31a831a8,0x33642e1f,
0x3400,0x3400,0x3400,0x3400,0xaa3e33d9,0xb07232a7,0xb2a73072,0xb3d92a3e,0xae1f3364,0xb3642e1f,0xb364ae1f,0xae1fb364,0xb07232a7,0xb3d9aa3e,0xaa3eb3d9,0x32a7b072,
0xb1a831a8,0xb1a8b1a8,0x31a8b1a8,0x31a831a8,0xb2a73072,0xaa3eb3d9,0x33d92a3e,0xb07232a7,0xb3642e1f,0x2e1fb364,0x2e1f3364,0xb364ae1f,0xb3d92a3e,0x32a7b072,0xb07232a7,0x2a3eb3d9,
0xb4000000,0x34008000,0xb4000000,0x34008000,0xb3d9aa3e,0x32a73072,0xb072b2a7,0x2a3e33d9,0xb364ae1f,0x2e1f3364,0x2e1fb364,0xb3642e1f,0xb2a7b072,0xaa3e33d9,0x33d9aa3e,0xb072b2a7,
0xb1a8b1a8,0xb1a831a8,0x31a831a8,0x31a8b1a8,0xb072b2a7,0xb3d92a3e,0xaa3e33d9,0x32a73072,0xae1fb364,0xb364ae1f,0xb3642e1f,0xae1f3364,0xaa3eb3d9,0xb072b2a7,0xb2a7b072,0xb3d9aa3e,
0x3400,0x3400,0x3400,0x3400,0xb3d9aa3e,0xb2a7b072,0xb072b2a7,0xaa3eb3d9,0x2e1fb364,0x3364ae1f,0x33642e1f,0x2e1f3364,0x32a73072,0xaa3e33d9,0xb3d92a3e,0xb072b2a7,
0xb1a831a8,0xb1a8b1a8,0x31a8b1a8,0x31a831a8,0xb072b2a7,0x33d9aa3e,0xaa3e33d9,0xb2a7b072,0x3364ae1f,0xae1f3364,0xae1fb364,0x33642e1f,0x2a3e33d9,0xb072b2a7,0x32a73072,0xb3d9aa3e,
0xb4000000,0x34008000,0xb4000000,0x34008000,0x2a3eb3d9,0xb07232a7,0x32a7b072,0xb3d92a3e,0x33642e1f,0xae1fb364,0xae1f3364,0x3364ae1f,0xb07232a7,0x33d92a3e,0xaa3eb3d9,0xb2a73072,
0xb1a8b1a8,0xb1a831a8,0x31a831a8,0x31a8b1a8,0x32a7b072,0xaa3eb3d9,0xb3d9aa3e,0xb07232a7,0x2e1f3364,0x33642e1f,0x3364ae1f,0x2e1fb364,0xb3d92a3e,0xb2a73072,0xb07232a7,0xaa3e33d9,
0x3400,0x3400,0x3400,0x3400,0x2a3eb3d9,0x3072b2a7,0x32a7b072,0x33d9aa3e,0xae1f3364,0xb3642e1f,0xb364ae1f,0xae1fb364,0x3072b2a7,0x33d92a3e,0x2a3e33d9,0xb2a73072,
0xb1a831a8,0xb1a8b1a8,0x31a8b1a8,0x31a831a8,0x32a7b072,0x2a3e33d9,0xb3d9aa3e,0x3072b2a7,0xb3642e1f,0x2e1fb364,0x2e1f3364,0xb364ae1f,0x33d9aa3e,0xb2a73072,0x3072b2a7,0xaa3e33d9,
0xb4000000,0x34008000,0xb4000000,0x34008000,0x33d92a3e,0xb2a7b072,0x307232a7,0xaa3eb3d9,0xb364ae1f,0x2e1f3364,0x2e1fb364,0xb3642e1f,0x32a73072,0x2a3eb3d9,0xb3d92a3e,0x307232a7,
0xb1a8b1a8,0xb1a831a8,0x31a831a8,0x31a8b1a8,0x307232a7,0x33d9aa3e,0x2a3eb3d9,0xb2a7b072,0xae1fb364,0xb364ae1f,0xb3642e1f,0xae1f3364,0x2a3e33d9,0x307232a7,0x32a73072,0x33d92a3e,
0x3400,0x3400,0x3400,0x3400,0x33d92a3e,0x32a73072,0x307232a7,0x2a3e33d9,0x2e1fb364,0x3364ae1f,0x33642e1f,0x2e1f3364,0xb2a7b072,0x2a3eb3d9,0x33d9aa3e,0x307232a7,
0xb1a831a8,0xb1a8b1a8,0x31a8b1a8,0x31a831a8,0x307232a7,0xb3d92a3e,0x2a3eb3d9,0x32a73072,0x3364ae1f,0xae1f3364,0xae1fb364,0x33642e1f,0xaa3eb3d9,0x307232a7,0xb2a7b072,0x33d92a3e,
0xb4000000,0x34008000,0xb4000000,0x34008000,0xaa3e33d9,0x3072b2a7,0xb2a73072,0x33d9aa3e,0x33642e1f,0xae1fb364,0xae1f3364,0x3364ae1f,0x3072b2a7,0xb3d9aa3e,0x2a3e33d9,0x32a7b072,
0xb1a8b1a8,0xb1a831a8,0x31a831a8,0x31a8b1a8,0xb2a73072,0x2a3e33d9,0x33d92a3e,0x3072b2a7,0x2e1f3364,0x33642e1f,0x3364ae1f,0x2e1fb364,0x33d9aa3e,0x32a7b072,0x3072b2a7,0x2a3eb3d9,
0x34000000,0x34000000,0x34000000,0x34000000,0x33d92a3e,0x32a73072,0x307232a7,0x2a3e33d9,0x33642e1f,0x2e1f3364,0xae1f3364,0xb3642e1f,0x32a73072,0xaa3e33d9,0xb3d92a3e,0xb072b2a7,
0x31a831a8,0xb1a831a8,0xb1a8b1a8,0x31a8b1a8,0x307232a7,0xb3d92a3e,0x2a3eb3d9,0x32a73072,0x2e1f3364,0xb364ae1f,0x3364ae1f,0xae1f3364,0x2a3e33d9,0xb072b2a7,0x32a73072,0xb3d9aa3e,
0x3400,0x8000b400,0x3400,0x8000b400,0xaa3e33d9,0x3072b2a7,0xb2a73072,0x33d9aa3e,0xae1f3364,0x3364ae1f,0xb364ae1f,0x2e1f3364,0xb07232a7,0x33d92a3e,0xaa3eb3d9,0xb2a73072,
0xb1a831a8,0x31a831a8,0x31a8b1a8,0xb1a8b1a8,0xb2a73072,0x2a3e33d9,0x33d92a3e,0x3072b2a7,0xb3642e1f,0xae1f3364,0x2e1f3364,0x33642e1f,0xb3d92a3e,0xb2a73072,0xb07232a7,0xaa3e33d9,
0x34000000,0x34000000,0x34000000,0x34000000,0xaa3e33d9,0xb07232a7,0xb2a73072,0xb3d92a3e,0xb364ae1f,0xae1fb364,0x2e1fb364,0x3364ae1f,0x3072b2a7,0x33d92a3e,0x2a3e33d9,0xb2a73072,
0x31a831a8,0xb1a831a8,0xb1a8b1a8,0x31a8b1a8,0xb2a73072,0xaa3eb3d9,0x33d92a3e,0xb07232a7,0xae1fb364,0x33642e1f,0xb3642e1f,0x2e1fb364,0x33d9aa3e,0xb2a73072,0x3072b2a7,0xaa3e33d9,
0x3400,0x8000b400,0x3400,0x8000b400,0xb3d9aa3e,0x32a73072,0xb072b2a7,0x2a3e33d9,0x2e1fb364,0xb3642e1f,0x33642e1f,0xae1fb364,0x32a73072,0x2a3eb3d9,0xb3d92a3e,0x307232a7,
0xb1a831a8,0x31a831a8,0x31a8b1a8,0xb1a8b1a8,0xb072b2a7,0xb3d92a3e,0xaa3e33d9,0x32a73072,0x3364ae1f,0x2e1fb364,0xae1fb364,0xb364ae1f,0x2a3e33d9,0x307232a7,0x32a73072,0x33d92a3e,
0x34000000,0x34000000,0x34000000,0x34000000,0xb3d9aa3e,0xb2a7b072,0xb072b2a7,0xaa3eb3d9,0x33642e1f,0x2e1f3364,0xae1f3364,0xb3642e1f,0xb2a7b072,0x2a3eb3d9,0x33d9aa3e,0x307232a7,
0x31a831a8,0xb1a831a8,0xb1a8b1a8,0x31a8b1a8,0xb072b2a7,0x33d9aa3e,0xaa3e33d9,0xb2a7b072,0x2e1f3364,0xb364ae1f,0x3364ae1f,0xae1f3364,0xaa3eb3d9,0x307232a7,0xb2a7b072,0x33d92a3e,
0x3400,0x8000b400,0x3400,0x8000b400,0x2a3eb3d9,0xb07232a7,0x32a7b072,0xb3d92a3e,0xae1f3364,0x3364ae1f,0xb364ae1f,0x2e1f3364,0x3072b2a7,0xb3d9aa3e,0x2a3e33d9,0x32a7b072,
0xb1a831a8,0x31a831a8,0x31a8b1a8,0xb1a8b1a8,0x32a7b072,0xaa3eb3d9,0xb3d9aa3e,0xb07232a7,0xb3642e1f,0xae1f3364,0x2e1f3364,0x33642e1f,0x33d9aa3e,0x32a7b072,0x3072b2a7,0x2a3eb3d9,
0x34000000,0x34000000,0x34000000,0x34000000,0x2a3eb3d9,0x3072b2a7,0x32a7b072,0x33d9aa3e,0xb364ae1f,0xae1fb364,0x2e1fb364,0x3364ae1f,0xb07232a7,0xb3d9aa3e,0xaa3eb3d9,0x32a7b072,
0x31a831a8,0xb1a831a8,0xb1a8b1a8,0x31a8b1a8,0x32a7b072,0x2a3e33d9,0xb3d9aa3e,0x3072b2a7,0xae1fb364,0x33642e1f,0xb3642e1f,0x2e1fb364,0xb3d92a3e,0x32a7b072,0xb07232a7,0x2a3eb3d9,
0x3400,0x8000b400,0x3400,0x8000b400,0x33d92a3e,0xb2a7b072,0x307232a7,0xaa3eb3d9,0x2e1fb364,0xb3642e1f,0x33642e1f,0xae1fb364,0xb2a7b072,0xaa3e33d9,0x33d9aa3e,0xb072b2a7,
0xb1a831a8,0x31a831a8,0x31a8b1a8,0xb1a8b1a8,0x307232a7,0x33d9aa3e,0x2a3eb3d9,0xb2a7b072,0x3364ae1f,0x2e1fb364,0xae1fb364,0xb364ae1f,0xaa3eb3d9,0xb072b2a7,0xb2a7b072,0xb3d9aa3e,
0x3f800000,0x3f7ffb11,0x3f7fec43,0x3f7fd397,0x3f800000,0x3f7fd397,0x3f7f4e6d,0x3f7e70b0,0x3f800000,0x3f7f84ab,0x3f7e1324,0x3f7baccd,0x3f800000,0x3f7f0e58,0x3f7c3b28,0x3f778bc5,
0x3f800000,0x3f7e70b0,0x3f79c79d,0x3f721352,0x3f800000,0x3f7dabcc,0x3f76ba07,0x3f6b4b0c,0x3f800000,0x3f7cbfc9,0x3f731447,0x3f633c5a,0x3f800000,0x3f7baccd,0x3f6ed89e,0x3f59f26a,
0x3f800000,0x3f7a7302,0x3f6a09a7,0x3f4f7a1f,0x3f800000,0x3f791298,0x3f64aa59,0x3f43e200,0x3f800000,0x3f778bc5,0x3f5ebe05,0x3f373a23,0x3f800000,0x3f75dec6,0x3f584853,0x3f299415,
0x3f800000,0x3f740bdd,0x3f514d3d,0x3f1b02c6,0x3f800000,0x3f721352,0x3f49d112,0x3f0b9a6b,0x3f800000,0x3f6ff573,0x3f41d870,0x3ef6e0cb,0x3f800000,0x3f6db293,0x3f396842,0x3ed53641,
0x3f7fb10f,0x3f7f84ab,0x3f7f4e6d,0x3f7f0e58,0x3f7d3aac,0x3f7baccd,0x3f79c79d,0x3f778bc5,0x3f7853f8,0x3f740bdd,0x3f6ed89e,0x3f68bf3c,0x3f710908,0x3f68bf3c,0x3f5ebe05,0x3f531849,
0x3f676bd8,0x3f59f26a,0x3f49d112,0x3f373a23,0x3f5b941a,0x3f47de65,0x3f3085bb,0x3f15f6d9,0x3f4d9f02,0x3f32c8c9,0x3f13682a,0x3ee0924f,0x3f3daef9,0x3f1b02c6,0x3ee63375,0x3e8e9a22,
0x3f2beb4a,0x3f00e7e4,0x3ea09ae5,0x3de1bc2e,0x3f187fc0,0x3ec9b953,0x3e2f10a2,0xbd7b2b74,0x3f039c3d,0x3e8e9a22,0x3cc90ab0,0xbe6c9a7f,0x3edae880,0x3e22abb6,0xbdfab273,0xbec9b953,
0x3eac7cd4,0x3d16c32c,0xbe888e93,0xbf0b9a6b,0x3e78cfcc,0xbdafb680,0xbecf7bca,0xbf2e3bde,0x3e164083,0xbe541501,0xbf08f59b,0xbf4bbbf8,0x3d48fb30,0xbea68f12,0xbf273656,0xbf633c5a,
0x3f7ec46d,0x3f7e70b0,0x3f7e1324,0x3f7dabcc,0x3f74fa0b,0x3f721352,0x3f6ed89e,0x3f6b4b0c,0x3f61c598,0x3f59f26a,0x3f514d3d,0x3f47de65,0x3f45e403,0x3f373a23,0x3f273656,0x3f15f6d9,
0x3f226799,0x3f0b9a6b,0x3ee63375,0x3eb263ef,0x3ef15aea,0x3eb263ef,0x3e605c13,0x3dafb680,0x3e94a031,0x3e09cf86,0xbcc90ab0,0xbe3b6ecf,0x3dc8bd36,0xbdafb680,0xbe888e93,0xbee0924f,
0xbdc8bd36,0xbe9aa086,0xbefc5d27,0xbf299415,0xbe94a031,0xbf00e7e4,0xbf3085bb,0xbf5695e5,0xbef15aea,0xbf2e3bde,0xbf584853,0xbf740bdd,0xbf226799,0xbf531849,0xbf731447,0xbf7fd397,
0xbf45e403,0xbf6db293,0xbf7f4e6d,0xbf791298,0xbf61c598,0xbf7cbfc9,0xbf7c3b28,0xbf604621,0xbf74fa0b,0xbf7f84ab,0xbf6a09a7,0xbf373a23,0xbf7ec46d,0xbf75dec6,0xbf49d112,0xbf00e7e4,
0x3f7d3aac,0x3f7cbfc9,0x3f7c3b28,0x3f7baccd,0x3f676bd8,0x3f633c5a,0x3f5ebe05,0x3f59f26a,0x3f3daef9,0x3f32c8c9,0x3f273656,0x3f1b02c6,0x3f039c3d,0x3ee0924f,0x3eb8442a,0x3e8e9a22,
0x3e78cfcc,0x3e09cf86,0x3cc90ab0,0xbdafb680,0xbd48fb30,0xbe3b6ecf,0xbea09ae5,0xbee0924f,0xbeac7cd4,0xbef6e0cb,0xbf1d7fd1,0xbf3b8f3b,0xbf187fc0,0xbf3b8f3b,0xbf584853,0xbf6db293,
0xbf4d9f02,0xbf68bf3c,0xbf79c79d,0xbf7ffb11,0xbf710908,0xbf7e70b0,0xbf7e1324,0xbf6ff573,0xbf7fb10f,0xbf7a7302,0xbf64aa59,0xbf3fc767,0xbf7853f8,0xbf5d2d53,0xbf3085bb,0xbeebcbbb,
0xbf5b941a,0xbf299415,0xbecf7bca,0xbde1bc2e,0xbf2beb4a,0xbec9b953,0xbd96a905,0x3e827dc0,0xbedae880,0xbdafb680,0x3e888e93,0x3f15f6d9,0xbe164083,0x3e6c9a7f,0x3f13682a,0x3f5695e5,
0x0,0x3c490e90,0x3cc90ab0,0x3d16c32c,0x0,0x3d16c32c,0x3d96a905,0x3de1bc2e,0x0,0x3d7b2b74,0x3dfab273,0x3e3b6ecf,0x0,0x3dafb680,0x3e2f10a2,0x3e827dc0,
0x0,0x3de1bc2e,0x3e605c13,0x3ea68f12,0x0,0x3e09cf86,0x3e888e93,0x3ec9b953,0x0,0x3e22abb6,0x3ea09ae5,0x3eebcbbb,0x0,0x3e3b6ecf,0x3eb8442a,0x3f064b82,
0x0,0x3e541501,0x3ecf7bca,0x3f15f6d9,0x0,0x3e6c9a7f,0x3ee63375,0x3f24d225,0x0,0x3e827dc0,0x3efc5d27,0x3f32c8c9,0x0,0x3e8e9a22,0x3f08f59b,0x3f3fc767,
0x0,0x3e9aa086,0x3f13682a,0x3f4bbbf8,0x0,0x3ea68f12,0x3f1d7fd1,0x3f5695e5,0x0,0x3eb263ef,0x3f273656,0x3f604621,0x0,0x3ebe1d4a,0x3f3085bb,0x3f68bf3c,
0x3d48fb30,0x3d7b2b74,0x3d96a905,0x3dafb680,0x3e164083,0x3e3b6ecf,0x3e605c13,0x3e827dc0,0x3e78cfcc,0x3e9aa086,0x3eb8442a,0x3ed53641,0x3eac7cd4,0x3ed53641,0x3efc5d27,0x3f10d3cd,
0x3edae880,0x3f064b82,0x3f1d7fd1,0x3f32c8c9,0x3f039c3d,0x3f1ff6cb,0x3f396842,0x3f4f7a1f,0x3f187fc0,0x3f373a23,0x3f514d3d,0x3f660f88,0x3f2beb4a,0x3f4bbbf8,0x3f64aa59,0x3f75dec6,
0x3f3daef9,0x3f5d2d53,0x3f731447,0x3f7e70b0,0x3f4d9f02,0x3f6b4b0c,0x3f7c3b28,0x3f7f84ab,0x3f5b941a,0x3f75dec6,0x3f7fec43,0x3f791298,0x3f676bd8,0x3f7cbfc9,0x3f7e1324,0x3f6b4b0c,
0x3f710908,0x3f7fd397,0x3f76ba07,0x3f5695e5,0x3f7853f8,0x3f7f0e58,0x3f6a09a7,0x3f3b8f3b,0x3f7d3aac,0x3f7a7302,0x3f584853,0x3f1b02c6,0x3f7fb10f,0x3f721352,0x3f41d870,0x3eebcbbb,
0x3dc8bd36,0x3de1bc2e,0x3dfab273,0x3e09cf86,0x3e94a031,0x3ea68f12,0x3eb8442a,0x3ec9b953,0x3ef15aea,0x3f064b82,0x3f13682a,0x3f1ff6cb,0x3f226799,0x3f32c8c9,0x3f41d870,0x3f4f7a1f,
0x3f45e403,0x3f5695e5,0x3f64aa59,0x3f6ff573,0x3f61c598,0x3f6ff573,0x3f79c79d,0x3f7f0e58,0x3f74fa0b,0x3f7dabcc,0x3f7fec43,0x3f7baccd,0x3f7ec46d,0x3f7f0e58,0x3f76ba07,0x3f660f88,
0x3f7ec46d,0x3f740bdd,0x3f5ebe05,0x3f3fc767,0x3f74fa0b,0x3f5d2d53,0x3f396842,0x3f0b9a6b,0x3f61c598,0x3f3b8f3b,0x3f08f59b,0x3e9aa086,0x3f45e403,0x3f10d3cd,0x3ea09ae5,0x3d16c32c,
0x3f226799,0x3ebe1d4a,0x3d96a905,0xbe6c9a7f,0x3ef15aea,0x3e22abb6,0xbe2f10a2,0xbef6e0cb,0x3e94a031,0xbd7b2b74,0xbecf7bca,0xbf32c8c9,0x3dc8bd36,0xbe8e9a22,0xbf1d7fd1,0xbf5d2d53,
0x3e164083,0x3e22abb6,0x3e2f10a2,0x3e3b6ecf,0x3edae880,0x3eebcbbb,0x3efc5d27,0x3f064b82,0x3f2beb4a,0x3f373a23,0x3f41d870,0x3f4bbbf8,0x3f5b941a,0x3f660f88,0x3f6ed89e,0x3f75dec6,
0x3f7853f8,0x3f7dabcc,0x3f7fec43,0x3f7f0e58,0x3f7fb10f,0x3f7baccd,0x3f731447,0x3f660f88,0x3f710908,0x3f604621,0x3f49d112,0x3f2e3bde,0x3f4d9f02,0x3f2e3bde,0x3f08f59b,0x3ebe1d4a,
0x3f187fc0,0x3ed53641,0x3e605c13,0x3c490e90,0x3eac7cd4,0x3de1bc2e,0xbdfab273,0xbeb263ef,0x3d48fb30,0xbe541501,0xbee63375,0xbf299415,0xbe78cfcc,0xbf00e7e4,0xbf396842,0xbf633c5a,
0xbf039c3d,0xbf3fc767,0xbf6a09a7,0xbf7e70b0,0xbf3daef9,0xbf6b4b0c,0xbf7f4e6d,0xbf778bc5,0xbf676bd8,0xbf7f0e58,0xbf76ba07,0xbf4f7a1f,0xbf7d3aac,0xbf791298,0xbf514d3d,0xbf0b9a6b,
0x3f800000,0x3f800000,0x3f800000,0x3f800000,0x3f7ffb11,0x3f7fd397,0x3f7f84ab,0x3f7f0e58,0x3f7fec43,0x3f7f4e6d,0x3f7e1324,0x3f7c3b28,0x3f7fd397,0x3f7e70b0,0x3f7baccd,0x3f778bc5,
0x3f7fb10f,0x3f7d3aac,0x3f7853f8,0x3f710908,0x3f7f84ab,0x3f7baccd,0x3f740bdd,0x3f68bf3c,0x3f7f4e6d,0x3f79c79d,0x3f6ed89e,0x3f5ebe05,0x3f7f0e58,0x3f778bc5,0x3f68bf3c,0x3f531849,
0x3f7ec46d,0x3f74fa0b,0x3f61c598,0x3f45e403,0x3f7e70b0,0x3f721352,0x3f59f26a,0x3f373a23,0x3f7e1324,0x3f6ed89e,0x3f514d3d,0x3f273656,0x3f7dabcc,0x3f6b4b0c,0x3f47de65,0x3f15f6d9,
0x3f7d3aac,0x3f676bd8,0x3f3daef9,0x3f039c3d,0x3f7cbfc9,0x3f633c5a,0x3f32c8c9,0x3ee0924f,0x3f7c3b28,0x3f5ebe05,0x3f273656,0x3eb8442a,0x3f7baccd,0x3f59f26a,0x3f1b02c6,0x3e8e9a22,
0x3f800000,0x3f800000,0x3f800000,0x3f800000,0x3f7e70b0,0x3f7dabcc,0x3f7cbfc9,0x3f7baccd,0x3f79c79d,0x3f76ba07,0x3f731447,0x3f6ed89e,0x3f721352,0x3f6b4b0c,0x3f633c5a,0x3f59f26a,
0x3f676bd8,0x3f5b941a,0x3f4d9f02,0x3f3daef9,0x3f59f26a,0x3f47de65,0x3f32c8c9,0x3f1b02c6,0x3f49d112,0x3f3085bb,0x3f13682a,0x3ee63375,0x3f373a23,0x3f15f6d9,0x3ee0924f,0x3e8e9a22,
0x3f226799,0x3ef15aea,0x3e94a031,0x3dc8bd36,0x3f0b9a6b,0x3eb263ef,0x3e09cf86,0xbdafb680,0x3ee63375,0x3e605c13,0xbcc90ab0,0xbe888e93,0x3eb263ef,0x3dafb680,0xbe3b6ecf,0xbee0924f,
0x3e78cfcc,0xbd48fb30,0xbeac7cd4,0xbf187fc0,0x3e09cf86,0xbe3b6ecf,0xbef6e0cb,0xbf3b8f3b,0x3cc90ab0,0xbea09ae5,0xbf1d7fd1,0xbf584853,0xbdafb680,0xbee0924f,0xbf3b8f3b,0xbf6db293,
0x3f800000,0x3f800000,0x3f800000,0x3f800000,0x3f7a7302,0x3f791298,0x3f778bc5,0x3f75dec6,0x3f6a09a7,0x3f64aa59,0x3f5ebe05,0x3f584853,0x3f4f7a1f,0x3f43e200,0x3f373a23,0x3f299415,
0x3f2beb4a,0x3f187fc0,0x3f039c3d,0x3edae880,0x3f00e7e4,0x3ec9b953,0x3e8e9a22,0x3e22abb6,0x3ea09ae5,0x3e2f10a2,0x3cc90ab0,0xbdfab273,0x3de1bc2e,0xbd7b2b74,0xbe6c9a7f,0xbec9b953,
0xbdc8bd36,0xbe94a031,0xbef15aea,0xbf226799,0xbe9aa086,0xbf00e7e4,0xbf2e3bde,0xbf531849,0xbefc5d27,0xbf3085bb,0xbf584853,0xbf731447,0xbf299415,0xbf5695e5,0xbf740bdd,0xbf7fd397,
0xbf4d9f02,0xbf710908,0xbf7fb10f,0xbf7853f8,0xbf68bf3c,0xbf7e70b0,0xbf7a7302,0xbf5d2d53,0xbf79c79d,0xbf7e1324,0xbf64aa59,0xbf3085bb,0xbf7ffb11,0xbf6ff573,0xbf3fc767,0xbeebcbbb,
0x3f800000,0x3f800000,0x3f800000,0x3f800000,0x3f740bdd,0x3f721352,0x3f6ff573,0x3f6db293,0x3f514d3d,0x3f49d112,0x3f41d870,0x3f396842,0x3f1b02c6,0x3f0b9a6b,0x3ef6e0cb,0x3ed53641,
0x3eac7cd4,0x3e78cfcc,0x3e164083,0x3d48fb30,0x3d16c32c,0xbdafb680,0xbe541501,0xbea68f12,0xbe888e93,0xbecf7bca,0xbf08f59b,0xbf273656,0xbf0b9a6b,0xbf2e3bde,0xbf4bbbf8,0xbf633c5a,
0xbf45e403,0xbf61c598,0xbf74fa0b,0xbf7ec46d,0xbf6db293,0xbf7cbfc9,0xbf7f84ab,0xbf75dec6,0xbf7f4e6d,0xbf7c3b28,0xbf6a09a7,0xbf49d112,0xbf791298,0xbf604621,0xbf373a23,0xbf00e7e4,
0xbf5b941a,0xbf2beb4a,0xbedae880,0xbe164083,0xbf299415,0xbec9b953,0xbdafb680,0x3e6c9a7f,0xbecf7bca,0xbd96a905,0x3e888e93,0x3f13682a,0xbde1bc2e,0x3e827dc0,0x3f15f6d9,0x3f5695e5,
0x0,0x0,0x0,0x0,0x3c490e90,0x3d16c32c,0x3d7b2b74,0x3dafb680,0x3cc90ab0,0x3d96a905,0x3dfab273,0x3e2f10a2,0x3d16c32c,0x3de1bc2e,0x3e3b6ecf,0x3e827dc0,
0x3d48fb30,0x3e164083,0x3e78cfcc,0x3eac7cd4,0x3d7b2b74,0x3e3b6ecf,0x3e9aa086,0x3ed53641,0x3d96a905,0x3e605c13,0x3eb8442a,0x3efc5d27,0x3dafb680,0x3e827dc0,0x3ed53641,0x3f10d3cd,
0x3dc8bd36,0x3e94a031,0x3ef15aea,0x3f226799,0x3de1bc2e,0x3ea68f12,0x3f064b82,0x3f32c8c9,0x3dfab273,0x3eb8442a,0x3f13682a,0x3f41d870,0x3e09cf86,0x3ec9b953,0x3f1ff6cb,0x3f4f7a1f,
0x3e164083,0x3edae880,0x3f2beb4a,0x3f5b941a,0x3e22abb6,0x3eebcbbb,0x3f373a23,0x3f660f88,0x3e2f10a2,0x3efc5d27,0x3f41d870,0x3f6ed89e,0x3e3b6ecf,0x3f064b82,0x3f4bbbf8,0x3f75dec6,
0x0,0x0,0x0,0x0,0x3de1bc2e,0x3e09cf86,0x3e22abb6,0x3e3b6ecf,0x3e605c13,0x3e888e93,0x3ea09ae5,0x3eb8442a,0x3ea68f12,0x3ec9b953,0x3eebcbbb,0x3f064b82,
0x3edae880,0x3f039c3d,0x3f187fc0,0x3f2beb4a,0x3f064b82,0x3f1ff6cb,0x3f373a23,0x3f4bbbf8,0x3f1d7fd1,0x3f396842,0x3f514d3d,0x3f64aa59,0x3f32c8c9,0x3f4f7a1f,0x3f660f88,0x3f75dec6,
0x3f45e403,0x3f61c598,0x3f74fa0b,0x3f7ec46d,0x3f5695e5,0x3f6ff573,0x3f7dabcc,0x3f7f0e58,0x3f64aa59,0x3f79c79d,0x3f7fec43,0x3f76ba07,0x3f6ff573,0x3f7f0e58,0x3f7baccd,0x3f660f88,
0x3f7853f8,0x3f7fb10f,0x3f710908,0x3f4d9f02,0x3f7dabcc,0x3f7baccd,0x3f604621,0x3f2e3bde,0x3f7fec43,0x3f731447,0x3f49d112,0x3f08f59b,0x3f7f0e58,0x3f660f88,0x3f2e3bde,0x3ebe1d4a,
0x0,0x0,0x0,0x0,0x3e541501,0x3e6c9a7f,0x3e827dc0,0x3e8e9a22,0x3ecf7bca,0x3ee63375,0x3efc5d27,0x3f08f59b,0x3f15f6d9,0x3f24d225,0x3f32c8c9,0x3f3fc767,
0x3f3daef9,0x3f4d9f02,0x3f5b941a,0x3f676bd8,0x3f5d2d53,0x3f6b4b0c,0x3f75dec6,0x3f7cbfc9,0x3f731447,0x3f7c3b28,0x3f7fec43,0x3f7e1324,0x3f7e70b0,0x3f7f84ab,0x3f791298,0x3f6b4b0c,
0x3f7ec46d,0x3f74fa0b,0x3f61c598,0x3f45e403,0x3f740bdd,0x3f5d2d53,0x3f3b8f3b,0x3f10d3cd,0x3f5ebe05,0x3f396842,0x3f08f59b,0x3ea09ae5,0x3f3fc767,0x3f0b9a6b,0x3e9aa086,0x3d16c32c,
0x3f187fc0,0x3eac7cd4,0x3d48fb30,0xbe78cfcc,0x3ed53641,0x3de1bc2e,0xbe541501,0xbf00e7e4,0x3e605c13,0xbdfab273,0xbee63375,0xbf396842,0x3c490e90,0xbeb263ef,0xbf299415,0xbf633c5a,
0x0,0x0,0x0,0x0,0x3e9aa086,0x3ea68f12,0x3eb263ef,0x3ebe1d4a,0x3f13682a,0x3f1d7fd1,0x3f273656,0x3f3085bb,0x3f4bbbf8,0x3f5695e5,0x3f604621,0x3f68bf3c,
0x3f710908,0x3f7853f8,0x3f7d3aac,0x3f7fb10f,0x3f7fd397,0x3f7f0e58,0x3f7a7302,0x3f721352,0x3f76ba07,0x3f6a09a7,0x3f584853,0x3f41d870,0x3f5695e5,0x3f3b8f3b,0x3f1b02c6,0x3eebcbbb,
0x3f226799,0x3ef15aea,0x3e94a031,0x3dc8bd36,0x3ebe1d4a,0x3e22abb6,0xbd7b2b74,0xbe8e9a22,0x3d96a905,0xbe2f10a2,0xbecf7bca,0xbf1d7fd1,0xbe6c9a7f,0xbef6e0cb,0xbf32c8c9,0xbf5d2d53,
0xbf039c3d,0xbf3daef9,0xbf676bd8,0xbf7d3aac,0xbf3fc767,0xbf6b4b0c,0xbf7f0e58,0xbf791298,0xbf6a09a7,0xbf7f4e6d,0xbf76ba07,0xbf514d3d,0xbf7e70b0,0xbf778bc5,0xbf4f7a1f,0xbf0b9a6b,
0x3400,0x2bc633c3,0x2f8b330e,0x315f31ed,0x3400,0x30c4326d,0x33a82ca5,0x3388ad64,0x3400,0x32dd301d,0x330eaf8b,0x2248b3fe,0x3400,0x33ea28b2,0x2ca5b3a8,0xb33baed7,
0x3400,0x33c3abc6,0xaf8bb30e,0xb1ed315f,0x3400,0x326db0c4,0xb3a8aca5,0x2d643388,0x3400,0x301db2dd,0xb30e2f8b,0x33fe2248,0x3400,0x28b2b3ea,0xaca533a8,0x2ed7b33b,
0x3400,0xabc6b3c3,0x2f8b330e,0xb15fb1ed,0x3400,0xb0c4b26d,0x33a82ca5,0xb3882d64,0x3400,0xb2ddb01d,0x330eaf8b,0xa24833fe,0x3400,0xb3eaa8b2,0x2ca5b3a8,0x333b2ed7,
0x3400,0xb3c32bc6,0xaf8bb30e,0x31edb15f,0x3400,0xb26d30c4,0xb3a8aca5,0xad64b388,0x3400,0xb01d32dd,0xb30e2f8b,0xb3fea248,0x3400,0xa8b233ea,0xaca533a8,0xaed7333b,
0x32a73072,0x33882d64,0x33f62646,0x33eaa8b2,0x3072b2a7,0xa248b3fe,0xb113b22f,0xb3c3abc6,0xb2a7b072,0xb33b2ed7,0xa64633f6,0x326d30c4,0xb07232a7,0x31ed315f,0x322fb113,0xb01db2dd,
0x32a73072,0x2d64b388,0xb3f6a646,0x28b233ea,0x3072b2a7,0xb3fe2248,0x3113322f,0x2bc6b3c3,0xb2a7b072,0x2ed7333b,0x2646b3f6,0xb0c4326d,0xb07232a7,0x315fb1ed,0xb22f3113,0x32ddb01d,
0x32a73072,0xb388ad64,0x33f62646,0xb3ea28b2,0x3072b2a7,0x224833fe,0xb113b22f,0x33c32bc6,0xb2a7b072,0x333baed7,0xa64633f6,0xb26db0c4,0xb07232a7,0xb1edb15f,0x322fb113,0x301d32dd,
0x32a73072,0xad643388,0xb3f6a646,0xa8b2b3ea,0x3072b2a7,0x33fea248,0x3113322f,0xabc633c3,0xb2a7b072,0xaed7b33b,0x2646b3f6,0x30c4b26d,0xb07232a7,0xb15f31ed,0xb22f3113,0xb2dd301d,
0x3364ae1f,0x326db0c4,0x3113b22f,0x2ed7b33b,0xb3642e1f,0xb01d32dd,0x264633f6,0x315f31ed,0x3364ae1f,0x28b2b3ea,0xb22fb113,0xb3882d64,0xb3642e1f,0x2bc633c3,0x33f6a646,0x2248b3fe,
0x3364ae1f,0xb0c4b26d,0xb113322f,0x333b2ed7,0xb3642e1f,0x32dd301d,0xa646b3f6,0xb1ed315f,0x3364ae1f,0xb3eaa8b2,0x322f3113,0xad64b388,0xb3642e1f,0x33c3abc6,0xb3f62646,0x33fe2248,
0x3364ae1f,0xb26d30c4,0x3113b22f,0xaed7333b,0xb3642e1f,0x301db2dd,0x264633f6,0xb15fb1ed,0x3364ae1f,0xa8b233ea,0xb22fb113,0x3388ad64,0xb3642e1f,0xabc6b3c3,0x33f6a646,0xa24833fe,
0x3364ae1f,0x30c4326d,0xb113322f,0xb33baed7,0xb3642e1f,0xb2ddb01d,0xa646b3f6,0x31edb15f,0x3364ae1f,0x33ea28b2,0x322f3113,0x2d643388,0xb3642e1f,0xb3c32bc6,0xb3f62646,0xb3fea248,
0x2a3eb3d9,0xa248b3fe,0xaca5b3a8,0xb01db2dd,0x33d92a3e,0x333baed7,0x2f8bb30e,0xa8b2b3ea,0xaa3e33d9,0x31ed315f,0x33a8aca5,0x2bc6b3c3,0xb3d9aa3e,0xad643388,0x330e2f8b,0x30c4b26d,
0x2a3eb3d9,0xb3fe2248,0x2ca533a8,0x32ddb01d,0x33d92a3e,0xaed7b33b,0xaf8b330e,0x33eaa8b2,0xaa3e33d9,0x315fb1ed,0xb3a82ca5,0x33c32bc6,0xb3d9aa3e,0x33882d64,0xb30eaf8b,0x326d30c4,
0x2a3eb3d9,0x224833fe,0xaca5b3a8,0x301d32dd,0x33d92a3e,0xb33b2ed7,0x2f8bb30e,0x28b233ea,0xaa3e33d9,0xb1edb15f,0x33a8aca5,0xabc633c3,0xb3d9aa3e,0x2d64b388,0x330e2f8b,0xb0c4326d,
0x2a3eb3d9,0x33fea248,0x2ca533a8,0xb2dd301d,0x33d92a3e,0x2ed7333b,0xaf8b330e,0xb3ea28b2,0xaa3e33d9,0xb15f31ed,0xb3a82ca5,0xb3c3abc6,0xb3d9aa3e,0xb388ad64,0xb30eaf8b,0xb26db0c4,
0x34000000,0x33c3abc6,0x330eaf8b,0x31edb15f,0x34000000,0x326db0c4,0x2ca5b3a8,0xad64b388,0x34000000,0x301db2dd,0xaf8bb30e,0xb3fea248,0x34000000,0x28b2b3ea,0xb3a8aca5,0xaed7333b,
0x34000000,0xabc6b3c3,0xb30e2f8b,0x315f31ed,0x34000000,0xb0c4b26d,0xaca533a8,0x3388ad64,0x34000000,0xb2ddb01d,0x2f8b330e,0x2248b3fe,0x34000000,0xb3eaa8b2,0x33a82ca5,0xb33baed7,
0x34000000,0xb3c32bc6,0x330eaf8b,0xb1ed315f,0x34000000,0xb26d30c4,0x2ca5b3a8,0x2d643388,0x34000000,0xb01d32dd,0xaf8bb30e,0x33fe2248,0x34000000,0xa8b233ea,0xb3a8aca5,0x2ed7b33b,
0x34000000,0x2bc633c3,0xb30e2f8b,0xb15fb1ed,0x34000000,0x30c4326d,0xaca533a8,0xb3882d64,0x34000000,0x32dd301d,0x2f8b330e,0xa24833fe,0x34000000,0x33ea28b2,0x33a82ca5,0x333b2ed7,
0x3072b2a7,0x2d64b388,0x2646b3f6,0xa8b2b3ea,0xb2a7b072,0xb3fe2248,0xb22f3113,0xabc633c3,0xb07232a7,0x2ed7333b,0x33f62646,0x30c4b26d,0x32a73072,0x315fb1ed,0xb113b22f,0xb2dd301d,
0x3072b2a7,0xb388ad64,0xa64633f6,0x33eaa8b2,0xb2a7b072,0x224833fe,0x322fb113,0xb3c3abc6,0xb07232a7,0x333baed7,0xb3f6a646,0x326d30c4,0x32a73072,0xb1edb15f,0x3113322f,0xb01db2dd,
0x3072b2a7,0xad643388,0x2646b3f6,0x28b233ea,0xb2a7b072,0x33fea248,0xb22f3113,0x2bc6b3c3,0xb07232a7,0xaed7b33b,0x33f62646,0xb0c4326d,0x32a73072,0xb15f31ed,0xb113b22f,0x32ddb01d,
0x3072b2a7,0x33882d64,0xa64633f6,0xb3ea28b2,0xb2a7b072,0xa248b3fe,0x322fb113,0x33c32bc6,0xb07232a7,0xb33b2ed7,0xb3f6a646,0xb26db0c4,0x32a73072,0x31ed315f,0x3113322f,0x301d32dd,
0xae1fb364,0xb0c4b26d,0xb22fb113,0xb33baed7,0x2e1f3364,0x32dd301d,0x33f6a646,0x31edb15f,0xae1fb364,0xb3eaa8b2,0xb113322f,0x2d643388,0x2e1f3364,0x33c3abc6,0xa646b3f6,0xb3fea248,
0xae1fb364,0xb26d30c4,0x322f3113,0x2ed7b33b,0x2e1f3364,0x301db2dd,0xb3f62646,0x315f31ed,0xae1fb364,0xa8b233ea,0x3113b22f,0xb3882d64,0x2e1f3364,0xabc6b3c3,0x264633f6,0x2248b3fe,
0xae1fb364,0x30c4326d,0xb22fb113,0x333b2ed7,0x2e1f3364,0xb2ddb01d,0x33f6a646,0xb1ed315f,0xae1fb364,0x33ea28b2,0xb113322f,0xad64b388,0x2e1f3364,0xb3c32bc6,0xa646b3f6,0x33fe2248,
0xae1fb364,0x326db0c4,0x322f3113,0xaed7333b,0x2e1f3364,0xb01d32dd,0xb3f62646,0xb15fb1ed,0xae1fb364,0x28b2b3ea,0x3113b22f,0x3388ad64,0x2e1f3364,0x2bc633c3,0x264633f6,0xa24833fe,
0xb3d9aa3e,0xb3fe2248,0xb3a82ca5,0xb2dd301d,0x2a3eb3d9,0xaed7b33b,0xb30eaf8b,0xb3ea28b2,0x33d92a3e,0x315fb1ed,0xaca5b3a8,0xb3c3abc6,0xaa3e33d9,0x33882d64,0x2f8bb30e,0xb26db0c4,
0xb3d9aa3e,0x224833fe,0x33a8aca5,0xb01db2dd,0x2a3eb3d9,0xb33b2ed7,0x330e2f8b,0xa8b2b3ea,0x33d92a3e,0xb1edb15f,0x2ca533a8,0x2bc6b3c3,0xaa3e33d9,0x2d64b388,0xaf8b330e,0x30c4b26d,
0xb3d9aa3e,0x33fea248,0xb3a82ca5,0x32ddb01d,0x2a3eb3d9,0x2ed7333b,0xb30eaf8b,0x33eaa8b2,0x33d92a3e,0xb15f31ed,0xaca5b3a8,0x33c32bc6,0xaa3e33d9,0xb388ad64,0x2f8bb30e,0x326d30c4,
0xb3d9aa3e,0xa248b3fe,0x33a8aca5,0x301d32dd,0x2a3eb3d9,0x333baed7,0x330e2f8b,0x28b233ea,0x33d92a3e,0x31ed315f,0x2ca533a8,0xabc633c3,0xaa3e33d9,0xad643388,0xaf8b330e,0xb0c4326d,
0x3400,0x3400,0x3400,0x3400,0x3400,0x2e1f3364,0x31a831a8,0x33642e1f,0x3400,0x31a831a8,0x34000000,0x31a8b1a8,0x3400,0x33642e1f,0x31a8b1a8,0xae1fb364,
0x3400,0x34000000,0xb400,0xb4008000,0x3400,0x3364ae1f,0xb1a8b1a8,0xae1f3364,0x3400,0x31a8b1a8,0xb4008000,0x31a831a8,0x3400,0x2e1fb364,0xb1a831a8,0x3364ae1f,
0x3400,0xb400,0x3400,0xb400,0x3400,0xae1fb364,0x31a831a8,0xb364ae1f,0x3400,0xb1a8b1a8,0x34000000,0xb1a831a8,0x3400,0xb364ae1f,0x31a8b1a8,0x2e1f3364,
0x3400,0xb4008000,0xb400,0x34000000,0x3400,0xb3642e1f,0xb1a8b1a8,0x2e1fb364,0x3400,0xb1a831a8,0xb4008000,0xb1a8b1a8,0x3400,0xae1f3364,0xb1a831a8,0xb3642e1f,
0x3400,0x3400,0x3400,0x3400,0x34000000,0x3364ae1f,0x31a8b1a8,0x2e1fb364,0xb400,0xb1a8b1a8,0xb4008000,0xb1a831a8,0xb4008000,0xae1f3364,0x31a831a8,0x3364ae1f,
0x3400,0x34000000,0xb400,0xb4008000,0x34000000,0xae1fb364,0xb1a831a8,0x33642e1f,0xb400,0xb1a831a8,0x34000000,0xb1a8b1a8,0xb4008000,0x33642e1f,0xb1a8b1a8,0x2e1f3364,
0x3400,0xb400,0x3400,0xb400,0x34000000,0xb3642e1f,0x31a8b1a8,0xae1f3364,0xb400,0x31a831a8,0xb4008000,0x31a8b1a8,0xb4008000,0x2e1fb364,0x31a831a8,0xb3642e1f,
0x3400,0xb4008000,0xb400,0x34000000,0x34000000,0x2e1f3364,0xb1a831a8,0xb364ae1f,0xb400,0x31a8b1a8,0x34000000,0x31a831a8,0xb4008000,0xb364ae1f,0xb1a8b1a8,0xae1fb364,
0x3400,0x3400,0x3400,0x3400,0xb400,0xae1fb364,0xb1a8b1a8,0xb364ae1f,0x3400,0x31a831a8,0x34000000,0x31a8b1a8,0xb400,0xb364ae1f,0xb1a831a8,0x2e1f3364,
0x3400,0x34000000,0xb400,0xb4008000,0xb400,0xb3642e1f,0x31a831a8,0x2e1fb364,0x3400,0x31a8b1a8,0xb4008000,0x31a831a8,0xb400,0xae1f3364,0x31a8b1a8,0xb3642e1f,
0x3400,0xb400,0x3400,0xb400,0xb400,0x2e1f3364,0xb1a8b1a8,0x33642e1f,0x3400,0xb1a8b1a8,0x34000000,0xb1a831a8,0xb400,0x33642e1f,0xb1a831a8,0xae1fb364,
0x3400,0xb4008000,0xb400,0x34000000,0xb400,0x3364ae1f,0x31a831a8,0xae1f3364,0x3400,0xb1a831a8,0xb4008000,0xb1a8b1a8,0xb400,0x2e1fb364,0x31a8b1a8,0x3364ae1f,
0x3400,0x3400,0x3400,0x3400,0xb4008000,0xb3642e1f,0xb1a831a8,0xae1f3364,0xb400,0xb1a8b1a8,0xb4008000,0xb1a831a8,0x34000000,0x2e1fb364,0xb1a8b1a8,0xb3642e1f,
0x3400,0x34000000,0xb400,0xb4008000,0xb4008000,0x2e1f3364,0x31a8b1a8,0xb364ae1f,0xb400,0xb1a831a8,0x34000000,0xb1a8b1a8,0x34000000,0xb364ae1f,0x31a831a8,0xae1fb364,
0x3400,0xb400,0x3400,0xb400,0xb4008000,0x3364ae1f,0xb1a831a8,0x2e1fb364,0xb400,0x31a831a8,0xb4008000,0x31a8b1a8,0x34000000,0xae1f3364,0xb1a8b1a8,0x3364ae1f,
0x3400,0xb4008000,0xb400,0x34000000,0xb4008000,0xae1fb364,0x31a8b1a8,0x33642e1f,0xb400,0x31a8b1a8,0x34000000,0x31a831a8,0x34000000,0x33642e1f,0x31a831a8,0x2e1f3364,
0x34000000,0x34000000,0x34000000,0x34000000,0x34000000,0x3364ae1f,0x31a8b1a8,0x2e1fb364,0x34000000,0x31a8b1a8,0xb400,0xb1a8b1a8,0x34000000,0x2e1fb364,0xb1a8b1a8,0xb3642e1f,
0x34000000,0xb400,0xb4008000,0x80003400,0x34000000,0xae1fb364,0xb1a831a8,0x33642e1f,0x34000000,0xb1a8b1a8,0x80003400,0x31a8b1a8,0x34000000,0xb364ae1f,0x31a831a8,0xae1fb364,
0x34000000,0xb4008000,0x34000000,0xb4008000,0x34000000,0xb3642e1f,0x31a8b1a8,0xae1f3364,0x34000000,0xb1a831a8,0xb400,0x31a831a8,0x34000000,0xae1f3364,0xb1a8b1a8,0x3364ae1f,
0x34000000,0x80003400,0xb4008000,0xb400,0x34000000,0x2e1f3364,0xb1a831a8,0xb364ae1f,0x34000000,0x31a831a8,0x80003400,0xb1a831a8,0x34000000,0x33642e1f,0x31a831a8,0x2e1f3364,
0x34000000,0x34000000,0x34000000,0x34000000,0xb400,0xae1fb364,0xb1a8b1a8,0xb364ae1f,0xb4008000,0xb1a831a8,0x80003400,0x31a831a8,0x80003400,0x33642e1f,0x31a8b1a8,0xae1fb364,
0x34000000,0xb400,0xb4008000,0x80003400,0xb400,0xb3642e1f,0x31a831a8,0x2e1fb364,0xb4008000,0x31a831a8,0xb400,0xb1a831a8,0x80003400,0x2e1fb364,0xb1a831a8,0x3364ae1f,
0x34000000,0xb4008000,0x34000000,0xb4008000,0xb400,0x2e1f3364,0xb1a8b1a8,0x33642e1f,0xb4008000,0x31a8b1a8,0x80003400,0xb1a8b1a8,0x80003400,0xb364ae1f,0x31a8b1a8,0x2e1f3364,
0x34000000,0x80003400,0xb4008000,0xb400,0xb400,0x3364ae1f,0x31a831a8,0xae1f3364,0xb4008000,0xb1a8b1a8,0xb400,0x31a8b1a8,0x80003400,0xae1f3364,0xb1a831a8,0xb3642e1f,
0x34000000,0x34000000,0x34000000,0x34000000,0xb4008000,0xb3642e1f,0xb1a831a8,0xae1f3364,0x34000000,0x31a8b1a8,0xb400,0xb1a8b1a8,0xb4008000,0xae1f3364,0x31a831a8,0x3364ae1f,
0x34000000,0xb400,0xb4008000,0x80003400,0xb4008000,0x2e1f3364,0x31a8b1a8,0xb364ae1f,0x34000000,0xb1a8b1a8,0x80003400,0x31a8b1a8,0xb4008000,0x33642e1f,0xb1a8b1a8,0x2e1f3364,
0x34000000,0xb4008000,0x34000000,0xb4008000,0xb4008000,0x3364ae1f,0xb1a831a8,0x2e1fb364,0x34000000,0xb1a831a8,0xb400,0x31a831a8,0xb4008000,0x2e1fb364,0x31a831a8,0xb3642e1f,
0x34000000,0x80003400,0xb4008000,0xb400,0xb4008000,0xae1fb364,0x31a8b1a8,0x33642e1f,0x34000000,0x31a831a8,0x80003400,0xb1a831a8,0xb4008000,0xb364ae1f,0xb1a8b1a8,0xae1fb364,
0x34000000,0x34000000,0x34000000,0x34000000,0x80003400,0x2e1f3364,0x31a831a8,0x33642e1f,0xb4008000,0xb1a831a8,0x80003400,0x31a831a8,0xb400,0xb364ae1f,0xb1a831a8,0x2e1f3364,
0x34000000,0xb400,0xb4008000,0x80003400,0x80003400,0x3364ae1f,0xb1a8b1a8,0xae1f3364,0xb4008000,0x31a831a8,0xb400,0xb1a831a8,0xb400,0xae1f3364,0x31a8b1a8,0xb3642e1f,
0x34000000,0xb4008000,0x34000000,0xb4008000,0x80003400,0xae1fb364,0x31a831a8,0xb364ae1f,0xb4008000,0x31a8b1a8,0x80003400,0xb1a8b1a8,0xb400,0x33642e1f,0xb1a831a8,0xae1fb364,
0x34000000,0x80003400,0xb4008000,0xb400,0x80003400,0xb3642e1f,0xb1a8b1a8,0x2e1fb364,0xb4008000,0xb1a8b1a8,0xb400,0x31a8b1a8,0xb400,0x2e1fb364,0x31a8b1a8,0x3364ae1f,
0x3400,0x3400,0x3400,0x3400,0x3400,0xae1f3364,0xb1a831a8,0xb3642e1f,0x3400,0xb1a831a8,0xb4000000,0xb1a8b1a8,0x3400,0xb3642e1f,0xb1a8b1a8,0x2e1fb364,
0x3400,0xb4000000,0x8000b400,0x34008000,0x3400,0xb364ae1f,0x31a8b1a8,0x2e1f3364,0x3400,0xb1a8b1a8,0x34008000,0xb1a831a8,0x3400,0xae1fb364,0x31a831a8,0xb364ae1f,
0x3400,0x8000b400,0x3400,0x8000b400,0x3400,0x2e1fb364,0xb1a831a8,0x3364ae1f,0x3400,0x31a8b1a8,0xb4000000,0x31a831a8,0x3400,0x3364ae1f,0xb1a8b1a8,0xae1f3364,
0x3400,0x34008000,0x8000b400,0xb4000000,0x3400,0x33642e1f,0x31a8b1a8,0xae1fb364,0x3400,0x31a831a8,0x34008000,0x31a8b1a8,0x3400,0x2e1f3364,0x31a831a8,0x33642e1f,
0x3400,0x3400,0x3400,0x3400,0xb4000000,0xb364ae1f,0xb1a8b1a8,0xae1fb364,0x8000b400,0x31a8b1a8,0x34008000,0x31a831a8,0x34008000,0x2e1f3364,0xb1a831a8,0xb364ae1f,
0x3400,0xb4000000,0x8000b400,0x34008000,0xb4000000,0x2e1fb364,0x31a831a8,0xb3642e1f,0x8000b400,0x31a831a8,0xb4000000,0x31a8b1a8,0x34008000,0xb3642e1f,0x31a8b1a8,0xae1f3364,
0x3400,0x8000b400,0x3400,0x8000b400,0xb4000000,0x33642e1f,0xb1a8b1a8,0x2e1f3364,0x8000b400,0xb1a831a8,0x34008000,0xb1a8b1a8,0x34008000,0xae1fb364,0xb1a831a8,0x33642e1f,
0x3400,0x34008000,0x8000b400,0xb4000000,0xb4000000,0xae1f3364,0x31a831a8,0x3364ae1f,0x8000b400,0xb1a8b1a8,0xb4000000,0xb1a831a8,0x34008000,0x3364ae1f,0x31a8b1a8,0x2e1fb364,
0x3400,0x3400,0x3400,0x3400,0x8000b400,0x2e1fb364,0x31a8b1a8,0x3364ae1f,0x3400,0xb1a831a8,0xb4000000,0xb1a8b1a8,0x8000b400,0x3364ae1f,0x31a831a8,0xae1f3364,
0x3400,0xb4000000,0x8000b400,0x34008000,0x8000b400,0x33642e1f,0xb1a831a8,0xae1fb364,0x3400,0xb1a8b1a8,0x34008000,0xb1a831a8,0x8000b400,0x2e1f3364,0xb1a8b1a8,0x33642e1f,
0x3400,0x8000b400,0x3400,0x8000b400,0x8000b400,0xae1f3364,0x31a8b1a8,0xb3642e1f,0x3400,0x31a8b1a8,0xb4000000,0x31a831a8,0x8000b400,0xb3642e1f,0x31a831a8,0x2e1fb364,
0x3400,0x34008000,0x8000b400,0xb4000000,0x8000b400,0xb364ae1f,0xb1a831a8,0x2e1f3364,0x3400,0x31a831a8,0x34008000,0x31a8b1a8,0x8000b400,0xae1fb364,0xb1a8b1a8,0xb364ae1f,
0x3400,0x3400,0x3400,0x3400,0x34008000,0x33642e1f,0x31a831a8,0x2e1f3364,0x8000b400,0x31a8b1a8,0x34008000,0x31a831a8,0xb4000000,0xae1fb364,0x31a8b1a8,0x33642e1f,
0x3400,0xb4000000,0x8000b400,0x34008000,0x34008000,0xae1f3364,0xb1a8b1a8,0x3364ae1f,0x8000b400,0x31a831a8,0xb4000000,0x31a8b1a8,0xb4000000,0x3364ae1f,0xb1a831a8,0x2e1fb364,
0x3400,0x8000b400,0x3400,0x8000b400,0x34008000,0xb364ae1f,0x31a831a8,0xae1fb364,0x8000b400,0xb1a831a8,0x34008000,0xb1a8b1a8,0xb4000000,0x2e1f3364,0x31a8b1a8,0xb364ae1f,
0x3400,0x34008000,0x8000b400,0xb4000000,0x34008000,0x2e1fb364,0xb1a8b1a8,0xb3642e1f,0x8000b400,0xb1a8b1a8,0xb4000000,0xb1a831a8,0xb4000000,0xb3642e1f,0xb1a831a8,0xae1f3364,
0x34000000,0x34000000,0x34000000,0x34000000,0x34000000,0x33642e1f,0x31a831a8,0x2e1f3364,0x34000000,0x31a831a8,0x3400,0xb1a831a8,0x34000000,0x2e1f3364,0xb1a831a8,0xb364ae1f,
0x34000000,0x3400,0xb4000000,0x8000b400,0x34000000,0xae1f3364,0xb1a8b1a8,0x3364ae1f,0x34000000,0xb1a831a8,0x8000b400,0x31a831a8,0x34000000,0xb3642e1f,0x31a8b1a8,0xae1f3364,
0x34000000,0xb4000000,0x34000000,0xb4000000,0x34000000,0xb364ae1f,0x31a831a8,0xae1fb364,0x34000000,0xb1a8b1a8,0x3400,0x31a8b1a8,0x34000000,0xae1fb364,0xb1a831a8,0x33642e1f,
0x34000000,0x8000b400,0xb4000000,0x3400,0x34000000,0x2e1fb364,0xb1a8b1a8,0xb3642e1f,0x34000000,0x31a8b1a8,0x8000b400,0xb1a8b1a8,0x34000000,0x3364ae1f,0x31a8b1a8,0x2e1fb364,
0x34000000,0x34000000,0x34000000,0x34000000,0x3400,0xae1f3364,0xb1a831a8,0xb3642e1f,0xb4000000,0xb1a8b1a8,0x8000b400,0x31a8b1a8,0x8000b400,0x3364ae1f,0x31a831a8,0xae1f3364,
0x34000000,0x3400,0xb4000000,0x8000b400,0x3400,0xb364ae1f,0x31a8b1a8,0x2e1f3364,0xb4000000,0x31a8b1a8,0x3400,0xb1a8b1a8,0x8000b400,0x2e1f3364,0xb1a8b1a8,0x33642e1f,
0x34000000,0xb4000000,0x34000000,0xb4000000,0x3400,0x2e1fb364,0xb1a831a8,0x3364ae1f,0xb4000000,0x31a831a8,0x8000b400,0xb1a831a8,0x8000b400,0xb3642e1f,0x31a831a8,0x2e1fb364,
0x34000000,0x8000b400,0xb4000000,0x3400,0x3400,0x33642e1f,0x31a8b1a8,0xae1fb364,0xb4000000,0xb1a831a8,0x3400,0x31a831a8,0x8000b400,0xae1fb364,0xb1a8b1a8,0xb364ae1f,
0x34000000,0x34000000,0x34000000,0x34000000,0xb4000000,0xb364ae1f,0xb1a8b1a8,0xae1fb364,0x34000000,0x31a831a8,0x3400,0xb1a831a8,0xb4000000,0xae1fb364,0x31a8b1a8,0x33642e1f,
0x34000000,0x3400,0xb4000000,0x8000b400,0xb4000000,0x2e1fb364,0x31a831a8,0xb3642e1f,0x34000000,0xb1a831a8,0x8000b400,0x31a831a8,0xb4000000,0x3364ae1f,0xb1a831a8,0x2e1fb364,
0x34000000,0xb4000000,0x34000000,0xb4000000,0xb4000000,0x33642e1f,0xb1a8b1a8,0x2e1f3364,0x34000000,0xb1a8b1a8,0x3400,0x31a8b1a8,0xb4000000,0x2e1f3364,0x31a8b1a8,0xb364ae1f,
0x34000000,0x8000b400,0xb4000000,0x3400,0xb4000000,0xae1f3364,0x31a831a8,0x3364ae1f,0x34000000,0x31a8b1a8,0x8000b400,0xb1a8b1a8,0xb4000000,0xb3642e1f,0xb1a831a8,0xae1f3364,
0x34000000,0x34000000,0x34000000,0x34000000,0x8000b400,0x2e1fb364,0x31a8b1a8,0x3364ae1f,0xb4000000,0xb1a8b1a8,0x8000b400,0x31a8b1a8,0x3400,0xb3642e1f,0xb1a8b1a8,0x2e1fb364,
0x34000000,0x3400,0xb4000000,0x8000b400,0x8000b400,0x33642e1f,0xb1a831a8,0xae1fb364,0xb4000000,0x31a8b1a8,0x3400,0xb1a8b1a8,0x3400,0xae1fb364,0x31a831a8,0xb364ae1f,
0x34000000,0xb4000000,0x34000000,0xb4000000,0x8000b400,0xae1f3364,0x31a8b1a8,0xb3642e1f,0xb4000000,0x31a831a8,0x8000b400,0xb1a831a8,0x3400,0x3364ae1f,0xb1a8b1a8,0xae1f3364,
0x34000000,0x8000b400,0xb4000000,0x3400,0x8000b400,0xb364ae1f,0xb1a831a8,0x2e1f3364,0xb4000000,0xb1a831a8,0x3400,0x31a831a8,0x3400,0x2e1f3364,0x31a831a8,0x33642e1f,
0x3400,0x3400,0x3400,0x3400,0xabc633c3,0xb0c4326d,0xb2dd301d,0xb3ea28b2,0xaf8b330e,0xb3a82ca5,0xb30eaf8b,0xaca5b3a8,0xb15f31ed,0xb388ad64,0xa248b3fe,0x333baed7,
0xb2a73072,0xb072b2a7,0x32a7b072,0x307232a7,0xb3882d64,0x2248b3fe,0x333b2ed7,0xb1ed315f,0xb3f62646,0x3113b22f,0x264633f6,0xb22fb113,0xb3eaa8b2,0x33c3abc6,0xb26d30c4,0x301db2dd,
0xb364ae1f,0x33642e1f,0xb364ae1f,0x33642e1f,0xb26db0c4,0x301d32dd,0xa8b2b3ea,0xabc633c3,0xb113b22f,0xa64633f6,0x322fb113,0xb3f6a646,0xaed7b33b,0xb15f31ed,0x33882d64,0xa248b3fe,
0xaa3eb3d9,0xb3d92a3e,0x2a3e33d9,0x33d9aa3e,0x2248b3fe,0xb33baed7,0xb1ed315f,0x2d643388,0x2ca5b3a8,0xaf8bb30e,0xb3a8aca5,0xb30e2f8b,0x301db2dd,0x28b2b3ea,0xabc6b3c3,0xb0c4b26d,
0x3400,0x3400,0x3400,0x3400,0xb3c3abc6,0xb26db0c4,0xb01db2dd,0xa8b2b3ea,0x2f8bb30e,0x33a8aca5,0x330e2f8b,0x2ca533a8,0x31ed315f,0xad643388,0xb3fe2248,0xaed7b33b,
0xb2a73072,0xb072b2a7,0x32a7b072,0x307232a7,0xad64b388,0x33fe2248,0xaed7333b,0xb15fb1ed,0x33f6a646,0xb113322f,0xa646b3f6,0x322f3113,0xa8b233ea,0xabc6b3c3,0x30c4326d,0xb2ddb01d,
0xb364ae1f,0x33642e1f,0xb364ae1f,0x33642e1f,0x30c4b26d,0xb2dd301d,0x33eaa8b2,0xb3c3abc6,0x3113322f,0x2646b3f6,0xb22f3113,0x33f62646,0xb33b2ed7,0x31ed315f,0x2d64b388,0xb3fe2248,
0xaa3eb3d9,0xb3d92a3e,0x2a3e33d9,0x33d9aa3e,0x33fe2248,0x2ed7b33b,0xb15fb1ed,0xb3882d64,0xaca533a8,0x2f8b330e,0x33a82ca5,0x330eaf8b,0xb2ddb01d,0xb3eaa8b2,0xb3c32bc6,0xb26d30c4,
0x3400,0x3400,0x3400,0x3400,0x2bc6b3c3,0x30c4b26d,0x32ddb01d,0x33eaa8b2,0xaf8b330e,0xb3a82ca5,0xb30eaf8b,0xaca5b3a8,0x315fb1ed,0x33882d64,0x224833fe,0xb33b2ed7,
0xb2a73072,0xb072b2a7,0x32a7b072,0x307232a7,0x3388ad64,0xa24833fe,0xb33baed7,0x31edb15f,0xb3f62646,0x3113b22f,0x264633f6,0xb22fb113,0x33ea28b2,0xb3c32bc6,0x326db0c4,0xb01d32dd,
0xb364ae1f,0x33642e1f,0xb364ae1f,0x33642e1f,0x326d30c4,0xb01db2dd,0x28b233ea,0x2bc6b3c3,0xb113b22f,0xa64633f6,0x322fb113,0xb3f6a646,0x2ed7333b,0x315fb1ed,0xb388ad64,0x224833fe,
0xaa3eb3d9,0xb3d92a3e,0x2a3e33d9,0x33d9aa3e,0xa24833fe,0x333b2ed7,0x31edb15f,0xad64b388,0x2ca5b3a8,0xaf8bb30e,0xb3a8aca5,0xb30e2f8b,0xb01d32dd,0xa8b233ea,0x2bc633c3,0x30c4326d,
0x3400,0x3400,0x3400,0x3400,0x33c32bc6,0x326d30c4,0x301d32dd,0x28b233ea,0x2f8bb30e,0x33a8aca5,0x330e2f8b,0x2ca533a8,0xb1edb15f,0x2d64b388,0x33fea248,0x2ed7333b,
0xb2a73072,0xb072b2a7,0x32a7b072,0x307232a7,0x2d643388,0xb3fea248,0x2ed7b33b,0x315f31ed,0x33f6a646,0xb113322f,0xa646b3f6,0x322f3113,0x28b2b3ea,0x2bc633c3,0xb0c4b26d,0x32dd301d,
0xb364ae1f,0x33642e1f,0xb364ae1f,0x33642e1f,0xb0c4326d,0x32ddb01d,0xb3ea28b2,0x33c32bc6,0x3113322f,0x2646b3f6,0xb22f3113,0x33f62646,0x333baed7,0xb1edb15f,0xad643388,0x33fea248,
0xaa3eb3d9,0xb3d92a3e,0x2a3e33d9,0x33d9aa3e,0xb3fea248,0xaed7333b,0x315f31ed,0x3388ad64,0xaca533a8,0x2f8b330e,0x33a82ca5,0x330eaf8b,0x32dd301d,0x33ea28b2,0x33c3abc6,0x326db0c4,
0x34000000,0x34000000,0x34000000,0x34000000,0x33c32bc6,0x326d30c4,0x301d32dd,0x28b233ea,0x330e2f8b,0x2ca533a8,0xaf8b330e,0xb3a82ca5,0x31ed315f,0xad643388,0xb3fe2248,0xaed7b33b,
0x307232a7,0xb2a73072,0xb072b2a7,0x32a7b072,0x2d643388,0xb3fea248,0x2ed7b33b,0x315f31ed,0x264633f6,0xb22fb113,0x33f6a646,0xb113322f,0xa8b233ea,0xabc6b3c3,0x30c4326d,0xb2ddb01d,
0xae1f3364,0x2e1fb364,0xae1f3364,0x2e1fb364,0xb0c4326d,0x32ddb01d,0xb3ea28b2,0x33c32bc6,0xb22f3113,0x33f62646,0xb113b22f,0xa64633f6,0xb33b2ed7,0x31ed315f,0x2d64b388,0xb3fe2248,
0xb3d92a3e,0x2a3e33d9,0x33d9aa3e,0xaa3eb3d9,0xb3fea248,0xaed7333b,0x315f31ed,0x3388ad64,0xb3a8aca5,0xb30e2f8b,0xaca533a8,0x2f8b330e,0xb2ddb01d,0xb3eaa8b2,0xb3c32bc6,0xb26d30c4,
0x34000000,0x34000000,0x34000000,0x34000000,0xabc633c3,0xb0c4326d,0xb2dd301d,0xb3ea28b2,0xb30eaf8b,0xaca5b3a8,0x2f8bb30e,0x33a8aca5,0x315fb1ed,0x33882d64,0x224833fe,0xb33b2ed7,
0x307232a7,0xb2a73072,0xb072b2a7,0x32a7b072,0xb3882d64,0x2248b3fe,0x333b2ed7,0xb1ed315f,0xa646b3f6,0x322f3113,0xb3f62646,0x3113b22f,0x33ea28b2,0xb3c32bc6,0x326db0c4,0xb01d32dd,
0xae1f3364,0x2e1fb364,0xae1f3364,0x2e1fb364,0xb26db0c4,0x301d32dd,0xa8b2b3ea,0xabc633c3,0x322fb113,0xb3f6a646,0x3113322f,0x2646b3f6,0x2ed7333b,0x315fb1ed,0xb388ad64,0x224833fe,
0xb3d92a3e,0x2a3e33d9,0x33d9aa3e,0xaa3eb3d9,0x2248b3fe,0xb33baed7,0xb1ed315f,0x2d643388,0x33a82ca5,0x330eaf8b,0x2ca5b3a8,0xaf8bb30e,0xb01d32dd,0xa8b233ea,0x2bc633c3,0x30c4326d,
0x34000000,0x34000000,0x34000000,0x34000000,0xb3c3abc6,0xb26db0c4,0xb01db2dd,0xa8b2b3ea,0x330e2f8b,0x2ca533a8,0xaf8b330e,0xb3a82ca5,0xb1edb15f,0x2d64b388,0x33fea248,0x2ed7333b,
0x307232a7,0xb2a73072,0xb072b2a7,0x32a7b072,0xad64b388,0x33fe2248,0xaed7333b,0xb15fb1ed,0x264633f6,0xb22fb113,0x33f6a646,0xb113322f,0x28b2b3ea,0x2bc633c3,0xb0c4b26d,0x32dd301d,
0xae1f3364,0x2e1fb364,0xae1f3364,0x2e1fb364,0x30c4b26d,0xb2dd301d,0x33eaa8b2,0xb3c3abc6,0xb22f3113,0x33f62646,0xb113b22f,0xa64633f6,0x333baed7,0xb1edb15f,0xad643388,0x33fea248,
0xb3d92a3e,0x2a3e33d9,0x33d9aa3e,0xaa3eb3d9,0x33fe2248,0x2ed7b33b,0xb15fb1ed,0xb3882d64,0xb3a8aca5,0xb30e2f8b,0xaca533a8,0x2f8b330e,0x32dd301d,0x33ea28b2,0x33c3abc6,0x326db0c4,
0x34000000,0x34000000,0x34000000,0x34000000,0x2bc6b3c3,0x30c4b26d,0x32ddb01d,0x33eaa8b2,0xb30eaf8b,0xaca5b3a8,0x2f8bb30e,0x33a8aca5,0xb15f31ed,0xb388ad64,0xa248b3fe,0x333baed7,
0x307232a7,0xb2a73072,0xb072b2a7,0x32a7b072,0x3388ad64,0xa24833fe,0xb33baed7,0x31edb15f,0xa646b3f6,0x322f3113,0xb3f62646,0x3113b22f,0xb3eaa8b2,0x33c3abc6,0xb26d30c4,0x301db2dd,
0xae1f3364,0x2e1fb364,0xae1f3364,0x2e1fb364,0x326d30c4,0xb01db2dd,0x28b233ea,0x2bc6b3c3,0x322fb113,0xb3f6a646,0x3113322f,0x2646b3f6,0xaed7b33b,0xb15f31ed,0x33882d64,0xa248b3fe,
0xb3d92a3e,0x2a3e33d9,0x33d9aa3e,0xaa3eb3d9,0xa24833fe,0x333b2ed7,0x31edb15f,0xad64b388,0x33a82ca5,0x330eaf8b,0x2ca5b3a8,0xaf8bb30e,0x301db2dd,0x28b2b3ea,0xabc6b3c3,0xb0c4b26d,
0x3f800000,0x3f7ff84a,0x3f7fe129,0x3f7fba9e,0x3f800000,0x3f7fcbe2,0x3f7f2f9d,0x3f7e2b72,0x3f800000,0x3f7f7808,0x3f7de0b1,0x3f7b3bab,0x3f800000,0x3f7efcc9,0x3f7bf531,0x3f76ef5b,
0x3f800000,0x3f7e5a38,0x3f796e4e,0x3f714c7a,0x3f800000,0x3f7d906e,0x3f764d97,0x3f6a5ad9,0x3f800000,0x3f7c9f8a,0x3f7294f8,0x3f62241a,0x3f800000,0x3f7b87b2,0x3f6e46be,0x3f58b3a1,
0x3f800000,0x3f7a4910,0x3f696591,0x3f4e1689,0x3f800000,0x3f78e3d6,0x3f63f473,0x3f425b89,0x3f800000,0x3f77583a,0x3f5df6be,0x3f3592e7,0x3f800000,0x3f75a67b,0x3f577026,0x3f27ce61,
0x3f800000,0x3f73ced9,0x3f5064af,0x3f19210f,0x3f800000,0x3f71d19f,0x3f48d8b3,0x3f099f4e,0x3f800000,0x3f6faf1b,0x3f40d0da,0x3ef2bd43,0x3f800000,0x3f6d67a1,0x3f385216,0x3ed0eb2a,
0x3f7f84ab,0x3f7f3f54,0x3f7eea9d,0x3f7e868b,0x3f7cbfc9,0x3f7aed37,0x3f78b47b,0x3f76167a,0x3f778bc5,0x3f72d4eb,0x3f6d1c1d,0x3f666771,0x3f6ff573,0x3f671599,0x3f5c61c7,0x3f4fefa8,
0x3f660f88,0x3f57dc7f,0x3f46e22a,0x3f33587a,0x3f59f26a,0x3f456439,0x3f2d1469,0x3f11796b,0x3f4bbbf8,0x3f2ff3e6,0x3f0f8784,0x3ed6a399,0x3f3b8f3b,0x3f17de12,0x3eddbe79,0x3e840270,
0x3f299415,0x3efafef7,0x3e97a117,0x3db5f8da,0x3f15f6d9,0x3ec27b55,0x3e1c76de,0xbda973ba,0x3f00e7e4,0x3e870ada,0x3bc90f88,0xbe80f8c0,0x3ed53641,0x3e1324ca,0xbe1008b7,0xbed3c867,
0x3ea68f12,0x3cafea69,0xbe919ddd,0xbf102dd5,0x3e6c9a7f,0xbdcefdb7,0xbed8106b,0xbf3238aa,0x3e09cf86,0xbe636c83,0xbf0cead0,0xbf4f0417,0x3d16c32c,0xbeadf73c,0xbf2ac082,0xbf65b710,
0x3f7e1324,0x3f7d906e,0x3f7cfe73,0x3f7c5d3a,0x3f731447,0x3f6faf1b,0x3f6be858,0x3f67c187,0x3f5ebe05,0x3f5627fe,0x3f4cae79,0x3f425b89,0x3f41d870,0x3f3238aa,0x3f212ff9,0x3f0ee0db,
0x3f1d7fd1,0x3f05a02c,0x3ed8106b,0x3ea21887,0x3ee63375,0x3ea5129f,0x3e419b37,0x3d55880e,0x3e888e93,0x3ddb7d37,0xbd621469,0xbe5d4b1a,0x3d96a905,0xbde7fa9a,0xbe97a117,0xbeeff7fb,
0xbdfab273,0xbea80b1f,0xbf04f484,0xbf2ff3e6,0xbea09ae5,0xbf06f686,0xbf36206c,0xbf5b2c79,0xbefc5d27,0xbf33587a,0xbf5c61c7,0xbf76841b,0xbf273656,0xbf570348,0xbf756d97,0xbf7fffb1,
0xbf49d112,0xbf703b37,0xbf7fc38f,0xbf76ef5b,0xbf64aa59,0xbf7dc68c,0xbf7ac516,0xbf5bfb34,0xbf76ba07,0xbf7efcc9,0xbf66becc,0xbf311722,0xbf7f4e6d,0xbf73ced9,0xbf44e3f5,0xbef2bd43,
0x3f7baccd,0x3f7aed37,0x3f7a1e84,0x3f7940c0,0x3f633c5a,0x3f5e5aa6,0x3f591e6a,0x3f5389c7,0x3f373a23,0x3f2b561b,0x3f1ebc12,0x3f11796b,0x3ef6e0cb,0x3ece0bea,0x3ea395c5,0x3e6fa8b2,
0x3e541501,0x3dc27c39,0xbc96c9b6,0xbe06b26e,0xbdafb680,0xbe636c83,0xbeb554ec,0xbef58035,0xbebe1d4a,0xbf044889,0xbf26050a,0xbf436051,0xbf1ff6cb,0xbf425b89,0xbf5df6be,0xbf71d19f,
0xbf531849,0xbf6cd007,0xbf7bf531,0xbf7fdaaf,0xbf740bdd,0xbf7f5ce9,0xbf7c7eb0,0xbf6b99fb,0xbf7ffb11,0xbf7822d1,0xbf5f8327,0xbf37c655,0xbf75dec6,0xbf57dc7f,0xbf286605,0xbed6a399,
0xbf5695e5,0xbf21cbfb,0xbebb31a0,0xbd83db0a,0xbf24d225,0xbeb6ccc3,0xbcfb49ba,0x3e9920fe,0xbec9b953,0xbd3c6dd5,0x3e9d9e78,0x3f1f599f,0xbde1bc2e,0x3e8a11f7,0x3f1c420c,0x3f5cc7d1,
0x0,0x3c7b514b,0x3cfb49ba,0x3d3c6dd5,0x0,0x3d2351cb,0x3da3308c,0x3df475c0,0x0,0x3d83db0a,0x3e039502,0x3e44b0b9,0x0,0x3db5f8da,0x3e354098,0x3e870ada,
0x0,0x3de7fa9a,0x3e667c66,0x3eab0201,0x0,0x3e0cec4a,0x3e8b9507,0x3ece0bea,0x0,0x3e25c58c,0x3ea395c5,0x3eeff7fb,0x0,0x3e3e853e,0x3ebb31a0,0x3f084b92,
0x0,0x3e57278f,0x3ed25a09,0x3f17de12,0x0,0x3e6fa8b2,0x3ee900b7,0x3f269de3,0x0,0x3e840270,0x3eff17b2,0x3f34768f,0x0,0x3e901c2c,0x3f0a48ad,0x3f4154e1,
0x0,0x3e9c1faf,0x3f14b039,0x3f4d26fd,0x0,0x3ea80b1f,0x3f1ebc12,0x3f57dc7f,0x0,0x3eb3dca5,0x3f286605,0x3f61668a,0x0,0x3ebf926f,0x3f31a81d,0x3f69b7e4,
0x3d7b2b74,0x3d9cecf9,0x3dbc3ac3,0x3ddb7d37,0x3e22abb6,0x3e4ada4f,0x3e72b651,0x3e8d17c0,0x3e827dc0,0x3ea21887,0x3ec1071e,0x3edf28a9,0x3eb263ef,0x3edc53c1,0x3f0242b1,0x3f1553b7,
0x3ee0924f,0x3f099f4e,0x3f212ff9,0x3f36ad7f,0x3f064b82,0x3f2302d3,0x3f3ca003,0x3f52a649,0x3f1b02c6,0x3f39f2ac,0x3f53fac3,0x3f686b39,0x3f2e3bde,0x3f4e1689,0x3f66becc,0x3f77583a,
0x3f3fc767,0x3f5f20db,0x3f748422,0x3f7efcc9,0x3f4f7a1f,0x3f6cd007,0x3f7cfe73,0x3f7f1f49,0x3f5d2d53,0x3f76ef5b,0x3f7ffec4,0x3f77beb7,0x3f68bf3c,0x3f7d57de,0x3f7d7474,0x3f6912ae,
0x3f721352,0x3f7ff0e3,0x3f756d97,0x3f5389c7,0x3f791298,0x3f7eb069,0x3f6816a8,0x3f37c655,0x3f7dabcc,0x3f799b43,0x3f55b993,0x3f16999f,0x3f7fd397,0x3f70c501,0x3f3ebc1b,0x3ee1fb6a,
0x3dfab273,0x3e0cec4a,0x3e1c76de,0x3e2bf804,0x3ea09ae5,0x3eb3dca5,0x3ec6d529,0x3ed97cb9,0x3efc5d27,0x3f0c42c9,0x3f19c200,0x3f269de3,0x3f273656,0x3f37c655,0x3f46e22a,0x3f546b3b,
0x3f49d112,0x3f5a5ba0,0x3f6816a8,0x3f72d4eb,0x3f64aa59,0x3f725470,0x3f7b61fc,0x3f7fa6e3,0x3f76ba07,0x3f7e868b,0x3f7f9c18,0x3f79f35e,0x3f7f4e6d,0x3f7e5a38,0x3f748422,0x3f62241a,
0x3f7e1324,0x3f71d19f,0x3f5ac450,0x3f39f2ac,0x3f731447,0x3f5988ad,0x3f33e7bc,0x3f044889,0x3f5ebe05,0x3f36ad7f,0x3f0242b1,0x3e8a11f7,0x3f41d870,0x3f0af1b7,0x3e919ddd,0x3b490fc6,
0x3f1d7fd1,0x3eb0eacb,0x3d2fe007,0xbe870ada,0x3ee63375,0x3e06b26e,0xbe4dee60,0xbf02ef9f,0x3e888e93,0xbdb5f8da,0xbeddbe79,0xbf38dd65,0x3d96a905,0xbe9c1faf,0xbf239da9,0xbf61668a,
0x3e3b6ecf,0x3e4ada4f,0x3e5a3997,0x3e698bba,0x3eebcbbb,0x3efdbabb,0x3f07a136,0x3f102dd5,0x3f32c8c9,0x3f3e35c5,0x3f48d8b3,0x3f52a649,0x3f604621,0x3f6a5ad9,0x3f7294f8,0x3f78e3d6,
0x3f7a7302,0x3f7ed7d4,0x3f7ff4e6,0x3f7dc68c,0x3f7f0e58,0x3f799b43,0x3f6f6830,0x3f60a6cf,0x3f6db293,0x3f5b2c79,0x3f42de29,0x3f256bcb,0x3f47de65,0x3f269de3,0x3eff17b2,0x3ea80b1f,
0x3f10d3cd,0x3ec27b55,0x3e354098,0xbd0a342f,0x3e9aa086,0x3d9064b4,0xbe28defc,0xbec8477c,0x3c490e90,0xbe7bdba4,0xbef9a02d,0xbf3238aa,0xbe8e9a22,0xbf099f4e,0xbf40d0da,0xbf686b39,
0xbf0b9a6b,0xbf466354,0xbf6e46be,0xbf7f7808,0xbf43e200,0xbf6f20b0,0xbf7fe129,0xbf74484b,0xbf6b4b0c,0xbf7fba9e,0xbf73913f,0xbf485bca,0xbf7e70b0,0xbf76841b,0xbf4ac77f,0xbf019573,
0x3f800000,0x3f800000,0x3f800000,0x3f800000,0x3f7ff84a,0x3f7fcbe2,0x3f7f7808,0x3f7efcc9,0x3f7fe129,0x3f7f2f9d,0x3f7de0b1,0x3f7bf531,0x3f7fba9e,0x3f7e2b72,0x3f7b3bab,0x3f76ef5b,
0x3f7f84ab,0x3f7cbfc9,0x3f778bc5,0x3f6ff573,0x3f7f3f54,0x3f7aed37,0x3f72d4eb,0x3f671599,0x3f7eea9d,0x3f78b47b,0x3f6d1c1d,0x3f5c61c7,0x3f7e868b,0x3f76167a,0x3f666771,0x3f4fefa8,
0x3f7e1324,0x3f731447,0x3f5ebe05,0x3f41d870,0x3f7d906e,0x3f6faf1b,0x3f5627fe,0x3f3238aa,0x3f7cfe73,0x3f6be858,0x3f4cae79,0x3f212ff9,0x3f7c5d3a,0x3f67c187,0x3f425b89,0x3f0ee0db,
0x3f7baccd,0x3f633c5a,0x3f373a23,0x3ef6e0cb,0x3f7aed37,0x3f5e5aa6,0x3f2b561b,0x3ece0bea,0x3f7a1e84,0x3f591e6a,0x3f1ebc12,0x3ea395c5,0x3f7940c0,0x3f5389c7,0x3f11796b,0x3e6fa8b2,
0x3f800000,0x3f800000,0x3f800000,0x3f800000,0x3f7e5a38,0x3f7d906e,0x3f7c9f8a,0x3f7b87b2,0x3f796e4e,0x3f764d97,0x3f7294f8,0x3f6e46be,0x3f714c7a,0x3f6a5ad9,0x3f62241a,0x3f58b3a1,
0x3f660f88,0x3f59f26a,0x3f4bbbf8,0x3f3b8f3b,0x3f57dc7f,0x3f456439,0x3f2ff3e6,0x3f17de12,0x3f46e22a,0x3f2d1469,0x3f0f8784,0x3eddbe79,0x3f33587a,0x3f11796b,0x3ed6a399,0x3e840270,
0x3f1d7fd1,0x3ee63375,0x3e888e93,0x3d96a905,0x3f05a02c,0x3ea5129f,0x3ddb7d37,0xbde7fa9a,0x3ed8106b,0x3e419b37,0xbd621469,0xbe97a117,0x3ea21887,0x3d55880e,0xbe5d4b1a,0xbeeff7fb,
0x3e541501,0xbdafb680,0xbebe1d4a,0xbf1ff6cb,0x3dc27c39,0xbe636c83,0xbf044889,0xbf425b89,0xbc96c9b6,0xbeb554ec,0xbf26050a,0xbf5df6be,0xbe06b26e,0xbef58035,0xbf436051,0xbf71d19f,
0x3f800000,0x3f800000,0x3f800000,0x3f800000,0x3f7a4910,0x3f78e3d6,0x3f77583a,0x3f75a67b,0x3f696591,0x3f63f473,0x3f5df6be,0x3f577026,0x3f4e1689,0x3f425b89,0x3f3592e7,0x3f27ce61,
0x3f299415,0x3f15f6d9,0x3f00e7e4,0x3ed53641,0x3efafef7,0x3ec27b55,0x3e870ada,0x3e1324ca,0x3e97a117,0x3e1c76de,0x3bc90f88,0xbe1008b7,0x3db5f8da,0xbda973ba,0xbe80f8c0,0xbed3c867,
0xbdfab273,0xbea09ae5,0xbefc5d27,0xbf273656,0xbea80b1f,0xbf06f686,0xbf33587a,0xbf570348,0xbf04f484,0xbf36206c,0xbf5c61c7,0xbf756d97,0xbf2ff3e6,0xbf5b2c79,0xbf76841b,0xbf7fffb1,
0xbf531849,0xbf740bdd,0xbf7ffb11,0xbf75dec6,0xbf6cd007,0xbf7f5ce9,0xbf7822d1,0xbf57dc7f,0xbf7bf531,0xbf7c7eb0,0xbf5f8327,0xbf286605,0xbf7fdaaf,0xbf6b99fb,0xbf37c655,0xbed6a399,
0x3f800000,0x3f800000,0x3f800000,0x3f800000,0x3f73ced9,0x3f71d19f,0x3f6faf1b,0x3f6d67a1,0x3f5064af,0x3f48d8b3,0x3f40d0da,0x3f385216,0x3f19210f,0x3f099f4e,0x3ef2bd43,0x3ed0eb2a,
0x3ea68f12,0x3e6c9a7f,0x3e09cf86,0x3d16c32c,0x3cafea69,0xbdcefdb7,0xbe636c83,0xbeadf73c,0xbe919ddd,0xbed8106b,0xbf0cead0,0xbf2ac082,0xbf102dd5,0xbf3238aa,0xbf4f0417,0xbf65b710,
0xbf49d112,0xbf64aa59,0xbf76ba07,0xbf7f4e6d,0xbf703b37,0xbf7dc68c,0xbf7efcc9,0xbf73ced9,0xbf7fc38f,0xbf7ac516,0xbf66becc,0xbf44e3f5,0xbf76ef5b,0xbf5bfb34,0xbf311722,0xbef2bd43,
0xbf5695e5,0xbf24d225,0xbec9b953,0xbde1bc2e,0xbf21cbfb,0xbeb6ccc3,0xbd3c6dd5,0x3e8a11f7,0xbebb31a0,0xbcfb49ba,0x3e9d9e78,0x3f1c420c,0xbd83db0a,0x3e9920fe,0x3f1f599f,0x3f5cc7d1,
0x0,0x0,0x0,0x0,0x3c7b514b,0x3d2351cb,0x3d83db0a,0x3db5f8da,0x3cfb49ba,0x3da3308c,0x3e039502,0x3e354098,0x3d3c6dd5,0x3df475c0,0x3e44b0b9,0x3e870ada,
0x3d7b2b74,0x3e22abb6,0x3e827dc0,0x3eb263ef,0x3d9cecf9,0x3e4ada4f,0x3ea21887,0x3edc53c1,0x3dbc3ac3,0x3e72b651,0x3ec1071e,0x3f0242b1,0x3ddb7d37,0x3e8d17c0,0x3edf28a9,0x3f1553b7,
0x3dfab273,0x3ea09ae5,0x3efc5d27,0x3f273656,0x3e0cec4a,0x3eb3dca5,0x3f0c42c9,0x3f37c655,0x3e1c76de,0x3ec6d529,0x3f19c200,0x3f46e22a,0x3e2bf804,0x3ed97cb9,0x3f269de3,0x3f546b3b,
0x3e3b6ecf,0x3eebcbbb,0x3f32c8c9,0x3f604621,0x3e4ada4f,0x3efdbabb,0x3f3e35c5,0x3f6a5ad9,0x3e5a3997,0x3f07a136,0x3f48d8b3,0x3f7294f8,0x3e698bba,0x3f102dd5,0x3f52a649,0x3f78e3d6,
0x0,0x0,0x0,0x0,0x3de7fa9a,0x3e0cec4a,0x3e25c58c,0x3e3e853e,0x3e667c66,0x3e8b9507,0x3ea395c5,0x3ebb31a0,0x3eab0201,0x3ece0bea,0x3eeff7fb,0x3f084b92,
0x3ee0924f,0x3f064b82,0x3f1b02c6,0x3f2e3bde,0x3f099f4e,0x3f2302d3,0x3f39f2ac,0x3f4e1689,0x3f212ff9,0x3f3ca003,0x3f53fac3,0x3f66becc,0x3f36ad7f,0x3f52a649,0x3f686b39,0x3f77583a,
0x3f49d112,0x3f64aa59,0x3f76ba07,0x3f7f4e6d,0x3f5a5ba0,0x3f725470,0x3f7e868b,0x3f7e5a38,0x3f6816a8,0x3f7b61fc,0x3f7f9c18,0x3f748422,0x3f72d4eb,0x3f7fa6e3,0x3f79f35e,0x3f62241a,
0x3f7a7302,0x3f7f0e58,0x3f6db293,0x3f47de65,0x3f7ed7d4,0x3f799b43,0x3f5b2c79,0x3f269de3,0x3f7ff4e6,0x3f6f6830,0x3f42de29,0x3eff17b2,0x3f7dc68c,0x3f60a6cf,0x3f256bcb,0x3ea80b1f,
0x0,0x0,0x0,0x0,0x3e57278f,0x3e6fa8b2,0x3e840270,0x3e901c2c,0x3ed25a09,0x3ee900b7,0x3eff17b2,0x3f0a48ad,0x3f17de12,0x3f269de3,0x3f34768f,0x3f4154e1,
0x3f3fc767,0x3f4f7a1f,0x3f5d2d53,0x3f68bf3c,0x3f5f20db,0x3f6cd007,0x3f76ef5b,0x3f7d57de,0x3f748422,0x3f7cfe73,0x3f7ffec4,0x3f7d7474,0x3f7efcc9,0x3f7f1f49,0x3f77beb7,0x3f6912ae,
0x3f7e1324,0x3f731447,0x3f5ebe05,0x3f41d870,0x3f71d19f,0x3f5988ad,0x3f36ad7f,0x3f0af1b7,0x3f5ac450,0x3f33e7bc,0x3f0242b1,0x3e919ddd,0x3f39f2ac,0x3f044889,0x3e8a11f7,0x3b490fc6,
0x3f10d3cd,0x3e9aa086,0x3c490e90,0xbe8e9a22,0x3ec27b55,0x3d9064b4,0xbe7bdba4,0xbf099f4e,0x3e354098,0xbe28defc,0xbef9a02d,0xbf40d0da,0xbd0a342f,0xbec8477c,0xbf3238aa,0xbf686b39,
0x0,0x0,0x0,0x0,0x3e9c1faf,0x3ea80b1f,0x3eb3dca5,0x3ebf926f,0x3f14b039,0x3f1ebc12,0x3f286605,0x3f31a81d,0x3f4d26fd,0x3f57dc7f,0x3f61668a,0x3f69b7e4,
0x3f721352,0x3f791298,0x3f7dabcc,0x3f7fd397,0x3f7ff0e3,0x3f7eb069,0x3f799b43,0x3f70c501,0x3f756d97,0x3f6816a8,0x3f55b993,0x3f3ebc1b,0x3f5389c7,0x3f37c655,0x3f16999f,0x3ee1fb6a,
0x3f1d7fd1,0x3ee63375,0x3e888e93,0x3d96a905,0x3eb0eacb,0x3e06b26e,0xbdb5f8da,0xbe9c1faf,0x3d2fe007,0xbe4dee60,0xbeddbe79,0xbf239da9,0xbe870ada,0xbf02ef9f,0xbf38dd65,0xbf61668a,
0xbf0b9a6b,0xbf43e200,0xbf6b4b0c,0xbf7e70b0,0xbf466354,0xbf6f20b0,0xbf7fba9e,0xbf76841b,0xbf6e46be,0xbf7fe129,0xbf73913f,0xbf4ac77f,0xbf7f7808,0xbf74484b,0xbf485bca,0xbf019573,
0x3400,0x2ca533a8,0x307232a7,0x322f3113,0x3400,0x3113322f,0x33d92a3e,0x330eaf8b,0x3400,0x330e2f8b,0x32a7b072,0xa646b3f6,0x3400,0x33f62646,0x2a3eb3d9,0xb3a8aca5,
0x3400,0x33a8aca5,0xb072b2a7,0xb113322f,0x3400,0x322fb113,0xb3d9aa3e,0x2f8b330e,0x3400,0x2f8bb30e,0xb2a73072,0x33f6a646,0x3400,0x2646b3f6,0xaa3e33d9,0x2ca5b3a8,
0x3400,0xaca5b3a8,0x307232a7,0xb22fb113,0x3400,0xb113b22f,0x33d92a3e,0xb30e2f8b,0x3400,0xb30eaf8b,0x32a7b072,0x264633f6,0x3400,0xb3f6a646,0x2a3eb3d9,0x33a82ca5,
0x3400,0xb3a82ca5,0xb072b2a7,0x3113b22f,0x3400,0xb22f3113,0xb3d9aa3e,0xaf8bb30e,0x3400,0xaf8b330e,0xb2a73072,0xb3f62646,0x3400,0xa64633f6,0xaa3e33d9,0xaca533a8,
0x33642e1f,0x33f62646,0x33d9aa3e,0x330eaf8b,0x2e1fb364,0xaca5b3a8,0xb2a7b072,0xb3f62646,0xb364ae1f,0xb22f3113,0x2a3e33d9,0x33a82ca5,0xae1f3364,0x330e2f8b,0x3072b2a7,0xb22fb113,
0x33642e1f,0x2646b3f6,0xb3d92a3e,0x2f8b330e,0x2e1fb364,0xb3a82ca5,0x32a73072,0xa646b3f6,0xb364ae1f,0x3113322f,0xaa3eb3d9,0xaca533a8,0xae1f3364,0x2f8bb30e,0xb07232a7,0x3113b22f,
0x33642e1f,0xb3f6a646,0x33d9aa3e,0xb30e2f8b,0x2e1fb364,0x2ca533a8,0xb2a7b072,0x33f6a646,0xb364ae1f,0x322fb113,0x2a3e33d9,0xb3a8aca5,0xae1f3364,0xb30eaf8b,0x3072b2a7,0x322f3113,
0x33642e1f,0xa64633f6,0xb3d92a3e,0xaf8bb30e,0x2e1fb364,0x33a8aca5,0x32a73072,0x264633f6,0xb364ae1f,0xb113b22f,0xaa3eb3d9,0x2ca5b3a8,0xae1f3364,0xaf8b330e,0xb07232a7,0xb113322f,
0x31a8b1a8,0x2f8bb30e,0x2a3eb3d9,0xa646b3f6,0xb1a831a8,0xa64633f6,0x307232a7,0x33a82ca5,0x31a8b1a8,0xaca5b3a8,0xb3d9aa3e,0xb113322f,0xb1a831a8,0x3113322f,0x32a7b072,0xaf8bb30e,
0x31a8b1a8,0xb30eaf8b,0xaa3e33d9,0x33f6a646,0xb1a831a8,0x33f62646,0xb072b2a7,0xaca533a8,0x31a8b1a8,0xb3a82ca5,0x33d92a3e,0xb22fb113,0xb1a831a8,0x322fb113,0xb2a73072,0x330eaf8b,
0x31a8b1a8,0xaf8b330e,0x2a3eb3d9,0x264633f6,0xb1a831a8,0x2646b3f6,0x307232a7,0xb3a8aca5,0x31a8b1a8,0x2ca533a8,0xb3d9aa3e,0x3113b22f,0xb1a831a8,0xb113b22f,0x32a7b072,0x2f8b330e,
0x31a8b1a8,0x330e2f8b,0xaa3e33d9,0xb3f62646,0xb1a831a8,0xb3f6a646,0xb072b2a7,0x2ca5b3a8,0x31a8b1a8,0x33a8aca5,0x33d92a3e,0x322f3113,0xb1a831a8,0xb22f3113,0xb2a73072,0xb30e2f8b,
0xae1fb364,0xb113b22f,0xb2a7b072,0xb3a8aca5,0x3364ae1f,0x2f8bb30e,0xaa3eb3d9,0xb22fb113,0x2e1f3364,0x33f62646,0x3072b2a7,0xaf8bb30e,0xb3642e1f,0x2ca533a8,0x33d9aa3e,0xa646b3f6,
0xae1fb364,0xb22f3113,0x32a73072,0x2ca5b3a8,0x3364ae1f,0xb30eaf8b,0x2a3e33d9,0x3113b22f,0x2e1f3364,0x2646b3f6,0xb07232a7,0x330eaf8b,0xb3642e1f,0x33a8aca5,0xb3d92a3e,0x33f6a646,
0xae1fb364,0x3113322f,0xb2a7b072,0x33a82ca5,0x3364ae1f,0xaf8b330e,0xaa3eb3d9,0x322f3113,0x2e1f3364,0xb3f6a646,0x3072b2a7,0x2f8b330e,0xb3642e1f,0xaca5b3a8,0x33d9aa3e,0x264633f6,
0xae1fb364,0x322fb113,0x32a73072,0xaca533a8,0x3364ae1f,0x330e2f8b,0x2a3e33d9,0xb113322f,0x2e1f3364,0xa64633f6,0xb07232a7,0xb30e2f8b,0xb3642e1f,0xb3a82ca5,0xb3d92a3e,0xb3f62646,
0x34000000,0x33a8aca5,0x32a7b072,0x3113b22f,0x34000000,0x322fb113,0x2a3eb3d9,0xaf8bb30e,0x34000000,0x2f8bb30e,0xb072b2a7,0xb3f62646,0x34000000,0x2646b3f6,0xb3d9aa3e,0xaca533a8,
0x34000000,0xaca5b3a8,0xb2a73072,0x322f3113,0x34000000,0xb113b22f,0xaa3e33d9,0x330eaf8b,0x34000000,0xb30eaf8b,0x307232a7,0xa646b3f6,0x34000000,0xb3f6a646,0x33d92a3e,0xb3a8aca5,
0x34000000,0xb3a82ca5,0x32a7b072,0xb113322f,0x34000000,0xb22f3113,0x2a3eb3d9,0x2f8b330e,0x34000000,0xaf8b330e,0xb072b2a7,0x33f6a646,0x34000000,0xa64633f6,0xb3d9aa3e,0x2ca5b3a8,
0x34000000,0x2ca533a8,0xb2a73072,0xb22fb113,0x34000000,0x3113322f,0xaa3e33d9,0xb30e2f8b,0x34000000,0x330e2f8b,0x307232a7,0x264633f6,0x34000000,0x33f62646,0x33d92a3e,0x33a82ca5,
0x2e1fb364,0x2646b3f6,0xaa3eb3d9,0xaf8bb30e,0xb364ae1f,0xb3a82ca5,0xb07232a7,0x264633f6,0xae1f3364,0x3113322f,0x33d9aa3e,0x2ca5b3a8,0x33642e1f,0x2f8bb30e,0xb2a7b072,0xb113322f,
0x2e1fb364,0xb3f6a646,0x2a3e33d9,0x330eaf8b,0xb364ae1f,0x2ca533a8,0x3072b2a7,0xb3f62646,0xae1f3364,0x322fb113,0xb3d92a3e,0x33a82ca5,0x33642e1f,0xb30eaf8b,0x32a73072,0xb22fb113,
0x2e1fb364,0xa64633f6,0xaa3eb3d9,0x2f8b330e,0xb364ae1f,0x33a8aca5,0xb07232a7,0xa646b3f6,0xae1f3364,0xb113b22f,0x33d9aa3e,0xaca533a8,0x33642e1f,0xaf8b330e,0xb2a7b072,0x3113b22f,
0x2e1fb364,0x33f62646,0x2a3e33d9,0xb30e2f8b,0xb364ae1f,0xaca5b3a8,0x3072b2a7,0x33f6a646,0xae1f3364,0xb22f3113,0xb3d92a3e,0xb3a8aca5,0x33642e1f,0x330e2f8b,0x32a73072,0x322f3113,
0xb1a8b1a8,0xb30eaf8b,0xb3d9aa3e,0xb3f62646,0x31a831a8,0x33f62646,0x32a7b072,0x2ca5b3a8,0xb1a8b1a8,0xb3a82ca5,0xaa3e33d9,0x322f3113,0x31a831a8,0x322fb113,0xb072b2a7,0xb30e2f8b,
0xb1a8b1a8,0xaf8b330e,0x33d92a3e,0xa646b3f6,0x31a831a8,0x2646b3f6,0xb2a73072,0x33a82ca5,0xb1a8b1a8,0x2ca533a8,0x2a3eb3d9,0xb113322f,0x31a831a8,0xb113b22f,0x307232a7,0xaf8bb30e,
0xb1a8b1a8,0x330e2f8b,0xb3d9aa3e,0x33f6a646,0x31a831a8,0xb3f6a646,0x32a7b072,0xaca533a8,0xb1a8b1a8,0x33a8aca5,0xaa3e33d9,0xb22fb113,0x31a831a8,0xb22f3113,0xb072b2a7,0x330eaf8b,
0xb1a8b1a8,0x2f8bb30e,0x33d92a3e,0x264633f6,0x31a831a8,0xa64633f6,0xb2a73072,0xb3a8aca5,0xb1a8b1a8,0xaca5b3a8,0x2a3eb3d9,0x3113b22f,0x31a831a8,0x3113322f,0x307232a7,0x2f8b330e,
0xb3642e1f,0xb22f3113,0xb07232a7,0xaca533a8,0xae1fb364,0xb30eaf8b,0xb3d92a3e,0xb113322f,0x3364ae1f,0x2646b3f6,0xb2a7b072,0xb30e2f8b,0x2e1f3364,0x33a8aca5,0xaa3eb3d9,0xb3f62646,
0xb3642e1f,0x3113322f,0x3072b2a7,0xb3a8aca5,0xae1fb364,0xaf8b330e,0x33d9aa3e,0xb22fb113,0x3364ae1f,0xb3f6a646,0x32a73072,0xaf8bb30e,0x2e1f3364,0xaca5b3a8,0x2a3e33d9,0xa646b3f6,
0xb3642e1f,0x322fb113,0xb07232a7,0x2ca5b3a8,0xae1fb364,0x330e2f8b,0xb3d92a3e,0x3113b22f,0x3364ae1f,0xa64633f6,0xb2a7b072,0x330eaf8b,0x2e1f3364,0xb3a82ca5,0xaa3eb3d9,0x33f6a646,
0xb3642e1f,0xb113b22f,0x3072b2a7,0x33a82ca5,0xae1fb364,0x2f8bb30e,0x33d9aa3e,0x322f3113,0x3364ae1f,0x33f62646,0x32a73072,0x2f8b330e,0x2e1f3364,0x2ca533a8,0x2a3e33d9,0x264633f6,
0x3400,0x3400,0x3400,0x3400,0x3400,0x2e1f3364,0x31a831a8,0x33642e1f,0x3400,0x31a831a8,0x34000000,0x31a8b1a8,0x3400,0x33642e1f,0x31a8b1a8,0xae1fb364,
0x3400,0x34000000,0xb400,0xb4008000,0x3400,0x3364ae1f,0xb1a8b1a8,0xae1f3364,0x3400,0x31a8b1a8,0xb4008000,0x31a831a8,0x3400,0x2e1fb364,0xb1a831a8,0x3364ae1f,
0x3400,0xb400,0x3400,0xb400,0x3400,0xae1fb364,0x31a831a8,0xb364ae1f,0x3400,0xb1a8b1a8,0x34000000,0xb1a831a8,0x3400,0xb364ae1f,0x31a8b1a8,0x2e1f3364,
0x3400,0xb4008000,0xb400,0x34000000,0x3400,0xb3642e1f,0xb1a8b1a8,0x2e1fb364,0x3400,0xb1a831a8,0xb4008000,0xb1a8b1a8,0x3400,0xae1f3364,0xb1a831a8,0xb3642e1f,
0x3400,0x3400,0x3400,0x3400,0x34000000,0x3364ae1f,0x31a8b1a8,0x2e1fb364,0xb400,0xb1a8b1a8,0xb4008000,0xb1a831a8,0xb4008000,0xae1f3364,0x31a831a8,0x3364ae1f,
0x3400,0x34000000,0xb400,0xb4008000,0x34000000,0xae1fb364,0xb1a831a8,0x33642e1f,0xb400,0xb1a831a8,0x34000000,0xb1a8b1a8,0xb4008000,0x33642e1f,0xb1a8b1a8,0x2e1f3364,
0x3400,0xb400,0x3400,0xb400,0x34000000,0xb3642e1f,0x31a8b1a8,0xae1f3364,0xb400,0x31a831a8,0xb4008000,0x31a8b1a8,0xb4008000,0x2e1fb364,0x31a831a8,0xb3642e1f,
0x3400,0xb4008000,0xb400,0x34000000,0x34000000,0x2e1f3364,0xb1a831a8,0xb364ae1f,0xb400,0x31a8b1a8,0x34000000,0x31a831a8,0xb4008000,0xb364ae1f,0xb1a8b1a8,0xae1fb364,
0x3400,0x3400,0x3400,0x3400,0xb400,0xae1fb364,0xb1a8b1a8,0xb364ae1f,0x3400,0x31a831a8,0x34000000,0x31a8b1a8,0xb400,0xb364ae1f,0xb1a831a8,0x2e1f3364,
0x3400,0x34000000,0xb400,0xb4008000,0xb400,0xb3642e1f,0x31a831a8,0x2e1fb364,0x3400,0x31a8b1a8,0xb4008000,0x31a831a8,0xb400,0xae1f3364,0x31a8b1a8,0xb3642e1f,
0x3400,0xb400,0x3400,0xb400,0xb400,0x2e1f3364,0xb1a8b1a8,0x33642e1f,0x3400,0xb1a8b1a8,0x34000000,0xb1a831a8,0xb400,0x33642e1f,0xb1a831a8,0xae1fb364,
0x3400,0xb4008000,0xb400,0x34000000,0xb400,0x3364ae1f,0x31a831a8,0xae1f3364,0x3400,0xb1a831a8,0xb4008000,0xb1a8b1a8,0xb400,0x2e1fb364,0x31a8b1a8,0x3364ae1f,
0x3400,0x3400,0x3400,0x3400,0xb4008000,0xb3642e1f,0xb1a831a8,0xae1f3364,0xb400,0xb1a8b1a8,0xb4008000,0xb1a831a8,0x34000000,0x2e1fb364,0xb1a8b1a8,0xb3642e1f,
0x3400,0x34000000,0xb400,0xb4008000,0xb4008000,0x2e1f3364,0x31a8b1a8,0xb364ae1f,0xb400,0xb1a831a8,0x34000000,0xb1a8b1a8,0x34000000,0xb364ae1f,0x31a831a8,0xae1fb364,
0x3400,0xb400,0x3400,0xb400,0xb4008000,0x3364ae1f,0xb1a831a8,0x2e1fb364,0xb400,0x31a831a8,0xb4008000,0x31a8b1a8,0x34000000,0xae1f3364,0xb1a8b1a8,0x3364ae1f,
0x3400,0xb4008000,0xb400,0x34000000,0xb4008000,0xae1fb364,0x31a8b1a8,0x33642e1f,0xb400,0x31a8b1a8,0x34000000,0x31a831a8,0x34000000,0x33642e1f,0x31a831a8,0x2e1f3364,
0x34000000,0x34000000,0x34000000,0x34000000,0x34000000,0x3364ae1f,0x31a8b1a8,0x2e1fb364,0x34000000,0x31a8b1a8,0xb400,0xb1a8b1a8,0x34000000,0x2e1fb364,0xb1a8b1a8,0xb3642e1f,
0x34000000,0xb400,0xb4008000,0x80003400,0x34000000,0xae1fb364,0xb1a831a8,0x33642e1f,0x34000000,0xb1a8b1a8,0x80003400,0x31a8b1a8,0x34000000,0xb364ae1f,0x31a831a8,0xae1fb364,
0x34000000,0xb4008000,0x34000000,0xb4008000,0x34000000,0xb3642e1f,0x31a8b1a8,0xae1f3364,0x34000000,0xb1a831a8,0xb400,0x31a831a8,0x34000000,0xae1f3364,0xb1a8b1a8,0x3364ae1f,
0x34000000,0x80003400,0xb4008000,0xb400,0x34000000,0x2e1f3364,0xb1a831a8,0xb364ae1f,0x34000000,0x31a831a8,0x80003400,0xb1a831a8,0x34000000,0x33642e1f,0x31a831a8,0x2e1f3364,
0x34000000,0x34000000,0x34000000,0x34000000,0xb400,0xae1fb364,0xb1a8b1a8,0xb364ae1f,0xb4008000,0xb1a831a8,0x80003400,0x31a831a8,0x80003400,0x33642e1f,0x31a8b1a8,0xae1fb364,
0x34000000,0xb400,0xb4008000,0x80003400,0xb400,0xb3642e1f,0x31a831a8,0x2e1fb364,0xb4008000,0x31a831a8,0xb400,0xb1a831a8,0x80003400,0x2e1fb364,0xb1a831a8,0x3364ae1f,
0x34000000,0xb4008000,0x34000000,0xb4008000,0xb400,0x2e1f3364,0xb1a8b1a8,0x33642e1f,0xb4008000,0x31a8b1a8,0x80003400,0xb1a8b1a8,0x80003400,0xb364ae1f,0x31a8b1a8,0x2e1f3364,
0x34000000,0x80003400,0xb4008000,0xb400,0xb400,0x3364ae1f,0x31a831a8,0xae1f3364,0xb4008000,0xb1a8b1a8,0xb400,0x31a8b1a8,0x80003400,0xae1f3364,0xb1a831a8,0xb3642e1f,
0x34000000,0x34000000,0x34000000,0x34000000,0xb4008000,0xb3642e1f,0xb1a831a8,0xae1f3364,0x34000000,0x31a8b1a8,0xb400,0xb1a8b1a8,0xb4008000,0xae1f3364,0x31a831a8,0x3364ae1f,
0x34000000,0xb400,0xb4008000,0x80003400,0xb4008000,0x2e1f3364,0x31a8b1a8,0xb364ae1f,0x34000000,0xb1a8b1a8,0x80003400,0x31a8b1a8,0xb4008000,0x33642e1f,0xb1a8b1a8,0x2e1f3364,
0x34000000,0xb4008000,0x34000000,0xb4008000,0xb4008000,0x3364ae1f,0xb1a831a8,0x2e1fb364,0x34000000,0xb1a831a8,0xb400,0x31a831a8,0xb4008000,0x2e1fb364,0x31a831a8,0xb3642e1f,
0x34000000,0x80003400,0xb4008000,0xb400,0xb4008000,0xae1fb364,0x31a8b1a8,0x33642e1f,0x34000000,0x31a831a8,0x80003400,0xb1a831a8,0xb4008000,0xb364ae1f,0xb1a8b1a8,0xae1fb364,
0x34000000,0x34000000,0x34000000,0x34000000,0x80003400,0x2e1f3364,0x31a831a8,0x33642e1f,0xb4008000,0xb1a831a8,0x80003400,0x31a831a8,0xb400,0xb364ae1f,0xb1a831a8,0x2e1f3364,
0x34000000,0xb400,0xb4008000,0x80003400,0x80003400,0x3364ae1f,0xb1a8b1a8,0xae1f3364,0xb4008000,0x31a831a8,0xb400,0xb1a831a8,0xb400,0xae1f3364,0x31a8b1a8,0xb3642e1f,
0x34000000,0xb4008000,0x34000000,0xb4008000,0x80003400,0xae1fb364,0x31a831a8,0xb364ae1f,0xb4008000,0x31a8b1a8,0x80003400,0xb1a8b1a8,0xb400,0x33642e1f,0xb1a831a8,0xae1fb364,
0x34000000,0x80003400,0xb4008000,0xb400,0x80003400,0xb3642e1f,0xb1a8b1a8,0x2e1fb364,0xb4008000,0xb1a8b1a8,0xb400,0x31a8b1a8,0xb400,0x2e1fb364,0x31a8b1a8,0x3364ae1f,
0x3400,0x3400,0x3400,0x3400,0x3400,0xae1f3364,0xb1a831a8,0xb3642e1f,0x3400,0xb1a831a8,0xb4000000,0xb1a8b1a8,0x3400,0xb3642e1f,0xb1a8b1a8,0x2e1fb364,
0x3400,0xb4000000,0x8000b400,0x34008000,0x3400,0xb364ae1f,0x31a8b1a8,0x2e1f3364,0x3400,0xb1a8b1a8,0x34008000,0xb1a831a8,0x3400,0xae1fb364,0x31a831a8,0xb364ae1f,
0x3400,0x8000b400,0x3400,0x8000b400,0x3400,0x2e1fb364,0xb1a831a8,0x3364ae1f,0x3400,0x31a8b1a8,0xb4000000,0x31a831a8,0x3400,0x3364ae1f,0xb1a8b1a8,0xae1f3364,
0x3400,0x34008000,0x8000b400,0xb4000000,0x3400,0x33642e1f,0x31a8b1a8,0xae1fb364,0x3400,0x31a831a8,0x34008000,0x31a8b1a8,0x3400,0x2e1f3364,0x31a831a8,0x33642e1f,
0x3400,0x3400,0x3400,0x3400,0xb4000000,0xb364ae1f,0xb1a8b1a8,0xae1fb364,0x8000b400,0x31a8b1a8,0x34008000,0x31a831a8,0x34008000,0x2e1f3364,0xb1a831a8,0xb364ae1f,
0x3400,0xb4000000,0x8000b400,0x34008000,0xb4000000,0x2e1fb364,0x31a831a8,0xb3642e1f,0x8000b400,0x31a831a8,0xb4000000,0x31a8b1a8,0x34008000,0xb3642e1f,0x31a8b1a8,0xae1f3364,
0x3400,0x8000b400,0x3400,0x8000b400,0xb4000000,0x33642e1f,0xb1a8b1a8,0x2e1f3364,0x8000b400,0xb1a831a8,0x34008000,0xb1a8b1a8,0x34008000,0xae1fb364,0xb1a831a8,0x33642e1f,
0x3400,0x34008000,0x8000b400,0xb4000000,0xb4000000,0xae1f3364,0x31a831a8,0x3364ae1f,0x8000b400,0xb1a8b1a8,0xb4000000,0xb1a831a8,0x34008000,0x3364ae1f,0x31a8b1a8,0x2e1fb364,
0x3400,0x3400,0x3400,0x3400,0x8000b400,0x2e1fb364,0x31a8b1a8,0x3364ae1f,0x3400,0xb1a831a8,0xb4000000,0xb1a8b1a8,0x8000b400,0x3364ae1f,0x31a831a8,0xae1f3364,
0x3400,0xb4000000,0x8000b400,0x34008000,0x8000b400,0x33642e1f,0xb1a831a8,0xae1fb364,0x3400,0xb1a8b1a8,0x34008000,0xb1a831a8,0x8000b400,0x2e1f3364,0xb1a8b1a8,0x33642e1f,
0x3400,0x8000b400,0x3400,0x8000b400,0x8000b400,0xae1f3364,0x31a8b1a8,0xb3642e1f,0x3400,0x31a8b1a8,0xb4000000,0x31a831a8,0x8000b400,0xb3642e1f,0x31a831a8,0x2e1fb364,
0x3400,0x34008000,0x8000b400,0xb4000000,0x8000b400,0xb364ae1f,0xb1a831a8,0x2e1f3364,0x3400,0x31a831a8,0x34008000,0x31a8b1a8,0x8000b400,0xae1fb364,0xb1a8b1a8,0xb364ae1f,
0x3400,0x3400,0x3400,0x3400,0x34008000,0x33642e1f,0x31a831a8,0x2e1f3364,0x8000b400,0x31a8b1a8,0x34008000,0x31a831a8,0xb4000000,0xae1fb364,0x31a8b1a8,0x33642e1f,
0x3400,0xb4000000,0x8000b400,0x34008000,0x34008000,0xae1f3364,0xb1a8b1a8,0x3364ae1f,0x8000b400,0x31a831a8,0xb4000000,0x31a8b1a8,0xb4000000,0x3364ae1f,0xb1a831a8,0x2e1fb364,
0x3400,0x8000b400,0x3400,0x8000b400,0x34008000,0xb364ae1f,0x31a831a8,0xae1fb364,0x8000b400,0xb1a831a8,0x34008000,0xb1a8b1a8,0xb4000000,0x2e1f3364,0x31a8b1a8,0xb364ae1f,
0x3400,0x34008000,0x8000b400,0xb4000000,0x34008000,0x2e1fb364,0xb1a8b1a8,0xb3642e1f,0x8000b400,0xb1a8b1a8,0xb4000000,0xb1a831a8,0xb4000000,0xb3642e1f,0xb1a831a8,0xae1f3364,
0x34000000,0x34000000,0x34000000,0x34000000,0x34000000,0x33642e1f,0x31a831a8,0x2e1f3364,0x34000000,0x31a831a8,0x3400,0xb1a831a8,0x34000000,0x2e1f3364,0xb1a831a8,0xb364ae1f,
0x34000000,0x3400,0xb4000000,0x8000b400,0x34000000,0xae1f3364,0xb1a8b1a8,0x3364ae1f,0x34000000,0xb1a831a8,0x8000b400,0x31a831a8,0x34000000,0xb3642e1f,0x31a8b1a8,0xae1f3364,
0x34000000,0xb4000000,0x34000000,0xb4000000,0x34000000,0xb364ae1f,0x31a831a8,0xae1fb364,0x34000000,0xb1a8b1a8,0x3400,0x31a8b1a8,0x34000000,0xae1fb364,0xb1a831a8,0x33642e1f,
0x34000000,0x8000b400,0xb4000000,0x3400,0x34000000,0x2e1fb364,0xb1a8b1a8,0xb3642e1f,0x34000000,0x31a8b1a8,0x8000b400,0xb1a8b1a8,0x34000000,0x3364ae1f,0x31a8b1a8,0x2e1fb364,
0x34000000,0x34000000,0x34000000,0x34000000,0x3400,0xae1f3364,0xb1a831a8,0xb3642e1f,0xb4000000,0xb1a8b1a8,0x8000b400,0x31a8b1a8,0x8000b400,0x3364ae1f,0x31a831a8,0xae1f3364,
0x34000000,0x3400,0xb4000000,0x8000b400,0x3400,0xb364ae1f,0x31a8b1a8,0x2e1f3364,0xb4000000,0x31a8b1a8,0x3400,0xb1a8b1a8,0x8000b400,0x2e1f3364,0xb1a8b1a8,0x33642e1f,
0x34000000,0xb4000000,0x34000000,0xb4000000,0x3400,0x2e1fb364,0xb1a831a8,0x3364ae1f,0xb4000000,0x31a831a8,0x8000b400,0xb1a831a8,0x8000b400,0xb3642e1f,0x31a831a8,0x2e1fb364,
0x34000000,0x8000b400,0xb4000000,0x3400,0x3400,0x33642e1f,0x31a8b1a8,0xae1fb364,0xb4000000,0xb1a831a8,0x3400,0x31a831a8,0x8000b400,0xae1fb364,0xb1a8b1a8,0xb364ae1f,
0x34000000,0x34000000,0x34000000,0x34000000,0xb4000000,0xb364ae1f,0xb1a8b1a8,0xae1fb364,0x34000000,0x31a831a8,0x3400,0xb1a831a8,0xb4000000,0xae1fb364,0x31a8b1a8,0x33642e1f,
0x34000000,0x3400,0xb4000000,0x8000b400,0xb4000000,0x2e1fb364,0x31a831a8,0xb3642e1f,0x34000000,0xb1a831a8,0x8000b400,0x31a831a8,0xb4000000,0x3364ae1f,0xb1a831a8,0x2e1fb364,
0x34000000,0xb4000000,0x34000000,0xb4000000,0xb4000000,0x33642e1f,0xb1a8b1a8,0x2e1f3364,0x34000000,0xb1a8b1a8,0x3400,0x31a8b1a8,0xb4000000,0x2e1f3364,0x31a8b1a8,0xb364ae1f,
0x34000000,0x8000b400,0xb4000000,0x3400,0xb4000000,0xae1f3364,0x31a831a8,0x3364ae1f,0x34000000,0x31a8b1a8,0x8000b400,0xb1a8b1a8,0xb4000000,0xb3642e1f,0xb1a831a8,0xae1f3364,
0x34000000,0x34000000,0x34000000,0x34000000,0x8000b400,0x2e1fb364,0x31a8b1a8,0x3364ae1f,0xb4000000,0xb1a8b1a8,0x8000b400,0x31a8b1a8,0x3400,0xb3642e1f,0xb1a8b1a8,0x2e1fb364,
0x34000000,0x3400,0xb4000000,0x8000b400,0x8000b400,0x33642e1f,0xb1a831a8,0xae1fb364,0xb4000000,0x31a8b1a8,0x3400,0xb1a8b1a8,0x3400,0xae1fb364,0x31a831a8,0xb364ae1f,
0x34000000,0xb4000000,0x34000000,0xb4000000,0x8000b400,0xae1f3364,0x31a8b1a8,0xb3642e1f,0xb4000000,0x31a831a8,0x8000b400,0xb1a831a8,0x3400,0x3364ae1f,0xb1a8b1a8,0xae1f3364,
0x34000000,0x8000b400,0xb4000000,0x3400,0x8000b400,0xb364ae1f,0xb1a831a8,0x2e1f3364,0xb4000000,0xb1a831a8,0x3400,0x31a831a8,0x3400,0x2e1f3364,0x31a831a8,0x33642e1f,
0x3400,0x3400,0x3400,0x3400,0xaca533a8,0xb113322f,0xb30e2f8b,0xb3f62646,0xb07232a7,0xb3d92a3e,0xb2a7b072,0xaa3eb3d9,0xb22f3113,0xb30eaf8b,0x2646b3f6,0x33a8aca5,
0xb3642e1f,0xae1fb364,0x3364ae1f,0x2e1f3364,0xb3f62646,0x2ca5b3a8,0x322f3113,0xb30e2f8b,0xb3d9aa3e,0x32a7b072,0xaa3e33d9,0xb072b2a7,0xb30eaf8b,0x33f62646,0xb3a82ca5,0x322fb113,
0xb1a8b1a8,0x31a831a8,0xb1a8b1a8,0x31a831a8,0xaf8bb30e,0x264633f6,0x2ca5b3a8,0xb113322f,0xaa3eb3d9,0xb07232a7,0x33d9aa3e,0xb2a7b072,0x2646b3f6,0xb3a82ca5,0x3113322f,0x2f8bb30e,
0x2e1fb364,0xb364ae1f,0xae1f3364,0x33642e1f,0x3113b22f,0xaf8bb30e,0xb3f62646,0xaca533a8,0x32a7b072,0x2a3eb3d9,0xb072b2a7,0xb3d9aa3e,0x33a8aca5,0x322fb113,0x2f8bb30e,0x2646b3f6,
0x3400,0x3400,0x3400,0x3400,0xb3a8aca5,0xb22fb113,0xaf8bb30e,0xa646b3f6,0x3072b2a7,0x33d9aa3e,0x32a73072,0x2a3e33d9,0x3113322f,0xaf8b330e,0xb3f6a646,0xaca5b3a8,
0xb3642e1f,0xae1fb364,0x3364ae1f,0x2e1f3364,0xa646b3f6,0x33a82ca5,0xb113322f,0xaf8bb30e,0x33d92a3e,0xb2a73072,0x2a3eb3d9,0x307232a7,0xaf8b330e,0x2646b3f6,0x2ca533a8,0xb113b22f,
0xb1a8b1a8,0x31a831a8,0xb1a8b1a8,0x31a831a8,0x330eaf8b,0xb3f62646,0x33a82ca5,0xb22fb113,0x2a3e33d9,0x3072b2a7,0xb3d92a3e,0x32a73072,0xb3f6a646,0x2ca533a8,0x322fb113,0xb30eaf8b,
0x2e1fb364,0xb364ae1f,0xae1f3364,0x33642e1f,0x322f3113,0x330eaf8b,0xa646b3f6,0xb3a8aca5,0xb2a73072,0xaa3e33d9,0x307232a7,0x33d92a3e,0xaca5b3a8,0xb113b22f,0xb30eaf8b,0xb3f6a646,
0x3400,0x3400,0x3400,0x3400,0x2ca5b3a8,0x3113b22f,0x330eaf8b,0x33f6a646,0xb07232a7,0xb3d92a3e,0xb2a7b072,0xaa3eb3d9,0x322fb113,0x330e2f8b,0xa64633f6,0xb3a82ca5,
0xb3642e1f,0xae1fb364,0x3364ae1f,0x2e1f3364,0x33f6a646,0xaca533a8,0xb22fb113,0x330eaf8b,0xb3d9aa3e,0x32a7b072,0xaa3e33d9,0xb072b2a7,0x330e2f8b,0xb3f6a646,0x33a8aca5,0xb22f3113,
0xb1a8b1a8,0x31a831a8,0xb1a8b1a8,0x31a831a8,0x2f8b330e,0xa646b3f6,0xaca533a8,0x3113b22f,0xaa3eb3d9,0xb07232a7,0x33d9aa3e,0xb2a7b072,0xa64633f6,0x33a8aca5,0xb113b22f,0xaf8b330e,
0x2e1fb364,0xb364ae1f,0xae1f3364,0x33642e1f,0xb113322f,0x2f8b330e,0x33f6a646,0x2ca5b3a8,0x32a7b072,0x2a3eb3d9,0xb072b2a7,0xb3d9aa3e,0xb3a82ca5,0xb22f3113,0xaf8b330e,0xa64633f6,
0x3400,0x3400,0x3400,0x3400,0x33a82ca5,0x322f3113,0x2f8b330e,0x264633f6,0x3072b2a7,0x33d9aa3e,0x32a73072,0x2a3e33d9,0xb113b22f,0x2f8bb30e,0x33f62646,0x2ca533a8,
0xb3642e1f,0xae1fb364,0x3364ae1f,0x2e1f3364,0x264633f6,0xb3a8aca5,0x3113b22f,0x2f8b330e,0x33d92a3e,0xb2a73072,0x2a3eb3d9,0x307232a7,0x2f8bb30e,0xa64633f6,0xaca5b3a8,0x3113322f,
0xb1a8b1a8,0x31a831a8,0xb1a8b1a8,0x31a831a8,0xb30e2f8b,0x33f6a646,0xb3a8aca5,0x322f3113,0x2a3e33d9,0x3072b2a7,0xb3d92a3e,0x32a73072,0x33f62646,0xaca5b3a8,0xb22f3113,0x330e2f8b,
0x2e1fb364,0xb364ae1f,0xae1f3364,0x33642e1f,0xb22fb113,0xb30e2f8b,0x264633f6,0x33a82ca5,0xb2a73072,0xaa3e33d9,0x307232a7,0x33d92a3e,0x2ca533a8,0x3113322f,0x330e2f8b,0x33f62646,
0x34000000,0x34000000,0x34000000,0x34000000,0x33a82ca5,0x322f3113,0x2f8b330e,0x264633f6,0x32a73072,0x2a3e33d9,0xb07232a7,0xb3d92a3e,0x3113322f,0xaf8b330e,0xb3f6a646,0xaca5b3a8,
0x2e1f3364,0xb3642e1f,0xae1fb364,0x3364ae1f,0x264633f6,0xb3a8aca5,0x3113b22f,0x2f8b330e,0xaa3e33d9,0xb072b2a7,0x33d92a3e,0xb2a73072,0xaf8b330e,0x2646b3f6,0x2ca533a8,0xb113b22f,
0xb1a831a8,0x31a8b1a8,0xb1a831a8,0x31a8b1a8,0xb30e2f8b,0x33f6a646,0xb3a8aca5,0x322f3113,0xb3d92a3e,0x32a73072,0xaa3eb3d9,0xb07232a7,0xb3f6a646,0x2ca533a8,0x322fb113,0xb30eaf8b,
0xb364ae1f,0xae1f3364,0x33642e1f,0x2e1fb364,0xb22fb113,0xb30e2f8b,0x264633f6,0x33a82ca5,0xb072b2a7,0xb3d9aa3e,0xb2a73072,0xaa3e33d9,0xaca5b3a8,0xb113b22f,0xb30eaf8b,0xb3f6a646,
0x34000000,0x34000000,0x34000000,0x34000000,0xaca533a8,0xb113322f,0xb30e2f8b,0xb3f62646,0xb2a7b072,0xaa3eb3d9,0x3072b2a7,0x33d9aa3e,0x322fb113,0x330e2f8b,0xa64633f6,0xb3a82ca5,
0x2e1f3364,0xb3642e1f,0xae1fb364,0x3364ae1f,0xb3f62646,0x2ca5b3a8,0x322f3113,0xb30e2f8b,0x2a3eb3d9,0x307232a7,0xb3d9aa3e,0x32a7b072,0x330e2f8b,0xb3f6a646,0x33a8aca5,0xb22f3113,
0xb1a831a8,0x31a8b1a8,0xb1a831a8,0x31a8b1a8,0xaf8bb30e,0x264633f6,0x2ca5b3a8,0xb113322f,0x33d9aa3e,0xb2a7b072,0x2a3e33d9,0x3072b2a7,0xa64633f6,0x33a8aca5,0xb113b22f,0xaf8b330e,
0xb364ae1f,0xae1f3364,0x33642e1f,0x2e1fb364,0x3113b22f,0xaf8bb30e,0xb3f62646,0xaca533a8,0x307232a7,0x33d92a3e,0x32a7b072,0x2a3eb3d9,0xb3a82ca5,0xb22f3113,0xaf8b330e,0xa64633f6,
0x34000000,0x34000000,0x34000000,0x34000000,0xb3a8aca5,0xb22fb113,0xaf8bb30e,0xa646b3f6,0x32a73072,0x2a3e33d9,0xb07232a7,0xb3d92a3e,0xb113b22f,0x2f8bb30e,0x33f62646,0x2ca533a8,
0x2e1f3364,0xb3642e1f,0xae1fb364,0x3364ae1f,0xa646b3f6,0x33a82ca5,0xb113322f,0xaf8bb30e,0xaa3e33d9,0xb072b2a7,0x33d92a3e,0xb2a73072,0x2f8bb30e,0xa64633f6,0xaca5b3a8,0x3113322f,
0xb1a831a8,0x31a8b1a8,0xb1a831a8,0x31a8b1a8,0x330eaf8b,0xb3f62646,0x33a82ca5,0xb22fb113,0xb3d92a3e,0x32a73072,0xaa3eb3d9,0xb07232a7,0x33f62646,0xaca5b3a8,0xb22f3113,0x330e2f8b,
0xb364ae1f,0xae1f3364,0x33642e1f,0x2e1fb364,0x322f3113,0x330eaf8b,0xa646b3f6,0xb3a8aca5,0xb072b2a7,0xb3d9aa3e,0xb2a73072,0xaa3e33d9,0x2ca533a8,0x3113322f,0x330e2f8b,0x33f62646,
0x34000000,0x34000000,0x34000000,0x34000000,0x2ca5b3a8,0x3113b22f,0x330eaf8b,0x33f6a646,0xb2a7b072,0xaa3eb3d9,0x3072b2a7,0x33d9aa3e,0xb22f3113,0xb30eaf8b,0x2646b3f6,0x33a8aca5,
0x2e1f3364,0xb3642e1f,0xae1fb364,0x3364ae1f,0x33f6a646,0xaca533a8,0xb22fb113,0x330eaf8b,0x2a3eb3d9,0x307232a7,0xb3d9aa3e,0x32a7b072,0xb30eaf8b,0x33f62646,0xb3a82ca5,0x322fb113,
0xb1a831a8,0x31a8b1a8,0xb1a831a8,0x31a8b1a8,0x2f8b330e,0xa646b3f6,0xaca533a8,0x3113b22f,0x33d9aa3e,0xb2a7b072,0x2a3e33d9,0x3072b2a7,0x2646b3f6,0xb3a82ca5,0x3113322f,0x2f8bb30e,
0xb364ae1f,0xae1f3364,0x33642e1f,0x2e1fb364,0xb113322f,0x2f8b330e,0x33f6a646,0x2ca5b3a8,0x307232a7,0x33d92a3e,0x32a7b072,0x2a3eb3d9,0x33a8aca5,0x322fb113,0x2f8bb30e,0x2646b3f6,
0x3f800000,0x3f7ff4e6,0x3f7fd397,0x3f7f9c18,0x3f800000,0x3f7fc38f,0x3f7f0e58,0x3f7de0b1,0x3f800000,0x3f7f6ac7,0x3f7dabcc,0x3f7ac516,0x3f800000,0x3f7eea9d,0x3f7baccd,0x3f764d97,
0x3f800000,0x3f7e4323,0x3f791298,0x3f708066,0x3f800000,0x3f7d7474,0x3f75dec6,0x3f696591,0x3f800000,0x3f7c7eb0,0x3f721352,0x3f6106f2,0x3f800000,0x3f7b61fc,0x3f6db293,0x3f577026,
0x3f800000,0x3f7a1e84,0x3f68bf3c,0x3f4cae79,0x3f800000,0x3f78b47b,0x3f633c5a,0x3f40d0da,0x3f800000,0x3f772417,0x3f5d2d53,0x3f33e7bc,0x3f800000,0x3f756d97,0x3f5695e5,0x3f26050a,
0x3f800000,0x3f73913f,0x3f4f7a1f,0x3f173c07,0x3f800000,0x3f718f57,0x3f47de65,0x3f07a136,0x3f800000,0x3f6f6830,0x3f3fc767,0x3eee9479,0x3f800000,0x3f6d1c1d,0x3f373a23,0x3ecc9b8b,
0x3f7f4e6d,0x3f7eea9d,0x3f7e70b0,0x3f7de0b1,0x3f7c3b28,0x3f7a1e84,0x3f778bc5,0x3f748422,0x3f76ba07,0x3f718f57,0x3f6b4b0c,0x3f63f473,0x3f6ed89e,0x3f655e0b,0x3f59f26a,0x3f4cae79,
0x3f64aa59,0x3f55b993,0x3f43e200,0x3f2f61a5,0x3f584853,0x3f42de29,0x3f299415,0x3f0cead0,0x3f49d112,0x3f2d1469,0x3f0b9a6b,0x3ecc9b8b,0x3f396842,0x3f14b039,0x3ed53641,0x3e72b651,
0x3f273656,0x3ef41f07,0x3e8e9a22,0x3d8a200a,0x3f13682a,0x3ebb31a0,0x3e09cf86,0xbdd53db9,0x3efc5d27,0x3e7ee6e1,0xbc490e90,0xbe8b9507,0x3ecf7bca,0x3e039502,0xbe22abb6,0xbeddbe79,
0x3ea09ae5,0x3bc90f88,0xbe9aa086,0xbf14b039,0x3e605c13,0xbdee3876,0xbee0924f,0xbf36206c,0x3dfab273,0xbe72b651,0xbf10d3cd,0xbf5233c6,0x3cc90ab0,0xbeb554ec,0xbf2e3bde,0xbf6816a8,
0x3f7d3aac,0x3f7c7eb0,0x3f7baccd,0x3f7ac516,0x3f710908,0x3f6d1c1d,0x3f68bf3c,0x3f63f473,0x3f5b941a,0x3f5233c6,0x3f47de65,0x3f3ca003,0x3f3daef9,0x3f2d1469,0x3f1b02c6,0x3f07a136,
0x3f187fc0,0x3eff17b2,0x3ec9b953,0x3e919ddd,0x3edae880,0x3e97a117,0x3e22abb6,0x3c96c9b6,0x3e78cfcc,0x3da3308c,0xbdafb680,0xbe7ee6e1,0x3d48fb30,0xbe1008b7,0xbea68f12,0xbeff17b2,
0xbe164083,0xbeb554ec,0xbf0b9a6b,0xbf36206c,0xbeac7cd4,0xbf0cead0,0xbf3b8f3b,0xbf5f8327,0xbf039c3d,0xbf385216,0xbf604621,0xbf78b47b,0xbf2beb4a,0xbf5ac450,0xbf778bc5,0xbf7fe129,
0xbf4d9f02,0xbf7294f8,0xbf7ffb11,0xbf748422,0xbf676bd8,0xbf7e9bc9,0xbf791298,0xbf577026,0xbf7853f8,0xbf7e4323,0xbf633c5a,0xbf2ac082,0xbf7fb10f,0xbf718f57,0xbf3fc767,0xbee363fa,
0x3f79c79d,0x3f78b47b,0x3f778bc5,0x3f764d97,0x3f5ebe05,0x3f591e6a,0x3f531849,0x3f4cae79,0x3f3085bb,0x3f239da9,0x3f15f6d9,0x3f07a136,0x3ee63375,0x3ebb31a0,0x3e8e9a22,0x3e419b37,
0x3e2f10a2,0x3d621469,0xbd7b2b74,0xbe354098,0xbdfab273,0xbe8586ce,0xbec9b953,0xbf04f484,0xbecf7bca,0xbf0cead0,0xbf2e3bde,0xbf4ac77f,0xbf273656,0xbf48d8b3,0xbf633c5a,0xbf756d97,
0xbf584853,0xbf708066,0xbf7dabcc,0xbf7f2f9d,0xbf76ba07,0xbf7fe129,0xbf7a7302,0xbf66becc,0xbf7fec43,0xbf756d97,0xbf59f26a,0xbf2f61a5,0xbf731447,0xbf5233c6,0xbf1ff6cb,0xbec1071e,
0xbf514d3d,0xbf19c200,0xbea68f12,0xbc96c9b6,0xbf1d7fd1,0xbea395c5,0x3c490e90,0x3eaf713a,0xbeb8442a,0xbbc90f88,0x3eb263ef,0x3f286605,0xbd96a905,0x3e9d9e78,0x3f24d225,0x3f628210,
0x0,0x3c96c9b6,0x3d16c32c,0x3d621469,0x0,0x3d2fe007,0x3dafb680,0x3e039502,0x0,0x3d8a200a,0x3e09cf86,0x3e4dee60,0x0,0x3dbc3ac3,0x3e3b6ecf,0x3e8b9507,
0x0,0x3dee3876,0x3e6c9a7f,0x3eaf713a,0x0,0x3e1008b7,0x3e8e9a22,0x3ed25a09,0x0,0x3e28defc,0x3ea68f12,0x3ef41f07,0x0,0x3e419b37,0x3ebe1d4a,0x3f0a48ad,
0x0,0x3e5a3997,0x3ed53641,0x3f19c200,0x0,0x3e72b651,0x3eebcbbb,0x3f286605,0x0,0x3e8586ce,0x3f00e7e4,0x3f36206c,0x0,0x3e919ddd,0x3f0b9a6b,0x3f42de29,
0x0,0x3e9d9e78,0x3f15f6d9,0x3f4e8d90,0x0,0x3ea986c4,0x3f1ff6cb,0x3f591e6a,0x0,0x3eb554ec,0x3f299415,0x3f628210,0x0,0x3ec1071e,0x3f32c8c9,0x3f6aab7b,
0x3d96a905,0x3dbc3ac3,0x3de1bc2e,0x3e039502,0x3e2f10a2,0x3e5a3997,0x3e827dc0,0x3e97a117,0x3e888e93,0x3ea986c4,0x3ec9b953,0x3ee900b7,0x3eb8442a,0x3ee363fa,0x3f064b82,0x3f19c200,
0x3ee63375,0x3f0cead0,0x3f24d225,0x3f3a7ca4,0x3f08f59b,0x3f26050a,0x3f3fc767,0x3f55b993,0x3f1d7fd1,0x3f3ca003,0x3f5695e5,0x3f6aab7b,0x3f3085bb,0x3f5064af,0x3f68bf3c,0x3f78b47b,
0x3f41d870,0x3f6106f2,0x3f75dec6,0x3f7f6ac7,0x3f514d3d,0x3f6e46be,0x3f7dabcc,0x3f7e9bc9,0x3f5ebe05,0x3f77f110,0x3f7ffb11,0x3f764d97,0x3f6a09a7,0x3f7de0b1,0x3f7cbfc9,0x3f66becc,
0x3f731447,0x3f7ffec4,0x3f740bdd,0x3f5064af,0x3f79c79d,0x3f7e4323,0x3f660f88,0x3f33e7bc,0x3f7e1324,0x3f78b47b,0x3f531849,0x3f121eb0,0x3f7fec43,0x3f6f6830,0x3f3b8f3b,0x3ed8106b,
0x3e164083,0x3e28defc,0x3e3b6ecf,0x3e4dee60,0x3eac7cd4,0x3ec1071e,0x3ed53641,0x3ee900b7,0x3f039c3d,0x3f121eb0,0x3f1ff6cb,0x3f2d1469,0x3f2beb4a,0x3f3ca003,0x3f4bbbf8,0x3f591e6a,
0x3f4d9f02,0x3f5df6be,0x3f6b4b0c,0x3f756d97,0x3f676bd8,0x3f748422,0x3f7cbfc9,0x3f7ff4e6,0x3f7853f8,0x3f7f2f9d,0x3f7f0e58,0x3f77f110,0x3f7fb10f,0x3f7d7474,0x3f721352,0x3f5df6be,
0x3f7d3aac,0x3f6f6830,0x3f5695e5,0x3f33e7bc,0x3f710908,0x3f55b993,0x3f2e3bde,0x3ef9a02d,0x3f5b941a,0x3f31a81d,0x3ef6e0cb,0x3e72b651,0x3f3daef9,0x3f04f484,0x3e827dc0,0xbcfb49ba,
0x3f187fc0,0x3ea395c5,0x3c490e90,0xbe97a117,0x3edae880,0x3dd53db9,0xbe6c9a7f,0xbf0a48ad,0x3e78cfcc,0xbdee3876,0xbeebcbbb,0xbf3ebc1b,0x3d48fb30,0xbea986c4,0xbf299415,0xbf655e0b,
0x3e605c13,0x3e72b651,0x3e827dc0,0x3e8b9507,0x3efc5d27,0x3f07a136,0x3f10d3cd,0x3f19c200,0x3f396842,0x3f44e3f5,0x3f4f7a1f,0x3f591e6a,0x3f64aa59,0x3f6e46be,0x3f75dec6,0x3f7b61fc,
0x3f7c3b28,0x3f7f9c18,0x3f7f84ab,0x3f7bf531,0x3f7e1324,0x3f772417,0x3f6b4b0c,0x3f5ac450,0x3f6a09a7,0x3f55b993,0x3f3b8f3b,0x3f1c420c,0x3f41d870,0x3f1ebc12,0x3eebcbbb,0x3e919ddd,
0x3f08f59b,0x3eaf713a,0x3e09cf86,0xbda3308c,0x3e888e93,0x3cfb49ba,0xbe541501,0xbeddbe79,0xbcc90ab0,0xbe919ddd,0xbf064b82,0xbf3a7ca4,0xbea09ae5,0xbf121eb0,0xbf47de65,0xbf6d1c1d,
0xbf13682a,0xbf4cae79,0xbf721352,0xbf7ff4e6,0xbf49d112,0xbf7294f8,0xbf7ffb11,0xbf708066,0xbf6ed89e,0xbf7ffec4,0xbf6ff573,0xbf40d0da,0xbf7f4e6d,0xbf73913f,0xbf43e200,0xbeee9479,
0x3f800000,0x3f800000,0x3f800000,0x3f800000,0x3f7ff4e6,0x3f7fc38f,0x3f7f6ac7,0x3f7eea9d,0x3f7fd397,0x3f7f0e58,0x3f7dabcc,0x3f7baccd,0x3f7f9c18,0x3f7de0b1,0x3f7ac516,0x3f764d97,
0x3f7f4e6d,0x3f7c3b28,0x3f76ba07,0x3f6ed89e,0x3f7eea9d,0x3f7a1e84,0x3f718f57,0x3f655e0b,0x3f7e70b0,0x3f778bc5,0x3f6b4b0c,0x3f59f26a,0x3f7de0b1,0x3f748422,0x3f63f473,0x3f4cae79,
0x3f7d3aac,0x3f710908,0x3f5b941a,0x3f3daef9,0x3f7c7eb0,0x3f6d1c1d,0x3f5233c6,0x3f2d1469,0x3f7baccd,0x3f68bf3c,0x3f47de65,0x3f1b02c6,0x3f7ac516,0x3f63f473,0x3f3ca003,0x3f07a136,
0x3f79c79d,0x3f5ebe05,0x3f3085bb,0x3ee63375,0x3f78b47b,0x3f591e6a,0x3f239da9,0x3ebb31a0,0x3f778bc5,0x3f531849,0x3f15f6d9,0x3e8e9a22,0x3f764d97,0x3f4cae79,0x3f07a136,0x3e419b37,
0x3f800000,0x3f800000,0x3f800000,0x3f800000,0x3f7e4323,0x3f7d7474,0x3f7c7eb0,0x3f7b61fc,0x3f791298,0x3f75dec6,0x3f721352,0x3f6db293,0x3f708066,0x3f696591,0x3f6106f2,0x3f577026,
0x3f64aa59,0x3f584853,0x3f49d112,0x3f396842,0x3f55b993,0x3f42de29,0x3f2d1469,0x3f14b039,0x3f43e200,0x3f299415,0x3f0b9a6b,0x3ed53641,0x3f2f61a5,0x3f0cead0,0x3ecc9b8b,0x3e72b651,
0x3f187fc0,0x3edae880,0x3e78cfcc,0x3d48fb30,0x3eff17b2,0x3e97a117,0x3da3308c,0xbe1008b7,0x3ec9b953,0x3e22abb6,0xbdafb680,0xbea68f12,0x3e919ddd,0x3c96c9b6,0xbe7ee6e1,0xbeff17b2,
0x3e2f10a2,0xbdfab273,0xbecf7bca,0xbf273656,0x3d621469,0xbe8586ce,0xbf0cead0,0xbf48d8b3,0xbd7b2b74,0xbec9b953,0xbf2e3bde,0xbf633c5a,0xbe354098,0xbf04f484,0xbf4ac77f,0xbf756d97,
0x3f800000,0x3f800000,0x3f800000,0x3f800000,0x3f7a1e84,0x3f78b47b,0x3f772417,0x3f756d97,0x3f68bf3c,0x3f633c5a,0x3f5d2d53,0x3f5695e5,0x3f4cae79,0x3f40d0da,0x3f33e7bc,0x3f26050a,
0x3f273656,0x3f13682a,0x3efc5d27,0x3ecf7bca,0x3ef41f07,0x3ebb31a0,0x3e7ee6e1,0x3e039502,0x3e8e9a22,0x3e09cf86,0xbc490e90,0xbe22abb6,0x3d8a200a,0xbdd53db9,0xbe8b9507,0xbeddbe79,
0xbe164083,0xbeac7cd4,0xbf039c3d,0xbf2beb4a,0xbeb554ec,0xbf0cead0,0xbf385216,0xbf5ac450,0xbf0b9a6b,0xbf3b8f3b,0xbf604621,0xbf778bc5,0xbf36206c,0xbf5f8327,0xbf78b47b,0xbf7fe129,
0xbf584853,0xbf76ba07,0xbf7fec43,0xbf731447,0xbf708066,0xbf7fe129,0xbf756d97,0xbf5233c6,0xbf7dabcc,0xbf7a7302,0xbf59f26a,0xbf1ff6cb,0xbf7f2f9d,0xbf66becc,0xbf2f61a5,0xbec1071e,
0x3f800000,0x3f800000,0x3f800000,0x3f800000,0x3f73913f,0x3f718f57,0x3f6f6830,0x3f6d1c1d,0x3f4f7a1f,0x3f47de65,0x3f3fc767,0x3f373a23,0x3f173c07,0x3f07a136,0x3eee9479,0x3ecc9b8b,
0x3ea09ae5,0x3e605c13,0x3dfab273,0x3cc90ab0,0x3bc90f88,0xbdee3876,0xbe72b651,0xbeb554ec,0xbe9aa086,0xbee0924f,0xbf10d3cd,0xbf2e3bde,0xbf14b039,0xbf36206c,0xbf5233c6,0xbf6816a8,
0xbf4d9f02,0xbf676bd8,0xbf7853f8,0xbf7fb10f,0xbf7294f8,0xbf7e9bc9,0xbf7e4323,0xbf718f57,0xbf7ffb11,0xbf791298,0xbf633c5a,0xbf3fc767,0xbf748422,0xbf577026,0xbf2ac082,0xbee363fa,
0xbf514d3d,0xbf1d7fd1,0xbeb8442a,0xbd96a905,0xbf19c200,0xbea395c5,0xbbc90f88,0x3e9d9e78,0xbea68f12,0x3c490e90,0x3eb263ef,0x3f24d225,0xbc96c9b6,0x3eaf713a,0x3f286605,0x3f628210,
0x0,0x0,0x0,0x0,0x3c96c9b6,0x3d2fe007,0x3d8a200a,0x3dbc3ac3,0x3d16c32c,0x3dafb680,0x3e09cf86,0x3e3b6ecf,0x3d621469,0x3e039502,0x3e4dee60,0x3e8b9507,
0x3d96a905,0x3e2f10a2,0x3e888e93,0x3eb8442a,0x3dbc3ac3,0x3e5a3997,0x3ea986c4,0x3ee363fa,0x3de1bc2e,0x3e827dc0,0x3ec9b953,0x3f064b82,0x3e039502,0x3e97a117,0x3ee900b7,0x3f19c200,
0x3e164083,0x3eac7cd4,0x3f039c3d,0x3f2beb4a,0x3e28defc,0x3ec1071e,0x3f121eb0,0x3f3ca003,0x3e3b6ecf,0x3ed53641,0x3f1ff6cb,0x3f4bbbf8,0x3e4dee60,0x3ee900b7,0x3f2d1469,0x3f591e6a,
0x3e605c13,0x3efc5d27,0x3f396842,0x3f64aa59,0x3e72b651,0x3f07a136,0x3f44e3f5,0x3f6e46be,0x3e827dc0,0x3f10d3cd,0x3f4f7a1f,0x3f75dec6,0x3e8b9507,0x3f19c200,0x3f591e6a,0x3f7b61fc,
0x0,0x0,0x0,0x0,0x3dee3876,0x3e1008b7,0x3e28defc,0x3e419b37,0x3e6c9a7f,0x3e8e9a22,0x3ea68f12,0x3ebe1d4a,0x3eaf713a,0x3ed25a09,0x3ef41f07,0x3f0a48ad,
0x3ee63375,0x3f08f59b,0x3f1d7fd1,0x3f3085bb,0x3f0cead0,0x3f26050a,0x3f3ca003,0x3f5064af,0x3f24d225,0x3f3fc767,0x3f5695e5,0x3f68bf3c,0x3f3a7ca4,0x3f55b993,0x3f6aab7b,0x3f78b47b,
0x3f4d9f02,0x3f676bd8,0x3f7853f8,0x3f7fb10f,0x3f5df6be,0x3f748422,0x3f7f2f9d,0x3f7d7474,0x3f6b4b0c,0x3f7cbfc9,0x3f7f0e58,0x3f721352,0x3f756d97,0x3f7ff4e6,0x3f77f110,0x3f5df6be,
0x3f7c3b28,0x3f7e1324,0x3f6a09a7,0x3f41d870,0x3f7f9c18,0x3f772417,0x3f55b993,0x3f1ebc12,0x3f7f84ab,0x3f6b4b0c,0x3f3b8f3b,0x3eebcbbb,0x3f7bf531,0x3f5ac450,0x3f1c420c,0x3e919ddd,
0x0,0x0,0x0,0x0,0x3e5a3997,0x3e72b651,0x3e8586ce,0x3e919ddd,0x3ed53641,0x3eebcbbb,0x3f00e7e4,0x3f0b9a6b,0x3f19c200,0x3f286605,0x3f36206c,0x3f42de29,
0x3f41d870,0x3f514d3d,0x3f5ebe05,0x3f6a09a7,0x3f6106f2,0x3f6e46be,0x3f77f110,0x3f7de0b1,0x3f75dec6,0x3f7dabcc,0x3f7ffb11,0x3f7cbfc9,0x3f7f6ac7,0x3f7e9bc9,0x3f764d97,0x3f66becc,
0x3f7d3aac,0x3f710908,0x3f5b941a,0x3f3daef9,0x3f6f6830,0x3f55b993,0x3f31a81d,0x3f04f484,0x3f5695e5,0x3f2e3bde,0x3ef6e0cb,0x3e827dc0,0x3f33e7bc,0x3ef9a02d,0x3e72b651,0xbcfb49ba,
0x3f08f59b,0x3e888e93,0xbcc90ab0,0xbea09ae5,0x3eaf713a,0x3cfb49ba,0xbe919ddd,0xbf121eb0,0x3e09cf86,0xbe541501,0xbf064b82,0xbf47de65,0xbda3308c,0xbeddbe79,0xbf3a7ca4,0xbf6d1c1d,
0x0,0x0,0x0,0x0,0x3e9d9e78,0x3ea986c4,0x3eb554ec,0x3ec1071e,0x3f15f6d9,0x3f1ff6cb,0x3f299415,0x3f32c8c9,0x3f4e8d90,0x3f591e6a,0x3f628210,0x3f6aab7b,
0x3f731447,0x3f79c79d,0x3f7e1324,0x3f7fec43,0x3f7ffec4,0x3f7e4323,0x3f78b47b,0x3f6f6830,0x3f740bdd,0x3f660f88,0x3f531849,0x3f3b8f3b,0x3f5064af,0x3f33e7bc,0x3f121eb0,0x3ed8106b,
0x3f187fc0,0x3edae880,0x3e78cfcc,0x3d48fb30,0x3ea395c5,0x3dd53db9,0xbdee3876,0xbea986c4,0x3c490e90,0xbe6c9a7f,0xbeebcbbb,0xbf299415,0xbe97a117,0xbf0a48ad,0xbf3ebc1b,0xbf655e0b,
0xbf13682a,0xbf49d112,0xbf6ed89e,0xbf7f4e6d,0xbf4cae79,0xbf7294f8,0xbf7ffec4,0xbf73913f,0xbf721352,0xbf7ffb11,0xbf6ff573,0xbf43e200,0xbf7ff4e6,0xbf708066,0xbf40d0da,0xbeee9479,
0x3400,0x2d643388,0x3113322f,0x32dd301d,0x3400,0x315f31ed,0x33f62646,0x326db0c4,0x3400,0x333b2ed7,0x322fb113,0xabc6b3c3,0x3400,0x33fe2248,0x2646b3f6,0xb3eaa8b2,
0x3400,0x3388ad64,0xb113b22f,0xb01d32dd,0x3400,0x31edb15f,0xb3f6a646,0x30c4326d,0x3400,0x2ed7b33b,0xb22f3113,0x33c3abc6,0x3400,0x2248b3fe,0xa64633f6,0x28b2b3ea,
0x3400,0xad64b388,0x3113322f,0xb2ddb01d,0x3400,0xb15fb1ed,0x33f62646,0xb26d30c4,0x3400,0xb33baed7,0x322fb113,0x2bc633c3,0x3400,0xb3fea248,0x2646b3f6,0x33ea28b2,
0x3400,0xb3882d64,0xb113b22f,0x301db2dd,0x3400,0xb1ed315f,0xb3f6a646,0xb0c4b26d,0x3400,0xaed7333b,0xb22f3113,0xb3c32bc6,0x3400,0xa24833fe,0xa64633f6,0xa8b233ea,
0x33d92a3e,0x33eaa8b2,0x330eaf8b,0x315fb1ed,0x2a3eb3d9,0xb01db2dd,0xb3a8aca5,0xb33b2ed7,0xb3d9aa3e,0xb0c4326d,0x2f8b330e,0x33fea248,0xaa3e33d9,0x33c32bc6,0x2ca5b3a8,0xb388ad64,
0x33d92a3e,0xa8b2b3ea,0xb30e2f8b,0x31ed315f,0x2a3eb3d9,0xb2dd301d,0x33a82ca5,0xaed7b33b,0xb3d9aa3e,0x326d30c4,0xaf8bb30e,0x224833fe,0xaa3e33d9,0x2bc6b3c3,0xaca533a8,0x2d64b388,
0x33d92a3e,0xb3ea28b2,0x330eaf8b,0xb15f31ed,0x2a3eb3d9,0x301d32dd,0xb3a8aca5,0x333baed7,0xb3d9aa3e,0x30c4b26d,0x2f8b330e,0xb3fe2248,0xaa3e33d9,0xb3c3abc6,0x2ca5b3a8,0x33882d64,
0x33d92a3e,0x28b233ea,0xb30e2f8b,0xb1edb15f,0x2a3eb3d9,0x32ddb01d,0x33a82ca5,0x2ed7333b,0xb3d9aa3e,0xb26db0c4,0xaf8bb30e,0xa248b3fe,0xaa3e33d9,0xabc633c3,0xaca533a8,0xad643388,
0x2e1fb364,0x2248b3fe,0xaca5b3a8,0xb0c4b26d,0xae1f3364,0x2d643388,0x330e2f8b,0x33c3abc6,0x2e1fb364,0xb15fb1ed,0xb3a82ca5,0xa8b233ea,0xae1f3364,0x333b2ed7,0x2f8bb30e,0xb2ddb01d,
0x2e1fb364,0xb3fea248,0x2ca533a8,0x326db0c4,0xae1f3364,0x3388ad64,0xb30eaf8b,0x2bc633c3,0x2e1fb364,0xb1ed315f,0x33a8aca5,0xb3eaa8b2,0xae1f3364,0x2ed7b33b,0xaf8b330e,0x301db2dd,
0x2e1fb364,0xa24833fe,0xaca5b3a8,0x30c4326d,0xae1f3364,0xad64b388,0x330e2f8b,0xb3c32bc6,0x2e1fb364,0x315f31ed,0xb3a82ca5,0x28b2b3ea,0xae1f3364,0xb33baed7,0x2f8bb30e,0x32dd301d,
0x2e1fb364,0x33fe2248,0x2ca533a8,0xb26d30c4,0xae1f3364,0xb3882d64,0xb30eaf8b,0xabc6b3c3,0x2e1fb364,0x31edb15f,0x33a8aca5,0x33ea28b2,0xae1f3364,0xaed7333b,0xaf8b330e,0xb01d32dd,
0xb2a7b072,0xb3c3abc6,0xb3f62646,0xb33b2ed7,0x3072b2a7,0xa8b2b3ea,0xb22fb113,0xb3fe2248,0x32a73072,0x32ddb01d,0xa646b3f6,0xb388ad64,0xb07232a7,0x326d30c4,0x3113b22f,0xb1edb15f,
0xb2a7b072,0xabc633c3,0x33f6a646,0xaed7b33b,0x3072b2a7,0xb3ea28b2,0x322f3113,0xa248b3fe,0x32a73072,0xb01db2dd,0x264633f6,0x2d64b388,0xb07232a7,0x30c4b26d,0xb113322f,0x315fb1ed,
0xb2a7b072,0x33c32bc6,0xb3f62646,0x333baed7,0x3072b2a7,0x28b233ea,0xb22fb113,0x33fea248,0x32a73072,0xb2dd301d,0xa646b3f6,0x33882d64,0xb07232a7,0xb26db0c4,0x3113b22f,0x31ed315f,
0xb2a7b072,0x2bc6b3c3,0x33f6a646,0x2ed7333b,0x3072b2a7,0x33eaa8b2,0x322f3113,0x224833fe,0x32a73072,0x301d32dd,0x264633f6,0xad643388,0xb07232a7,0xb0c4326d,0xb113322f,0xb15f31ed,
0x34000000,0x3388ad64,0x322fb113,0x301db2dd,0x34000000,0x31edb15f,0x2646b3f6,0xb0c4b26d,0x34000000,0x2ed7b33b,0xb113b22f,0xb3c32bc6,0x34000000,0x2248b3fe,0xb3f6a646,0xa8b233ea,
0x34000000,0xad64b388,0xb22f3113,0x32dd301d,0x34000000,0xb15fb1ed,0xa64633f6,0x326db0c4,0x34000000,0xb33baed7,0x3113322f,0xabc6b3c3,0x34000000,0xb3fea248,0x33f62646,0xb3eaa8b2,
0x34000000,0xb3882d64,0x322fb113,0xb01d32dd,0x34000000,0xb1ed315f,0x2646b3f6,0x30c4326d,0x34000000,0xaed7333b,0xb113b22f,0x33c3abc6,0x34000000,0xa24833fe,0xb3f6a646,0x28b2b3ea,
0x34000000,0x2d643388,0xb22f3113,0xb2ddb01d,0x34000000,0x315f31ed,0xa64633f6,0xb26d30c4,0x34000000,0x333b2ed7,0x3113322f,0x2bc633c3,0x34000000,0x33fe2248,0x33f62646,0x33ea28b2,
0x2a3eb3d9,0xa8b2b3ea,0xaf8bb30e,0xb1edb15f,0xb3d9aa3e,0xb2dd301d,0xaca533a8,0x2ed7333b,0xaa3e33d9,0x326d30c4,0x330eaf8b,0xa248b3fe,0x33d92a3e,0x2bc6b3c3,0xb3a8aca5,0xad643388,
0x2a3eb3d9,0xb3ea28b2,0x2f8b330e,0x315fb1ed,0xb3d9aa3e,0x301d32dd,0x2ca5b3a8,0xb33b2ed7,0xaa3e33d9,0x30c4b26d,0xb30e2f8b,0x33fea248,0x33d92a3e,0xb3c3abc6,0x33a82ca5,0xb388ad64,
0x2a3eb3d9,0x28b233ea,0xaf8bb30e,0x31ed315f,0xb3d9aa3e,0x32ddb01d,0xaca533a8,0xaed7b33b,0xaa3e33d9,0xb26db0c4,0x330eaf8b,0x224833fe,0x33d92a3e,0xabc633c3,0xb3a8aca5,0x2d64b388,
0x2a3eb3d9,0x33eaa8b2,0x2f8b330e,0xb15f31ed,0xb3d9aa3e,0xb01db2dd,0x2ca5b3a8,0x333baed7,0xaa3e33d9,0xb0c4326d,0xb30e2f8b,0xb3fe2248,0x33d92a3e,0x33c32bc6,0x33a82ca5,0x33882d64,
0xb364ae1f,0xb3fea248,0xb3a82ca5,0xb26d30c4,0x33642e1f,0x3388ad64,0x2f8bb30e,0xabc6b3c3,0xb364ae1f,0xb1ed315f,0x2ca533a8,0x33ea28b2,0x33642e1f,0x2ed7b33b,0xb30eaf8b,0xb01d32dd,
0xb364ae1f,0xa24833fe,0x33a8aca5,0xb0c4b26d,0x33642e1f,0xad64b388,0xaf8b330e,0x33c3abc6,0xb364ae1f,0x315f31ed,0xaca5b3a8,0xa8b233ea,0x33642e1f,0xb33baed7,0x330e2f8b,0xb2ddb01d,
0xb364ae1f,0x33fe2248,0xb3a82ca5,0x326db0c4,0x33642e1f,0xb3882d64,0x2f8bb30e,0x2bc633c3,0xb364ae1f,0x31edb15f,0x2ca533a8,0xb3eaa8b2,0x33642e1f,0xaed7333b,0xb30eaf8b,0x301db2dd,
0xb364ae1f,0x2248b3fe,0x33a8aca5,0x30c4326d,0x33642e1f,0x2d643388,0xaf8b330e,0xb3c32bc6,0xb364ae1f,0xb15fb1ed,0xaca5b3a8,0x28b2b3ea,0x33642e1f,0x333b2ed7,0x330e2f8b,0x32dd301d,
0xb07232a7,0xabc633c3,0x264633f6,0x2ed7333b,0xb2a7b072,0xb3ea28b2,0xb113322f,0x224833fe,0x3072b2a7,0xb01db2dd,0xb3f62646,0xad643388,0x32a73072,0x30c4b26d,0xb22fb113,0xb15f31ed,
0xb07232a7,0x33c32bc6,0xa646b3f6,0xb33b2ed7,0xb2a7b072,0x28b233ea,0x3113b22f,0xb3fe2248,0x3072b2a7,0xb2dd301d,0x33f6a646,0xb388ad64,0x32a73072,0xb26db0c4,0x322f3113,0xb1edb15f,
0xb07232a7,0x2bc6b3c3,0x264633f6,0xaed7b33b,0xb2a7b072,0x33eaa8b2,0xb113322f,0xa248b3fe,0x3072b2a7,0x301d32dd,0xb3f62646,0x2d64b388,0x32a73072,0xb0c4326d,0xb22fb113,0x315fb1ed,
0xb07232a7,0xb3c3abc6,0xa646b3f6,0x333baed7,0xb2a7b072,0xa8b2b3ea,0x3113b22f,0x33fea248,0x3072b2a7,0x32ddb01d,0x33f6a646,0x33882d64,0x32a73072,0x326d30c4,0x322f3113,0x31ed315f,
0x3400,0x3400,0x3400,0x3400,0x3400,0x2e1f3364,0x31a831a8,0x33642e1f,0x3400,0x31a831a8,0x34000000,0x31a8b1a8,0x3400,0x33642e1f,0x31a8b1a8,0xae1fb364,
0x3400,0x34000000,0xb400,0xb4008000,0x3400,0x3364ae1f,0xb1a8b1a8,0xae1f3364,0x3400,0x31a8b1a8,0xb4008000,0x31a831a8,0x3400,0x2e1fb364,0xb1a831a8,0x3364ae1f,
0x3400,0xb400,0x3400,0xb400,0x3400,0xae1fb364,0x31a831a8,0xb364ae1f,0x3400,0xb1a8b1a8,0x34000000,0xb1a831a8,0x3400,0xb364ae1f,0x31a8b1a8,0x2e1f3364,
0x3400,0xb4008000,0xb400,0x34000000,0x3400,0xb3642e1f,0xb1a8b1a8,0x2e1fb364,0x3400,0xb1a831a8,0xb4008000,0xb1a8b1a8,0x3400,0xae1f3364,0xb1a831a8,0xb3642e1f,
0x3400,0x3400,0x3400,0x3400,0x34000000,0x3364ae1f,0x31a8b1a8,0x2e1fb364,0xb400,0xb1a8b1a8,0xb4008000,0xb1a831a8,0xb4008000,0xae1f3364,0x31a831a8,0x3364ae1f,
0x3400,0x34000000,0xb400,0xb4008000,0x34000000,0xae1fb364,0xb1a831a8,0x33642e1f,0xb400,0xb1a831a8,0x34000000,0xb1a8b1a8,0xb4008000,0x33642e1f,0xb1a8b1a8,0x2e1f3364,
0x3400,0xb400,0x3400,0xb400,0x34000000,0xb3642e1f,0x31a8b1a8,0xae1f3364,0xb400,0x31a831a8,0xb4008000,0x31a8b1a8,0xb4008000,0x2e1fb364,0x31a831a8,0xb3642e1f,
0x3400,0xb4008000,0xb400,0x34000000,0x34000000,0x2e1f3364,0xb1a831a8,0xb364ae1f,0xb400,0x31a8b1a8,0x34000000,0x31a831a8,0xb4008000,0xb364ae1f,0xb1a8b1a8,0xae1fb364,
0x3400,0x3400,0x3400,0x3400,0xb400,0xae1fb364,0xb1a8b1a8,0xb364ae1f,0x3400,0x31a831a8,0x34000000,0x31a8b1a8,0xb400,0xb364ae1f,0xb1a831a8,0x2e1f3364,
0x3400,0x34000000,0xb400,0xb4008000,0xb400,0xb3642e1f,0x31a831a8,0x2e1fb364,0x3400,0x31a8b1a8,0xb4008000,0x31a831a8,0xb400,0xae1f3364,0x31a8b1a8,0xb3642e1f,
0x3400,0xb400,0x3400,0xb400,0xb400,0x2e1f3364,0xb1a8b1a8,0x33642e1f,0x3400,0xb1a8b1a8,0x34000000,0xb1a831a8,0xb400,0x33642e1f,0xb1a831a8,0xae1fb364,
0x3400,0xb4008000,0xb400,0x34000000,0xb400,0x3364ae1f,0x31a831a8,0xae1f3364,0x3400,0xb1a831a8,0xb4008000,0xb1a8b1a8,0xb400,0x2e1fb364,0x31a8b1a8,0x3364ae1f,
0x3400,0x3400,0x3400,0x3400,0xb4008000,0xb3642e1f,0xb1a831a8,0xae1f3364,0xb400,0xb1a8b1a8,0xb4008000,0xb1a831a8,0x34000000,0x2e1fb364,0xb1a8b1a8,0xb3642e1f,
0x3400,0x34000000,0xb400,0xb4008000,0xb4008000,0x2e1f3364,0x31a8b1a8,0xb364ae1f,0xb400,0xb1a831a8,0x34000000,0xb1a8b1a8,0x34000000,0xb364ae1f,0x31a831a8,0xae1fb364,
0x3400,0xb400,0x3400,0xb400,0xb4008000,0x3364ae1f,0xb1a831a8,0x2e1fb364,0xb400,0x31a831a8,0xb4008000,0x31a8b1a8,0x34000000,0xae1f3364,0xb1a8b1a8,0x3364ae1f,
0x3400,0xb4008000,0xb400,0x34000000,0xb4008000,0xae1fb364,0x31a8b1a8,0x33642e1f,0xb400,0x31a8b1a8,0x34000000,0x31a831a8,0x34000000,0x33642e1f,0x31a831a8,0x2e1f3364,
0x34000000,0x34000000,0x34000000,0x34000000,0x34000000,0x3364ae1f,0x31a8b1a8,0x2e1fb364,0x34000000,0x31a8b1a8,0xb400,0xb1a8b1a8,0x34000000,0x2e1fb364,0xb1a8b1a8,0xb3642e1f,
0x34000000,0xb400,0xb4008000,0x80003400,0x34000000,0xae1fb364,0xb1a831a8,0x33642e1f,0x34000000,0xb1a8b1a8,0x80003400,0x31a8b1a8,0x34000000,0xb364ae1f,0x31a831a8,0xae1fb364,
0x34000000,0xb4008000,0x34000000,0xb4008000,0x34000000,0xb3642e1f,0x31a8b1a8,0xae1f3364,0x34000000,0xb1a831a8,0xb400,0x31a831a8,0x34000000,0xae1f3364,0xb1a8b1a8,0x3364ae1f,
0x34000000,0x80003400,0xb4008000,0xb400,0x34000000,0x2e1f3364,0xb1a831a8,0xb364ae1f,0x34000000,0x31a831a8,0x80003400,0xb1a831a8,0x34000000,0x33642e1f,0x31a831a8,0x2e1f3364,
0x34000000,0x34000000,0x34000000,0x34000000,0xb400,0xae1fb364,0xb1a8b1a8,0xb364ae1f,0xb4008000,0xb1a831a8,0x80003400,0x31a831a8,0x80003400,0x33642e1f,0x31a8b1a8,0xae1fb364,
0x34000000,0xb400,0xb4008000,0x80003400,0xb400,0xb3642e1f,0x31a831a8,0x2e1fb364,0xb4008000,0x31a831a8,0xb400,0xb1a831a8,0x80003400,0x2e1fb364,0xb1a831a8,0x3364ae1f,
0x34000000,0xb4008000,0x34000000,0xb4008000,0xb400,0x2e1f3364,0xb1a8b1a8,0x33642e1f,0xb4008000,0x31a8b1a8,0x80003400,0xb1a8b1a8,0x80003400,0xb364ae1f,0x31a8b1a8,0x2e1f3364,
0x34000000,0x80003400,0xb4008000,0xb400,0xb400,0x3364ae1f,0x31a831a8,0xae1f3364,0xb4008000,0xb1a8b1a8,0xb400,0x31a8b1a8,0x80003400,0xae1f3364,0xb1a831a8,0xb3642e1f,
0x34000000,0x34000000,0x34000000,0x34000000,0xb4008000,0xb3642e1f,0xb1a831a8,0xae1f3364,0x34000000,0x31a8b1a8,0xb400,0xb1a8b1a8,0xb4008000,0xae1f3364,0x31a831a8,0x3364ae1f,
0x34000000,0xb400,0xb4008000,0x80003400,0xb4008000,0x2e1f3364,0x31a8b1a8,0xb364ae1f,0x34000000,0xb1a8b1a8,0x80003400,0x31a8b1a8,0xb4008000,0x33642e1f,0xb1a8b1a8,0x2e1f3364,
0x34000000,0xb4008000,0x34000000,0xb4008000,0xb4008000,0x3364ae1f,0xb1a831a8,0x2e1fb364,0x34000000,0xb1a831a8,0xb400,0x31a831a8,0xb4008000,0x2e1fb364,0x31a831a8,0xb3642e1f,
0x34000000,0x80003400,0xb4008000,0xb400,0xb4008000,0xae1fb364,0x31a8b1a8,0x33642e1f,0x34000000,0x31a831a8,0x80003400,0xb1a831a8,0xb4008000,0xb364ae1f,0xb1a8b1a8,0xae1fb364,
0x34000000,0x34000000,0x34000000,0x34000000,0x80003400,0x2e1f3364,0x31a831a8,0x33642e1f,0xb4008000,0xb1a831a8,0x80003400,0x31a831a8,0xb400,0xb364ae1f,0xb1a831a8,0x2e1f3364,
0x34000000,0xb400,0xb4008000,0x80003400,0x80003400,0x3364ae1f,0xb1a8b1a8,0xae1f3364,0xb4008000,0x31a831a8,0xb400,0xb1a831a8,0xb400,0xae1f3364,0x31a8b1a8,0xb3642e1f,
0x34000000,0xb4008000,0x34000000,0xb4008000,0x80003400,0xae1fb364,0x31a831a8,0xb364ae1f,0xb4008000,0x31a8b1a8,0x80003400,0xb1a8b1a8,0xb400,0x33642e1f,0xb1a831a8,0xae1fb364,
0x34000000,0x80003400,0xb4008000,0xb400,0x80003400,0xb3642e1f,0xb1a8b1a8,0x2e1fb364,0xb4008000,0xb1a8b1a8,0xb400,0x31a8b1a8,0xb400,0x2e1fb364,0x31a8b1a8,0x3364ae1f,
0x3400,0x3400,0x3400,0x3400,0x3400,0xae1f3364,0xb1a831a8,0xb3642e1f,0x3400,0xb1a831a8,0xb4000000,0xb1a8b1a8,0x3400,0xb3642e1f,0xb1a8b1a8,0x2e1fb364,
0x3400,0xb4000000,0x8000b400,0x34008000,0x3400,0xb364ae1f,0x31a8b1a8,0x2e1f3364,0x3400,0xb1a8b1a8,0x34008000,0xb1a831a8,0x3400,0xae1fb364,0x31a831a8,0xb364ae1f,
0x3400,0x8000b400,0x3400,0x8000b400,0x3400,0x2e1fb364,0xb1a831a8,0x3364ae1f,0x3400,0x31a8b1a8,0xb4000000,0x31a831a8,0x3400,0x3364ae1f,0xb1a8b1a8,0xae1f3364,
0x3400,0x34008000,0x8000b400,0xb4000000,0x3400,0x33642e1f,0x31a8b1a8,0xae1fb364,0x3400,0x31a831a8,0x34008000,0x31a8b1a8,0x3400,0x2e1f3364,0x31a831a8,0x33642e1f,
0x3400,0x3400,0x3400,0x3400,0xb4000000,0xb364ae1f,0xb1a8b1a8,0xae1fb364,0x8000b400,0x31a8b1a8,0x34008000,0x31a831a8,0x34008000,0x2e1f3364,0xb1a831a8,0xb364ae1f,
0x3400,0xb4000000,0x8000b400,0x34008000,0xb4000000,0x2e1fb364,0x31a831a8,0xb3642e1f,0x8000b400,0x31a831a8,0xb4000000,0x31a8b1a8,0x34008000,0xb3642e1f,0x31a8b1a8,0xae1f3364,
0x3400,0x8000b400,0x3400,0x8000b400,0xb4000000,0x33642e1f,0xb1a8b1a8,0x2e1f3364,0x8000b400,0xb1a831a8,0x34008000,0xb1a8b1a8,0x34008000,0xae1fb364,0xb1a831a8,0x33642e1f,
0x3400,0x34008000,0x8000b400,0xb4000000,0xb4000000,0xae1f3364,0x31a831a8,0x3364ae1f,0x8000b400,0xb1a8b1a8,0xb4000000,0xb1a831a8,0x34008000,0x3364ae1f,0x31a8b1a8,0x2e1fb364,
0x3400,0x3400,0x3400,0x3400,0x8000b400,0x2e1fb364,0x31a8b1a8,0x3364ae1f,0x3400,0xb1a831a8,0xb4000000,0xb1a8b1a8,0x8000b400,0x3364ae1f,0x31a831a8,0xae1f3364,
0x3400,0xb4000000,0x8000b400,0x34008000,0x8000b400,0x33642e1f,0xb1a831a8,0xae1fb364,0x3400,0xb1a8b1a8,0x34008000,0xb1a831a8,0x8000b400,0x2e1f3364,0xb1a8b1a8,0x33642e1f,
0x3400,0x8000b400,0x3400,0x8000b400,0x8000b400,0xae1f3364,0x31a8b1a8,0xb3642e1f,0x3400,0x31a8b1a8,0xb4000000,0x31a831a8,0x8000b400,0xb3642e1f,0x31a831a8,0x2e1fb364,
0x3400,0x34008000,0x8000b400,0xb4000000,0x8000b400,0xb364ae1f,0xb1a831a8,0x2e1f3364,0x3400,0x31a831a8,0x34008000,0x31a8b1a8,0x8000b400,0xae1fb364,0xb1a8b1a8,0xb364ae1f,
0x3400,0x3400,0x3400,0x3400,0x34008000,0x33642e1f,0x31a831a8,0x2e1f3364,0x8000b400,0x31a8b1a8,0x34008000,0x31a831a8,0xb4000000,0xae1fb364,0x31a8b1a8,0x33642e1f,
0x3400,0xb4000000,0x8000b400,0x34008000,0x34008000,0xae1f3364,0xb1a8b1a8,0x3364ae1f,0x8000b400,0x31a831a8,0xb4000000,0x31a8b1a8,0xb4000000,0x3364ae1f,0xb1a831a8,0x2e1fb364,
0x3400,0x8000b400,0x3400,0x8000b400,0x34008000,0xb364ae1f,0x31a831a8,0xae1fb364,0x8000b400,0xb1a831a8,0x34008000,0xb1a8b1a8,0xb4000000,0x2e1f3364,0x31a8b1a8,0xb364ae1f,
0x3400,0x34008000,0x8000b400,0xb4000000,0x34008000,0x2e1fb364,0xb1a8b1a8,0xb3642e1f,0x8000b400,0xb1a8b1a8,0xb4000000,0xb1a831a8,0xb4000000,0xb3642e1f,0xb1a831a8,0xae1f3364,
0x34000000,0x34000000,0x34000000,0x34000000,0x34000000,0x33642e1f,0x31a831a8,0x2e1f3364,0x34000000,0x31a831a8,0x3400,0xb1a831a8,0x34000000,0x2e1f3364,0xb1a831a8,0xb364ae1f,
0x34000000,0x3400,0xb4000000,0x8000b400,0x34000000,0xae1f3364,0xb1a8b1a8,0x3364ae1f,0x34000000,0xb1a831a8,0x8000b400,0x31a831a8,0x34000000,0xb3642e1f,0x31a8b1a8,0xae1f3364,
0x34000000,0xb4000000,0x34000000,0xb4000000,0x34000000,0xb364ae1f,0x31a831a8,0xae1fb364,0x34000000,0xb1a8b1a8,0x3400,0x31a8b1a8,0x34000000,0xae1fb364,0xb1a831a8,0x33642e1f,
0x34000000,0x8000b400,0xb4000000,0x3400,0x34000000,0x2e1fb364,0xb1a8b1a8,0xb3642e1f,0x34000000,0x31a8b1a8,0x8000b400,0xb1a8b1a8,0x34000000,0x3364ae1f,0x31a8b1a8,0x2e1fb364,
0x34000000,0x34000000,0x34000000,0x34000000,0x3400,0xae1f3364,0xb1a831a8,0xb3642e1f,0xb4000000,0xb1a8b1a8,0x8000b400,0x31a8b1a8,0x8000b400,0x3364ae1f,0x31a831a8,0xae1f3364,
0x34000000,0x3400,0xb4000000,0x8000b400,0x3400,0xb364ae1f,0x31a8b1a8,0x2e1f3364,0xb4000000,0x31a8b1a8,0x3400,0xb1a8b1a8,0x8000b400,0x2e1f3364,0xb1a8b1a8,0x33642e1f,
0x34000000,0xb4000000,0x34000000,0xb4000000,0x3400,0x2e1fb364,0xb1a831a8,0x3364ae1f,0xb4000000,0x31a831a8,0x8000b400,0xb1a831a8,0x8000b400,0xb3642e1f,0x31a831a8,0x2e1fb364,
0x34000000,0x8000b400,0xb4000000,0x3400,0x3400,0x33642e1f,0x31a8b1a8,0xae1fb364,0xb4000000,0xb1a831a8,0x3400,0x31a831a8,0x8000b400,0xae1fb364,0xb1a8b1a8,0xb364ae1f,
0x34000000,0x34000000,0x34000000,0x34000000,0xb4000000,0xb364ae1f,0xb1a8b1a8,0xae1fb364,0x34000000,0x31a831a8,0x3400,0xb1a831a8,0xb4000000,0xae1fb364,0x31a8b1a8,0x33642e1f,
0x34000000,0x3400,0xb4000000,0x8000b400,0xb4000000,0x2e1fb364,0x31a831a8,0xb3642e1f,0x34000000,0xb1a831a8,0x8000b400,0x31a831a8,0xb4000000,0x3364ae1f,0xb1a831a8,0x2e1fb364,
0x34000000,0xb4000000,0x34000000,0xb4000000,0xb4000000,0x33642e1f,0xb1a8b1a8,0x2e1f3364,0x34000000,0xb1a8b1a8,0x3400,0x31a8b1a8,0xb4000000,0x2e1f3364,0x31a8b1a8,0xb364ae1f,
0x34000000,0x8000b400,0xb4000000,0x3400,0xb4000000,0xae1f3364,0x31a831a8,0x3364ae1f,0x34000000,0x31a8b1a8,0x8000b400,0xb1a8b1a8,0xb4000000,0xb3642e1f,0xb1a831a8,0xae1f3364,
0x34000000,0x34000000,0x34000000,0x34000000,0x8000b400,0x2e1fb364,0x31a8b1a8,0x3364ae1f,0xb4000000,0xb1a8b1a8,0x8000b400,0x31a8b1a8,0x3400,0xb3642e1f,0xb1a8b1a8,0x2e1fb364,
0x34000000,0x3400,0xb4000000,0x8000b400,0x8000b400,0x33642e1f,0xb1a831a8,0xae1fb364,0xb4000000,0x31a8b1a8,0x3400,0xb1a8b1a8,0x3400,0xae1fb364,0x31a831a8,0xb364ae1f,
0x34000000,0xb4000000,0x34000000,0xb4000000,0x8000b400,0xae1f3364,0x31a8b1a8,0xb3642e1f,0xb4000000,0x31a831a8,0x8000b400,0xb1a831a8,0x3400,0x3364ae1f,0xb1a8b1a8,0xae1f3364,
0x34000000,0x8000b400,0xb4000000,0x3400,0x8000b400,0xb364ae1f,0xb1a831a8,0x2e1f3364,0xb4000000,0xb1a831a8,0x3400,0x31a831a8,0x3400,0x2e1f3364,0x31a831a8,0x33642e1f,
0x3400,0x3400,0x3400,0x3400,0xad643388,0xb15f31ed,0xb33b2ed7,0xb3fe2248,0xb113322f,0xb3f62646,0xb22fb113,0xa646b3f6,0xb2dd301d,0xb26db0c4,0x2bc6b3c3,0x33eaa8b2,
0xb3d92a3e,0xaa3eb3d9,0x33d9aa3e,0x2a3e33d9,0xb3eaa8b2,0x301db2dd,0x30c4326d,0xb3c32bc6,0xb30eaf8b,0x33a8aca5,0xaf8b330e,0xaca5b3a8,0xb15fb1ed,0x333b2ed7,0xb3fea248,0x3388ad64,
0xae1fb364,0x2e1f3364,0xae1fb364,0x2e1f3364,0xa248b3fe,0xad643388,0x315fb1ed,0xb33b2ed7,0x2ca5b3a8,0xb30e2f8b,0x33a82ca5,0xaf8bb30e,0x30c4b26d,0xb3c3abc6,0x28b233ea,0x32ddb01d,
0x32a7b072,0xb072b2a7,0xb2a73072,0x307232a7,0x33c3abc6,0x28b2b3ea,0xb2ddb01d,0xb26d30c4,0x33f62646,0x322fb113,0x2646b3f6,0xb113b22f,0x333b2ed7,0x33fe2248,0x3388ad64,0x31edb15f,
0x3400,0x3400,0x3400,0x3400,0xb388ad64,0xb1edb15f,0xaed7b33b,0xa248b3fe,0x3113b22f,0x33f6a646,0x322f3113,0x264633f6,0x301d32dd,0xb0c4326d,0xb3c3abc6,0xa8b2b3ea,
0xb3d92a3e,0xaa3eb3d9,0x33d9aa3e,0x2a3e33d9,0x28b2b3ea,0x32dd301d,0xb26d30c4,0xabc6b3c3,0x330e2f8b,0xb3a82ca5,0x2f8bb30e,0x2ca533a8,0xb1ed315f,0x2ed7b33b,0xa24833fe,0xad64b388,
0xae1fb364,0x2e1f3364,0xae1fb364,0x2e1f3364,0x33fea248,0xb388ad64,0x31ed315f,0xaed7b33b,0xaca533a8,0x330eaf8b,0xb3a8aca5,0x2f8b330e,0xb26db0c4,0xabc633c3,0x33eaa8b2,0xb01db2dd,
0x32a7b072,0xb072b2a7,0xb2a73072,0x307232a7,0x2bc633c3,0x33ea28b2,0x301db2dd,0xb0c4b26d,0xb3f6a646,0xb22f3113,0xa64633f6,0x3113322f,0x2ed7b33b,0x2248b3fe,0xad64b388,0xb15fb1ed,
0x3400,0x3400,0x3400,0x3400,0x2d64b388,0x315fb1ed,0x333baed7,0x33fea248,0xb113322f,0xb3f62646,0xb22fb113,0xa646b3f6,0x32ddb01d,0x326d30c4,0xabc633c3,0xb3ea28b2,
0xb3d92a3e,0xaa3eb3d9,0x33d9aa3e,0x2a3e33d9,0x33ea28b2,0xb01d32dd,0xb0c4b26d,0x33c3abc6,0xb30eaf8b,0x33a8aca5,0xaf8b330e,0xaca5b3a8,0x315f31ed,0xb33baed7,0x33fe2248,0xb3882d64,
0xae1fb364,0x2e1f3364,0xae1fb364,0x2e1f3364,0x224833fe,0x2d64b388,0xb15f31ed,0x333baed7,0x2ca5b3a8,0xb30e2f8b,0x33a82ca5,0xaf8bb30e,0xb0c4326d,0x33c32bc6,0xa8b2b3ea,0xb2dd301d,
0x32a7b072,0xb072b2a7,0xb2a73072,0x307232a7,0xb3c32bc6,0xa8b233ea,0x32dd301d,0x326db0c4,0x33f62646,0x322fb113,0x2646b3f6,0xb113b22f,0xb33baed7,0xb3fea248,0xb3882d64,0xb1ed315f,
0x3400,0x3400,0x3400,0x3400,0x33882d64,0x31ed315f,0x2ed7333b,0x224833fe,0x3113b22f,0x33f6a646,0x322f3113,0x264633f6,0xb01db2dd,0x30c4b26d,0x33c32bc6,0x28b233ea,
0xb3d92a3e,0xaa3eb3d9,0x33d9aa3e,0x2a3e33d9,0xa8b233ea,0xb2ddb01d,0x326db0c4,0x2bc633c3,0x330e2f8b,0xb3a82ca5,0x2f8bb30e,0x2ca533a8,0x31edb15f,0xaed7333b,0x2248b3fe,0x2d643388,
0xae1fb364,0x2e1f3364,0xae1fb364,0x2e1f3364,0xb3fe2248,0x33882d64,0xb1edb15f,0x2ed7333b,0xaca533a8,0x330eaf8b,0xb3a8aca5,0x2f8b330e,0x326d30c4,0x2bc6b3c3,0xb3ea28b2,0x301d32dd,
0x32a7b072,0xb072b2a7,0xb2a73072,0x307232a7,0xabc6b3c3,0xb3eaa8b2,0xb01d32dd,0x30c4326d,0xb3f6a646,0xb22f3113,0xa64633f6,0x3113322f,0xaed7333b,0xa24833fe,0x2d643388,0x315f31ed,
0x34000000,0x34000000,0x34000000,0x34000000,0x33882d64,0x31ed315f,0x2ed7333b,0x224833fe,0x322f3113,0x264633f6,0xb113322f,0xb3f62646,0x301d32dd,0xb0c4326d,0xb3c3abc6,0xa8b2b3ea,
0x2a3e33d9,0xb3d92a3e,0xaa3eb3d9,0x33d9aa3e,0xa8b233ea,0xb2ddb01d,0x326db0c4,0x2bc633c3,0xaf8b330e,0xaca5b3a8,0x330e2f8b,0xb3a82ca5,0xb1ed315f,0x2ed7b33b,0xa24833fe,0xad64b388,
0xb3642e1f,0x3364ae1f,0xb3642e1f,0x3364ae1f,0xb3fe2248,0x33882d64,0xb1edb15f,0x2ed7333b,0xb3a8aca5,0x2f8b330e,0x2ca5b3a8,0xb30e2f8b,0xb26db0c4,0xabc633c3,0x33eaa8b2,0xb01db2dd,
0xb072b2a7,0xb2a73072,0x307232a7,0x32a7b072,0xabc6b3c3,0xb3eaa8b2,0xb01d32dd,0x30c4326d,0x2646b3f6,0xb113b22f,0xb3f6a646,0xb22f3113,0x2ed7b33b,0x2248b3fe,0xad64b388,0xb15fb1ed,
0x34000000,0x34000000,0x34000000,0x34000000,0xad643388,0xb15f31ed,0xb33b2ed7,0xb3fe2248,0xb22fb113,0xa646b3f6,0x3113b22f,0x33f6a646,0x32ddb01d,0x326d30c4,0xabc633c3,0xb3ea28b2,
0x2a3e33d9,0xb3d92a3e,0xaa3eb3d9,0x33d9aa3e,0xb3eaa8b2,0x301db2dd,0x30c4326d,0xb3c32bc6,0x2f8bb30e,0x2ca533a8,0xb30eaf8b,0x33a8aca5,0x315f31ed,0xb33baed7,0x33fe2248,0xb3882d64,
0xb3642e1f,0x3364ae1f,0xb3642e1f,0x3364ae1f,0xa248b3fe,0xad643388,0x315fb1ed,0xb33b2ed7,0x33a82ca5,0xaf8bb30e,0xaca533a8,0x330eaf8b,0xb0c4326d,0x33c32bc6,0xa8b2b3ea,0xb2dd301d,
0xb072b2a7,0xb2a73072,0x307232a7,0x32a7b072,0x33c3abc6,0x28b2b3ea,0xb2ddb01d,0xb26d30c4,0xa64633f6,0x3113322f,0x33f62646,0x322fb113,0xb33baed7,0xb3fea248,0xb3882d64,0xb1ed315f,
0x34000000,0x34000000,0x34000000,0x34000000,0xb388ad64,0xb1edb15f,0xaed7b33b,0xa248b3fe,0x322f3113,0x264633f6,0xb113322f,0xb3f62646,0xb01db2dd,0x30c4b26d,0x33c32bc6,0x28b233ea,
0x2a3e33d9,0xb3d92a3e,0xaa3eb3d9,0x33d9aa3e,0x28b2b3ea,0x32dd301d,0xb26d30c4,0xabc6b3c3,0xaf8b330e,0xaca5b3a8,0x330e2f8b,0xb3a82ca5,0x31edb15f,0xaed7333b,0x2248b3fe,0x2d643388,
0xb3642e1f,0x3364ae1f,0xb3642e1f,0x3364ae1f,0x33fea248,0xb388ad64,0x31ed315f,0xaed7b33b,0xb3a8aca5,0x2f8b330e,0x2ca5b3a8,0xb30e2f8b,0x326d30c4,0x2bc6b3c3,0xb3ea28b2,0x301d32dd,
0xb072b2a7,0xb2a73072,0x307232a7,0x32a7b072,0x2bc633c3,0x33ea28b2,0x301db2dd,0xb0c4b26d,0x2646b3f6,0xb113b22f,0xb3f6a646,0xb22f3113,0xaed7333b,0xa24833fe,0x2d643388,0x315f31ed,
0x34000000,0x34000000,0x34000000,0x34000000,0x2d64b388,0x315fb1ed,0x333baed7,0x33fea248,0xb22fb113,0xa646b3f6,0x3113b22f,0x33f6a646,0xb2dd301d,0xb26db0c4,0x2bc6b3c3,0x33eaa8b2,
0x2a3e33d9,0xb3d92a3e,0xaa3eb3d9,0x33d9aa3e,0x33ea28b2,0xb01d32dd,0xb0c4b26d,0x33c3abc6,0x2f8bb30e,0x2ca533a8,0xb30eaf8b,0x33a8aca5,0xb15fb1ed,0x333b2ed7,0xb3fea248,0x3388ad64,
0xb3642e1f,0x3364ae1f,0xb3642e1f,0x3364ae1f,0x224833fe,0x2d64b388,0xb15f31ed,0x333baed7,0x33a82ca5,0xaf8bb30e,0xaca533a8,0x330eaf8b,0x30c4b26d,0xb3c3abc6,0x28b233ea,0x32ddb01d,
0xb072b2a7,0xb2a73072,0x307232a7,0x32a7b072,0xb3c32bc6,0xa8b233ea,0x32dd301d,0x326db0c4,0xa64633f6,0x3113322f,0x33f62646,0x322fb113,0x333b2ed7,0x33fe2248,0x3388ad64,0x31edb15f,
0x3f800000,0x3f7ff0e3,0x3f7fc38f,0x3f7f7808,0x3f800000,0x3f7fba9e,0x3f7eea9d,0x3f7d906e,0x3f800000,0x3f7f5ce9,0x3f7d7474,0x3f7a4910,0x3f800000,0x3f7ed7d4,0x3f7b61fc,0x3f75a67b,
0x3f800000,0x3f7e2b72,0x3f78b47b,0x3f6faf1b,0x3f800000,0x3f7d57de,0x3f756d97,0x3f686b39,0x3f800000,0x3f7c5d3a,0x3f718f57,0x3f5fe4e9,0x3f800000,0x3f7b3bab,0x3f6d1c1d,0x3f5627fe,
0x3f800000,0x3f79f35e,0x3f6816a8,0x3f4b41fa,0x3f800000,0x3f788486,0x3f628210,0x3f3f41fc,0x3f800000,0x3f76ef5b,0x3f5c61c7,0x3f3238aa,0x3f800000,0x3f75341d,0x3f55b993,0x3f24381a,
0x3f800000,0x3f73530e,0x3f4e8d90,0x3f1553b7,0x3f800000,0x3f714c7a,0x3f46e22a,0x3f05a02c,0x3f800000,0x3f6f20b0,0x3f3ebc1b,0x3eea6681,0x3f800000,0x3f6cd007,0x3f36206c,0x3ec8477c,
0x3f7f0e58,0x3f7e868b,0x3f7de0b1,0x3f7d1cdd,0x3f7baccd,0x3f7940c0,0x3f764d97,0x3f72d4eb,0x3f75dec6,0x3f703b37,0x3f696591,0x3f61668a,0x3f6db293,0x3f6398ac,0x3f577026,0x3f495521,
0x3f633c5a,0x3f5389c7,0x3f40d0da,0x3f2b561b,0x3f5695e5,0x3f404c5c,0x3f26050a,0x3f084b92,0x3f47de65,0x3f2a2a80,0x3f07a136,0x3ec27b55,0x3f373a23,0x3f11796b,0x3ecc9b8b,0x3e5d4b1a,
0x3f24d225,0x3eed3063,0x3e8586ce,0x3d3c6dd5,0x3f10d3cd,0x3eb3dca5,0x3dee3876,0xbe007745,0x3ef6e0cb,0x3e6fa8b2,0xbcfb49ba,0xbe9620d2,0x3ec9b953,0x3de7fa9a,0xbe354098,0xbee79a5d,
0x3e9aa086,0xbc16cb58,0xbea395c5,0xbf19210f,0x3e541501,0xbe06b26e,0xbee900b7,0xbf39f2ac,0x3de1bc2e,0xbe80f8c0,0xbf14b039,0xbf554aa4,0x3c490e90,0xbebca7af,0xbf31a81d,0xbf6a5ad9,
0x3f7c3b28,0x3f7b3bab,0x3f7a1e84,0x3f78e3d6,0x3f6ed89e,0x3f6a5ad9,0x3f655e0b,0x3f5fe4e9,0x3f584853,0x3f4e1689,0x3f42de29,0x3f36ad7f,0x3f396842,0x3f27ce61,0x3f14b039,0x3f003a06,
0x3f13682a,0x3ef2bd43,0x3ebb31a0,0x3e80f8c0,0x3ecf7bca,0x3e8a11f7,0x3e039502,0xbc7b514b,0x3e605c13,0x3d55880e,0xbdee3876,0xbe901c2c,0x3cc90ab0,0xbe2bf804,0xbeb554ec,0xbf06f686,
0xbe2f10a2,0xbec27b55,0xbf121eb0,0xbf3c17d9,0xbeb8442a,0xbf12c39a,0xbf40d0da,0xbf6398ac,0xbf08f59b,0xbf3d27b8,0xbf63f473,0xbf7a9c59,0xbf3085bb,0xbf5e5aa6,0xbf796e4e,0xbf7f7808,
0xbf514d3d,0xbf74bf62,0xbf7ff4e6,0xbf71d19f,0xbf6a09a7,0xbf7f3f54,0xbf772417,0xbf52a649,0xbf79c79d,0xbf7d57de,0xbf5f8327,0xbf24381a,0xbf7fec43,0xbf6f20b0,0xbf3a7ca4,0xbed3c867,
0x3f778bc5,0x3f76167a,0x3f748422,0x3f72d4eb,0x3f59f26a,0x3f5389c7,0x3f4cae79,0x3f456439,0x3f299415,0x3f1ba299,0x3f0cead0,0x3efafef7,0x3ed53641,0x3ea80b1f,0x3e72b651,0x3e1324ca,
0x3e09cf86,0x3c7b514b,0xbdd53db9,0xbe636c83,0xbe22abb6,0xbe9920fe,0xbeddbe79,0xbf0ee0db,0xbee0924f,0xbf1553b7,0xbf36206c,0xbf51c0c2,0xbf2e3bde,0xbf4f0417,0xbf6816a8,0xbf788486,
0xbf5d2d53,0xbf73ced9,0xbf7eea9d,0xbf7dfa38,0xbf791298,0xbf7ffd39,0xbf77f110,0xbf61668a,0xbf7f84ab,0xbf725470,0xbf53fac3,0xbf269de3,0xbf6ff573,0xbf4c3578,0xbf173c07,0xbeab0201,
0xbf4bbbf8,0xbf11796b,0xbe919ddd,0x3ce22a7a,0xbf15f6d9,0xbe901c2c,0x3d621469,0x3ec5625c,0xbea68f12,0x3d0a342f,0x3ec6d529,0x3f311722,0xbd16c32c,0x3eb0eacb,0x3f2d1469,0x3f67c187,
0x0,0x3cafea69,0x3d2fe007,0x3d83db0a,0x0,0x3d3c6dd5,0x3dbc3ac3,0x3e0cec4a,0x0,0x3d9064b4,0x3e1008b7,0x3e57278f,0x0,0x3dc27c39,0x3e419b37,0x3e901c2c,
0x0,0x3df475c0,0x3e72b651,0x3eb3dca5,0x0,0x3e1324ca,0x3e919ddd,0x3ed6a399,0x0,0x3e2bf804,0x3ea986c4,0x3ef840c8,0x0,0x3e44b0b9,0x3ec1071e,0x3f0c42c9,
0x0,0x3e5d4b1a,0x3ed8106b,0x3f1ba299,0x0,0x3e75c35a,0x3eee9479,0x3f2a2a80,0x0,0x3e870ada,0x3f0242b1,0x3f37c655,0x0,0x3e931f35,0x3f0cead0,0x3f446337,
0x0,0x3e9f1cdf,0x3f173c07,0x3f4fefa8,0x0,0x3eab0201,0x3f212ff9,0x3f5a5ba0,0x0,0x3eb6ccc3,0x3f2ac082,0x3f6398ac,0x0,0x3ec27b55,0x3f33e7bc,0x3f6b99fb,
0x3dafb680,0x3ddb7d37,0x3e039502,0x3e195be0,0x3e3b6ecf,0x3e698bba,0x3e8b9507,0x3ea21887,0x3e8e9a22,0x3eb0eacb,0x3ed25a09,0x3ef2bd43,0x3ebe1d4a,0x3eea6681,0x3f0a48ad,0x3f1e1e22,
0x3eebcbbb,0x3f102dd5,0x3f286605,0x3f3e35c5,0x3f0b9a6b,0x3f28fd41,0x3f42de29,0x3f58b3a1,0x3f1ff6cb,0x3f3f41fc,0x3f591e6a,0x3f6cd007,0x3f32c8c9,0x3f52a649,0x3f6aab7b,0x3f79f35e,
0x3f43e200,0x3f62df7b,0x3f772417,0x3f7fba9e,0x3f531849,0x3f6faf1b,0x3f7e4323,0x3f7dfa38,0x3f604621,0x3f78e3d6,0x3f7fe129,0x3f74bf62,0x3f6b4b0c,0x3f7e5a38,0x3f7bf531,0x3f644fac,
0x3f740bdd,0x3f7ffd39,0x3f7294f8,0x3f4d26fd,0x3f7a7302,0x3f7dc68c,0x3f63f473,0x3f2ff3e6,0x3f7e70b0,0x3f77beb7,0x3f5064af,0x3f0d9281,0x3f7ffb11,0x3f6dfcf2,0x3f385216,0x3ece0bea,
0x3e2f10a2,0x3e44b0b9,0x3e5a3997,0x3e6fa8b2,0x3eb8442a,0x3ece0bea,0x3ee363fa,0x3ef840c8,0x3f08f59b,0x3f17de12,0x3f26050a,0x3f33587a,0x3f3085bb,0x3f4154e1,0x3f5064af,0x3f5d924d,
0x3f514d3d,0x3f61668a,0x3f6e46be,0x3f77beb7,0x3f6a09a7,0x3f76841b,0x3f7de0b1,0x3f7ff84a,0x3f79c79d,0x3f7fa6e3,0x3f7e4323,0x3f75a67b,0x3f7fec43,0x3f7c5d3a,0x3f6f6830,0x3f5988ad,
0x3f7c3b28,0x3f6cd007,0x3f5233c6,0x3f2da859,0x3f6ed89e,0x3f51c0c2,0x3f286605,0x3eea6681,0x3f584853,0x3f2c800f,0x3ee900b7,0x3e5101f1,0x3f396842,0x3efdbabb,0x3e667c66,0xbd83db0a,
0x3f13682a,0x3e9620d2,0xbc96c9b6,0xbea80b1f,0x3ecf7bca,0x3d9cecf9,0xbe8586ce,0xbf11796b,0x3e605c13,0xbe1324ca,0xbef9a02d,0xbf446337,0x3cc90ab0,0xbeb6ccc3,0xbf2f61a5,0xbf6912ae,
0x3e827dc0,0x3e8d17c0,0x3e97a117,0x3ea21887,0x3f064b82,0x3f102dd5,0x3f19c200,0x3f2302d3,0x3f3fc767,0x3f4b41fa,0x3f55b993,0x3f5f20db,0x3f68bf3c,0x3f71d19f,0x3f78b47b,0x3f7d57de,
0x3f7dabcc,0x3f7ff84a,0x3f7e9bc9,0x3f799b43,0x3f7cbfc9,0x3f74484b,0x3f66becc,0x3f546b3b,0x3f660f88,0x3f4fefa8,0x3f33e7bc,0x3f12c39a,0x3f3b8f3b,0x3f16999f,0x3ed8106b,0x3e75c35a,
0x3f00e7e4,0x3e9c1faf,0x3dbc3ac3,0xbe007745,0x3e6c9a7f,0xbc16cb58,0xbe7ee6e1,0xbef2bd43,0xbd7b2b74,0xbea5129f,0xbf0f8784,0xbf425b89,0xbeb263ef,0xbf1a6293,0xbf4e8d90,0xbf714c7a,
0xbf1b02c6,0xbf52a649,0xbf756d97,0xbf7fe705,0xbf4f7a1f,0xbf75a67b,0xbf7f9c18,0xbf6c3624,0xbf721352,0xbf7fdaaf,0xbf6be858,0xbf38dd65,0xbf7fd397,0xbf703b37,0xbf3ca003,0xbed97cb9,
0x3f800000,0x3f800000,0x3f800000,0x3f800000,0x3f7ff0e3,0x3f7fba9e,0x3f7f5ce9,0x3f7ed7d4,0x3f7fc38f,0x3f7eea9d,0x3f7d7474,0x3f7b61fc,0x3f7f7808,0x3f7d906e,0x3f7a4910,0x3f75a67b,
0x3f7f0e58,0x3f7baccd,0x3f75dec6,0x3f6db293,0x3f7e868b,0x3f7940c0,0x3f703b37,0x3f6398ac,0x3f7de0b1,0x3f764d97,0x3f696591,0x3f577026,0x3f7d1cdd,0x3f72d4eb,0x3f61668a,0x3f495521,
0x3f7c3b28,0x3f6ed89e,0x3f584853,0x3f396842,0x3f7b3bab,0x3f6a5ad9,0x3f4e1689,0x3f27ce61,0x3f7a1e84,0x3f655e0b,0x3f42de29,0x3f14b039,0x3f78e3d6,0x3f5fe4e9,0x3f36ad7f,0x3f003a06,
0x3f778bc5,0x3f59f26a,0x3f299415,0x3ed53641,0x3f76167a,0x3f5389c7,0x3f1ba299,0x3ea80b1f,0x3f748422,0x3f4cae79,0x3f0cead0,0x3e72b651,0x3f72d4eb,0x3f456439,0x3efafef7,0x3e1324ca,
0x3f800000,0x3f800000,0x3f800000,0x3f800000,0x3f7e2b72,0x3f7d57de,0x3f7c5d3a,0x3f7b3bab,0x3f78b47b,0x3f756d97,0x3f718f57,0x3f6d1c1d,0x3f6faf1b,0x3f686b39,0x3f5fe4e9,0x3f5627fe,
0x3f633c5a,0x3f5695e5,0x3f47de65,0x3f373a23,0x3f5389c7,0x3f404c5c,0x3f2a2a80,0x3f11796b,0x3f40d0da,0x3f26050a,0x3f07a136,0x3ecc9b8b,0x3f2b561b,0x3f084b92,0x3ec27b55,0x3e5d4b1a,
0x3f13682a,0x3ecf7bca,0x3e605c13,0x3cc90ab0,0x3ef2bd43,0x3e8a11f7,0x3d55880e,0xbe2bf804,0x3ebb31a0,0x3e039502,0xbdee3876,0xbeb554ec,0x3e80f8c0,0xbc7b514b,0xbe901c2c,0xbf06f686,
0x3e09cf86,0xbe22abb6,0xbee0924f,0xbf2e3bde,0x3c7b514b,0xbe9920fe,0xbf1553b7,0xbf4f0417,0xbdd53db9,0xbeddbe79,0xbf36206c,0xbf6816a8,0xbe636c83,0xbf0ee0db,0xbf51c0c2,0xbf788486,
0x3f800000,0x3f800000,0x3f800000,0x3f800000,0x3f79f35e,0x3f788486,0x3f76ef5b,0x3f75341d,0x3f6816a8,0x3f628210,0x3f5c61c7,0x3f55b993,0x3f4b41fa,0x3f3f41fc,0x3f3238aa,0x3f24381a,
0x3f24d225,0x3f10d3cd,0x3ef6e0cb,0x3ec9b953,0x3eed3063,0x3eb3dca5,0x3e6fa8b2,0x3de7fa9a,0x3e8586ce,0x3dee3876,0xbcfb49ba,0xbe354098,0x3d3c6dd5,0xbe007745,0xbe9620d2,0xbee79a5d,
0xbe2f10a2,0xbeb8442a,0xbf08f59b,0xbf3085bb,0xbec27b55,0xbf12c39a,0xbf3d27b8,0xbf5e5aa6,0xbf121eb0,0xbf40d0da,0xbf63f473,0xbf796e4e,0xbf3c17d9,0xbf6398ac,0xbf7a9c59,0xbf7f7808,
0xbf5d2d53,0xbf791298,0xbf7f84ab,0xbf6ff573,0xbf73ced9,0xbf7ffd39,0xbf725470,0xbf4c3578,0xbf7eea9d,0xbf77f110,0xbf53fac3,0xbf173c07,0xbf7dfa38,0xbf61668a,0xbf269de3,0xbeab0201,
0x3f800000,0x3f800000,0x3f800000,0x3f800000,0x3f73530e,0x3f714c7a,0x3f6f20b0,0x3f6cd007,0x3f4e8d90,0x3f46e22a,0x3f3ebc1b,0x3f36206c,0x3f1553b7,0x3f05a02c,0x3eea6681,0x3ec8477c,
0x3e9aa086,0x3e541501,0x3de1bc2e,0x3c490e90,0xbc16cb58,0xbe06b26e,0xbe80f8c0,0xbebca7af,0xbea395c5,0xbee900b7,0xbf14b039,0xbf31a81d,0xbf19210f,0xbf39f2ac,0xbf554aa4,0xbf6a5ad9,
0xbf514d3d,0xbf6a09a7,0xbf79c79d,0xbf7fec43,0xbf74bf62,0xbf7f3f54,0xbf7d57de,0xbf6f20b0,0xbf7ff4e6,0xbf772417,0xbf5f8327,0xbf3a7ca4,0xbf71d19f,0xbf52a649,0xbf24381a,0xbed3c867,
0xbf4bbbf8,0xbf15f6d9,0xbea68f12,0xbd16c32c,0xbf11796b,0xbe901c2c,0x3d0a342f,0x3eb0eacb,0xbe919ddd,0x3d621469,0x3ec6d529,0x3f2d1469,0x3ce22a7a,0x3ec5625c,0x3f311722,0x3f67c187,
0x0,0x0,0x0,0x0,0x3cafea69,0x3d3c6dd5,0x3d9064b4,0x3dc27c39,0x3d2fe007,0x3dbc3ac3,0x3e1008b7,0x3e419b37,0x3d83db0a,0x3e0cec4a,0x3e57278f,0x3e901c2c,
0x3dafb680,0x3e3b6ecf,0x3e8e9a22,0x3ebe1d4a,0x3ddb7d37,0x3e698bba,0x3eb0eacb,0x3eea6681,0x3e039502,0x3e8b9507,0x3ed25a09,0x3f0a48ad,0x3e195be0,0x3ea21887,0x3ef2bd43,0x3f1e1e22,
0x3e2f10a2,0x3eb8442a,0x3f08f59b,0x3f3085bb,0x3e44b0b9,0x3ece0bea,0x3f17de12,0x3f4154e1,0x3e5a3997,0x3ee363fa,0x3f26050a,0x3f5064af,0x3e6fa8b2,0x3ef840c8,0x3f33587a,0x3f5d924d,
0x3e827dc0,0x3f064b82,0x3f3fc767,0x3f68bf3c,0x3e8d17c0,0x3f102dd5,0x3f4b41fa,0x3f71d19f,0x3e97a117,0x3f19c200,0x3f55b993,0x3f78b47b,0x3ea21887,0x3f2302d3,0x3f5f20db,0x3f7d57de,
0x0,0x0,0x0,0x0,0x3df475c0,0x3e1324ca,0x3e2bf804,0x3e44b0b9,0x3e72b651,0x3e919ddd,0x3ea986c4,0x3ec1071e,0x3eb3dca5,0x3ed6a399,0x3ef840c8,0x3f0c42c9,
0x3eebcbbb,0x3f0b9a6b,0x3f1ff6cb,0x3f32c8c9,0x3f102dd5,0x3f28fd41,0x3f3f41fc,0x3f52a649,0x3f286605,0x3f42de29,0x3f591e6a,0x3f6aab7b,0x3f3e35c5,0x3f58b3a1,0x3f6cd007,0x3f79f35e,
0x3f514d3d,0x3f6a09a7,0x3f79c79d,0x3f7fec43,0x3f61668a,0x3f76841b,0x3f7fa6e3,0x3f7c5d3a,0x3f6e46be,0x3f7de0b1,0x3f7e4323,0x3f6f6830,0x3f77beb7,0x3f7ff84a,0x3f75a67b,0x3f5988ad,
0x3f7dabcc,0x3f7cbfc9,0x3f660f88,0x3f3b8f3b,0x3f7ff84a,0x3f74484b,0x3f4fefa8,0x3f16999f,0x3f7e9bc9,0x3f66becc,0x3f33e7bc,0x3ed8106b,0x3f799b43,0x3f546b3b,0x3f12c39a,0x3e75c35a,
0x0,0x0,0x0,0x0,0x3e5d4b1a,0x3e75c35a,0x3e870ada,0x3e931f35,0x3ed8106b,0x3eee9479,0x3f0242b1,0x3f0cead0,0x3f1ba299,0x3f2a2a80,0x3f37c655,0x3f446337,
0x3f43e200,0x3f531849,0x3f604621,0x3f6b4b0c,0x3f62df7b,0x3f6faf1b,0x3f78e3d6,0x3f7e5a38,0x3f772417,0x3f7e4323,0x3f7fe129,0x3f7bf531,0x3f7fba9e,0x3f7dfa38,0x3f74bf62,0x3f644fac,
0x3f7c3b28,0x3f6ed89e,0x3f584853,0x3f396842,0x3f6cd007,0x3f51c0c2,0x3f2c800f,0x3efdbabb,0x3f5233c6,0x3f286605,0x3ee900b7,0x3e667c66,0x3f2da859,0x3eea6681,0x3e5101f1,0xbd83db0a,
0x3f00e7e4,0x3e6c9a7f,0xbd7b2b74,0xbeb263ef,0x3e9c1faf,0xbc16cb58,0xbea5129f,0xbf1a6293,0x3dbc3ac3,0xbe7ee6e1,0xbf0f8784,0xbf4e8d90,0xbe007745,0xbef2bd43,0xbf425b89,0xbf714c7a,
0x0,0x0,0x0,0x0,0x3e9f1cdf,0x3eab0201,0x3eb6ccc3,0x3ec27b55,0x3f173c07,0x3f212ff9,0x3f2ac082,0x3f33e7bc,0x3f4fefa8,0x3f5a5ba0,0x3f6398ac,0x3f6b99fb,
0x3f740bdd,0x3f7a7302,0x3f7e70b0,0x3f7ffb11,0x3f7ffd39,0x3f7dc68c,0x3f77beb7,0x3f6dfcf2,0x3f7294f8,0x3f63f473,0x3f5064af,0x3f385216,0x3f4d26fd,0x3f2ff3e6,0x3f0d9281,0x3ece0bea,
0x3f13682a,0x3ecf7bca,0x3e605c13,0x3cc90ab0,0x3e9620d2,0x3d9cecf9,0xbe1324ca,0xbeb6ccc3,0xbc96c9b6,0xbe8586ce,0xbef9a02d,0xbf2f61a5,0xbea80b1f,0xbf11796b,0xbf446337,0xbf6912ae,
0xbf1b02c6,0xbf4f7a1f,0xbf721352,0xbf7fd397,0xbf52a649,0xbf75a67b,0xbf7fdaaf,0xbf703b37,0xbf756d97,0xbf7f9c18,0xbf6be858,0xbf3ca003,0xbf7fe705,0xbf6c3624,0xbf38dd65,0xbed97cb9,
0x3f80,0x3f80,0x3c00,0x3c00,0x3f800000,0x3f800000,0x3c000000,0x3c000000,0x3f80,0xbf353f35,0x3c00,0xb9a839a8,0x3f800000,0x3f353f35,0x3c000000,0x39a839a8,
0x3f80,0xbf80248d,0x3c00,0xbc000000,0x3f800000,0x248d3f80,0x3c000000,0x3c00,0x3f80,0xbf35bf35,0x3c00,0xb9a8b9a8,0x3f800000,0xbf353f35,0x3c000000,0xb9a839a8,
0x3f80,0xa50dbf80,0x3c00,0x8000bc00,0x3f800000,0xbf80250d,0x3c000000,0xbc000000,0x3f80,0x3f35bf35,0x3c00,0x39a8b9a8,0x3f800000,0xbf35bf35,0x3c000000,0xb9a8b9a8,
0x3f80,0x3f80a554,0x3c00,0x3c008000,0x3f800000,0xa554bf80,0x3c000000,0x8000bc00,0x3f80,0x3f353f35,0x3c00,0x39a839a8,0x3f800000,0x3f35bf35,0x3c000000,0x39a8b9a8,
0x3f80,0x3f80,0x3c00,0x3c00,0x3f800000,0x3f800000,0x3c000000,0x3c000000,0xbf80248d,0xbf35bf35,0xbc000000,0xb9a8b9a8,0x248d3f80,0xbf353f35,0x3c00,0xb9a839a8,
0xa50dbf80,0x3f80a554,0x8000bc00,0x3c008000,0xbf80250d,0xa554bf80,0xbc000000,0x8000bc00,0x3f80a554,0xbf353f35,0x3c008000,0xb9a839a8,0xa554bf80,0x3f353f35,0x8000bc00,0x39a839a8,
0x3f80,0xa50dbf80,0x3c00,0x8000bc00,0x3f800000,0xbf80250d,0x3c000000,0xbc000000,0xbf80248d,0x3f353f35,0xbc000000,0x39a839a8,0x248d3f80,0x3f35bf35,0x3c00,0x39a8b9a8,
0xa50dbf80,0xbf80248d,0x8000bc00,0xbc000000,0xbf80250d,0x248d3f80,0xbc000000,0x3c00,0x3f80a554,0x3f35bf35,0x3c008000,0x39a8b9a8,0xa554bf80,0xbf35bf35,0x8000bc00,0xb9a8b9a8,
0x3f80,0x3f80,0x3c00,0x3c00,0x3f800000,0x3f800000,0x3c000000,0x3c000000,0xa50dbf80,0x3f35bf35,0x8000bc00,0x39a8b9a8,0xbf80250d,0xbf35bf35,0xbc000000,0xb9a8b9a8,
0x3f80,0xbf80248d,0x3c00,0xbc000000,0x3f800000,0x248d3f80,0x3c000000,0x3c00,0xa50dbf80,0x3f353f35,0x8000bc00,0x39a839a8,0xbf80250d,0x3f35bf35,0xbc000000,0x39a8b9a8,
0x3f80,0xa50dbf80,0x3c00,0x8000bc00,0x3f800000,0xbf80250d,0x3c000000,0xbc000000,0xa50dbf80,0xbf353f35,0x8000bc00,0xb9a839a8,0xbf80250d,0x3f353f35,0xbc000000,0x39a839a8,
0x3f80,0x3f80a554,0x3c00,0x3c008000,0x3f800000,0xa554bf80,0x3c000000,0x8000bc00,0xa50dbf80,0xbf35bf35,0x8000bc00,0xb9a8b9a8,0xbf80250d,0xbf353f35,0xbc000000,0xb9a839a8,
0x3f80,0x3f80,0x3c00,0x3c00,0x3f800000,0x3f800000,0x3c000000,0x3c000000,0x3f80a554,0x3f353f35,0x3c008000,0x39a839a8,0xa554bf80,0x3f35bf35,0x8000bc00,0x39a8b9a8,
0xa50dbf80,0x3f80a554,0x8000bc00,0x3c008000,0xbf80250d,0xa554bf80,0xbc000000,0x8000bc00,0xbf80248d,0x3f35bf35,0xbc000000,0x39a8b9a8,0x248d3f80,0xbf35bf35,0x3c00,0xb9a8b9a8,
0x3f80,0xa50dbf80,0x3c00,0x8000bc00,0x3f800000,0xbf80250d,0x3c000000,0xbc000000,0x3f80a554,0xbf35bf35,0x3c008000,0xb9a8b9a8,0xa554bf80,0xbf353f35,0x8000bc00,0xb9a839a8,
0xa50dbf80,0xbf80248d,0x8000bc00,0xbc000000,0xbf80250d,0x248d3f80,0xbc000000,0x3c00,0xbf80248d,0xbf353f35,0xbc000000,0xb9a839a8,0x248d3f80,0x3f353f35,0x3c00,0x39a839a8,
0x3f80,0x3f80,0x3f800000,0x3f800000,0x3f80,0xbec43f6d,0x3f800000,0x3f6d3ec4,0x3f80,0xbf353f35,0x3f800000,0x3f353f35,0x3f80,0xbf6d3ec4,0x3f800000,0x3ec43f6d,
0x3f80,0xbf80248d,0x3f800000,0x248d3f80,0x3f80,0xbf6dbec4,0x3f800000,0xbec43f6d,0x3f80,0xbf35bf35,0x3f800000,0xbf353f35,0x3f80,0xbec4bf6d,0x3f800000,0xbf6d3ec4,
0x3f80,0xa50dbf80,0x3f800000,0xbf80250d,0x3f80,0x3ec4bf6d,0x3f800000,0xbf6dbec4,0x3f80,0x3f35bf35,0x3f800000,0xbf35bf35,0x3f80,0x3f6dbec4,0x3f800000,0xbec4bf6d,
0x3f80,0x3f80a554,0x3f800000,0xa554bf80,0x3f80,0x3f6d3ec4,0x3f800000,0x3ec4bf6d,0x3f80,0x3f353f35,0x3f800000,0x3f35bf35,0x3f80,0x3ec43f6d,0x3f800000,0x3f6dbec4,
0x3f80,0x3f80,0x3f800000,0x3f800000,0xbf353f35,0xbf6d3ec4,0x3f353f35,0x3ec43f6d,0xbf80248d,0xbf35bf35,0x248d3f80,0xbf353f35,0xbf35bf35,0x3ec4bf6d,0xbf353f35,0xbf6dbec4,
0xa50dbf80,0x3f80a554,0xbf80250d,0xa554bf80,0x3f35bf35,0x3ec43f6d,0xbf35bf35,0x3f6dbec4,0x3f80a554,0xbf353f35,0xa554bf80,0x3f353f35,0x3f353f35,0xbf6dbec4,0x3f35bf35,0xbec43f6d,
0x3f80,0xa50dbf80,0x3f800000,0xbf80250d,0xbf353f35,0x3f6dbec4,0x3f353f35,0xbec4bf6d,0xbf80248d,0x3f353f35,0x248d3f80,0x3f35bf35,0xbf35bf35,0xbec43f6d,0xbf353f35,0x3f6d3ec4,
0xa50dbf80,0xbf80248d,0xbf80250d,0x248d3f80,0x3f35bf35,0xbec4bf6d,0xbf35bf35,0xbf6d3ec4,0x3f80a554,0x3f35bf35,0xa554bf80,0xbf35bf35,0x3f353f35,0x3f6d3ec4,0x3f35bf35,0x3ec4bf6d,
0x3f80,0x3f80,0x3f800000,0x3f800000,0xbf80248d,0xbf6dbec4,0x248d3f80,0xbec43f6d,0xa50dbf80,0x3f35bf35,0xbf80250d,0xbf35bf35,0x3f80a554,0x3ec43f6d,0xa554bf80,0x3f6dbec4,
0x3f80,0xbf80248d,0x3f800000,0x248d3f80,0xbf80248d,0x3ec4bf6d,0x248d3f80,0xbf6dbec4,0xa50dbf80,0x3f353f35,0xbf80250d,0x3f35bf35,0x3f80a554,0xbf6d3ec4,0xa554bf80,0x3ec43f6d,
0x3f80,0xa50dbf80,0x3f800000,0xbf80250d,0xbf80248d,0x3f6d3ec4,0x248d3f80,0x3ec4bf6d,0xa50dbf80,0xbf353f35,0xbf80250d,0x3f353f35,0x3f80a554,0xbec4bf6d,0xa554bf80,0xbf6d3ec4,
0x3f80,0x3f80a554,0x3f800000,0xa554bf80,0xbf80248d,0xbec43f6d,0x248d3f80,0x3f6d3ec4,0xa50dbf80,0xbf35bf35,0xbf80250d,0xbf353f35,0x3f80a554,0x3f6dbec4,0xa554bf80,0xbec4bf6d,
0x3f80,0x3f80,0x3f800000,0x3f800000,0xbf35bf35,0xbec4bf6d,0xbf353f35,0xbf6d3ec4,0x3f80a554,0x3f353f35,0xa554bf80,0x3f35bf35,0xbf353f35,0xbf6dbec4,0x3f353f35,0xbec43f6d,
0xa50dbf80,0x3f80a554,0xbf80250d,0xa554bf80,0x3f353f35,0xbf6d3ec4,0x3f35bf35,0x3ec43f6d,0xbf80248d,0x3f35bf35,0x248d3f80,0xbf35bf35,0x3f35bf35,0xbec43f6d,0xbf35bf35,0x3f6d3ec4,
0x3f80,0xa50dbf80,0x3f800000,0xbf80250d,0xbf35bf35,0x3ec43f6d,0xbf353f35,0x3f6dbec4,0x3f80a554,0xbf35bf35,0xa554bf80,0xbf353f35,0xbf353f35,0x3f6d3ec4,0x3f353f35,0x3ec4bf6d,
0xa50dbf80,0xbf80248d,0xbf80250d,0x248d3f80,0x3f353f35,0x3f6dbec4,0x3f35bf35,0xbec4bf6d,0xbf80248d,0xbf353f35,0x248d3f80,0x3f353f35,0x3f35bf35,0x3ec4bf6d,0xbf35bf35,0xbf6dbec4,
0x3d800000,0x3d800000,0x3d800000,0x3d800000,0x3d800000,0x3d7ffb11,0x3d7fec43,0x3d7fd397,0x3d800000,0x3d7fec43,0x3d7fb10f,0x3d7f4e6d,0x3d800000,0x3d7fd397,0x3d7f4e6d,0x3d7e70b0,
0x3d800000,0x3d7fb10f,0x3d7ec46d,0x3d7d3aac,0x3d800000,0x3d7f84ab,0x3d7e1324,0x3d7baccd,0x3d800000,0x3d7f4e6d,0x3d7d3aac,0x3d79c79d,0x3d800000,0x3d7f0e58,0x3d7c3b28,0x3d778bc5,
0x3d800000,0x3d7ec46d,0x3d7b14be,0x3d74fa0b,0x3d800000,0x3d7e70b0,0x3d79c79d,0x3d721352,0x3d800000,0x3d7e1324,0x3d7853f8,0x3d6ed89e,0x3d800000,0x3d7dabcc,0x3d76ba07,0x3d6b4b0c,
0x3d800000,0x3d7d3aac,0x3d74fa0b,0x3d676bd8,0x3d800000,0x3d7cbfc9,0x3d731447,0x3d633c5a,0x3d800000,0x3d7c3b28,0x3d710908,0x3d5ebe05,0x3d800000,0x3d7baccd,0x3d6ed89e,0x3d59f26a,
0x3d800000,0x3d800000,0x3d800000,0x3d800000,0x3d7fb10f,0x3d7f84ab,0x3d7f4e6d,0x3d7f0e58,0x3d7ec46d,0x3d7e1324,0x3d7d3aac,0x3d7c3b28,0x3d7d3aac,0x3d7baccd,0x3d79c79d,0x3d778bc5,
0x3d7b14be,0x3d7853f8,0x3d74fa0b,0x3d710908,0x3d7853f8,0x3d740bdd,0x3d6ed89e,0x3d68bf3c,0x3d74fa0b,0x3d6ed89e,0x3d676bd8,0x3d5ebe05,0x3d710908,0x3d68bf3c,0x3d5ebe05,0x3d531849,
0x3d6c835e,0x3d61c598,0x3d54db31,0x3d45e403,0x3d676bd8,0x3d59f26a,0x3d49d112,0x3d373a23,0x3d61c598,0x3d514d3d,0x3d3daef9,0x3d273656,0x3d5b941a,0x3d47de65,0x3d3085bb,0x3d15f6d9,
0x3d54db31,0x3d3daef9,0x3d226799,0x3d039c3d,0x3d4d9f02,0x3d32c8c9,0x3d13682a,0x3ce0924f,0x3d45e403,0x3d273656,0x3d039c3d,0x3cb8442a,0x3d3daef9,0x3d1b02c6,0x3ce63375,0x3c8e9a22,
0x3d800000,0x3d800000,0x3d800000,0x3d800000,0x3d7ec46d,0x3d7e70b0,0x3d7e1324,0x3d7dabcc,0x3d7b14be,0x3d79c79d,0x3d7853f8,0x3d76ba07,0x3d74fa0b,0x3d721352,0x3d6ed89e,0x3d6b4b0c,
0x3d6c835e,0x3d676bd8,0x3d61c598,0x3d5b941a,0x3d61c598,0x3d59f26a,0x3d514d3d,0x3d47de65,0x3d54db31,0x3d49d112,0x3d3daef9,0x3d3085bb,0x3d45e403,0x3d373a23,0x3d273656,0x3d15f6d9,
0x3d3504f3,0x3d226799,0x3d0e39da,0x3cf15aea,0x3d226799,0x3d0b9a6b,0x3ce63375,0x3cb263ef,0x3d0e39da,0x3ce63375,0x3cac7cd4,0x3c605c13,0x3cf15aea,0x3cb263ef,0x3c605c13,0x3bafb680,
0x3cc3ef15,0x3c78cfcc,0x3bc8bd36,0xbb48fb30,0x3c94a031,0x3c09cf86,0xbac90ab0,0xbc3b6ecf,0x3c47c5c2,0x3ac90ab0,0xbc164083,0xbca09ae5,0x3bc8bd36,0xbbafb680,0xbc888e93,0xbce0924f,
0x3d800000,0x3d800000,0x3d800000,0x3d800000,0x3d7d3aac,0x3d7cbfc9,0x3d7c3b28,0x3d7baccd,0x3d74fa0b,0x3d731447,0x3d710908,0x3d6ed89e,0x3d676bd8,0x3d633c5a,0x3d5ebe05,0x3d59f26a,
0x3d54db31,0x3d4d9f02,0x3d45e403,0x3d3daef9,0x3d3daef9,0x3d32c8c9,0x3d273656,0x3d1b02c6,0x3d226799,0x3d13682a,0x3d039c3d,0x3ce63375,0x3d039c3d,0x3ce0924f,0x3cb8442a,0x3c8e9a22,
0x3cc3ef15,0x3c94a031,0x3c47c5c2,0x3bc8bd36,0x3c78cfcc,0x3c09cf86,0x3ac90ab0,0xbbafb680,0x3bc8bd36,0xbac90ab0,0xbc164083,0xbc888e93,0xbb48fb30,0xbc3b6ecf,0xbca09ae5,0xbce0924f,
0xbc47c5c2,0xbcac7cd4,0xbcf15aea,0xbd187fc0,0xbcac7cd4,0xbcf6e0cb,0xbd1d7fd1,0xbd3b8f3b,0xbcf15aea,0xbd1d7fd1,0xbd3daef9,0xbd584853,0xbd187fc0,0xbd3b8f3b,0xbd584853,0xbd6db293,
0x0,0x0,0x0,0x0,0x0,0x3a490e90,0x3ac90ab0,0x3b16c32c,0x0,0x3ac90ab0,0x3b48fb30,0x3b96a905,0x0,0x3b16c32c,0x3b96a905,0x3be1bc2e,
0x0,0x3b48fb30,0x3bc8bd36,0x3c164083,0x0,0x3b7b2b74,0x3bfab273,0x3c3b6ecf,0x0,0x3b96a905,0x3c164083,0x3c605c13,0x0,0x3bafb680,0x3c2f10a2,0x3c827dc0,
0x0,0x3bc8bd36,0x3c47c5c2,0x3c94a031,0x0,0x3be1bc2e,0x3c605c13,0x3ca68f12,0x0,0x3bfab273,0x3c78cfcc,0x3cb8442a,0x0,0x3c09cf86,0x3c888e93,0x3cc9b953,
0x0,0x3c164083,0x3c94a031,0x3cdae880,0x0,0x3c22abb6,0x3ca09ae5,0x3cebcbbb,0x0,0x3c2f10a2,0x3cac7cd4,0x3cfc5d27,0x0,0x3c3b6ecf,0x3cb8442a,0x3d064b82,
0x0,0x0,0x0,0x0,0x3b48fb30,0x3b7b2b74,0x3b96a905,0x3bafb680,0x3bc8bd36,0x3bfab273,0x3c164083,0x3c2f10a2,0x3c164083,0x3c3b6ecf,0x3c605c13,0x3c827dc0,
0x3c47c5c2,0x3c78cfcc,0x3c94a031,0x3cac7cd4,0x3c78cfcc,0x3c9aa086,0x3cb8442a,0x3cd53641,0x3c94a031,0x3cb8442a,0x3cdae880,0x3cfc5d27,0x3cac7cd4,0x3cd53641,0x3cfc5d27,0x3d10d3cd,
0x3cc3ef15,0x3cf15aea,0x3d0e39da,0x3d226799,0x3cdae880,0x3d064b82,0x3d1d7fd1,0x3d32c8c9,0x3cf15aea,0x3d13682a,0x3d2beb4a,0x3d41d870,0x3d039c3d,0x3d1ff6cb,0x3d396842,0x3d4f7a1f,
0x3d0e39da,0x3d2beb4a,0x3d45e403,0x3d5b941a,0x3d187fc0,0x3d373a23,0x3d514d3d,0x3d660f88,0x3d226799,0x3d41d870,0x3d5b941a,0x3d6ed89e,0x3d2beb4a,0x3d4bbbf8,0x3d64aa59,0x3d75dec6,
0x0,0x0,0x0,0x0,0x3bc8bd36,0x3be1bc2e,0x3bfab273,0x3c09cf86,0x3c47c5c2,0x3c605c13,0x3c78cfcc,0x3c888e93,0x3c94a031,0x3ca68f12,0x3cb8442a,0x3cc9b953,
0x3cc3ef15,0x3cdae880,0x3cf15aea,0x3d039c3d,0x3cf15aea,0x3d064b82,0x3d13682a,0x3d1ff6cb,0x3d0e39da,0x3d1d7fd1,0x3d2beb4a,0x3d396842,0x3d226799,0x3d32c8c9,0x3d41d870,0x3d4f7a1f,
0x3d3504f3,0x3d45e403,0x3d54db31,0x3d61c598,0x3d45e403,0x3d5695e5,0x3d64aa59,0x3d6ff573,0x3d54db31,0x3d64aa59,0x3d710908,0x3d79c79d,0x3d61c598,0x3d6ff573,0x3d79c79d,0x3d7f0e58,
0x3d6c835e,0x3d7853f8,0x3d7ec46d,0x3d7fb10f,0x3d74fa0b,0x3d7dabcc,0x3d7fec43,0x3d7baccd,0x3d7b14be,0x3d7fec43,0x3d7d3aac,0x3d731447,0x3d7ec46d,0x3d7f0e58,0x3d76ba07,0x3d660f88,
0x0,0x0,0x0,0x0,0x3c164083,0x3c22abb6,0x3c2f10a2,0x3c3b6ecf,0x3c94a031,0x3ca09ae5,0x3cac7cd4,0x3cb8442a,0x3cdae880,0x3cebcbbb,0x3cfc5d27,0x3d064b82,
0x3d0e39da,0x3d187fc0,0x3d226799,0x3d2beb4a,0x3d2beb4a,0x3d373a23,0x3d41d870,0x3d4bbbf8,0x3d45e403,0x3d514d3d,0x3d5b941a,0x3d64aa59,0x3d5b941a,0x3d660f88,0x3d6ed89e,0x3d75dec6,
0x3d6c835e,0x3d74fa0b,0x3d7b14be,0x3d7ec46d,0x3d7853f8,0x3d7dabcc,0x3d7fec43,0x3d7f0e58,0x3d7ec46d,0x3d7fec43,0x3d7d3aac,0x3d76ba07,0x3d7fb10f,0x3d7baccd,0x3d731447,0x3d660f88,
0x3d7b14be,0x3d710908,0x3d61c598,0x3d4d9f02,0x3d710908,0x3d604621,0x3d49d112,0x3d2e3bde,0x3d61c598,0x3d49d112,0x3d2beb4a,0x3d08f59b,0x3d4d9f02,0x3d2e3bde,0x3d08f59b,0x3cbe1d4a,
0x3d800000,0x3d800000,0x3d800000,0x3d800000,0x3d7b14be,0x3d7a7302,0x3d79c79d,0x3d791298,0x3d6c835e,0x3d6a09a7,0x3d676bd8,0x3d64aa59,0x3d54db31,0x3d4f7a1f,0x3d49d112,0x3d43e200,
0x3d3504f3,0x3d2beb4a,0x3d226799,0x3d187fc0,0x3d0e39da,0x3d00e7e4,0x3ce63375,0x3cc9b953,0x3cc3ef15,0x3ca09ae5,0x3c78cfcc,0x3c2f10a2,0x3c47c5c2,0x3be1bc2e,0x3ac90ab0,0xbb7b2b74,
0x228d3132,0xbbc8bd36,0xbc47c5c2,0xbc94a031,0xbc47c5c2,0xbc9aa086,0xbccf7bca,0xbd00e7e4,0xbcc3ef15,0xbcfc5d27,0xbd187fc0,0xbd3085bb,0xbd0e39da,0xbd299415,0xbd41d870,0xbd5695e5,
0xbd3504f3,0xbd4d9f02,0xbd61c598,0xbd710908,0xbd54db31,0xbd68bf3c,0xbd76ba07,0xbd7e70b0,0xbd6c835e,0xbd79c79d,0xbd7fb10f,0xbd7e1324,0xbd7b14be,0xbd7ffb11,0xbd7c3b28,0xbd6ff573,
0x3d800000,0x3d800000,0x3d800000,0x3d800000,0x3d7853f8,0x3d778bc5,0x3d76ba07,0x3d75dec6,0x3d61c598,0x3d5ebe05,0x3d5b941a,0x3d584853,0x3d3daef9,0x3d373a23,0x3d3085bb,0x3d299415,
0x3d0e39da,0x3d039c3d,0x3cf15aea,0x3cdae880,0x3cac7cd4,0x3c8e9a22,0x3c605c13,0x3c22abb6,0x3bc8bd36,0x3ac90ab0,0xbb48fb30,0xbbfab273,0xbc164083,0xbc6c9a7f,0xbca09ae5,0xbcc9b953,
0xbcc3ef15,0xbcf15aea,0xbd0e39da,0xbd226799,0xbd187fc0,0xbd2e3bde,0xbd41d870,0xbd531849,0xbd45e403,0xbd584853,0xbd676bd8,0xbd731447,0xbd676bd8,0xbd740bdd,0xbd7c3b28,0xbd7fd397,
0xbd7b14be,0xbd7fb10f,0xbd7ec46d,0xbd7853f8,0xbd7fb10f,0xbd7a7302,0xbd6ed89e,0xbd5d2d53,0xbd74fa0b,0xbd64aa59,0xbd4d9f02,0xbd3085bb,0xbd5b941a,0xbd3fc767,0xbd1d7fd1,0xbcebcbbb,
0x3d800000,0x3d800000,0x3d800000,0x3d800000,0x3d74fa0b,0x3d740bdd,0x3d731447,0x3d721352,0x3d54db31,0x3d514d3d,0x3d4d9f02,0x3d49d112,0x3d226799,0x3d1b02c6,0x3d13682a,0x3d0b9a6b,
0x3cc3ef15,0x3cac7cd4,0x3c94a031,0x3c78cfcc,0x3bc8bd36,0x3b16c32c,0xbac90ab0,0xbbafb680,0xbc47c5c2,0xbc888e93,0xbcac7cd4,0xbccf7bca,0xbcf15aea,0xbd0b9a6b,0xbd1d7fd1,0xbd2e3bde,
0xbd3504f3,0xbd45e403,0xbd54db31,0xbd61c598,0xbd61c598,0xbd6db293,0xbd76ba07,0xbd7cbfc9,0xbd7b14be,0xbd7f4e6d,0xbd7fb10f,0xbd7c3b28,0xbd7ec46d,0xbd791298,0xbd6ed89e,0xbd604621,
0xbd6c835e,0xbd5b941a,0xbd45e403,0xbd2beb4a,0xbd45e403,0xbd299415,0xbd08f59b,0xbcc9b953,0xbd0e39da,0xbccf7bca,0xbc78cfcc,0xbb96a905,0xbc94a031,0xbbe1bc2e,0x3b96a905,0x3c827dc0,
0x3d800000,0x3d800000,0x3d800000,0x3d800000,0x3d710908,0x3d6ff573,0x3d6ed89e,0x3d6db293,0x3d45e403,0x3d41d870,0x3d3daef9,0x3d396842,0x3d039c3d,0x3cf6e0cb,0x3ce63375,0x3cd53641,
0x3c47c5c2,0x3c164083,0x3bc8bd36,0x3b48fb30,0xbc164083,0xbc541501,0xbc888e93,0xbca68f12,0xbcf15aea,0xbd08f59b,0xbd187fc0,0xbd273656,0xbd3daef9,0xbd4bbbf8,0xbd584853,0xbd633c5a,
0xbd6c835e,0xbd74fa0b,0xbd7b14be,0xbd7ec46d,0xbd7fb10f,0xbd7f84ab,0xbd7c3b28,0xbd75dec6,0xbd74fa0b,0xbd6a09a7,0xbd5b941a,0xbd49d112,0xbd4d9f02,0xbd373a23,0xbd1d7fd1,0xbd00e7e4,
0xbd0e39da,0xbcdae880,0xbc94a031,0xbc164083,0xbc78cfcc,0xbbafb680,0x3b96a905,0x3c6c9a7f,0x3bc8bd36,0x3c888e93,0x3cdae880,0x3d13682a,0x3cdae880,0x3d15f6d9,0x3d396842,0x3d5695e5,
0x0,0x0,0x0,0x0,0x3c47c5c2,0x3c541501,0x3c605c13,0x3c6c9a7f,0x3cc3ef15,0x3ccf7bca,0x3cdae880,0x3ce63375,0x3d0e39da,0x3d15f6d9,0x3d1d7fd1,0x3d24d225,
0x3d3504f3,0x3d3daef9,0x3d45e403,0x3d4d9f02,0x3d54db31,0x3d5d2d53,0x3d64aa59,0x3d6b4b0c,0x3d6c835e,0x3d731447,0x3d7853f8,0x3d7c3b28,0x3d7b14be,0x3d7e70b0,0x3d7fec43,0x3d7f84ab,
0x3d800000,0x3d7ec46d,0x3d7b14be,0x3d74fa0b,0x3d7b14be,0x3d740bdd,0x3d6a09a7,0x3d5d2d53,0x3d6c835e,0x3d5ebe05,0x3d4d9f02,0x3d396842,0x3d54db31,0x3d3fc767,0x3d273656,0x3d0b9a6b,
0x3d3504f3,0x3d187fc0,0x3cf15aea,0x3cac7cd4,0x3d0e39da,0x3cd53641,0x3c888e93,0x3be1bc2e,0x3cc3ef15,0x3c605c13,0x3b48fb30,0xbbfab273,0x3c47c5c2,0x3a490e90,0xbc2f10a2,0xbcb263ef,
0x0,0x0,0x0,0x0,0x3c78cfcc,0x3c827dc0,0x3c888e93,0x3c8e9a22,0x3cf15aea,0x3cfc5d27,0x3d039c3d,0x3d08f59b,0x3d2beb4a,0x3d32c8c9,0x3d396842,0x3d3fc767,
0x3d54db31,0x3d5b941a,0x3d61c598,0x3d676bd8,0x3d710908,0x3d75dec6,0x3d79c79d,0x3d7cbfc9,0x3d7ec46d,0x3d7fec43,0x3d7fb10f,0x3d7e1324,0x3d7d3aac,0x3d791298,0x3d731447,0x3d6b4b0c,
0x3d6c835e,0x3d61c598,0x3d54db31,0x3d45e403,0x3d4d9f02,0x3d3b8f3b,0x3d273656,0x3d10d3cd,0x3d226799,0x3d08f59b,0x3cdae880,0x3ca09ae5,0x3cdae880,0x3c9aa086,0x3c2f10a2,0x3b16c32c,
0x3c47c5c2,0x3b48fb30,0xbbc8bd36,0xbc78cfcc,0xbb48fb30,0xbc541501,0xbcb8442a,0xbd00e7e4,0xbc94a031,0xbce63375,0xbd187fc0,0xbd396842,0xbd039c3d,0xbd299415,0xbd49d112,0xbd633c5a,
0x0,0x0,0x0,0x0,0x3c94a031,0x3c9aa086,0x3ca09ae5,0x3ca68f12,0x3d0e39da,0x3d13682a,0x3d187fc0,0x3d1d7fd1,0x3d45e403,0x3d4bbbf8,0x3d514d3d,0x3d5695e5,
0x3d6c835e,0x3d710908,0x3d74fa0b,0x3d7853f8,0x3d7ec46d,0x3d7fd397,0x3d7fec43,0x3d7f0e58,0x3d7b14be,0x3d76ba07,0x3d710908,0x3d6a09a7,0x3d61c598,0x3d5695e5,0x3d49d112,0x3d3b8f3b,
0x3d3504f3,0x3d226799,0x3d0e39da,0x3cf15aea,0x3cf15aea,0x3cbe1d4a,0x3c888e93,0x3c22abb6,0x3c47c5c2,0x3b96a905,0xbb48fb30,0xbc2f10a2,0xbbc8bd36,0xbc6c9a7f,0xbcb8442a,0xbcf6e0cb,
0xbcc3ef15,0xbd039c3d,0xbd226799,0xbd3daef9,0xbd226799,0xbd3fc767,0xbd584853,0xbd6b4b0c,0xbd54db31,0xbd6a09a7,0xbd7853f8,0xbd7f4e6d,0xbd74fa0b,0xbd7e70b0,0xbd7f4e6d,0xbd778bc5,
0x0,0x0,0x0,0x0,0x3cac7cd4,0x3cb263ef,0x3cb8442a,0x3cbe1d4a,0x3d226799,0x3d273656,0x3d2beb4a,0x3d3085bb,0x3d5b941a,0x3d604621,0x3d64aa59,0x3d68bf3c,
0x3d7b14be,0x3d7d3aac,0x3d7ec46d,0x3d7fb10f,0x3d7d3aac,0x3d7a7302,0x3d76ba07,0x3d721352,0x3d61c598,0x3d584853,0x3d4d9f02,0x3d41d870,0x3d2beb4a,0x3d1b02c6,0x3d08f59b,0x3cebcbbb,
0x3cc3ef15,0x3c94a031,0x3c47c5c2,0x3bc8bd36,0x3b48fb30,0xbb7b2b74,0xbc2f10a2,0xbc8e9a22,0xbc94a031,0xbccf7bca,0xbd039c3d,0xbd1d7fd1,0xbd187fc0,0xbd32c8c9,0xbd49d112,0xbd5d2d53,
0xbd54db31,0xbd676bd8,0xbd74fa0b,0xbd7d3aac,0xbd7853f8,0xbd7f0e58,0xbd7f4e6d,0xbd791298,0xbd7ec46d,0xbd76ba07,0xbd676bd8,0xbd514d3d,0xbd676bd8,0xbd4f7a1f,0xbd3085bb,0xbd0b9a6b,
0x3f80,0x3f80,0x3f80,0x3f80,0x3f80,0xbe483f7b,0xbec43f6d,0xbf0e3f55,0x3f80,0xbec43f6d,0xbf353f35,0xbf6d3ec4,0x3f80,0xbf0e3f55,0xbf6d3ec4,0xbf7bbe48,
0x3f80,0xbf353f35,0xbf80248d,0xbf35bf35,0x3f80,0xbf553f0e,0xbf6dbec4,0xbe48bf7b,0x3f80,0xbf6d3ec4,0xbf35bf35,0x3ec4bf6d,0x3f80,0xbf7b3e48,0xbec4bf6d,0x3f55bf0e,
0x3f80,0xbf80248d,0xa50dbf80,0x3f80a554,0x3f80,0xbf7bbe48,0x3ec4bf6d,0x3f553f0e,0x3f80,0xbf6dbec4,0x3f35bf35,0x3ec43f6d,0x3f80,0xbf55bf0e,0x3f6dbec4,0xbe483f7b,
0x3f80,0xbf35bf35,0x3f80a554,0xbf353f35,0x3f80,0xbf0ebf55,0x3f6d3ec4,0xbf7b3e48,0x3f80,0xbec4bf6d,0x3f353f35,0xbf6dbec4,0x3f80,0xbe48bf7b,0x3ec43f6d,0xbf0ebf55,
0x3f80,0x3f80,0x3f80,0x3f80,0xbf353f35,0xbf553f0e,0xbf6d3ec4,0xbf7b3e48,0xbf80248d,0xbf6dbec4,0xbf35bf35,0xbec4bf6d,0xbf35bf35,0xbe48bf7b,0x3ec4bf6d,0x3f55bf0e,
0xa50dbf80,0x3f35bf35,0x3f80a554,0x3f353f35,0x3f35bf35,0x3f7b3e48,0x3ec43f6d,0xbf0e3f55,0x3f80a554,0x3ec43f6d,0xbf353f35,0xbf6dbec4,0x3f353f35,0xbf0e3f55,0xbf6dbec4,0x3e48bf7b,
0x3f80,0xbf80248d,0xa50dbf80,0x3f80a554,0xbf353f35,0xbf0ebf55,0x3f6dbec4,0x3e483f7b,0xbf80248d,0x3ec4bf6d,0x3f353f35,0xbf6d3ec4,0xbf35bf35,0x3f7bbe48,0xbec43f6d,0xbf0ebf55,
0xa50dbf80,0x3f353f35,0xbf80248d,0x3f35bf35,0x3f35bf35,0xbe483f7b,0xbec4bf6d,0x3f553f0e,0x3f80a554,0xbf6d3ec4,0x3f35bf35,0xbec43f6d,0x3f353f35,0xbf55bf0e,0x3f6d3ec4,0xbf7bbe48,
0x3f80,0x3f80,0x3f80,0x3f80,0xbf80248d,0xbf7bbe48,0xbf6dbec4,0xbf55bf0e,0xa50dbf80,0x3ec4bf6d,0x3f35bf35,0x3f6dbec4,0x3f80a554,0x3f553f0e,0x3ec43f6d,0xbe483f7b,
0x3f80,0xbf353f35,0xbf80248d,0xbf35bf35,0xbf80248d,0xbf0ebf55,0x3ec4bf6d,0x3f7bbe48,0xa50dbf80,0x3f6dbec4,0x3f353f35,0xbec43f6d,0x3f80a554,0x3e483f7b,0xbf6d3ec4,0xbf0ebf55,
0x3f80,0xbf80248d,0xa50dbf80,0x3f80a554,0xbf80248d,0x3e48bf7b,0x3f6d3ec4,0xbf0e3f55,0xa50dbf80,0x3f6d3ec4,0xbf353f35,0xbec4bf6d,0x3f80a554,0xbf0e3f55,0xbec4bf6d,0x3f7b3e48,
0x3f80,0xbf35bf35,0x3f80a554,0xbf353f35,0xbf80248d,0x3f55bf0e,0xbec43f6d,0xbe48bf7b,0xa50dbf80,0x3ec43f6d,0xbf35bf35,0x3f6d3ec4,0x3f80a554,0xbf7b3e48,0x3f6dbec4,0xbf553f0e,
0x3f80,0x3f80,0x3f80,0x3f80,0xbf35bf35,0xbf0ebf55,0xbec4bf6d,0xbe48bf7b,0x3f80a554,0x3f6d3ec4,0x3f353f35,0x3ec43f6d,0xbf353f35,0xbf7b3e48,0xbf6dbec4,0xbf0ebf55,
0xa50dbf80,0x3f35bf35,0x3f80a554,0x3f353f35,0x3f353f35,0xbe483f7b,0xbf6d3ec4,0xbf55bf0e,0xbf80248d,0xbec4bf6d,0x3f35bf35,0x3f6d3ec4,0x3f35bf35,0x3f553f0e,0xbec43f6d,0xbf7bbe48,
0x3f80,0xbf80248d,0xa50dbf80,0x3f80a554,0xbf35bf35,0x3f55bf0e,0x3ec43f6d,0xbf7b3e48,0x3f80a554,0xbec43f6d,0xbf35bf35,0x3f6dbec4,0xbf353f35,0xbe48bf7b,0x3f6d3ec4,0xbf553f0e,
0xa50dbf80,0x3f353f35,0xbf80248d,0x3f35bf35,0x3f353f35,0xbf7bbe48,0x3f6dbec4,0xbf0e3f55,0xbf80248d,0x3f6dbec4,0xbf353f35,0x3ec4bf6d,0x3f35bf35,0xbf0e3f55,0x3ec4bf6d,0xbe483f7b,
0x3f80,0xa50dbf80,0x3f80,0xa50dbf80,0x3f80,0x3e48bf7b,0xbec43f6d,0x3f0ebf55,0x3f80,0x3ec4bf6d,0xbf353f35,0x3f6dbec4,0x3f80,0x3f0ebf55,0xbf6d3ec4,0x3f7b3e48,
0x3f80,0x3f35bf35,0xbf80248d,0x3f353f35,0x3f80,0x3f55bf0e,0xbf6dbec4,0x3e483f7b,0x3f80,0x3f6dbec4,0xbf35bf35,0xbec43f6d,0x3f80,0x3f7bbe48,0xbec4bf6d,0xbf553f0e,
0x3f80,0x3f80a554,0xa50dbf80,0xbf80248d,0x3f80,0x3f7b3e48,0x3ec4bf6d,0xbf55bf0e,0x3f80,0x3f6d3ec4,0x3f35bf35,0xbec4bf6d,0x3f80,0x3f553f0e,0x3f6dbec4,0x3e48bf7b,
0x3f80,0x3f353f35,0x3f80a554,0x3f35bf35,0x3f80,0x3f0e3f55,0x3f6d3ec4,0x3f7bbe48,0x3f80,0x3ec43f6d,0x3f353f35,0x3f6d3ec4,0x3f80,0x3e483f7b,0x3ec43f6d,0x3f0e3f55,
0x3f80,0xa50dbf80,0x3f80,0xa50dbf80,0xbf353f35,0x3f55bf0e,0xbf6d3ec4,0x3f7bbe48,0xbf80248d,0x3f6d3ec4,0xbf35bf35,0x3ec43f6d,0xbf35bf35,0x3e483f7b,0x3ec4bf6d,0xbf553f0e,
0xa50dbf80,0xbf353f35,0x3f80a554,0xbf35bf35,0x3f35bf35,0xbf7bbe48,0x3ec43f6d,0x3f0ebf55,0x3f80a554,0xbec4bf6d,0xbf353f35,0x3f6d3ec4,0x3f353f35,0x3f0ebf55,0xbf6dbec4,0xbe483f7b,
0x3f80,0x3f80a554,0xa50dbf80,0xbf80248d,0xbf353f35,0x3f0e3f55,0x3f6dbec4,0xbe48bf7b,0xbf80248d,0xbec43f6d,0x3f353f35,0x3f6dbec4,0xbf35bf35,0xbf7b3e48,0xbec43f6d,0x3f0e3f55,
0xa50dbf80,0xbf35bf35,0xbf80248d,0xbf353f35,0x3f35bf35,0x3e48bf7b,0xbec4bf6d,0xbf55bf0e,0x3f80a554,0x3f6dbec4,0x3f35bf35,0x3ec4bf6d,0x3f353f35,0x3f553f0e,0x3f6d3ec4,0x3f7b3e48,
0x3f80,0xa50dbf80,0x3f80,0xa50dbf80,0xbf80248d,0x3f7b3e48,0xbf6dbec4,0x3f553f0e,0xa50dbf80,0xbec43f6d,0x3f35bf35,0xbf6d3ec4,0x3f80a554,0xbf55bf0e,0x3ec43f6d,0x3e48bf7b,
0x3f80,0x3f35bf35,0xbf80248d,0x3f353f35,0xbf80248d,0x3f0e3f55,0x3ec4bf6d,0xbf7b3e48,0xa50dbf80,0xbf6d3ec4,0x3f353f35,0x3ec4bf6d,0x3f80a554,0xbe48bf7b,0xbf6d3ec4,0x3f0e3f55,
0x3f80,0x3f80a554,0xa50dbf80,0xbf80248d,0xbf80248d,0xbe483f7b,0x3f6d3ec4,0x3f0ebf55,0xa50dbf80,0xbf6dbec4,0xbf353f35,0x3ec43f6d,0x3f80a554,0x3f0ebf55,0xbec4bf6d,0xbf7bbe48,
0x3f80,0x3f353f35,0x3f80a554,0x3f35bf35,0xbf80248d,0xbf553f0e,0xbec43f6d,0x3e483f7b,0xa50dbf80,0xbec4bf6d,0xbf35bf35,0xbf6dbec4,0x3f80a554,0x3f7bbe48,0x3f6dbec4,0x3f55bf0e,
0x3f80,0xa50dbf80,0x3f80,0xa50dbf80,0xbf35bf35,0x3f0e3f55,0xbec4bf6d,0x3e483f7b,0x3f80a554,0xbf6dbec4,0x3f353f35,0xbec4bf6d,0xbf353f35,0x3f7bbe48,0xbf6dbec4,0x3f0e3f55,
0xa50dbf80,0xbf353f35,0x3f80a554,0xbf35bf35,0x3f353f35,0x3e48bf7b,0xbf6d3ec4,0x3f553f0e,0xbf80248d,0x3ec43f6d,0x3f35bf35,0xbf6dbec4,0x3f35bf35,0xbf55bf0e,0xbec43f6d,0x3f7b3e48,
0x3f80,0x3f80a554,0xa50dbf80,0xbf80248d,0xbf35bf35,0xbf553f0e,0x3ec43f6d,0x3f7bbe48,0x3f80a554,0x3ec4bf6d,0xbf35bf35,0xbf6d3ec4,0xbf353f35,0x3e483f7b,0x3f6d3ec4,0x3f55bf0e,
0xa50dbf80,0xbf35bf35,0xbf80248d,0xbf353f35,0x3f353f35,0x3f7b3e48,0x3f6dbec4,0x3f0ebf55,0xbf80248d,0xbf6d3ec4,0xbf353f35,0xbec43f6d,0x3f35bf35,0x3f0ebf55,0x3ec4bf6d,0x3e48bf7b,
0x3f80,0x3f80,0x3f80,0x3f80,0xa50dbf80,0x3e48bf7b,0x3ec4bf6d,0x3f0ebf55,0x3f80,0xbec43f6d,0xbf353f35,0xbf6d3ec4,0xa50dbf80,0x3f0ebf55,0x3f6dbec4,0x3f7b3e48,
0x3f80,0xbf353f35,0xbf80248d,0xbf35bf35,0xa50dbf80,0x3f55bf0e,0x3f6d3ec4,0x3e483f7b,0x3f80,0xbf6d3ec4,0xbf35bf35,0x3ec4bf6d,0xa50dbf80,0x3f7bbe48,0x3ec43f6d,0xbf553f0e,
0x3f80,0xbf80248d,0xa50dbf80,0x3f80a554,0xa50dbf80,0x3f7b3e48,0xbec43f6d,0xbf55bf0e,0x3f80,0xbf6dbec4,0x3f35bf35,0x3ec43f6d,0xa50dbf80,0x3f553f0e,0xbf6d3ec4,0x3e48bf7b,
0x3f80,0xbf35bf35,0x3f80a554,0xbf353f35,0xa50dbf80,0x3f0e3f55,0xbf6dbec4,0x3f7bbe48,0x3f80,0xbec4bf6d,0x3f353f35,0xbf6dbec4,0xa50dbf80,0x3e483f7b,0xbec4bf6d,0x3f0e3f55,
0x3f80,0x3f80,0x3f80,0x3f80,0x3f35bf35,0x3f55bf0e,0x3f6dbec4,0x3f7bbe48,0xbf80248d,0xbf6dbec4,0xbf35bf35,0xbec4bf6d,0x3f353f35,0x3e483f7b,0xbec43f6d,0xbf553f0e,
0xa50dbf80,0x3f35bf35,0x3f80a554,0x3f353f35,0xbf353f35,0xbf7bbe48,0xbec4bf6d,0x3f0ebf55,0x3f80a554,0x3ec43f6d,0xbf353f35,0xbf6dbec4,0xbf35bf35,0x3f0ebf55,0x3f6d3ec4,0xbe483f7b,
0x3f80,0xbf80248d,0xa50dbf80,0x3f80a554,0x3f35bf35,0x3f0e3f55,0xbf6d3ec4,0xbe48bf7b,0xbf80248d,0x3ec4bf6d,0x3f353f35,0xbf6d3ec4,0x3f353f35,0xbf7b3e48,0x3ec4bf6d,0x3f0e3f55,
0xa50dbf80,0x3f353f35,0xbf80248d,0x3f35bf35,0xbf353f35,0x3e48bf7b,0x3ec43f6d,0xbf55bf0e,0x3f80a554,0xbf6d3ec4,0x3f35bf35,0xbec43f6d,0xbf35bf35,0x3f553f0e,0xbf6dbec4,0x3f7b3e48,
0x3f80,0x3f80,0x3f80,0x3f80,0x3f80a554,0x3f7b3e48,0x3f6d3ec4,0x3f553f0e,0xa50dbf80,0x3ec4bf6d,0x3f35bf35,0x3f6dbec4,0xbf80248d,0xbf55bf0e,0xbec4bf6d,0x3e48bf7b,
0x3f80,0xbf353f35,0xbf80248d,0xbf35bf35,0x3f80a554,0x3f0e3f55,0xbec43f6d,0xbf7b3e48,0xa50dbf80,0x3f6dbec4,0x3f353f35,0xbec43f6d,0xbf80248d,0xbe48bf7b,0x3f6dbec4,0x3f0e3f55,
0x3f80,0xbf80248d,0xa50dbf80,0x3f80a554,0x3f80a554,0xbe483f7b,0xbf6dbec4,0x3f0ebf55,0xa50dbf80,0x3f6d3ec4,0xbf353f35,0xbec4bf6d,0xbf80248d,0x3f0ebf55,0x3ec43f6d,0xbf7bbe48,
0x3f80,0xbf35bf35,0x3f80a554,0xbf353f35,0x3f80a554,0xbf553f0e,0x3ec4bf6d,0x3e483f7b,0xa50dbf80,0x3ec43f6d,0xbf35bf35,0x3f6d3ec4,0xbf80248d,0x3f7bbe48,0xbf6d3ec4,0x3f55bf0e,
0x3f80,0x3f80,0x3f80,0x3f80,0x3f353f35,0x3f0e3f55,0x3ec43f6d,0x3e483f7b,0x3f80a554,0x3f6d3ec4,0x3f353f35,0x3ec43f6d,0x3f35bf35,0x3f7bbe48,0x3f6d3ec4,0x3f0e3f55,
0xa50dbf80,0x3f35bf35,0x3f80a554,0x3f353f35,0xbf35bf35,0x3e48bf7b,0x3f6dbec4,0x3f553f0e,0xbf80248d,0xbec4bf6d,0x3f35bf35,0x3f6d3ec4,0xbf353f35,0xbf55bf0e,0x3ec4bf6d,0x3f7b3e48,
0x3f80,0xbf80248d,0xa50dbf80,0x3f80a554,0x3f353f35,0xbf553f0e,0xbec4bf6d,0x3f7bbe48,0x3f80a554,0xbec43f6d,0xbf35bf35,0x3f6dbec4,0x3f35bf35,0x3e483f7b,0xbf6dbec4,0x3f55bf0e,
0xa50dbf80,0x3f353f35,0xbf80248d,0x3f35bf35,0xbf35bf35,0x3f7b3e48,0xbf6d3ec4,0x3f0ebf55,0xbf80248d,0x3f6dbec4,0xbf353f35,0x3ec4bf6d,0xbf353f35,0x3f0ebf55,0xbec43f6d,0x3e48bf7b,
0x3f80,0xa50dbf80,0x3f80,0xa50dbf80,0xa50dbf80,0xbe483f7b,0x3ec4bf6d,0xbf0e3f55,0x3f80,0x3ec4bf6d,0xbf353f35,0x3f6dbec4,0xa50dbf80,0xbf0e3f55,0x3f6dbec4,0xbf7bbe48,
0x3f80,0x3f35bf35,0xbf80248d,0x3f353f35,0xa50dbf80,0xbf553f0e,0x3f6d3ec4,0xbe48bf7b,0x3f80,0x3f6dbec4,0xbf35bf35,0xbec43f6d,0xa50dbf80,0xbf7b3e48,0x3ec43f6d,0x3f55bf0e,
0x3f80,0x3f80a554,0xa50dbf80,0xbf80248d,0xa50dbf80,0xbf7bbe48,0xbec43f6d,0x3f553f0e,0x3f80,0x3f6d3ec4,0x3f35bf35,0xbec4bf6d,0xa50dbf80,0xbf55bf0e,0xbf6d3ec4,0xbe483f7b,
0x3f80,0x3f353f35,0x3f80a554,0x3f35bf35,0xa50dbf80,0xbf0ebf55,0xbf6dbec4,0xbf7b3e48,0x3f80,0x3ec43f6d,0x3f353f35,0x3f6d3ec4,0xa50dbf80,0xbe48bf7b,0xbec4bf6d,0xbf0ebf55,
0x3f80,0xa50dbf80,0x3f80,0xa50dbf80,0x3f35bf35,0xbf553f0e,0x3f6dbec4,0xbf7b3e48,0xbf80248d,0x3f6d3ec4,0xbf35bf35,0x3ec43f6d,0x3f353f35,0xbe48bf7b,0xbec43f6d,0x3f55bf0e,
0xa50dbf80,0xbf353f35,0x3f80a554,0xbf35bf35,0xbf353f35,0x3f7b3e48,0xbec4bf6d,0xbf0e3f55,0x3f80a554,0xbec4bf6d,0xbf353f35,0x3f6d3ec4,0xbf35bf35,0xbf0e3f55,0x3f6d3ec4,0x3e48bf7b,
0x3f80,0x3f80a554,0xa50dbf80,0xbf80248d,0x3f35bf35,0xbf0ebf55,0xbf6d3ec4,0x3e483f7b,0xbf80248d,0xbec43f6d,0x3f353f35,0x3f6dbec4,0x3f353f35,0x3f7bbe48,0x3ec4bf6d,0xbf0ebf55,
0xa50dbf80,0xbf35bf35,0xbf80248d,0xbf353f35,0xbf353f35,0xbe483f7b,0x3ec43f6d,0x3f553f0e,0x3f80a554,0x3f6dbec4,0x3f35bf35,0x3ec4bf6d,0xbf35bf35,0xbf55bf0e,0xbf6dbec4,0xbf7bbe48,
0x3f80,0xa50dbf80,0x3f80,0xa50dbf80,0x3f80a554,0xbf7bbe48,0x3f6d3ec4,0xbf55bf0e,0xa50dbf80,0xbec43f6d,0x3f35bf35,0xbf6d3ec4,0xbf80248d,0x3f553f0e,0xbec4bf6d,0xbe483f7b,
0x3f80,0x3f35bf35,0xbf80248d,0x3f353f35,0x3f80a554,0xbf0ebf55,0xbec43f6d,0x3f7bbe48,0xa50dbf80,0xbf6d3ec4,0x3f353f35,0x3ec4bf6d,0xbf80248d,0x3e483f7b,0x3f6dbec4,0xbf0ebf55,
0x3f80,0x3f80a554,0xa50dbf80,0xbf80248d,0x3f80a554,0x3e48bf7b,0xbf6dbec4,0xbf0e3f55,0xa50dbf80,0xbf6dbec4,0xbf353f35,0x3ec43f6d,0xbf80248d,0xbf0e3f55,0x3ec43f6d,0x3f7b3e48,
0x3f80,0x3f353f35,0x3f80a554,0x3f35bf35,0x3f80a554,0x3f55bf0e,0x3ec4bf6d,0xbe48bf7b,0xa50dbf80,0xbec4bf6d,0xbf35bf35,0xbf6dbec4,0xbf80248d,0xbf7b3e48,0xbf6d3ec4,0xbf553f0e,
0x3f80,0xa50dbf80,0x3f80,0xa50dbf80,0x3f353f35,0xbf0ebf55,0x3ec43f6d,0xbe48bf7b,0x3f80a554,0xbf6dbec4,0x3f353f35,0xbec4bf6d,0x3f35bf35,0xbf7b3e48,0x3f6d3ec4,0xbf0ebf55,
0xa50dbf80,0xbf353f35,0x3f80a554,0xbf35bf35,0xbf35bf35,0xbe483f7b,0x3f6dbec4,0xbf55bf0e,0xbf80248d,0x3ec43f6d,0x3f35bf35,0xbf6dbec4,0xbf353f35,0x3f553f0e,0x3ec4bf6d,0xbf7bbe48,
0x3f80,0x3f80a554,0xa50dbf80,0xbf80248d,0x3f353f35,0x3f55bf0e,0xbec4bf6d,0xbf7b3e48,0x3f80a554,0x3ec4bf6d,0xbf35bf35,0xbf6d3ec4,0x3f35bf35,0xbe48bf7b,0xbf6dbec4,0xbf553f0e,
0xa50dbf80,0xbf35bf35,0xbf80248d,0xbf353f35,0xbf35bf35,0xbf7bbe48,0xbf6d3ec4,0xbf0e3f55,0xbf80248d,0xbf6d3ec4,0xbf353f35,0xbec43f6d,0xbf353f35,0xbf0e3f55,0xbec43f6d,0xbe483f7b,
0x35a8,0x35a8,0x0,0x0,0x35a8,0x34003400,0x0,0x0,0x35a8,0x35a80000,0x0,0x0,0x35a8,0x3400b400,0x0,0x0,
0x35a8,0xb5a8,0x0,0x0,0x35a8,0xb400b400,0x0,0x0,0x35a8,0xb5a88000,0x0,0x0,0x35a8,0xb4003400,0x0,0x0,
0x0,0x0,0x35a8,0x35a8,0x0,0x0,0x35a8,0x34003400,0x0,0x0,0x35a8,0x35a80000,0x0,0x0,0x35a8,0x3400b400,
0x0,0x0,0x35a8,0xb5a8,0x0,0x0,0x35a8,0xb400b400,0x0,0x0,0x35a8,0xb5a88000,0x0,0x0,0x35a8,0xb4003400,
0x35a8,0x35a8,0x0,0x0,0x35a80000,0x3400b400,0x0,0x0,0xb5a8,0xb5a88000,0x0,0x0,0xb5a88000,0x34003400,0x0,0x0,
0x35a8,0xb5a8,0x0,0x0,0x35a80000,0xb4003400,0x0,0x0,0xb5a8,0x35a80000,0x0,0x0,0xb5a88000,0xb400b400,0x0,0x0,
0x0,0x0,0x35a8,0x35a8,0x0,0x0,0x35a80000,0x3400b400,0x0,0x0,0xb5a8,0xb5a88000,0x0,0x0,0xb5a88000,0x34003400,
0x0,0x0,0x35a8,0xb5a8,0x0,0x0,0x35a80000,0xb4003400,0x0,0x0,0xb5a8,0x35a80000,0x0,0x0,0xb5a88000,0xb400b400,
0x35a8,0x35a8,0x0,0x0,0xb5a8,0xb400b400,0x0,0x0,0x35a8,0x35a80000,0x0,0x0,0xb5a8,0xb4003400,0x0,0x0,
0x35a8,0xb5a8,0x0,0x0,0xb5a8,0x34003400,0x0,0x0,0x35a8,0xb5a88000,0x0,0x0,0xb5a8,0x3400b400,0x0,0x0,
0x0,0x0,0x35a8,0x35a8,0x0,0x0,0xb5a8,0xb400b400,0x0,0x0,0x35a8,0x35a80000,0x0,0x0,0xb5a8,0xb4003400,
0x0,0x0,0x35a8,0xb5a8,0x0,0x0,0xb5a8,0x34003400,0x0,0x0,0x35a8,0xb5a88000,0x0,0x0,0xb5a8,0x3400b400,
0x35a8,0x35a8,0x0,0x0,0xb5a88000,0xb4003400,0x0,0x0,0xb5a8,0xb5a88000,0x0,0x0,0x35a80000,0xb400b400,0x0,0x0,
0x35a8,0xb5a8,0x0,0x0,0xb5a88000,0x3400b400,0x0,0x0,0xb5a8,0x35a80000,0x0,0x0,0x35a80000,0x34003400,0x0,0x0,
0x0,0x0,0x35a8,0x35a8,0x0,0x0,0xb5a88000,0xb4003400,0x0,0x0,0xb5a8,0xb5a88000,0x0,0x0,0x35a80000,0xb400b400,
0x0,0x0,0x35a8,0xb5a8,0x0,0x0,0xb5a88000,0x3400b400,0x0,0x0,0xb5a8,0x35a80000,0x0,0x0,0x35a80000,0x34003400,
0x35a80000,0x35a80000,0x0,0x0,0x35a80000,0x3400b400,0x0,0x0,0x35a80000,0xb5a8,0x0,0x0,0x35a80000,0xb400b400,0x0,0x0,
0x35a80000,0xb5a88000,0x0,0x0,0x35a80000,0xb4003400,0x0,0x0,0x35a80000,0x800035a8,0x0,0x0,0x35a80000,0x34003400,0x0,0x0,
0x0,0x0,0x35a80000,0x35a80000,0x0,0x0,0x35a80000,0x3400b400,0x0,0x0,0x35a80000,0xb5a8,0x0,0x0,0x35a80000,0xb400b400,
0x0,0x0,0x35a80000,0xb5a88000,0x0,0x0,0x35a80000,0xb4003400,0x0,0x0,0x35a80000,0x800035a8,0x0,0x0,0x35a80000,0x34003400,
0x35a80000,0x35a80000,0x0,0x0,0xb5a8,0xb400b400,0x0,0x0,0xb5a88000,0x800035a8,0x0,0x0,0x800035a8,0x3400b400,0x0,0x0,
0x35a80000,0xb5a88000,0x0,0x0,0xb5a8,0x34003400,0x0,0x0,0xb5a88000,0xb5a8,0x0,0x0,0x800035a8,0xb4003400,0x0,0x0,
0x0,0x0,0x35a80000,0x35a80000,0x0,0x0,0xb5a8,0xb400b400,0x0,0x0,0xb5a88000,0x800035a8,0x0,0x0,0x800035a8,0x3400b400,
0x0,0x0,0x35a80000,0xb5a88000,0x0,0x0,0xb5a8,0x34003400,0x0,0x0,0xb5a88000,0xb5a8,0x0,0x0,0x800035a8,0xb4003400,
0x35a80000,0x35a80000,0x0,0x0,0xb5a88000,0xb4003400,0x0,0x0,0x35a80000,0xb5a8,0x0,0x0,0xb5a88000,0x34003400,0x0,0x0,
0x35a80000,0xb5a88000,0x0,0x0,0xb5a88000,0x3400b400,0x0,0x0,0x35a80000,0x800035a8,0x0,0x0,0xb5a88000,0xb400b400,0x0,0x0,
0x0,0x0,0x35a80000,0x35a80000,0x0,0x0,0xb5a88000,0xb4003400,0x0,0x0,0x35a80000,0xb5a8,0x0,0x0,0xb5a88000,0x34003400,
0x0,0x0,0x35a80000,0xb5a88000,0x0,0x0,0xb5a88000,0x3400b400,0x0,0x0,0x35a80000,0x800035a8,0x0,0x0,0xb5a88000,0xb400b400,
0x35a80000,0x35a80000,0x0,0x0,0x800035a8,0x34003400,0x0,0x0,0xb5a88000,0x800035a8,0x0,0x0,0xb5a8,0xb4003400,0x0,0x0,
0x35a80000,0xb5a88000,0x0,0x0,0x800035a8,0xb400b400,0x0,0x0,0xb5a88000,0xb5a8,0x0,0x0,0xb5a8,0x3400b400,0x0,0x0,
0x0,0x0,0x35a80000,0x35a80000,0x0,0x0,0x800035a8,0x34003400,0x0,0x0,0xb5a88000,0x800035a8,0x0,0x0,0xb5a8,0xb4003400,
0x0,0x0,0x35a80000,0xb5a88000,0x0,0x0,0x800035a8,0xb400b400,0x0,0x0,0xb5a88000,0xb5a8,0x0,0x0,0xb5a8,0x3400b400,
0x3c003c00,0x3c003c00,0x3c003c00,0x3c003c00,0x3bd93c00,0x3aa73b64,0x387239a8,0x323e361f,0x3b643c00,0x361f39a8,0xb61f0000,0xbb64b9a8,0x3aa73c00,0xb23e361f,0xbbd9b9a8,0xb872bb64,
0x39a83c00,0xb9a80000,0xb9a8bc00,0x39a88000,0x38723c00,0xbbd9b61f,0x323eb9a8,0x3aa73b64,0x361f3c00,0xbb64b9a8,0x3b648000,0xb61f39a8,0x323e3c00,0xb872bb64,0x3aa739a8,0xbbd9b61f,
0x3c00,0x8000bc00,0x3c00,0x8000bc00,0xb23e3c00,0x3872bb64,0xbaa739a8,0x3bd9b61f,0xb61f3c00,0x3b64b9a8,0xbb640000,0x361f39a8,0xb8723c00,0x3bd9b61f,0xb23eb9a8,0xbaa73b64,
0xb9a83c00,0x39a88000,0x39a8bc00,0xb9a80000,0xbaa73c00,0x323e361f,0x3bd9b9a8,0x3872bb64,0xbb643c00,0xb61f39a8,0x361f8000,0x3b64b9a8,0xbbd93c00,0xbaa73b64,0xb87239a8,0xb23e361f,
0x3c003c00,0x3c003c00,0x3c003c00,0x3c003c00,0xb23e0000,0xb872b61f,0xbaa7b9a8,0xbbd9bb64,0xbb64bc00,0xb61fb9a8,0x361f8000,0x3b6439a8,0x38728000,0x3bd93b64,0x323e39a8,0xbaa7b61f,
0x39a83c00,0xb9a80000,0xb9a8bc00,0x39a88000,0xbaa70000,0xb23ebb64,0x3bd939a8,0xb872361f,0xb61fbc00,0x3b6439a8,0xbb640000,0x361fb9a8,0x3bd98000,0xbaa7361f,0x3872b9a8,0xb23e3b64,
0x3c00,0x8000bc00,0x3c00,0x8000bc00,0xbbd90000,0x3aa7361f,0xb872b9a8,0x323e3b64,0x361fbc00,0xbb6439a8,0x3b648000,0xb61fb9a8,0x3aa78000,0x323ebb64,0xbbd939a8,0x3872361f,
0xb9a83c00,0x39a88000,0x39a8bc00,0xb9a80000,0xb8720000,0xbbd93b64,0xb23e39a8,0x3aa7b61f,0x3b64bc00,0x361fb9a8,0xb61f0000,0xbb6439a8,0x323e8000,0x3872b61f,0x3aa7b9a8,0x3bd9bb64,
0x3c003c00,0x3c003c00,0x3c003c00,0x3c003c00,0xbbd9bc00,0xbaa7bb64,0xb872b9a8,0xb23eb61f,0x3b643c00,0x361f39a8,0xb61f0000,0xbb64b9a8,0xbaa7bc00,0x323eb61f,0x3bd939a8,0x38723b64,
0x39a83c00,0xb9a80000,0xb9a8bc00,0x39a88000,0xb872bc00,0x3bd9361f,0xb23e39a8,0xbaa7bb64,0x361f3c00,0xbb64b9a8,0x3b648000,0xb61f39a8,0xb23ebc00,0x38723b64,0xbaa7b9a8,0x3bd9361f,
0x3c00,0x8000bc00,0x3c00,0x8000bc00,0x323ebc00,0xb8723b64,0x3aa7b9a8,0xbbd9361f,0xb61f3c00,0x3b64b9a8,0xbb640000,0x361f39a8,0x3872bc00,0xbbd9361f,0x323e39a8,0x3aa7bb64,
0xb9a83c00,0x39a88000,0x39a8bc00,0xb9a80000,0x3aa7bc00,0xb23eb61f,0xbbd939a8,0xb8723b64,0xbb643c00,0xb61f39a8,0x361f8000,0x3b64b9a8,0x3bd9bc00,0x3aa7bb64,0x3872b9a8,0x323eb61f,
0x3c003c00,0x3c003c00,0x3c003c00,0x3c003c00,0x323e8000,0x3872361f,0x3aa739a8,0x3bd93b64,0xbb64bc00,0xb61fb9a8,0x361f8000,0x3b6439a8,0xb8720000,0xbbd9bb64,0xb23eb9a8,0x3aa7361f,
0x39a83c00,0xb9a80000,0xb9a8bc00,0x39a88000,0x3aa78000,0x323e3b64,0xbbd9b9a8,0x3872b61f,0xb61fbc00,0x3b6439a8,0xbb640000,0x361fb9a8,0xbbd90000,0x3aa7b61f,0xb87239a8,0x323ebb64,
0x3c00,0x8000bc00,0x3c00,0x8000bc00,0x3bd98000,0xbaa7b61f,0x387239a8,0xb23ebb64,0x361fbc00,0xbb6439a8,0x3b648000,0xb61fb9a8,0xbaa70000,0xb23e3b64,0x3bd9b9a8,0xb872b61f,
0xb9a83c00,0x39a88000,0x39a8bc00,0xb9a80000,0x38728000,0x3bd9bb64,0x323eb9a8,0xbaa7361f,0x3b64bc00,0x361fb9a8,0xb61f0000,0xbb6439a8,0xb23e0000,0xb872361f,0xbaa739a8,0xbbd93b64,
0xbc003c00,0xbc003c00,0xbc003c00,0xbc003c00,0xbbd93c00,0xbaa73b64,0xb87239a8,0xb23e361f,0xbb643c00,0xb61f39a8,0x361f0000,0x3b64b9a8,0xbaa73c00,0x323e361f,0x3bd9b9a8,0x3872bb64,
0xb9a83c00,0x39a80000,0x39a8bc00,0xb9a88000,0xb8723c00,0x3bd9b61f,0xb23eb9a8,0xbaa73b64,0xb61f3c00,0x3b64b9a8,0xbb648000,0x361f39a8,0xb23e3c00,0x3872bb64,0xbaa739a8,0x3bd9b61f,
0x80003c00,0xbc00,0x80003c00,0xbc00,0x323e3c00,0xb872bb64,0x3aa739a8,0xbbd9b61f,0x361f3c00,0xbb64b9a8,0x3b640000,0xb61f39a8,0x38723c00,0xbbd9b61f,0x323eb9a8,0x3aa73b64,
0x39a83c00,0xb9a88000,0xb9a8bc00,0x39a80000,0x3aa73c00,0xb23e361f,0xbbd9b9a8,0xb872bb64,0x3b643c00,0x361f39a8,0xb61f8000,0xbb64b9a8,0x3bd93c00,0x3aa73b64,0x387239a8,0x323e361f,
0xbc003c00,0xbc003c00,0xbc003c00,0xbc003c00,0x323e0000,0x3872b61f,0x3aa7b9a8,0x3bd9bb64,0x3b64bc00,0x361fb9a8,0xb61f8000,0xbb6439a8,0xb8728000,0xbbd93b64,0xb23e39a8,0x3aa7b61f,
0xb9a83c00,0x39a80000,0x39a8bc00,0xb9a88000,0x3aa70000,0x323ebb64,0xbbd939a8,0x3872361f,0x361fbc00,0xbb6439a8,0x3b640000,0xb61fb9a8,0xbbd98000,0x3aa7361f,0xb872b9a8,0x323e3b64,
0x80003c00,0xbc00,0x80003c00,0xbc00,0x3bd90000,0xbaa7361f,0x3872b9a8,0xb23e3b64,0xb61fbc00,0x3b6439a8,0xbb648000,0x361fb9a8,0xbaa78000,0xb23ebb64,0x3bd939a8,0xb872361f,
0x39a83c00,0xb9a88000,0xb9a8bc00,0x39a80000,0x38720000,0x3bd93b64,0x323e39a8,0xbaa7b61f,0xbb64bc00,0xb61fb9a8,0x361f0000,0x3b6439a8,0xb23e8000,0xb872b61f,0xbaa7b9a8,0xbbd9bb64,
0xbc003c00,0xbc003c00,0xbc003c00,0xbc003c00,0x3bd9bc00,0x3aa7bb64,0x3872b9a8,0x323eb61f,0xbb643c00,0xb61f39a8,0x361f0000,0x3b64b9a8,0x3aa7bc00,0xb23eb61f,0xbbd939a8,0xb8723b64,
0xb9a83c00,0x39a80000,0x39a8bc00,0xb9a88000,0x3872bc00,0xbbd9361f,0x323e39a8,0x3aa7bb64,0xb61f3c00,0x3b64b9a8,0xbb648000,0x361f39a8,0x323ebc00,0xb8723b64,0x3aa7b9a8,0xbbd9361f,
0x80003c00,0xbc00,0x80003c00,0xbc00,0xb23ebc00,0x38723b64,0xbaa7b9a8,0x3bd9361f,0x361f3c00,0xbb64b9a8,0x3b640000,0xb61f39a8,0xb872bc00,0x3bd9361f,0xb23e39a8,0xbaa7bb64,
0x39a83c00,0xb9a88000,0xb9a8bc00,0x39a80000,0xbaa7bc00,0x323eb61f,0x3bd939a8,0x38723b64,0x3b643c00,0x361f39a8,0xb61f8000,0xbb64b9a8,0xbbd9bc00,0xbaa7bb64,0xb872b9a8,0xb23eb61f,
0xbc003c00,0xbc003c00,0xbc003c00,0xbc003c00,0xb23e8000,0xb872361f,0xbaa739a8,0xbbd93b64,0x3b64bc00,0x361fb9a8,0xb61f8000,0xbb6439a8,0x38720000,0x3bd9bb64,0x323eb9a8,0xbaa7361f,
0xb9a83c00,0x39a80000,0x39a8bc00,0xb9a88000,0xbaa78000,0xb23e3b64,0x3bd9b9a8,0xb872b61f,0x361fbc00,0xbb6439a8,0x3b640000,0xb61fb9a8,0x3bd90000,0xbaa7b61f,0x387239a8,0xb23ebb64,
0x80003c00,0xbc00,0x80003c00,0xbc00,0xbbd98000,0x3aa7b61f,0xb87239a8,0x323ebb64,0xb61fbc00,0x3b6439a8,0xbb648000,0x361fb9a8,0x3aa70000,0x323e3b64,0xbbd9b9a8,0x3872b61f,
0x39a83c00,0xb9a88000,0xb9a8bc00,0x39a80000,0xb8728000,0xbbd9bb64,0xb23eb9a8,0x3aa7361f,0xbb64bc00,0xb61fb9a8,0x361f0000,0x3b6439a8,0x323e0000,0x3872361f,0x3aa739a8,0x3bd93b64,
0x0,0x0,0x0,0x0,0xb23e0000,0xb872b61f,0xbaa7b9a8,0xbbd9bb64,0xb61f0000,0xbb64b9a8,0xbb64bc00,0xb61fb9a8,0xb8720000,0xbbd9bb64,0xb23eb9a8,0x3aa7361f,
0xb9a80000,0xb9a8bc00,0x39a88000,0x39a83c00,0xbaa70000,0xb23ebb64,0x3bd939a8,0xb872361f,0xbb640000,0x361fb9a8,0x361f3c00,0xbb64b9a8,0xbbd90000,0x3aa7b61f,0xb87239a8,0x323ebb64,
0xbc000000,0x3c008000,0xbc000000,0x3c008000,0xbbd90000,0x3aa7361f,0xb872b9a8,0x323e3b64,0xbb640000,0x361f39a8,0x361fbc00,0xbb6439a8,0xbaa70000,0xb23e3b64,0x3bd9b9a8,0xb872b61f,
0xb9a80000,0xb9a83c00,0x39a88000,0x39a8bc00,0xb8720000,0xbbd93b64,0xb23e39a8,0x3aa7b61f,0xb61f0000,0xbb6439a8,0xbb643c00,0xb61f39a8,0xb23e0000,0xb872361f,0xbaa739a8,0xbbd93b64,
0x0,0x0,0x0,0x0,0xbbd9bc00,0xbaa7bb64,0xb872b9a8,0xb23eb61f,0x361f8000,0x3b6439a8,0x3b643c00,0x361f39a8,0x3aa73c00,0xb23e361f,0xbbd9b9a8,0xb872bb64,
0xb9a80000,0xb9a8bc00,0x39a88000,0x39a83c00,0xb872bc00,0x3bd9361f,0xb23e39a8,0xbaa7bb64,0x3b648000,0xb61f39a8,0xb61fbc00,0x3b6439a8,0x323e3c00,0xb872bb64,0x3aa739a8,0xbbd9b61f,
0xbc000000,0x3c008000,0xbc000000,0x3c008000,0x323ebc00,0xb8723b64,0x3aa7b9a8,0xbbd9361f,0x3b648000,0xb61fb9a8,0xb61f3c00,0x3b64b9a8,0xb8723c00,0x3bd9b61f,0xb23eb9a8,0xbaa73b64,
0xb9a80000,0xb9a83c00,0x39a88000,0x39a8bc00,0x3aa7bc00,0xb23eb61f,0xbbd939a8,0xb8723b64,0x361f8000,0x3b64b9a8,0x3b64bc00,0x361fb9a8,0xbbd93c00,0xbaa73b64,0xb87239a8,0xb23e361f,
0x0,0x0,0x0,0x0,0x323e8000,0x3872361f,0x3aa739a8,0x3bd93b64,0xb61f0000,0xbb64b9a8,0xbb64bc00,0xb61fb9a8,0x38728000,0x3bd93b64,0x323e39a8,0xbaa7b61f,
0xb9a80000,0xb9a8bc00,0x39a88000,0x39a83c00,0x3aa78000,0x323e3b64,0xbbd9b9a8,0x3872b61f,0xbb640000,0x361fb9a8,0x361f3c00,0xbb64b9a8,0x3bd98000,0xbaa7361f,0x3872b9a8,0xb23e3b64,
0xbc000000,0x3c008000,0xbc000000,0x3c008000,0x3bd98000,0xbaa7b61f,0x387239a8,0xb23ebb64,0xbb640000,0x361f39a8,0x361fbc00,0xbb6439a8,0x3aa78000,0x323ebb64,0xbbd939a8,0x3872361f,
0xb9a80000,0xb9a83c00,0x39a88000,0x39a8bc00,0x38728000,0x3bd9bb64,0x323eb9a8,0xbaa7361f,0xb61f0000,0xbb6439a8,0xbb643c00,0xb61f39a8,0x323e8000,0x3872b61f,0x3aa7b9a8,0x3bd9bb64,
0x0,0x0,0x0,0x0,0x3bd93c00,0x3aa73b64,0x387239a8,0x323e361f,0x361f8000,0x3b6439a8,0x3b643c00,0x361f39a8,0xbaa7bc00,0x323eb61f,0x3bd939a8,0x38723b64,
0xb9a80000,0xb9a8bc00,0x39a88000,0x39a83c00,0x38723c00,0xbbd9b61f,0x323eb9a8,0x3aa73b64,0x3b648000,0xb61f39a8,0xb61fbc00,0x3b6439a8,0xb23ebc00,0x38723b64,0xbaa7b9a8,0x3bd9361f,
0xbc000000,0x3c008000,0xbc000000,0x3c008000,0xb23e3c00,0x3872bb64,0xbaa739a8,0x3bd9b61f,0x3b648000,0xb61fb9a8,0xb61f3c00,0x3b64b9a8,0x3872bc00,0xbbd9361f,0x323e39a8,0x3aa7bb64,
0xb9a80000,0xb9a83c00,0x39a88000,0x39a8bc00,0xbaa73c00,0x323e361f,0x3bd9b9a8,0x3872bb64,0x361f8000,0x3b64b9a8,0x3b64bc00,0x361fb9a8,0x3bd9bc00,0x3aa7bb64,0x3872b9a8,0x323eb61f,
0x80000000,0x80000000,0x80000000,0x80000000,0x323e0000,0x3872b61f,0x3aa7b9a8,0x3bd9bb64,0x361f0000,0x3b64b9a8,0x3b64bc00,0x361fb9a8,0x38720000,0x3bd9bb64,0x323eb9a8,0xbaa7361f,
0x39a80000,0x39a8bc00,0xb9a88000,0xb9a83c00,0x3aa70000,0x323ebb64,0xbbd939a8,0x3872361f,0x3b640000,0xb61fb9a8,0xb61f3c00,0x3b64b9a8,0x3bd90000,0xbaa7b61f,0x387239a8,0xb23ebb64,
0x3c000000,0xbc008000,0x3c000000,0xbc008000,0x3bd90000,0xbaa7361f,0x3872b9a8,0xb23e3b64,0x3b640000,0xb61f39a8,0xb61fbc00,0x3b6439a8,0x3aa70000,0x323e3b64,0xbbd9b9a8,0x3872b61f,
0x39a80000,0x39a83c00,0xb9a88000,0xb9a8bc00,0x38720000,0x3bd93b64,0x323e39a8,0xbaa7b61f,0x361f0000,0x3b6439a8,0x3b643c00,0x361f39a8,0x323e0000,0x3872361f,0x3aa739a8,0x3bd93b64,
0x80000000,0x80000000,0x80000000,0x80000000,0x3bd9bc00,0x3aa7bb64,0x3872b9a8,0x323eb61f,0xb61f8000,0xbb6439a8,0xbb643c00,0xb61f39a8,0xbaa73c00,0x323e361f,0x3bd9b9a8,0x3872bb64,
0x39a80000,0x39a8bc00,0xb9a88000,0xb9a83c00,0x3872bc00,0xbbd9361f,0x323e39a8,0x3aa7bb64,0xbb648000,0x361f39a8,0x361fbc00,0xbb6439a8,0xb23e3c00,0x3872bb64,0xbaa739a8,0x3bd9b61f,
0x3c000000,0xbc008000,0x3c000000,0xbc008000,0xb23ebc00,0x38723b64,0xbaa7b9a8,0x3bd9361f,0xbb648000,0x361fb9a8,0x361f3c00,0xbb64b9a8,0x38723c00,0xbbd9b61f,0x323eb9a8,0x3aa73b64,
0x39a80000,0x39a83c00,0xb9a88000,0xb9a8bc00,0xbaa7bc00,0x323eb61f,0x3bd939a8,0x38723b64,0xb61f8000,0xbb64b9a8,0xbb64bc00,0xb61fb9a8,0x3bd93c00,0x3aa73b64,0x387239a8,0x323e361f,
0x80000000,0x80000000,0x80000000,0x80000000,0xb23e8000,0xb872361f,0xbaa739a8,0xbbd93b64,0x361f0000,0x3b64b9a8,0x3b64bc00,0x361fb9a8,0xb8728000,0xbbd93b64,0xb23e39a8,0x3aa7b61f,
0x39a80000,0x39a8bc00,0xb9a88000,0xb9a83c00,0xbaa78000,0xb23e3b64,0x3bd9b9a8,0xb872b61f,0x3b640000,0xb61fb9a8,0xb61f3c00,0x3b64b9a8,0xbbd98000,0x3aa7361f,0xb872b9a8,0x323e3b64,
0x3c000000,0xbc008000,0x3c000000,0xbc008000,0xbbd98000,0x3aa7b61f,0xb87239a8,0x323ebb64,0x3b640000,0xb61f39a8,0xb61fbc00,0x3b6439a8,0xbaa78000,0xb23ebb64,0x3bd939a8,0xb872361f,
0x39a80000,0x39a83c00,0xb9a88000,0xb9a8bc00,0xb8728000,0xbbd9bb64,0xb23eb9a8,0x3aa7361f,0x361f0000,0x3b6439a8,0x3b643c00,0x361f39a8,0xb23e8000,0xb872b61f,0xbaa7b9a8,0xbbd9bb64,
0x80000000,0x80000000,0x80000000,0x80000000,0xbbd93c00,0xbaa73b64,0xb87239a8,0xb23e361f,0xb61f8000,0xbb6439a8,0xbb643c00,0xb61f39a8,0x3aa7bc00,0xb23eb61f,0xbbd939a8,0xb8723b64,
0x39a80000,0x39a8bc00,0xb9a88000,0xb9a83c00,0xb8723c00,0x3bd9b61f,0xb23eb9a8,0xbaa73b64,0xbb648000,0x361f39a8,0x361fbc00,0xbb6439a8,0x323ebc00,0xb8723b64,0x3aa7b9a8,0xbbd9361f,
0x3c000000,0xbc008000,0x3c000000,0xbc008000,0x323e3c00,0xb872bb64,0x3aa739a8,0xbbd9b61f,0xbb648000,0x361fb9a8,0x361f3c00,0xbb64b9a8,0xb872bc00,0x3bd9361f,0xb23e39a8,0xbaa7bb64,
0x39a80000,0x39a83c00,0xb9a88000,0xb9a8bc00,0x3aa73c00,0xb23e361f,0xbbd9b9a8,0xb872bb64,0xb61f8000,0xbb64b9a8,0xbb64bc00,0xb61fb9a8,0xbbd9bc00,0xbaa7bb64,0xb872b9a8,0xb23eb61f,
0x3c00,0x3c00,0x3c00,0x3c00,0x3c000000,0x3c000000,0x3c000000,0x3c000000,0x3c00,0x361f3b64,0x39a839a8,0x3b64361f,0x3c000000,0x3b64b61f,0x39a8b9a8,0x361fbb64,
0x3c00,0x39a839a8,0x3c000000,0x39a8b9a8,0x3c000000,0x39a8b9a8,0xbc00,0xb9a8b9a8,0x3c00,0x3b64361f,0x39a8b9a8,0xb61fbb64,0x3c000000,0x361fbb64,0xb9a8b9a8,0xbb64361f,
0x3c00,0x3c000000,0xbc00,0xbc008000,0x3c000000,0xbc00,0xbc008000,0x80003c00,0x3c00,0x3b64b61f,0xb9a8b9a8,0xb61f3b64,0x3c000000,0xb61fbb64,0xb9a839a8,0x3b64361f,
0x3c00,0x39a8b9a8,0xbc008000,0x39a839a8,0x3c000000,0xb9a8b9a8,0x80003c00,0x39a8b9a8,0x3c00,0x361fbb64,0xb9a839a8,0x3b64b61f,0x3c000000,0xbb64b61f,0x39a839a8,0xb61fbb64,
0x3c00,0x3c00,0x3c00,0x3c00,0x3c000000,0x3c000000,0x3c000000,0x3c000000,0x3c000000,0x3b64b61f,0x39a8b9a8,0x361fbb64,0xbc00,0xb61fbb64,0xb9a8b9a8,0xbb64b61f,
0xbc00,0xb9a8b9a8,0xbc008000,0xb9a839a8,0xbc008000,0xb9a839a8,0x80003c00,0x39a839a8,0xbc008000,0xb61f3b64,0x39a839a8,0x3b64b61f,0x80003c00,0x3b64361f,0x39a8b9a8,0xb61fbb64,
0x3c00,0x3c000000,0xbc00,0xbc008000,0x3c000000,0xbc00,0xbc008000,0x80003c00,0x3c000000,0xb61fbb64,0xb9a839a8,0x3b64361f,0xbc00,0xbb64361f,0x39a839a8,0x361fbb64,
0xbc00,0xb9a839a8,0x3c000000,0xb9a8b9a8,0xbc008000,0x39a839a8,0xbc00,0xb9a839a8,0xbc008000,0x3b64361f,0xb9a8b9a8,0x361f3b64,0x80003c00,0x361fbb64,0xb9a839a8,0x3b64b61f,
0x3c00,0x3c00,0x3c00,0x3c00,0x3c000000,0x3c000000,0x3c000000,0x3c000000,0xbc00,0xb61fbb64,0xb9a8b9a8,0xbb64b61f,0xbc008000,0xbb64361f,0xb9a839a8,0xb61f3b64,
0x3c00,0x39a839a8,0x3c000000,0x39a8b9a8,0x3c000000,0x39a8b9a8,0xbc00,0xb9a8b9a8,0xbc00,0xbb64b61f,0xb9a839a8,0x361f3b64,0xbc008000,0xb61f3b64,0x39a839a8,0x3b64b61f,
0x3c00,0x3c000000,0xbc00,0xbc008000,0x3c000000,0xbc00,0xbc008000,0x80003c00,0xbc00,0xbb64361f,0x39a839a8,0x361fbb64,0xbc008000,0x361f3b64,0x39a8b9a8,0xbb64b61f,
0x3c00,0x39a8b9a8,0xbc008000,0x39a839a8,0x3c000000,0xb9a8b9a8,0x80003c00,0x39a8b9a8,0xbc00,0xb61f3b64,0x39a8b9a8,0xbb64361f,0xbc008000,0x3b64361f,0xb9a8b9a8,0x361f3b64,
0x3c00,0x3c00,0x3c00,0x3c00,0x3c000000,0x3c000000,0x3c000000,0x3c000000,0xbc008000,0xbb64361f,0xb9a839a8,0xb61f3b64,0x80003c00,0x361f3b64,0x39a839a8,0x3b64361f,
0xbc00,0xb9a8b9a8,0xbc008000,0xb9a839a8,0xbc008000,0xb9a839a8,0x80003c00,0x39a839a8,0x3c000000,0x361fbb64,0xb9a8b9a8,0xbb64361f,0xbc00,0xbb64b61f,0xb9a839a8,0x361f3b64,
0x3c00,0x3c000000,0xbc00,0xbc008000,0x3c000000,0xbc00,0xbc008000,0x80003c00,0xbc008000,0x361f3b64,0x39a8b9a8,0xbb64b61f,0x80003c00,0x3b64b61f,0xb9a8b9a8,0xb61f3b64,
0xbc00,0xb9a839a8,0x3c000000,0xb9a8b9a8,0xbc008000,0x39a839a8,0xbc00,0xb9a839a8,0x3c000000,0xbb64b61f,0x39a839a8,0xb61fbb64,0xbc00,0xb61f3b64,0x39a8b9a8,0xbb64361f,
0x3d800000,0x3d800000,0x3d800000,0x3d800000,0x3d800000,0x3d7ffb11,0x3d7fec43,0x3d7fd397,0x3d800000,0x3d7fec43,0x3d7fb10f,0x3d7f4e6d,0x3d800000,0x3d7fd397,0x3d7f4e6d,0x3d7e70b0,
0x3d800000,0x3d7fb10f,0x3d7ec46d,0x3d7d3aac,0x3d800000,0x3d7f84ab,0x3d7e1324,0x3d7baccd,0x3d800000,0x3d7f4e6d,0x3d7d3aac,0x3d79c79d,0x3d800000,0x3d7f0e58,0x3d7c3b28,0x3d778bc5,
0x3d800000,0x3d7ec46d,0x3d7b14be,0x3d74fa0b,0x3d800000,0x3d7e70b0,0x3d79c79d,0x3d721352,0x3d800000,0x3d7e1324,0x3d7853f8,0x3d6ed89e,0x3d800000,0x3d7dabcc,0x3d76ba07,0x3d6b4b0c,
0x3d800000,0x3d7d3aac,0x3d74fa0b,0x3d676bd8,0x3d800000,0x3d7cbfc9,0x3d731447,0x3d633c5a,0x3d800000,0x3d7c3b28,0x3d710908,0x3d5ebe05,0x3d800000,0x3d7baccd,0x3d6ed89e,0x3d59f26a,
0x3d800000,0x3d800000,0x3d800000,0x3d800000,0x3d7fb10f,0x3d7f84ab,0x3d7f4e6d,0x3d7f0e58,0x3d7ec46d,0x3d7e1324,0x3d7d3aac,0x3d7c3b28,0x3d7d3aac,0x3d7baccd,0x3d79c79d,0x3d778bc5,
0x3d7b14be,0x3d7853f8,0x3d74fa0b,0x3d710908,0x3d7853f8,0x3d740bdd,0x3d6ed89e,0x3d68bf3c,0x3d74fa0b,0x3d6ed89e,0x3d676bd8,0x3d5ebe05,0x3d710908,0x3d68bf3c,0x3d5ebe05,0x3d531849,
0x3d6c835e,0x3d61c598,0x3d54db31,0x3d45e403,0x3d676bd8,0x3d59f26a,0x3d49d112,0x3d373a23,0x3d61c598,0x3d514d3d,0x3d3daef9,0x3d273656,0x3d5b941a,0x3d47de65,0x3d3085bb,0x3d15f6d9,
0x3d54db31,0x3d3daef9,0x3d226799,0x3d039c3d,0x3d4d9f02,0x3d32c8c9,0x3d13682a,0x3ce0924f,0x3d45e403,0x3d273656,0x3d039c3d,0x3cb8442a,0x3d3daef9,0x3d1b02c6,0x3ce63375,0x3c8e9a22,
0x3d800000,0x3d800000,0x3d800000,0x3d800000,0x3d7ec46d,0x3d7e70b0,0x3d7e1324,0x3d7dabcc,0x3d7b14be,0x3d79c79d,0x3d7853f8,0x3d76ba07,0x3d74fa0b,0x3d721352,0x3d6ed89e,0x3d6b4b0c,
0x3d6c835e,0x3d676bd8,0x3d61c598,0x3d5b941a,0x3d61c598,0x3d59f26a,0x3d514d3d,0x3d47de65,0x3d54db31,0x3d49d112,0x3d3daef9,0x3d3085bb,0x3d45e403,0x3d373a23,0x3d273656,0x3d15f6d9,
0x3d3504f3,0x3d226799,0x3d0e39da,0x3cf15aea,0x3d226799,0x3d0b9a6b,0x3ce63375,0x3cb263ef,0x3d0e39da,0x3ce63375,0x3cac7cd4,0x3c605c13,0x3cf15aea,0x3cb263ef,0x3c605c13,0x3bafb680,
0x3cc3ef15,0x3c78cfcc,0x3bc8bd36,0xbb48fb30,0x3c94a031,0x3c09cf86,0xbac90ab0,0xbc3b6ecf,0x3c47c5c2,0x3ac90ab0,0xbc164083,0xbca09ae5,0x3bc8bd36,0xbbafb680,0xbc888e93,0xbce0924f,
0x3d800000,0x3d800000,0x3d800000,0x3d800000,0x3d7d3aac,0x3d7cbfc9,0x3d7c3b28,0x3d7baccd,0x3d74fa0b,0x3d731447,0x3d710908,0x3d6ed89e,0x3d676bd8,0x3d633c5a,0x3d5ebe05,0x3d59f26a,
0x3d54db31,0x3d4d9f02,0x3d45e403,0x3d3daef9,0x3d3daef9,0x3d32c8c9,0x3d273656,0x3d1b02c6,0x3d226799,0x3d13682a,0x3d039c3d,0x3ce63375,0x3d039c3d,0x3ce0924f,0x3cb8442a,0x3c8e9a22,
0x3cc3ef15,0x3c94a031,0x3c47c5c2,0x3bc8bd36,0x3c78cfcc,0x3c09cf86,0x3ac90ab0,0xbbafb680,0x3bc8bd36,0xbac90ab0,0xbc164083,0xbc888e93,0xbb48fb30,0xbc3b6ecf,0xbca09ae5,0xbce0924f,
0xbc47c5c2,0xbcac7cd4,0xbcf15aea,0xbd187fc0,0xbcac7cd4,0xbcf6e0cb,0xbd1d7fd1,0xbd3b8f3b,0xbcf15aea,0xbd1d7fd1,0xbd3daef9,0xbd584853,0xbd187fc0,0xbd3b8f3b,0xbd584853,0xbd6db293,
0x0,0x0,0x0,0x0,0x0,0x3a490e90,0x3ac90ab0,0x3b16c32c,0x0,0x3ac90ab0,0x3b48fb30,0x3b96a905,0x0,0x3b16c32c,0x3b96a905,0x3be1bc2e,
0x0,0x3b48fb30,0x3bc8bd36,0x3c164083,0x0,0x3b7b2b74,0x3bfab273,0x3c3b6ecf,0x0,0x3b96a905,0x3c164083,0x3c605c13,0x0,0x3bafb680,0x3c2f10a2,0x3c827dc0,
0x0,0x3bc8bd36,0x3c47c5c2,0x3c94a031,0x0,0x3be1bc2e,0x3c605c13,0x3ca68f12,0x0,0x3bfab273,0x3c78cfcc,0x3cb8442a,0x0,0x3c09cf86,0x3c888e93,0x3cc9b953,
0x0,0x3c164083,0x3c94a031,0x3cdae880,0x0,0x3c22abb6,0x3ca09ae5,0x3cebcbbb,0x0,0x3c2f10a2,0x3cac7cd4,0x3cfc5d27,0x0,0x3c3b6ecf,0x3cb8442a,0x3d064b82,
0x0,0x0,0x0,0x0,0x3b48fb30,0x3b7b2b74,0x3b96a905,0x3bafb680,0x3bc8bd36,0x3bfab273,0x3c164083,0x3c2f10a2,0x3c164083,0x3c3b6ecf,0x3c605c13,0x3c827dc0,
0x3c47c5c2,0x3c78cfcc,0x3c94a031,0x3cac7cd4,0x3c78cfcc,0x3c9aa086,0x3cb8442a,0x3cd53641,0x3c94a031,0x3cb8442a,0x3cdae880,0x3cfc5d27,0x3cac7cd4,0x3cd53641,0x3cfc5d27,0x3d10d3cd,
0x3cc3ef15,0x3cf15aea,0x3d0e39da,0x3d226799,0x3cdae880,0x3d064b82,0x3d1d7fd1,0x3d32c8c9,0x3cf15aea,0x3d13682a,0x3d2beb4a,0x3d41d870,0x3d039c3d,0x3d1ff6cb,0x3d396842,0x3d4f7a1f,
0x3d0e39da,0x3d2beb4a,0x3d45e403,0x3d5b941a,0x3d187fc0,0x3d373a23,0x3d514d3d,0x3d660f88,0x3d226799,0x3d41d870,0x3d5b941a,0x3d6ed89e,0x3d2beb4a,0x3d4bbbf8,0x3d64aa59,0x3d75dec6,
0x0,0x0,0x0,0x0,0x3bc8bd36,0x3be1bc2e,0x3bfab273,0x3c09cf86,0x3c47c5c2,0x3c605c13,0x3c78cfcc,0x3c888e93,0x3c94a031,0x3ca68f12,0x3cb8442a,0x3cc9b953,
0x3cc3ef15,0x3cdae880,0x3cf15aea,0x3d039c3d,0x3cf15aea,0x3d064b82,0x3d13682a,0x3d1ff6cb,0x3d0e39da,0x3d1d7fd1,0x3d2beb4a,0x3d396842,0x3d226799,0x3d32c8c9,0x3d41d870,0x3d4f7a1f,
0x3d3504f3,0x3d45e403,0x3d54db31,0x3d61c598,0x3d45e403,0x3d5695e5,0x3d64aa59,0x3d6ff573,0x3d54db31,0x3d64aa59,0x3d710908,0x3d79c79d,0x3d61c598,0x3d6ff573,0x3d79c79d,0x3d7f0e58,
0x3d6c835e,0x3d7853f8,0x3d7ec46d,0x3d7fb10f,0x3d74fa0b,0x3d7dabcc,0x3d7fec43,0x3d7baccd,0x3d7b14be,0x3d7fec43,0x3d7d3aac,0x3d731447,0x3d7ec46d,0x3d7f0e58,0x3d76ba07,0x3d660f88,
0x0,0x0,0x0,0x0,0x3c164083,0x3c22abb6,0x3c2f10a2,0x3c3b6ecf,0x3c94a031,0x3ca09ae5,0x3cac7cd4,0x3cb8442a,0x3cdae880,0x3cebcbbb,0x3cfc5d27,0x3d064b82,
0x3d0e39da,0x3d187fc0,0x3d226799,0x3d2beb4a,0x3d2beb4a,0x3d373a23,0x3d41d870,0x3d4bbbf8,0x3d45e403,0x3d514d3d,0x3d5b941a,0x3d64aa59,0x3d5b941a,0x3d660f88,0x3d6ed89e,0x3d75dec6,
0x3d6c835e,0x3d74fa0b,0x3d7b14be,0x3d7ec46d,0x3d7853f8,0x3d7dabcc,0x3d7fec43,0x3d7f0e58,0x3d7ec46d,0x3d7fec43,0x3d7d3aac,0x3d76ba07,0x3d7fb10f,0x3d7baccd,0x3d731447,0x3d660f88,
0x3d7b14be,0x3d710908,0x3d61c598,0x3d4d9f02,0x3d710908,0x3d604621,0x3d49d112,0x3d2e3bde,0x3d61c598,0x3d49d112,0x3d2beb4a,0x3d08f59b,0x3d4d9f02,0x3d2e3bde,0x3d08f59b,0x3cbe1d4a,
0x3d800000,0x3d7b14be,0x3d6c835e,0x3d54db31,0x3d800000,0x3d7a7302,0x3d6a09a7,0x3d4f7a1f,0x3d800000,0x3d79c79d,0x3d676bd8,0x3d49d112,0x3d800000,0x3d791298,0x3d64aa59,0x3d43e200,
0x3d800000,0x3d7853f8,0x3d61c598,0x3d3daef9,0x3d800000,0x3d778bc5,0x3d5ebe05,0x3d373a23,0x3d800000,0x3d76ba07,0x3d5b941a,0x3d3085bb,0x3d800000,0x3d75dec6,0x3d584853,0x3d299415,
0x3d800000,0x3d74fa0b,0x3d54db31,0x3d226799,0x3d800000,0x3d740bdd,0x3d514d3d,0x3d1b02c6,0x3d800000,0x3d731447,0x3d4d9f02,0x3d13682a,0x3d800000,0x3d721352,0x3d49d112,0x3d0b9a6b,
0x3d800000,0x3d710908,0x3d45e403,0x3d039c3d,0x3d800000,0x3d6ff573,0x3d41d870,0x3cf6e0cb,0x3d800000,0x3d6ed89e,0x3d3daef9,0x3ce63375,0x3d800000,0x3d6db293,0x3d396842,0x3cd53641,
0x3d3504f3,0x3d0e39da,0x3cc3ef15,0x3c47c5c2,0x3d2beb4a,0x3d00e7e4,0x3ca09ae5,0x3be1bc2e,0x3d226799,0x3ce63375,0x3c78cfcc,0x3ac90ab0,0x3d187fc0,0x3cc9b953,0x3c2f10a2,0xbb7b2b74,
0x3d0e39da,0x3cac7cd4,0x3bc8bd36,0xbc164083,0x3d039c3d,0x3c8e9a22,0x3ac90ab0,0xbc6c9a7f,0x3cf15aea,0x3c605c13,0xbb48fb30,0xbca09ae5,0x3cdae880,0x3c22abb6,0xbbfab273,0xbcc9b953,
0x3cc3ef15,0x3bc8bd36,0xbc47c5c2,0xbcf15aea,0x3cac7cd4,0x3b16c32c,0xbc888e93,0xbd0b9a6b,0x3c94a031,0xbac90ab0,0xbcac7cd4,0xbd1d7fd1,0x3c78cfcc,0xbbafb680,0xbccf7bca,0xbd2e3bde,
0x3c47c5c2,0xbc164083,0xbcf15aea,0xbd3daef9,0x3c164083,0xbc541501,0xbd08f59b,0xbd4bbbf8,0x3bc8bd36,0xbc888e93,0xbd187fc0,0xbd584853,0x3b48fb30,0xbca68f12,0xbd273656,0xbd633c5a,
0x228d3132,0xbc47c5c2,0xbcc3ef15,0xbd0e39da,0xbbc8bd36,0xbc9aa086,0xbcfc5d27,0xbd299415,0xbc47c5c2,0xbccf7bca,0xbd187fc0,0xbd41d870,0xbc94a031,0xbd00e7e4,0xbd3085bb,0xbd5695e5,
0xbcc3ef15,0xbd187fc0,0xbd45e403,0xbd676bd8,0xbcf15aea,0xbd2e3bde,0xbd584853,0xbd740bdd,0xbd0e39da,0xbd41d870,0xbd676bd8,0xbd7c3b28,0xbd226799,0xbd531849,0xbd731447,0xbd7fd397,
0xbd3504f3,0xbd61c598,0xbd7b14be,0xbd7ec46d,0xbd45e403,0xbd6db293,0xbd7f4e6d,0xbd791298,0xbd54db31,0xbd76ba07,0xbd7fb10f,0xbd6ed89e,0xbd61c598,0xbd7cbfc9,0xbd7c3b28,0xbd604621,
0xbd6c835e,0xbd7fb10f,0xbd74fa0b,0xbd4d9f02,0xbd74fa0b,0xbd7f84ab,0xbd6a09a7,0xbd373a23,0xbd7b14be,0xbd7c3b28,0xbd5b941a,0xbd1d7fd1,0xbd7ec46d,0xbd75dec6,0xbd49d112,0xbd00e7e4,
0xbd3504f3,0xbd54db31,0xbd6c835e,0xbd7b14be,0xbd4d9f02,0xbd68bf3c,0xbd79c79d,0xbd7ffb11,0xbd61c598,0xbd76ba07,0xbd7fb10f,0xbd7c3b28,0xbd710908,0xbd7e70b0,0xbd7e1324,0xbd6ff573,
0xbd7b14be,0xbd7fb10f,0xbd74fa0b,0xbd5b941a,0xbd7fb10f,0xbd7a7302,0xbd64aa59,0xbd3fc767,0xbd7ec46d,0xbd6ed89e,0xbd4d9f02,0xbd1d7fd1,0xbd7853f8,0xbd5d2d53,0xbd3085bb,0xbcebcbbb,
0xbd6c835e,0xbd45e403,0xbd0e39da,0xbc94a031,0xbd5b941a,0xbd299415,0xbccf7bca,0xbbe1bc2e,0xbd45e403,0xbd08f59b,0xbc78cfcc,0x3b96a905,0xbd2beb4a,0xbcc9b953,0xbb96a905,0x3c827dc0,
0xbd0e39da,0xbc78cfcc,0x3bc8bd36,0x3cdae880,0xbcdae880,0xbbafb680,0x3c888e93,0x3d15f6d9,0xbc94a031,0x3b96a905,0x3cdae880,0x3d396842,0xbc164083,0x3c6c9a7f,0x3d13682a,0x3d5695e5,
0x0,0x3c47c5c2,0x3cc3ef15,0x3d0e39da,0x0,0x3c541501,0x3ccf7bca,0x3d15f6d9,0x0,0x3c605c13,0x3cdae880,0x3d1d7fd1,0x0,0x3c6c9a7f,0x3ce63375,0x3d24d225,
0x0,0x3c78cfcc,0x3cf15aea,0x3d2beb4a,0x0,0x3c827dc0,0x3cfc5d27,0x3d32c8c9,0x0,0x3c888e93,0x3d039c3d,0x3d396842,0x0,0x3c8e9a22,0x3d08f59b,0x3d3fc767,
0x0,0x3c94a031,0x3d0e39da,0x3d45e403,0x0,0x3c9aa086,0x3d13682a,0x3d4bbbf8,0x0,0x3ca09ae5,0x3d187fc0,0x3d514d3d,0x0,0x3ca68f12,0x3d1d7fd1,0x3d5695e5,
0x0,0x3cac7cd4,0x3d226799,0x3d5b941a,0x0,0x3cb263ef,0x3d273656,0x3d604621,0x0,0x3cb8442a,0x3d2beb4a,0x3d64aa59,0x0,0x3cbe1d4a,0x3d3085bb,0x3d68bf3c,
0x3d3504f3,0x3d54db31,0x3d6c835e,0x3d7b14be,0x3d3daef9,0x3d5d2d53,0x3d731447,0x3d7e70b0,0x3d45e403,0x3d64aa59,0x3d7853f8,0x3d7fec43,0x3d4d9f02,0x3d6b4b0c,0x3d7c3b28,0x3d7f84ab,
0x3d54db31,0x3d710908,0x3d7ec46d,0x3d7d3aac,0x3d5b941a,0x3d75dec6,0x3d7fec43,0x3d791298,0x3d61c598,0x3d79c79d,0x3d7fb10f,0x3d731447,0x3d676bd8,0x3d7cbfc9,0x3d7e1324,0x3d6b4b0c,
0x3d6c835e,0x3d7ec46d,0x3d7b14be,0x3d61c598,0x3d710908,0x3d7fd397,0x3d76ba07,0x3d5695e5,0x3d74fa0b,0x3d7fec43,0x3d710908,0x3d49d112,0x3d7853f8,0x3d7f0e58,0x3d6a09a7,0x3d3b8f3b,
0x3d7b14be,0x3d7d3aac,0x3d61c598,0x3d2beb4a,0x3d7d3aac,0x3d7a7302,0x3d584853,0x3d1b02c6,0x3d7ec46d,0x3d76ba07,0x3d4d9f02,0x3d08f59b,0x3d7fb10f,0x3d721352,0x3d41d870,0x3cebcbbb,
0x3d800000,0x3d7b14be,0x3d6c835e,0x3d54db31,0x3d7ec46d,0x3d740bdd,0x3d5ebe05,0x3d3fc767,0x3d7b14be,0x3d6a09a7,0x3d4d9f02,0x3d273656,0x3d74fa0b,0x3d5d2d53,0x3d396842,0x3d0b9a6b,
0x3d6c835e,0x3d4d9f02,0x3d226799,0x3cdae880,0x3d61c598,0x3d3b8f3b,0x3d08f59b,0x3c9aa086,0x3d54db31,0x3d273656,0x3cdae880,0x3c2f10a2,0x3d45e403,0x3d10d3cd,0x3ca09ae5,0x3b16c32c,
0x3d3504f3,0x3cf15aea,0x3c47c5c2,0xbbc8bd36,0x3d226799,0x3cbe1d4a,0x3b96a905,0xbc6c9a7f,0x3d0e39da,0x3c888e93,0xbb48fb30,0xbcb8442a,0x3cf15aea,0x3c22abb6,0xbc2f10a2,0xbcf6e0cb,
0x3cc3ef15,0x3b48fb30,0xbc94a031,0xbd187fc0,0x3c94a031,0xbb7b2b74,0xbccf7bca,0xbd32c8c9,0x3c47c5c2,0xbc2f10a2,0xbd039c3d,0xbd49d112,0x3bc8bd36,0xbc8e9a22,0xbd1d7fd1,0xbd5d2d53,
0x3d3504f3,0x3d0e39da,0x3cc3ef15,0x3c47c5c2,0x3d187fc0,0x3cd53641,0x3c605c13,0x3a490e90,0x3cf15aea,0x3c888e93,0x3b48fb30,0xbc2f10a2,0x3cac7cd4,0x3be1bc2e,0xbbfab273,0xbcb263ef,
0x3c47c5c2,0xbb48fb30,0xbc94a031,0xbd039c3d,0x3b48fb30,0xbc541501,0xbce63375,0xbd299415,0xbbc8bd36,0xbcb8442a,0xbd187fc0,0xbd49d112,0xbc78cfcc,0xbd00e7e4,0xbd396842,0xbd633c5a,
0xbcc3ef15,0xbd226799,0xbd54db31,0xbd74fa0b,0xbd039c3d,0xbd3fc767,0xbd6a09a7,0xbd7e70b0,0xbd226799,0xbd584853,0xbd7853f8,0xbd7f4e6d,0xbd3daef9,0xbd6b4b0c,0xbd7f4e6d,0xbd778bc5,
0xbd54db31,0xbd7853f8,0xbd7ec46d,0xbd676bd8,0xbd676bd8,0xbd7f0e58,0xbd76ba07,0xbd4f7a1f,0xbd74fa0b,0xbd7f4e6d,0xbd676bd8,0xbd3085bb,0xbd7d3aac,0xbd791298,0xbd514d3d,0xbd0b9a6b,
};

struct Params {
  const void* x; const void* wr; const void* wi; const void* wq; const void* wk; const void* wv; const void* wo;
  void* out; char* ws;
};
extern __shared__ __attribute__((aligned(16))) char smem_raw[];

__global__ void __launch_bounds__(512, 2) k_main(Params p) {
  unsigned tid = threadIdx.x;
  asm volatile(KERNEL_BODY ADUMP : "+{v0}"(tid)
               : [b] "s"(blockIdx.x), [ka] "s"(__builtin_amdgcn_kernarg_segment_ptr()), [tab] "s"(TABLES)
               : "memory", "scc", "vcc", KERNEL_CLOB);
}

extern "C" void kernel_launch(void* const* d_in, const int* in_sizes, int n_in, void* d_out, int out_size, void* d_ws, size_t ws_size, hipStream_t stream) {
  Params p;
  p.x = d_in[0]; p.wr = d_in[1]; p.wi = d_in[2]; p.wq = d_in[4]; p.wk = d_in[5]; p.wv = d_in[6]; p.wo = d_in[7];
  p.out = d_out; p.ws = (char*)d_ws;
  (void)hipFuncSetAttribute((const void*)k_main, hipFuncAttributeMaxDynamicSharedMemorySize, 163840);
  hipLaunchKernelGGL(k_main, dim3(256), dim3(512), 163840, stream, p);
}
```
